```python
import torch

def _softmax(x, axis=-1):
    return torch.softmax(x, dim=axis)

def _gelu(x):
    return torch.nn.functional.gelu(x, approximate='tanh')

def _silu(x):
    return torch.nn.functional.silu(x)

def _sigmoid(x):
    return torch.sigmoid(x)

def _layernorm(x, axis=-1, eps=1e-05):
    m = x.mean(dim=axis, keepdim=True)
    v = x.var(dim=axis, keepdim=True, unbiased=False)
    return (x - m) / torch.sqrt(v + eps)

def _scatter_add(tgt, idx, src, axis=0):
    return tgt.clone().index_add_(axis, idx, src)

def _segment_sum(src, seg, n):
    out = torch.zeros((n,) + src.shape[1:], dtype=src.dtype)
    return out.index_add_(0, seg, src)

def _pad(x, pw):
    flat = tuple((v for pair in reversed(pw) for v in pair))
    return torch.nn.functional.pad(x, flat)
kernel_name = 'adaLN_cond__ssm_conv_s4__glu_gated_reglu__prompt'

def setup_inputs():

    _w = lambda a: (lambda t: t.to(torch.bfloat16) if t.is_floating_point() else t)(torch.as_tensor(a))

    def _A_setup():
        B, L, D, N = (1, 2048, 576, 128)
        A_re = -(lambda _x: torch.exp(torch.as_tensor(_x)))((lambda *s: torch.randn(*s, dtype=torch.bfloat16) * 3.2)(D, N))
        A_im = torch.pi * (lambda *s: torch.randn(*s, dtype=torch.bfloat16) * 0.05)(D, N)
        B_re = (lambda *s: torch.randn(*s, dtype=torch.bfloat16) * 0.1)(D, N)
        B_im = (lambda *s: torch.randn(*s, dtype=torch.bfloat16) * 0.1)(D, N)
        C_re = (lambda *s: torch.randn(*s, dtype=torch.bfloat16) * 0.1)(D, N)
        C_im = (lambda *s: torch.randn(*s, dtype=torch.bfloat16) * 0.1)(D, N)
        log_dt = (lambda *s: torch.randn(*s, dtype=torch.bfloat16) * 0.05)(D) * 0.5 - 4.0
        Dskip = (lambda *s: torch.randn(*s, dtype=torch.bfloat16) * 0.05)(D)
        return {'u': _w((lambda *s: torch.randn(*s, dtype=torch.bfloat16) * 0.05)(B, L, D)), 'A_re': _w(A_re), 'A_im': _w(A_im), 'B_re': _w(B_re), 'B_im': _w(B_im), 'C_re': _w(C_re), 'C_im': _w(C_im), 'log_dt': _w(log_dt), 'Dskip': _w(Dskip)}
    _A_in = _A_setup()

    def _B_setup():
        B, S, D, F = (1, 2048, 576, 4608)
        return {'x': _w((lambda *s: torch.randn(*s, dtype=torch.bfloat16) * 0.05)(B, S, D)), 'W_gate': _w((lambda *s: torch.randn(*s, dtype=torch.bfloat16) * 0.8)(D, F) * (2.0 / D) ** 0.5), 'W_up': _w((lambda *s: torch.randn(*s, dtype=torch.bfloat16) * 0.8)(D, F) * (2.0 / D) ** 0.5), 'W_down': _w((lambda *s: torch.randn(*s, dtype=torch.bfloat16) * 0.8)(F, D) * (1.0 / F) ** 0.5)}
    _B_in = _B_setup()
    return {'A_u': _A_in['u'], 'A_A_re': _A_in['A_re'], 'A_A_im': _A_in['A_im'], 'A_B_re': _A_in['B_re'], 'A_B_im': _A_in['B_im'], 'A_C_re': _A_in['C_re'], 'A_C_im': _A_in['C_im'], 'A_log_dt': _A_in['log_dt'], 'A_Dskip': _A_in['Dskip'], 'B_x': _B_in['x'], 'B_W_gate': _B_in['W_gate'], 'B_W_up': _B_in['W_up'], 'B_W_down': _B_in['W_down'], 'c': _w((lambda *s: torch.randn(*s, dtype=torch.bfloat16) * 0.2)(1, 576) * 8.0), 'w_mod': _w((lambda *s: torch.randn(*s, dtype=torch.bfloat16) * 0.2)(576, 6 * 576) * 576 ** (-0.5) * 8.0)}

def reference(A_u, A_A_re, A_A_im, A_B_re, A_B_im, A_C_re, A_C_im, A_log_dt, A_Dskip, B_x, B_W_gate, B_W_up, B_W_down, c, w_mod):

    def _A_ref(u, A_re, A_im, B_re, B_im, C_re, C_im, log_dt, Dskip):
        Bsz, L, D = u.shape
        dt = torch.exp(log_dt)[:, None]
        A = A_re + 1j * A_im
        Bc = B_re + 1j * B_im
        Cc = C_re + 1j * C_im
        logdA = dt * A
        dB = dt * Bc
        l = (lambda x, dt: x.to(dt))(torch.arange(L), A_re.dtype)
        P = torch.exp(logdA[:, :, None] * l[None, None, :])
        K = torch.einsum('dn,dnl,dn->dl', Cc, P, dB).real
        L2 = 2 * L
        Kp = _pad(K, ((0, 0), (0, L)))
        up = (lambda x, *d: x.permute(*d))(u, 0, 2, 1)
        up = _pad(up, ((0, 0), (0, 0), (0, L)))
        Kf = (lambda x, axes=(-2, -1): torch.fft.rfft2(x.float(), dim=axes))(Kp[:, None, :], axes=(-2, -1))
        Uf = (lambda x, axes=(-2, -1): torch.fft.rfft2(x.float(), dim=axes))(up[:, :, None, :], axes=(-2, -1))
        Yf = Uf * Kf[None, :, :, :]
        y = (lambda x, s, axes=(-2, -1): torch.fft.irfft2(x, s=s, dim=axes).to(torch.bfloat16))(Yf, (1, L2), axes=(-2, -1))[:, :, 0, :L]
        y = (lambda x, *d: x.permute(*d))(y, 0, 2, 1) + u * Dskip[None, None, :]
        return y

    def _B_ref(x, W_gate, W_up, W_down):
        gate = torch.einsum('bsd,df->bsf', x, W_gate)
        up = torch.einsum('bsd,df->bsf', x, W_up)
        gate = gate * (lambda x, dt: x.to(dt))(gate > 0, gate.dtype)
        h = gate * up
        return torch.einsum('bsf,fd->bsd', h, W_down)
    x = A_u
    mod = (_silu(c) @ w_mod).reshape(c.shape[0], 6, 1, -1)
    sa, ca, ga, sb, cb, gb = (mod[:, 0].clone(), mod[:, 1].clone(), mod[:, 2].clone(), mod[:, 3].clone(), mod[:, 4].clone(), mod[:, 5].clone())
    h = _layernorm(x, axis=-1) * (1 + ca) + sa
    x = x + ga * _A_ref(h, A_A_re, A_A_im, A_B_re, A_B_im, A_C_re, A_C_im, A_log_dt, A_Dskip)
    h = _layernorm(x, axis=-1) * (1 + cb) + sb
    x = x + gb * _B_ref(h, B_W_gate, B_W_up, B_W_down)
    return x
```

```cpp
#include <hip/hip_runtime.h>
#include <hip/hip_bf16.h>
#include <stdint.h>

typedef unsigned short u16;
constexpr int L = 2048, D = 576, N = 128, F = 4608;

__device__ __forceinline__ float bf2f(u16 x) { return __uint_as_float(((unsigned)x) << 16); }
__device__ __forceinline__ u16 f2bf(float x) {
  unsigned u = __float_as_uint(x);
  u += 0x7fffu + ((u >> 16) & 1u);
  return (u16)(u >> 16);
}
__device__ __forceinline__ float rbf(float x) { return bf2f(f2bf(x)); }

__global__ void k_mod(const u16* c, const u16* w, float* mod) {
  int j = blockIdx.x * 64 + threadIdx.x;
  float acc = 0.f;
  for (int k = 0; k < D; ++k) {
    float cv = bf2f(c[k]);
    float s = rbf(cv / (1.f + __expf(-cv)));
    acc += s * bf2f(w[k * 6 * D + j]);
  }
  mod[j] = rbf(acc);
}

__global__ void k_gen(const u16* Are, const u16* Aim, const u16* Bre, const u16* Bim, const u16* Cre,
                      const u16* Cim, const u16* logdt, float* K) {
  int d = blockIdx.x;
  __shared__ float sa[N], sb[N], cr[N], ci[N];
  float dt = rbf(expf(bf2f(logdt[d])));
  if (threadIdx.x < N) {
    int n = threadIdx.x;
    sa[n] = dt * bf2f(Are[d * N + n]);
    sb[n] = dt * bf2f(Aim[d * N + n]);
    float br = dt * bf2f(Bre[d * N + n]), bi = dt * bf2f(Bim[d * N + n]);
    float c_r = bf2f(Cre[d * N + n]), c_i = bf2f(Cim[d * N + n]);
    cr[n] = c_r * br - c_i * bi;
    ci[n] = c_r * bi + c_i * br;
  }
  __syncthreads();
  for (int l = threadIdx.x; l < L; l += blockDim.x) {
    float lq = rbf((float)l);
    float acc = 0.f;
    for (int n = 0; n < N; ++n) {
      float e = expf(sa[n] * lq);
      float s, c;
      sincosf(sb[n] * lq, &s, &c);
      acc += e * (cr[n] * c - ci[n] * s);
    }
    K[d * L + l] = acc;
  }
}

__global__ void k_ln(const u16* x, const float* shift, const float* scale, u16* h) {
  int t = blockIdx.x * 4 + (threadIdx.x >> 6);
  int lane = threadIdx.x & 63;
  float v[9];
  float s = 0.f;
  for (int i = 0; i < 9; ++i) { v[i] = bf2f(x[t * D + lane + 64 * i]); s += v[i]; }
  for (int o = 32; o; o >>= 1) s += __shfl_xor(s, o);
  float m = s / D;
  float q = 0.f;
  for (int i = 0; i < 9; ++i) { float dd = v[i] - m; q += dd * dd; }
  for (int o = 32; o; o >>= 1) q += __shfl_xor(q, o);
  float r = rsqrtf(q / D + 1e-5f);
  for (int i = 0; i < 9; ++i) {
    int d = lane + 64 * i;
    h[t * D + d] = f2bf((v[i] - m) * r * rbf(1.f + scale[d]) + shift[d]);
  }
}

__global__ void k_conv(const u16* x, const u16* h, const float* K, const u16* Dskip, const float* ga, u16* x1) {
  int d = blockIdx.x, tb = blockIdx.y;
  __shared__ float sK[L], sh[L];
  int tmax = tb * 256 + 256;
  for (int i = threadIdx.x; i < tmax; i += 256) { sK[i] = K[d * L + i]; sh[i] = bf2f(h[i * D + d]); }
  __syncthreads();
  int t = tb * 256 + threadIdx.x;
  float acc = 0.f;
  for (int s = 0; s <= t; ++s) acc += sK[t - s] * sh[s];
  float y = rbf(rbf(acc) + rbf(sh[t] * bf2f(Dskip[d])));
  x1[t * D + d] = f2bf(bf2f(x[t * D + d]) + rbf(ga[d] * y));
}

typedef __attribute__((ext_vector_type(8))) short bf16x8;
typedef __attribute__((ext_vector_type(16))) float f32x16;

__global__ void k_gemm1(const u16* h, const u16* Wg, const u16* Wu, u16* hff) {
  int lane = threadIdx.x & 63, w = threadIdx.x >> 6;
  int m0 = blockIdx.y * 128 + w * 32, n0 = blockIdx.x * 32;
  int r = lane & 31, hh = lane >> 5;
  f32x16 g = {}, u = {};
  for (int k0 = 0; k0 < D; k0 += 16) {
    bf16x8 a = *(const bf16x8*)(h + (m0 + r) * D + k0 + 8 * hh);
    bf16x8 bg, bu;
    for (int j = 0; j < 8; ++j) {
      bg[j] = Wg[(k0 + 8 * hh + j) * F + n0 + r];
      bu[j] = Wu[(k0 + 8 * hh + j) * F + n0 + r];
    }
    g = __builtin_amdgcn_mfma_f32_32x32x16_bf16(a, bg, g, 0, 0, 0);
    u = __builtin_amdgcn_mfma_f32_32x32x16_bf16(a, bu, u, 0, 0, 0);
  }
  for (int i = 0; i < 16; ++i) {
    int row = (i & 3) + 8 * (i >> 2) + 4 * hh;
    float gv = rbf(g[i]), uv = rbf(u[i]);
    hff[(m0 + row) * F + n0 + r] = f2bf(fmaxf(gv, 0.f) * uv);
  }
}
__global__ void k_gemm2(const u16* hff, const u16* Wd, const u16* x1, const float* gb, u16* out) {
  int lane = threadIdx.x & 63, w = threadIdx.x >> 6;
  int m0 = blockIdx.y * 128 + w * 32, n0 = blockIdx.x * 32;
  int r = lane & 31, hh = lane >> 5;
  f32x16 g = {};
  for (int k0 = 0; k0 < F; k0 += 16) {
    bf16x8 a = *(const bf16x8*)(hff + (m0 + r) * F + k0 + 8 * hh);
    bf16x8 b;
    for (int j = 0; j < 8; ++j) b[j] = Wd[(k0 + 8 * hh + j) * D + n0 + r];
    g = __builtin_amdgcn_mfma_f32_32x32x16_bf16(a, b, g, 0, 0, 0);
  }
  for (int i = 0; i < 16; ++i) {
    int row = (i & 3) + 8 * (i >> 2) + 4 * hh;
    int idx = (m0 + row) * D + n0 + r;
    out[idx] = f2bf(bf2f(x1[idx]) + rbf(gb[n0 + r] * rbf(g[i])));
  }
}

extern "C" void kernel_launch(void* const* d_in, const int* in_sizes, int n_in, void* d_out, int out_size,
                              void* d_ws, size_t ws_size, hipStream_t stream) {
  const u16* u = (const u16*)d_in[0];
  const u16 *Are = (const u16*)d_in[1], *Aim = (const u16*)d_in[2], *Bre = (const u16*)d_in[3],
            *Bim = (const u16*)d_in[4], *Cre = (const u16*)d_in[5], *Cim = (const u16*)d_in[6],
            *logdt = (const u16*)d_in[7], *Dskip = (const u16*)d_in[8];
  const u16 *Wg = (const u16*)d_in[10], *Wu = (const u16*)d_in[11], *Wd = (const u16*)d_in[12];
  const u16 *c = (const u16*)d_in[13], *wmod = (const u16*)d_in[14];
  char* ws = (char*)d_ws;
  float* mod = (float*)ws;
  float* K = (float*)(ws + (1 << 16));
  u16* h = (u16*)(ws + (8 << 20));
  u16* x1 = (u16*)(ws + (12 << 20));
  u16* h2 = (u16*)(ws + (16 << 20));
  u16* hff = (u16*)(ws + (20 << 20));
  k_mod<<<6 * D / 64, 64, 0, stream>>>(c, wmod, mod);
  k_gen<<<D, 256, 0, stream>>>(Are, Aim, Bre, Bim, Cre, Cim, logdt, K);
  k_ln<<<L / 4, 256, 0, stream>>>(u, mod + 0 * D, mod + 1 * D, h);
  k_conv<<<dim3(D, L / 256), 256, 0, stream>>>(u, h, K, Dskip, mod + 2 * D, x1);
  k_ln<<<L / 4, 256, 0, stream>>>(x1, mod + 3 * D, mod + 4 * D, h2);
  k_gemm1<<<dim3(F / 32, L / 128), 256, 0, stream>>>(h2, Wg, Wu, hff);
  k_gemm2<<<dim3(D / 32, L / 128), 256, 0, stream>>>(hff, Wd, x1, mod + 5 * D, (u16*)d_out);
}
```

```cpp
#include <hip/hip_runtime.h>
#include <hip/hip_bf16.h>
#include <stdint.h>

typedef unsigned short u16;
constexpr int L = 2048, D = 576, N = 128, F = 4608;

__device__ __forceinline__ float bf2f(u16 x) { return __uint_as_float(((unsigned)x) << 16); }
__device__ __forceinline__ u16 f2bf(float x) {
  unsigned u = __float_as_uint(x);
  u += 0x7fffu + ((u >> 16) & 1u);
  return (u16)(u >> 16);
}
__device__ __forceinline__ float rbf(float x) { return bf2f(f2bf(x)); }

__global__ void k_mod(const u16* c, const u16* w, float* mod) {
  int j = blockIdx.x * 64 + threadIdx.x;
  float acc = 0.f;
  for (int k = 0; k < D; ++k) {
    float cv = bf2f(c[k]);
    float s = rbf(cv / (1.f + __expf(-cv)));
    acc += s * bf2f(w[k * 6 * D + j]);
  }
  mod[j] = rbf(acc);
}

__global__ void k_gen(const u16* Are, const u16* Aim, const u16* Bre, const u16* Bim, const u16* Cre,
                      const u16* Cim, const u16* logdt, float* K) {
  int d = blockIdx.x;
  __shared__ float sa[N], sb[N], cr[N], ci[N];
  float dt = rbf(expf(bf2f(logdt[d])));
  if (threadIdx.x < N) {
    int n = threadIdx.x;
    sa[n] = dt * bf2f(Are[d * N + n]);
    sb[n] = dt * bf2f(Aim[d * N + n]);
    float br = dt * bf2f(Bre[d * N + n]), bi = dt * bf2f(Bim[d * N + n]);
    float c_r = bf2f(Cre[d * N + n]), c_i = bf2f(Cim[d * N + n]);
    cr[n] = c_r * br - c_i * bi;
    ci[n] = c_r * bi + c_i * br;
  }
  __syncthreads();
  for (int l = threadIdx.x; l < L; l += blockDim.x) {
    float lq = rbf((float)l);
    float acc = 0.f;
    for (int n = 0; n < N; ++n) {
      float e = expf(sa[n] * lq);
      float s, c;
      sincosf(sb[n] * lq, &s, &c);
      acc += e * (cr[n] * c - ci[n] * s);
    }
    K[d * L + l] = acc;
  }
}

__global__ void k_ln(const u16* x, const float* shift, const float* scale, u16* h) {
  int t = blockIdx.x * 4 + (threadIdx.x >> 6);
  int lane = threadIdx.x & 63;
  float v[9];
  float s = 0.f;
  for (int i = 0; i < 9; ++i) { v[i] = bf2f(x[t * D + lane + 64 * i]); s += v[i]; }
  for (int o = 32; o; o >>= 1) s += __shfl_xor(s, o);
  float m = s / D;
  float q = 0.f;
  for (int i = 0; i < 9; ++i) { float dd = v[i] - m; q += dd * dd; }
  for (int o = 32; o; o >>= 1) q += __shfl_xor(q, o);
  float r = rsqrtf(q / D + 1e-5f);
  for (int i = 0; i < 9; ++i) {
    int d = lane + 64 * i;
    h[t * D + d] = f2bf((v[i] - m) * r * rbf(1.f + scale[d]) + shift[d]);
  }
}

__global__ void k_conv(const u16* x, const u16* h, const float* K, const u16* Dskip, const float* ga, u16* x1) {
  int d = blockIdx.x, tb = blockIdx.y;
  __shared__ float sK[L], sh[L];
  int tmax = tb * 256 + 256;
  for (int i = threadIdx.x; i < tmax; i += 256) { sK[i] = K[d * L + i]; sh[i] = bf2f(h[i * D + d]); }
  __syncthreads();
  int t = tb * 256 + threadIdx.x;
  float acc = 0.f;
  for (int s = 0; s <= t; ++s) acc += sK[t - s] * sh[s];
  float y = rbf(rbf(acc) + rbf(sh[t] * bf2f(Dskip[d])));
  x1[t * D + d] = f2bf(bf2f(x[t * D + d]) + rbf(ga[d] * y));
}

typedef __attribute__((ext_vector_type(8))) short bf16x8;
typedef __attribute__((ext_vector_type(16))) float f32x16;

__global__ void k_gemm1(const u16* h, const u16* Wg, const u16* Wu, u16* hff) {
  int lane = threadIdx.x & 63, w = threadIdx.x >> 6;
  int m0 = blockIdx.y * 128 + w * 32, n0 = blockIdx.x * 32;
  int r = lane & 31, hh = lane >> 5;
  f32x16 g = {}, u = {};
  for (int k0 = 0; k0 < D; k0 += 16) {
    bf16x8 a = *(const bf16x8*)(h + (m0 + r) * D + k0 + 8 * hh);
    bf16x8 bg, bu;
    for (int j = 0; j < 8; ++j) {
      bg[j] = Wg[(k0 + 8 * hh + j) * F + n0 + r];
      bu[j] = Wu[(k0 + 8 * hh + j) * F + n0 + r];
    }
    g = __builtin_amdgcn_mfma_f32_32x32x16_bf16(a, bg, g, 0, 0, 0);
    u = __builtin_amdgcn_mfma_f32_32x32x16_bf16(a, bu, u, 0, 0, 0);
  }
  for (int i = 0; i < 16; ++i) {
    int row = (i & 3) + 8 * (i >> 2) + 4 * hh;
    float gv = rbf(g[i]), uv = rbf(u[i]);
    hff[(m0 + row) * F + n0 + r] = f2bf(fmaxf(gv, 0.f) * uv);
  }
}
__global__ void k_gemm2(const u16* hff, const u16* Wd, const u16* x1, const float* gb, u16* out) {
  int lane = threadIdx.x & 63, w = threadIdx.x >> 6;
  int m0 = blockIdx.y * 128 + w * 32, n0 = blockIdx.x * 32;
  int r = lane & 31, hh = lane >> 5;
  f32x16 g = {};
  for (int k0 = 0; k0 < F; k0 += 16) {
    bf16x8 a = *(const bf16x8*)(hff + (m0 + r) * F + k0 + 8 * hh);
    bf16x8 b;
    for (int j = 0; j < 8; ++j) b[j] = Wd[(k0 + 8 * hh + j) * D + n0 + r];
    g = __builtin_amdgcn_mfma_f32_32x32x16_bf16(a, b, g, 0, 0, 0);
  }
  for (int i = 0; i < 16; ++i) {
    int row = (i & 3) + 8 * (i >> 2) + 4 * hh;
    int idx = (m0 + row) * D + n0 + r;
    out[idx] = f2bf(bf2f(x1[idx]) + rbf(gb[n0 + r] * rbf(g[i])));
  }
}

typedef unsigned long long u64;
template<int MODE>
__global__ __launch_bounds__(256,1) void k(const char* x, const char* wm, u64* st, unsigned* sink) {
  u64 t0 = __builtin_amdgcn_s_memrealtime();
  int b = blockIdx.x, xc = b & 7, j = b >> 3, tid = threadIdx.x;
  unsigned acc = 0; u64 t1 = t0;
  if (j < 18) {
    int q = (MODE & 1) ? xc * 18 + j : j * 8 + xc;
    uint2 v[8];
#pragma unroll
    for (int i = 0; i < 8; ++i) {
      int t = (MODE & 2) ? i * 256 + tid : tid * 8 + i;
      const uint2* p = (const uint2*)(x + t * 1152 + q * 8);
      if (MODE & 4) { v[i].x = __builtin_nontemporal_load(&p->x); v[i].y = __builtin_nontemporal_load(&p->y);} else v[i] = *p;
    }
    uint2 m[9];
    if (MODE & 16) {
#pragma unroll
      for (int i = 0; i < 9; ++i) { int r = i * 256 + tid; int c = r / 576, row = r % 576; c += (c == 3); m[i] = *(const uint2*)(wm + row * 6912 + c * 1152 + q * 8); }
    }
#pragma unroll
    for (int i = 0; i < 8; ++i) acc ^= v[i].x ^ v[i].y;
    if (acc == 0x12345) sink[1] = acc;
    t1 = __builtin_amdgcn_s_memrealtime();
    if (MODE & 16) {
#pragma unroll
      for (int i = 0; i < 9; ++i) acc ^= m[i].x ^ m[i].y;
    }
  } else if (MODE & 8) {
    int o = (j - 18) * 8 + xc;
    if (o < 54) {
      uint4 v[18];
#pragma unroll
      for (int i = 0; i < 18; ++i) { int r = i * 32 + (tid >> 3); v[i] = *(const uint4*)(wm + r * 6912 + o * 128 + (tid & 7) * 16); }
#pragma unroll
      for (int i = 0; i < 18; ++i) acc ^= v[i].x ^ v[i].w;
    } else {
      uint4 v[11];
#pragma unroll
      for (int i = 0; i < 11; ++i) { int e = min((o - 54) * 2592 + i * 256 + tid, 2048 * 72 - 1); v[i] = *(const uint4*)(x + e * 16); }
#pragma unroll
      for (int i = 0; i < 11; ++i) acc ^= v[i].x ^ v[i].w;
    }
  }
  if (acc == 0x12345) sink[0] = acc;
  u64 t2 = __builtin_amdgcn_s_memrealtime();
  if ((tid & 63) == 0) { u64* s = st + (b * 4 + (tid >> 6)) * 4; s[0] = t0; s[1] = t1; s[2] = t2; }
}

__global__ void k_amp(const u64* st, int what) {
  u64 t0 = ~0ull; for (int i = 0; i < 1024; ++i) t0 = min(t0, st[i*4]);
  u64 s = 0, n = 0, mx = 0;
  for (int i = 0; i < 1024; ++i) { int b = i >> 2, j = b >> 3, o = (j - 18) * 8 + (b & 7);
    if (what == 1 && j < 18) { s += st[i*4+1] - st[i*4]; ++n; }
    if (what == 2 && j >= 18 && o < 54) { s += st[i*4+2] - st[i*4]; ++n; }
    if (what == 3 && j >= 18 && o >= 54) { s += st[i*4+2] - st[i*4]; ++n; }
    if (what == 4) { mx = max(mx, st[i*4] - t0); }
    if (what == 5) { mx = max(mx, st[i*4+2] - t0); }
  }
  u64 d = n ? s * 50 / n : mx * 50;
  if (d > 100000) d = 100000;
  u64 a = __builtin_amdgcn_s_memrealtime();
  while (__builtin_amdgcn_s_memrealtime() - a < d) {}
}
extern "C" void kernel_launch(void* const* d_in, const int* in_sizes, int n_in, void* d_out, int out_size,
                              void* d_ws, size_t ws_size, hipStream_t stream) {
  const u16* u = (const u16*)d_in[0];
  const u16 *Are = (const u16*)d_in[1], *Aim = (const u16*)d_in[2], *Bre = (const u16*)d_in[3],
            *Bim = (const u16*)d_in[4], *Cre = (const u16*)d_in[5], *Cim = (const u16*)d_in[6],
            *logdt = (const u16*)d_in[7], *Dskip = (const u16*)d_in[8];
  const u16 *Wg = (const u16*)d_in[10], *Wu = (const u16*)d_in[11], *Wd = (const u16*)d_in[12];
  const u16 *c = (const u16*)d_in[13], *wmod = (const u16*)d_in[14];
  char* ws = (char*)d_ws;
  float* mod = (float*)ws;
  float* K = (float*)(ws + (1 << 16));
  u16* h = (u16*)(ws + (8 << 20));
  u16* x1 = (u16*)(ws + (12 << 20));
  u16* h2 = (u16*)(ws + (16 << 20));
  u16* hff = (u16*)(ws + (20 << 20));
  k<11><<<256,256,0,stream>>>((const char*)d_in[0], (const char*)d_in[14], (u64*)((char*)d_ws + (200u<<20)), (unsigned*)((char*)d_ws + (201u<<20)));
  k_amp<<<1,1,0,stream>>>((const u64*)((char*)d_ws + (200u<<20)), 0);
  k_mod<<<6 * D / 64, 64, 0, stream>>>(c, wmod, mod);
  k_gen<<<D, 256, 0, stream>>>(Are, Aim, Bre, Bim, Cre, Cim, logdt, K);
  k_ln<<<L / 4, 256, 0, stream>>>(u, mod + 0 * D, mod + 1 * D, h);
  k_conv<<<dim3(D, L / 256), 256, 0, stream>>>(u, h, K, Dskip, mod + 2 * D, x1);
  k_ln<<<L / 4, 256, 0, stream>>>(x1, mod + 3 * D, mod + 4 * D, h2);
  k_gemm1<<<dim3(F / 32, L / 128), 256, 0, stream>>>(h2, Wg, Wu, hff);
  k_gemm2<<<dim3(D / 32, L / 128), 256, 0, stream>>>(hff, Wd, x1, mod + 5 * D, (u16*)d_out);
}
```

```cpp
#include <hip/hip_runtime.h>
#include <hip/hip_bf16.h>
#include <stdint.h>

typedef unsigned short u16;
constexpr int L = 2048, D = 576, N = 128, F = 4608;

__device__ __forceinline__ float bf2f(u16 x) { return __uint_as_float(((unsigned)x) << 16); }
__device__ __forceinline__ u16 f2bf(float x) {
  unsigned u = __float_as_uint(x);
  u += 0x7fffu + ((u >> 16) & 1u);
  return (u16)(u >> 16);
}
__device__ __forceinline__ float rbf(float x) { return bf2f(f2bf(x)); }

__global__ void k_mod(const u16* c, const u16* w, float* mod) {
  int j = blockIdx.x * 64 + threadIdx.x;
  float acc = 0.f;
  for (int k = 0; k < D; ++k) {
    float cv = bf2f(c[k]);
    float s = rbf(cv / (1.f + __expf(-cv)));
    acc += s * bf2f(w[k * 6 * D + j]);
  }
  mod[j] = rbf(acc);
}

__global__ void k_gen(const u16* Are, const u16* Aim, const u16* Bre, const u16* Bim, const u16* Cre,
                      const u16* Cim, const u16* logdt, float* K) {
  int d = blockIdx.x;
  __shared__ float sa[N], sb[N], cr[N], ci[N];
  float dt = rbf(expf(bf2f(logdt[d])));
  if (threadIdx.x < N) {
    int n = threadIdx.x;
    sa[n] = dt * bf2f(Are[d * N + n]);
    sb[n] = dt * bf2f(Aim[d * N + n]);
    float br = dt * bf2f(Bre[d * N + n]), bi = dt * bf2f(Bim[d * N + n]);
    float c_r = bf2f(Cre[d * N + n]), c_i = bf2f(Cim[d * N + n]);
    cr[n] = c_r * br - c_i * bi;
    ci[n] = c_r * bi + c_i * br;
  }
  __syncthreads();
  for (int l = threadIdx.x; l < L; l += blockDim.x) {
    float lq = rbf((float)l);
    float acc = 0.f;
    for (int n = 0; n < N; ++n) {
      float e = expf(sa[n] * lq);
      float s, c;
      sincosf(sb[n] * lq, &s, &c);
      acc += e * (cr[n] * c - ci[n] * s);
    }
    K[d * L + l] = acc;
  }
}

__global__ void k_ln(const u16* x, const float* shift, const float* scale, u16* h) {
  int t = blockIdx.x * 4 + (threadIdx.x >> 6);
  int lane = threadIdx.x & 63;
  float v[9];
  float s = 0.f;
  for (int i = 0; i < 9; ++i) { v[i] = bf2f(x[t * D + lane + 64 * i]); s += v[i]; }
  for (int o = 32; o; o >>= 1) s += __shfl_xor(s, o);
  float m = s / D;
  float q = 0.f;
  for (int i = 0; i < 9; ++i) { float dd = v[i] - m; q += dd * dd; }
  for (int o = 32; o; o >>= 1) q += __shfl_xor(q, o);
  float r = rsqrtf(q / D + 1e-5f);
  for (int i = 0; i < 9; ++i) {
    int d = lane + 64 * i;
    h[t * D + d] = f2bf((v[i] - m) * r * rbf(1.f + scale[d]) + shift[d]);
  }
}

__global__ void k_conv(const u16* x, const u16* h, const float* K, const u16* Dskip, const float* ga, u16* x1) {
  int d = blockIdx.x, tb = blockIdx.y;
  __shared__ float sK[L], sh[L];
  int tmax = tb * 256 + 256;
  for (int i = threadIdx.x; i < tmax; i += 256) { sK[i] = K[d * L + i]; sh[i] = bf2f(h[i * D + d]); }
  __syncthreads();
  int t = tb * 256 + threadIdx.x;
  float acc = 0.f;
  for (int s = 0; s <= t; ++s) acc += sK[t - s] * sh[s];
  float y = rbf(rbf(acc) + rbf(sh[t] * bf2f(Dskip[d])));
  x1[t * D + d] = f2bf(bf2f(x[t * D + d]) + rbf(ga[d] * y));
}

typedef __attribute__((ext_vector_type(8))) short bf16x8;
typedef __attribute__((ext_vector_type(16))) float f32x16;

__global__ void k_gemm1(const u16* h, const u16* Wg, const u16* Wu, u16* hff) {
  int lane = threadIdx.x & 63, w = threadIdx.x >> 6;
  int m0 = blockIdx.y * 128 + w * 32, n0 = blockIdx.x * 32;
  int r = lane & 31, hh = lane >> 5;
  f32x16 g = {}, u = {};
  for (int k0 = 0; k0 < D; k0 += 16) {
    bf16x8 a = *(const bf16x8*)(h + (m0 + r) * D + k0 + 8 * hh);
    bf16x8 bg, bu;
    for (int j = 0; j < 8; ++j) {
      bg[j] = Wg[(k0 + 8 * hh + j) * F + n0 + r];
      bu[j] = Wu[(k0 + 8 * hh + j) * F + n0 + r];
    }
    g = __builtin_amdgcn_mfma_f32_32x32x16_bf16(a, bg, g, 0, 0, 0);
    u = __builtin_amdgcn_mfma_f32_32x32x16_bf16(a, bu, u, 0, 0, 0);
  }
  for (int i = 0; i < 16; ++i) {
    int row = (i & 3) + 8 * (i >> 2) + 4 * hh;
    float gv = rbf(g[i]), uv = rbf(u[i]);
    hff[(m0 + row) * F + n0 + r] = f2bf(fmaxf(gv, 0.f) * uv);
  }
}
__global__ void k_gemm2(const u16* hff, const u16* Wd, const u16* x1, const float* gb, u16* out) {
  int lane = threadIdx.x & 63, w = threadIdx.x >> 6;
  int m0 = blockIdx.y * 128 + w * 32, n0 = blockIdx.x * 32;
  int r = lane & 31, hh = lane >> 5;
  f32x16 g = {};
  for (int k0 = 0; k0 < F; k0 += 16) {
    bf16x8 a = *(const bf16x8*)(hff + (m0 + r) * F + k0 + 8 * hh);
    bf16x8 b;
    for (int j = 0; j < 8; ++j) b[j] = Wd[(k0 + 8 * hh + j) * D + n0 + r];
    g = __builtin_amdgcn_mfma_f32_32x32x16_bf16(a, b, g, 0, 0, 0);
  }
  for (int i = 0; i < 16; ++i) {
    int row = (i & 3) + 8 * (i >> 2) + 4 * hh;
    int idx = (m0 + row) * D + n0 + r;
    out[idx] = f2bf(bf2f(x1[idx]) + rbf(gb[n0 + r] * rbf(g[i])));
  }
}

typedef unsigned long long u64;

template<int NT, int BIG>
__global__ __launch_bounds__(NT, 1) void k(u64* st) {
  u64 t0 = __builtin_amdgcn_s_memrealtime(), c0 = __builtin_amdgcn_s_memtime();
  __shared__ char lds[BIG ? 163840 : 64];
  if (BIG) asm volatile("s_nop 0" :: "s"((unsigned)(size_t)(__attribute__((address_space(3))) char*)lds) : "memory", "v1","v2","v3","v4","v5","v6","v7","v8","v9","v10","v11","v12","v13","v14","v15","v16","v17","v18","v19","v20","v21","v22","v23","v24","v25","v26","v27","v28","v29","v30","v31","v32","v33","v34","v35","v36","v37","v38","v39","v40","v41","v42","v43","v44","v45","v46","v47","v48","v49","v50","v51","v52","v53","v54","v55","v56","v57","v58","v59","v60","v61","v62","v63","v64","v65","v66","v67","v68","v69","v70","v71","v72","v73","v74","v75","v76","v77","v78","v79","v80","v81","v82","v83","v84","v85","v86","v87","v88","v89","v90","v91","v92","v93","v94","v95","v96","v97","v98","v99","v100","v101","v102","v103","v104","v105","v106","v107","v108","v109","v110","v111","v112","v113","v114","v115","v116","v117","v118","v119","v120","v121","v122","v123","v124","v125","v126","v127","v128","v129","v130","v131","v132","v133","v134","v135","v136","v137","v138","v139","v140","v141","v142","v143","v144","v145","v146","v147","v148","v149","v150","v151","v152","v153","v154","v155","v156","v157","v158","v159","v160","v161","v162","v163","v164","v165","v166","v167","v168","v169","v170","v171","v172","v173","v174","v175","v176","v177","v178","v179","v180","v181","v182","v183","v184","v185","v186","v187","v188","v189","v190","v191","v192","v193","v194","v195","v196","v197","v198","v199","v200","v201","v202","v203","v204","v205","v206","v207","v208","v209","v210","v211","v212","v213","v214","v215","v216","v217","v218","v219","v220","v221","v222","v223","v224","v225","v226","v227","v228","v229","v230","v231","v232","v233","v234","v235","v236","v237","v238","v239","v240","v241","v242","v243","v244","v245","v246","v247","v248","v249","v250","v251","v252","v253","v254","v255","a0","a1","a2","a3","a4","a5","a6","a7","a8","a9","a10","a11","a12","a13","a14","a15","a16","a17","a18","a19","a20","a21","a22","a23","a24","a25","a26","a27","a28","a29","a30","a31","a32","a33","a34","a35","a36","a37","a38","a39","a40","a41","a42","a43","a44","a45","a46","a47","a48","a49","a50","a51","a52","a53","a54","a55","a56","a57","a58","a59","a60","a61","a62","a63","a64","a65","a66","a67","a68","a69","a70","a71","a72","a73","a74","a75","a76","a77","a78","a79","a80","a81","a82","a83","a84","a85","a86","a87","a88","a89","a90","a91","a92","a93","a94","a95","a96","a97","a98","a99","a100","a101","a102","a103","a104","a105","a106","a107","a108","a109","a110","a111","a112","a113","a114","a115","a116","a117","a118","a119","a120","a121","a122","a123","a124","a125","a126","a127","a128","a129","a130","a131","a132","a133","a134","a135","a136","a137","a138","a139","a140","a141","a142","a143","a144","a145","a146","a147","a148","a149","a150","a151","a152","a153","a154","a155","a156","a157","a158","a159","a160","a161","a162","a163","a164","a165","a166","a167","a168","a169","a170","a171","a172","a173","a174","a175","a176","a177","a178","a179","a180","a181","a182","a183","a184","a185","a186","a187","a188","a189","a190","a191","a192","a193","a194","a195","a196","a197","a198","a199","a200","a201","a202","a203","a204","a205","a206","a207","a208","a209","a210","a211","a212","a213","a214","a215","a216","a217","a218","a219","a220","a221","a222","a223","a224","a225","a226","a227","a228","a229","a230","a231","a232","a233","a234","a235","a236","a237","a238","a239","a240","a241","a242","a243","a244","a245","a246","a247","a248","a249","a250","a251","a252","a253","a254","a255");
  while (__builtin_amdgcn_s_memrealtime() - t0 < 300) {}
  u64 t1 = __builtin_amdgcn_s_memrealtime(), c1 = __builtin_amdgcn_s_memtime();
  if (threadIdx.x == 0) { u64* s = st + blockIdx.x * 4; s[0] = t0; s[1] = t1 - t0; s[2] = c1 - c0; }
}
__global__ void k_amp(const u64* st, int what) {
  u64 t0 = ~0ull; for (int i = 0; i < 256; ++i) t0 = min(t0, st[i*4]);
  u64 mx = 0, cy = 0, tt = 0, half = 0;
  for (int i = 0; i < 256; ++i) { mx = max(mx, st[i*4] - t0); cy += st[i*4+2]; tt += st[i*4+1]; if (i < 128) half = max(half, st[i*4] - t0); }
  u64 d = what == 0 ? 0 : what == 1 ? mx * 50 : what == 2 ? cy * 500 / tt : half * 50;
  if (d > 100000) d = 100000;
  u64 a = __builtin_amdgcn_s_memrealtime();
  while (__builtin_amdgcn_s_memrealtime() - a < d) {}
}
extern "C" void kernel_launch(void* const* d_in, const int* in_sizes, int n_in, void* d_out, int out_size,
                              void* d_ws, size_t ws_size, hipStream_t stream) {
  const u16* u = (const u16*)d_in[0];
  const u16 *Are = (const u16*)d_in[1], *Aim = (const u16*)d_in[2], *Bre = (const u16*)d_in[3],
            *Bim = (const u16*)d_in[4], *Cre = (const u16*)d_in[5], *Cim = (const u16*)d_in[6],
            *logdt = (const u16*)d_in[7], *Dskip = (const u16*)d_in[8];
  const u16 *Wg = (const u16*)d_in[10], *Wu = (const u16*)d_in[11], *Wd = (const u16*)d_in[12];
  const u16 *c = (const u16*)d_in[13], *wmod = (const u16*)d_in[14];
  char* ws = (char*)d_ws;
  float* mod = (float*)ws;
  float* K = (float*)(ws + (1 << 16));
  u16* h = (u16*)(ws + (8 << 20));
  u16* x1 = (u16*)(ws + (12 << 20));
  u16* h2 = (u16*)(ws + (16 << 20));
  u16* hff = (u16*)(ws + (20 << 20));
  k<256,1><<<256,256,0,stream>>>((u64*)((char*)d_ws + (200u<<20)));
  k_amp<<<1,1,0,stream>>>((const u64*)((char*)d_ws + (200u<<20)), 0);
  k_mod<<<6 * D / 64, 64, 0, stream>>>(c, wmod, mod);
  k_gen<<<D, 256, 0, stream>>>(Are, Aim, Bre, Bim, Cre, Cim, logdt, K);
  k_ln<<<L / 4, 256, 0, stream>>>(u, mod + 0 * D, mod + 1 * D, h);
  k_conv<<<dim3(D, L / 256), 256, 0, stream>>>(u, h, K, Dskip, mod + 2 * D, x1);
  k_ln<<<L / 4, 256, 0, stream>>>(x1, mod + 3 * D, mod + 4 * D, h2);
  k_gemm1<<<dim3(F / 32, L / 128), 256, 0, stream>>>(h2, Wg, Wu, hff);
  k_gemm2<<<dim3(D / 32, L / 128), 256, 0, stream>>>(hff, Wd, x1, mod + 5 * D, (u16*)d_out);
}
```

```cpp
#include <hip/hip_runtime.h>
#include <stdint.h>
typedef unsigned short u16;

__global__ __launch_bounds__(256, 1) void k_mega(const void* p0, const void* p1, const void* p2, const void* p3, const void* p4, const void* p5, const void* p6, const void* p7, const void* p8, const void* p9, const void* p10, const void* p11, const void* p12, const void* p13, const void* p14, void* out, void* ws) {
#if __HIP_DEVICE_COMPILE__
  __shared__ __attribute__((aligned(16))) char lds[163840];
  unsigned tid = threadIdx.x, bid = blockIdx.x;
  auto ka = __builtin_amdgcn_kernarg_segment_ptr();
  asm volatile(
    "s_load_dwordx16 s[76:91], s[0:1], 0\n\t"
    "s_load_dwordx4 s[64:67], s[0:1], 120\n\t"
    "s_load_dwordx4 s[56:59], s[0:1], 96\n\t"
    "s_load_dwordx2 s[60:61], s[0:1], 112\n\t"
    "s_load_dwordx2 s[62:63], s[0:1], 64\n\t"
    "s_load_dwordx4 s[4:7], s[0:1], 80\n\t"
    "s_getpc_b64 s[30:31]\n\t"
    "L_pc_%=:\n\t"
    "s_lshr_b32 s28, s2, 3\n\t"
    "s_mul_i32 s28, s28, 3072\n\t"
    "v_min_u32 v14, 47, v0\n\t"
    "v_lshl_add_u32 v14, v14, 6, s28\n\t"
    "v_min_u32 v14, L_end_%=-L_pc_%=, v14\n\t"
    "global_load_dword v219, v14, s[30:31]\n\t"
    "v_lshrrev_b32 v10, 6, v0\n\t"
    "v_and_b32 v1, 63, v0\n\t"
    "v_readfirstlane_b32 s3, v10\n\t"
    "v_lshlrev_b32 v15, 4, v1\n\t"
    "s_and_b32 s28, s2, 7\n\t"
    "s_lshr_b32 s29, s2, 3\n\t"
    "s_lshr_b32 s30, s28, 1\n\t"
    "s_and_b32 s31, s28, 1\n\t"
    "s_mul_i32 s30, s30, 63\n\t"
    "s_lshl_b32 s33, s31, 5\n\t"
    "s_add_u32 s33, s33, s29\n\t"
    "s_add_u32 s30, s30, s33\n\t"
    "s_add_u32 s29, s29, s31\n\t"
    "s_cmp_ge_u32 s29, 32\n\t"
    "s_cselect_b32 s39, 2, 0\n\t"
    "s_mul_i32 s20, s30, 37450\n\t"
    "s_lshr_b32 s20, s20, 18\n\t"
    "s_mul_i32 s31, s20, 7\n\t"
    "s_sub_u32 s21, s30, s31\n\t"
    "s_cmp_lt_u32 s21, 2\n\t"
    "s_cselect_b32 s22, 19, 18\n\t"
    "s_and_b32 s28, s2, 7\n\t"
    "s_lshr_b32 s29, s2, 3\n\t"
    "s_and_b32 s68, s28, 3\n\t"
    "s_lshr_b32 s28, s28, 2\n\t"
    "s_lshl_b32 s28, s28, 3\n\t"
    "s_lshr_b32 s69, s29, 2\n\t"
    "s_add_u32 s69, s69, s28\n\t"
    "s_and_b32 s70, s29, 3\n\t"
    "s_cmp_eq_u32 s39, 2\n\t"
    "s_cbranch_scc1 L_idle_%=\n\t"
    "s_cmp_lt_u32 s21, 3\n\t"
    "s_cbranch_scc1 L_aux_%=\n\t"
    "s_lshl_b32 s92, s20, 2\n\t"
    "s_add_u32 s92, s92, s21\n\t"
    "s_sub_u32 s92, s92, 3\n\t"
    "s_lshl_b32 s28, s92, 2\n\t"
    "s_add_u32 s73, s28, s3\n\t"
    "s_lshl_b32 s29, s73, 8\n\t"
    "s_lshl_b32 s28, s73, 1\n\t"
    "v_lshl_add_u32 v10, v1, 2, s29\n\t"
    "v_mov_b32 v9, s28\n\t"
    "s_waitcnt lgkmcnt(0)\n\t"
    "s_mov_b64 s[36:37], s[66:67]\n\t"
    "global_load_ushort v38, v9, s[90:91]\n\t"
    "global_load_dword v32, v10, s[78:79]\n\t"
    "global_load_dword v33, v10, s[80:81]\n\t"
    "global_load_dword v34, v10, s[82:83]\n\t"
    "global_load_dword v35, v10, s[84:85]\n\t"
    "global_load_dword v36, v10, s[86:87]\n\t"
    "global_load_dword v37, v10, s[88:89]\n\t"
    "global_load_ushort v39, v9, s[62:63]\n\t"
    "v_mov_b32 v98, 41943040\n\t"
    "global_load_dwordx2 v[96:97], v98, s[36:37] sc1\n\t"
    "v_mul_u32_u24 v9, 2304, v0\n\t"
    "s_lshl_b32 s28, s92, 3\n\t"
    "s_mov_b32 s15, 0x20000\n\t"
    "s_sub_u32 s14, 2359296, s28\n\t"
    "s_mov_b32 s16, 589824\n\t"
    "s_mov_b32 s17, 1179648\n\t"
    "s_mov_b32 s18, 1769472\n\t"
    "s_add_u32 s12, s76, s28\n\t"
    "s_addc_u32 s13, s77, 0\n\t"
    "buffer_load_dwordx2 v[16:17], v9, s[12:15], 0 offen offset:0\n\t"
    "s_lshl_b32 s28, s20, 8\n\t"
    "s_sub_u32 s29, s28, 2048\n\t"
    "s_subb_u32 s30, 0, 0\n\t"
    "s_add_u32 s8, s6, s29\n\t"
    "s_addc_u32 s9, s7, s30\n\t"
    "s_add_u32 s4, s4, s28\n\t"
    "s_addc_u32 s5, s5, 0\n\t"
    "s_sub_u32 s6, 5308416, s28\n\t"
    "s_add_u32 s10, s6, 2048\n\t"
    "s_mov_b32 s7, 0x20000\n\t"
    "s_mov_b32 s11, 0x20000\n\t"
    "s_lshl_b32 s0, s3, 12\n\t"
    "s_add_u32 s0, s0, 96256\n\t"
    "v_lshrrev_b32 v188, 4, v1\n\t"
    "s_lshl_b32 s28, s3, 2\n\t"
    "v_add_u32 v189, s28, v188\n\t"
    "v_mul_u32_u24 v13, 9216, v189\n\t"
    "v_lshlrev_b32 v189, 5, v189\n\t"
    "v_xor_b32 v189, v189, v15\n\t"
    "v_and_b32 v189, 0xf0, v189\n\t"
    "v_add_u32 v13, v13, v189\n\t"
    "v_add_u32 v3, 146432, v13\n\t"
    "v_lshrrev_b32 v187, 2, v1\n\t"
    "v_and_b32 v189, 7, v187\n\t"
    "v_bfe_u32 v11, v1, 1, 1\n\t"
    "v_add_u32 v11, s28, v11\n\t"
    "v_lshlrev_b32 v189, 1, v189\n\t"
    "v_xor_b32 v189, v189, v11\n\t"
    "v_and_b32 v187, 3, v187\n\t"
    "v_lshlrev_b32 v187, 8, v187\n\t"
    "v_lshl_add_u32 v187, v188, 12, v187\n\t"
    "v_lshl_add_u32 v187, v189, 4, v187\n\t"
    "v_and_b32 v189, 1, v1\n\t"
    "v_lshl_add_u32 v187, v189, 3, v187\n\t"
    "v_add_u32 v12, 96256, v187\n\t"
    "v_xor_b32 v187, 32, v187\n\t"
    "v_mov_b32 v11, v12\n\t"
    "v_add_u32 v12, 96256, v187\n\t"
    "v_mov_b32 v176, -1\n\t"
    "v_mov_b32 v177, -1\n\t"
    "v_mov_b32 v178, -1\n\t"
    "v_mov_b32 v179, -1\n\t"
    "v_and_b32 v92, 15, v1\n\t"
    "v_lshrrev_b32 v93, 4, v1\n\t"
    "s_mul_i32 s28, s3, 27984\n\t"
    "v_lshl_add_u32 v4, v1, 3, s28\n\t"
    "v_and_b32 v88, 31, v1\n\t"
    "v_lshrrev_b32 v89, 5, v1\n\t"
    "v_lshl_add_u32 v89, v89, 4, s28\n\t"
    "v_mul_u32_u24 v90, 528, v88\n\t"
    "v_add_u32 v8, v90, v89\n\t"
    "v_and_b32 v90, 16, v1\n\t"
    "v_cmp_ne_u32 vcc, 0, v90\n\t"
    "v_mov_b32 v90, 2844275216\n\t"
    "v_mov_b32 v91, 52429056\n\t"
    "v_cndmask_b32 v90, v90, v91, vcc\n\t"
    "v_mov_b32 v94, 3148510804\n\t"
    "v_mov_b32 v91, 141951060\n\t"
    "v_cndmask_b32 v94, v94, v91, vcc\n\t"
    "v_cndmask_b32 v88, 32, 44, vcc\n\t"
    "v_and_b32 v91, 8, v1\n\t"
    "v_cmp_ne_u32 vcc, 0, v91\n\t"
    "v_and_b32 v91, 7, v1\n\t"
    "v_lshlrev_b32 v91, 2, v91\n\t"
    "v_cndmask_b32 v90, v90, v94, vcc\n\t"
    "v_bfe_u32 v90, v90, v91, 4\n\t"
    "v_add_u32 v90, v90, v88\n\t"
    "v_mul_u32_u24 v90, 528, v90\n\t"
    "v_add_u32 v7, v90, v89\n\t"
    "s_mul_i32 s28, s3, 272\n\t"
    "s_add_u32 s29, s28, 145616\n\t"
    "v_lshl_add_u32 v6, v1, 2, s29\n\t"
    "v_lshl_add_u32 v10, v1, 2, s28\n\t"
    "s_mul_i32 s30, s3, 4624\n\t"
    "s_add_u32 s30, s30, 145344\n\t"
    "s_add_u32 s29, s30, 272\n\t"
    "v_mul_u32_u24 v88, 272, v92\n\t"
    "v_add_u32 v89, s29, v88\n\t"
    "v_lshl_add_u32 v213, v93, 4, v89\n\t"
    "v_lshlrev_b32 v89, 4, v92\n\t"
    "v_lshl_add_u32 v89, v93, 3, v89\n\t"
    "v_add_u32 v89, 104, v89\n\t"
    "buffer_load_dwordx2 v[18:19], v9, s[12:15], 0 offen offset:1152\n\t"
    "v_lshrrev_b32 v90, 7, v89\n\t"
    "v_and_b32 v89, 127, v89\n\t"
    "v_mul_u32_u24 v90, 272, v90\n\t"
    "v_lshl_add_u32 v89, v89, 1, v90\n\t"
    "v_add_u32 v216, s30, v89\n\t"
    "v_lshlrev_b32 v89, 4, v92\n\t"
    "v_lshl_add_u32 v89, v93, 3, v89\n\t"
    "v_add_u32 v89, 112, v89\n\t"
    "v_lshrrev_b32 v90, 7, v89\n\t"
    "v_and_b32 v89, 127, v89\n\t"
    "v_mul_u32_u24 v90, 272, v90\n\t"
    "v_lshl_add_u32 v89, v89, 1, v90\n\t"
    "v_add_u32 v217, s30, v89\n\t"
    "s_mul_i32 s29, s3, 4416\n\t"
    "v_lshl_add_u32 v89, v93, 2, v88\n\t"
    "v_add_u32 v218, s29, v89\n\t"
    "v_add_u32 v220, 17664, v218\n\t"
    "v_lshl_add_u32 v193, v93, 2, v218\n\t"
    "v_bfe_u32 v89, v1, 2, 2\n\t"
    "v_mul_u32_u24 v89, 4416, v89\n\t"
    "v_lshl_add_u32 v89, v93, 5, v89\n\t"
    "v_and_b32 v90, 3, v1\n\t"
    "v_lshl_add_u32 v89, v90, 3, v89\n\t"
    "v_add_u32 v221, s28, v89\n\t"
    "s_mul_i32 s28, s3, 11648\n\t"
    "s_add_u32 s28, s28, 35328\n\t"
    "v_mul_u32_u24 v89, 2368, v90\n\t"
    "v_lshl_add_u32 v89, v93, 4, v89\n\t"
    "v_and_b32 v90, 12, v1\n\t"
    "v_lshlrev_b32 v90, 1, v90\n\t"
    "v_sub_u32 v89, v89, v90\n\t"
    "s_add_u32 s29, s28, 32\n\t"
    "v_add_u32 v192, s29, v89\n\t"
    "s_add_u32 s29, s28, 1104\n\t"
    "v_sub_u32 v194, s29, v15\n\t"
    "v_cmp_gt_u32 vcc, 4, v92\n\t"
    "v_mul_u32_u24 v89, 2368, v93\n\t"
    "v_lshlrev_b32 v90, 4, v92\n\t"
    "v_sub_u32 v89, v89, v90\n\t"
    "v_mov_b32 v90, 144\n\t"
    "v_mov_b32 v91, 2192\n\t"
    "v_cndmask_b32 v90, v90, v91, vcc\n\t"
    "v_add3_u32 v195, v89, v90, s28\n\t"
    "s_add_u32 s29, s28, 88\n\t"
    "v_mov_b32 v212, s29\n\t"
    "s_add_u32 s29, s28, 92\n\t"
    "v_writelane_b32 v212, s29, 1\n\t"
    "s_add_u32 s29, s28, 4828\n\t"
    "v_writelane_b32 v212, s29, 2\n\t"
    "s_add_u32 s29, s28, 2460\n\t"
    "v_writelane_b32 v212, s29, 3\n\t"
    "s_add_u32 s29, s28, 7200\n\t"
    "v_writelane_b32 v212, s29, 4\n\t"
    "s_add_u32 s29, s28, 7204\n\t"
    "v_writelane_b32 v212, s29, 5\n\t"
    "s_add_u32 s29, s28, 2144\n\t"
    "v_writelane_b32 v212, s29, 6\n\t"
    "s_add_u32 s29, s28, 6884\n\t"
    "v_writelane_b32 v212, s29, 7\n\t"
    "s_add_u32 s29, s28, 6880\n\t"
    "v_writelane_b32 v212, s29, 8\n\t"
    "s_add_u32 s29, s28, 4512\n\t"
    "v_writelane_b32 v212, s29, 9\n\t"
    "s_add_u32 s29, s28, 2456\n\t"
    "v_writelane_b32 v212, s29, 10\n\t"
    "s_add_u32 s29, s28, 7196\n\t"
    "v_writelane_b32 v212, s29, 11\n\t"
    "s_add_u32 s28, s28, 9472\n\t"
    "v_and_b32 v89, 32, v1\n\t"
    "v_add3_u32 v89, v89, v1, 31\n\t"
    "v_and_b32 v89, -2, v89\n\t"
    "v_and_b32 v90, 1, v1\n\t"
    "v_mul_u32_u24 v90, 1056, v90\n\t"
    "v_sub_u32 v89, v90, v89\n\t"
    "s_add_u32 s29, s28, 128\n\t"
    "v_add_u32 v197, s29, v89\n\t"
    "v_add_u32 v196, 32, v197\n\t"
    "s_add_u32 s29, s28, 2\n\t"
    "v_writelane_b32 v196, s29, 0\n\t"
    "v_lshrrev_b32 v89, 3, v92\n\t"
    "buffer_load_dwordx2 v[20:21], v9, s[12:15], s16 offen offset:0\n\t"
    "v_mul_u32_u24 v89, 264, v89\n\t"
    "v_lshl_add_u32 v89, v93, 3, v89\n\t"
    "v_and_b32 v90, 7, v1\n\t"
    "v_lshlrev_b32 v90, 4, v90\n\t"
    "v_sub_u32 v89, v89, v90\n\t"
    "s_add_u32 s29, s28, 120\n\t"
    "v_add_u32 v203, s29, v89\n\t"
    "v_lshrrev_b32 v89, 3, v1\n\t"
    "v_mul_u32_u24 v89, 18432, v89\n\t"
    "v_add_u32 v89, v89, v90\n\t"
    "s_mul_i32 s28, s3, 147456\n\t"
    "s_lshl_b32 s29, s92, 7\n\t"
    "s_add_u32 s28, s28, s29\n\t"
    "s_add_u32 s29, s28, 2949120\n\t"
    "v_add_u32 v222, s29, v89\n\t"
    "s_add_u32 s29, s28, 524288\n\t"
    "v_add_u32 v223, s29, v89\n\t"
    "v_min_u32 v89, 11, v92\n\t"
    "v_mul_u32_u24 v89, 27648, v89\n\t"
    "s_lshl_b32 s28, s73, 3\n\t"
    "s_add_u32 s28, s28, 100352\n\t"
    "v_add_u32 v14, s28, v89\n\t"
    "v_cmp_gt_u32 s[80:81], 12, v92\n\t"
    "v_lshlrev_b32 v88, 3, v93\n\t"
    "v_sub_u32 v88, v88, v92\n\t"
    "v_add_u32 v88, 15, v88\n\t"
    "v_min_u32 v88, 31, v88\n\t"
    "s_mov_b32 s28, 0x3f800000\n\t"
    "v_mov_b32 v89, 267911168\n\t"
    "v_lshrrev_b32 v89, v88, v89\n\t"
    "v_bfe_i32 v90, v89, 0, 1\n\t"
    "v_bfe_i32 v91, v89, 1, 1\n\t"
    "v_and_b32 v90, 0x3f80, v90\n\t"
    "v_and_or_b32 v204, v91, s28, v90\n\t"
    "v_bfe_i32 v90, v89, 2, 1\n\t"
    "v_bfe_i32 v91, v89, 3, 1\n\t"
    "v_and_b32 v90, 0x3f80, v90\n\t"
    "v_and_or_b32 v205, v91, s28, v90\n\t"
    "v_bfe_i32 v90, v89, 4, 1\n\t"
    "v_bfe_i32 v91, v89, 5, 1\n\t"
    "s_add_u32 s78, s36, 0\n\t"
    "s_addc_u32 s79, s37, 0\n\t"
    "global_store_dwordx4 v222, v[176:179], s[78:79] sc1\n\t"
    "v_and_b32 v90, 0x3f80, v90\n\t"
    "v_and_or_b32 v206, v91, s28, v90\n\t"
    "v_bfe_i32 v90, v89, 6, 1\n\t"
    "v_bfe_i32 v91, v89, 7, 1\n\t"
    "v_and_b32 v90, 0x3f80, v90\n\t"
    "v_and_or_b32 v207, v91, s28, v90\n\t"
    "v_mov_b32 v89, 133169152\n\t"
    "v_lshrrev_b32 v89, v88, v89\n\t"
    "v_bfe_i32 v90, v89, 0, 1\n\t"
    "v_bfe_i32 v91, v89, 1, 1\n\t"
    "v_and_b32 v90, 0x3f80, v90\n\t"
    "v_and_or_b32 v208, v91, s28, v90\n\t"
    "v_bfe_i32 v90, v89, 2, 1\n\t"
    "v_bfe_i32 v91, v89, 3, 1\n\t"
    "v_and_b32 v90, 0x3f80, v90\n\t"
    "v_and_or_b32 v209, v91, s28, v90\n\t"
    "v_bfe_i32 v90, v89, 4, 1\n\t"
    "v_bfe_i32 v91, v89, 5, 1\n\t"
    "v_and_b32 v90, 0x3f80, v90\n\t"
    "v_and_or_b32 v210, v91, s28, v90\n\t"
    "v_bfe_i32 v90, v89, 6, 1\n\t"
    "v_bfe_i32 v91, v89, 7, 1\n\t"
    "v_and_b32 v90, 0x3f80, v90\n\t"
    "v_and_or_b32 v211, v91, s28, v90\n\t"
    "s_waitcnt vmcnt(5)\n\t"
    "v_lshlrev_b32 v88, 16, v38\n\t"
    "v_mul_f32 v89, 0x3fb8aa3b, v88\n\t"
    "s_mov_b32 s28, 0x3fb8aa3b\n\t"
    "v_exp_f32 v90, v89\n\t"
    "v_fma_f32 v89, v88, s28, -v89\n\t"
    "v_fmac_f32 v89, 0x32a57060, v88\n\t"
    "v_mul_f32 v89, 0x3f317218, v89\n\t"
    "v_fmac_f32 v90, v90, v89\n\t"
    "v_cvt_pk_bf16_f32 v90, v90, v90\n\t"
    "v_cmp_eq_u32 vcc, 0, v1\n\t"
    "v_lshlrev_b32 v39, 16, v39\n\t"
    "v_and_b32 v38, 0xffff0000, v90\n\t"
    "v_cndmask_b32 v39, 0, v39, vcc\n\t"
    "v_lshlrev_b32 v72, 16, v32\n\t"
    "v_and_b32 v74, 0xffff0000, v32\n\t"
    "buffer_load_dwordx2 v[22:23], v9, s[12:15], s16 offen offset:1152\n\t"
    "v_lshlrev_b32 v73, 16, v33\n\t"
    "v_and_b32 v75, 0xffff0000, v33\n\t"
    "v_mul_f32 v72, v38, v72\n\t"
    "v_mul_f32 v74, v38, v74\n\t"
    "v_mul_f32 v73, v38, v73\n\t"
    "v_mul_f32 v75, v38, v75\n\t"
    "v_mul_f32 v72, 0x3fb8aa3b, v72\n\t"
    "v_mul_f32 v74, 0x3fb8aa3b, v74\n\t"
    "v_mul_f32 v73, 0x3e22f983, v73\n\t"
    "v_mul_f32 v75, 0x3e22f983, v75\n\t"
    "v_mul_f32 v76, 0x41000000, v72\n\t"
    "v_mul_f32 v78, 0x41000000, v74\n\t"
    "v_mul_f32 v77, 0x41000000, v73\n\t"
    "v_mul_f32 v79, 0x41000000, v75\n\t"
    "v_exp_f32 v76, v76\n\t"
    "v_exp_f32 v78, v78\n\t"
    "v_fract_f32 v77, v77\n\t"
    "v_fract_f32 v79, v79\n\t"
    "v_cos_f32 v52, v77\n\t"
    "v_cos_f32 v54, v79\n\t"
    "v_sin_f32 v53, v77\n\t"
    "v_sin_f32 v55, v79\n\t"
    "v_mul_f32 v52, v76, v52\n\t"
    "v_mul_f32 v54, v78, v54\n\t"
    "v_mul_f32 v53, v76, v53\n\t"
    "v_mul_f32 v55, v78, v55\n\t"
    "v_exp_f32 v76, v72\n\t"
    "v_exp_f32 v78, v74\n\t"
    "v_fract_f32 v77, v73\n\t"
    "v_fract_f32 v79, v75\n\t"
    "v_cos_f32 v40, v77\n\t"
    "v_cos_f32 v42, v79\n\t"
    "v_sin_f32 v41, v77\n\t"
    "v_sin_f32 v43, v79\n\t"
    "v_mul_f32 v40, v76, v40\n\t"
    "v_mul_f32 v42, v78, v42\n\t"
    "v_mul_f32 v41, v76, v41\n\t"
    "v_mul_f32 v43, v78, v43\n\t"
    "v_mul_f32 v76, 0x43800000, v72\n\t"
    "v_mul_f32 v78, 0x43800000, v74\n\t"
    "v_mul_f32 v77, 0x43800000, v73\n\t"
    "v_mul_f32 v79, 0x43800000, v75\n\t"
    "v_exp_f32 v76, v76\n\t"
    "v_exp_f32 v78, v78\n\t"
    "v_fract_f32 v77, v77\n\t"
    "v_fract_f32 v79, v79\n\t"
    "v_cos_f32 v56, v77\n\t"
    "v_cos_f32 v58, v79\n\t"
    "v_sin_f32 v57, v77\n\t"
    "v_sin_f32 v59, v79\n\t"
    "s_add_u32 s78, s36, 589824\n\t"
    "s_addc_u32 s79, s37, 0\n\t"
    "global_store_dwordx4 v222, v[176:179], s[78:79] sc1\n\t"
    "v_mul_f32 v56, v76, v56\n\t"
    "v_mul_f32 v58, v78, v58\n\t"
    "v_mul_f32 v57, v76, v57\n\t"
    "v_mul_f32 v59, v78, v59\n\t"
    "v_lshlrev_b32 v68, 16, v34\n\t"
    "v_and_b32 v70, 0xffff0000, v34\n\t"
    "v_lshlrev_b32 v69, 16, v35\n\t"
    "v_and_b32 v71, 0xffff0000, v35\n\t"
    "v_lshlrev_b32 v76, 16, v36\n\t"
    "v_and_b32 v78, 0xffff0000, v36\n\t"
    "v_lshlrev_b32 v77, 16, v37\n\t"
    "v_and_b32 v79, 0xffff0000, v37\n\t"
    "v_mul_f32 v68, v38, v68\n\t"
    "v_mul_f32 v70, v38, v70\n\t"
    "v_mul_f32 v69, v38, v69\n\t"
    "v_mul_f32 v71, v38, v71\n\t"
    "v_pk_mul_f32 v[72:73], v[68:69], v[76:77] op_sel:[0,0] op_sel_hi:[0,1]\n\t"
    "v_pk_mul_f32 v[74:75], v[70:71], v[78:79] op_sel:[0,0] op_sel_hi:[0,1]\n\t"
    "v_pk_fma_f32 v[64:65], v[68:69], v[76:77], v[72:73] op_sel:[1,1,0] op_sel_hi:[1,0,1] neg_lo:[0,1,0]\n\t"
    "v_pk_fma_f32 v[66:67], v[70:71], v[78:79], v[74:75] op_sel:[1,1,0] op_sel_hi:[1,0,1] neg_lo:[0,1,0]\n\t"
    "s_waitcnt vmcnt(6)\n\t"
    "v_readfirstlane_b32 s66, v96\n\t"
    "v_readfirstlane_b32 s67, v97\n\t"
    "s_nop 0\n\t"
    "s_add_u32 s66, s66, 1\n\t"
    "s_add_u32 s67, s67, 1\n\t"
    "v_mul_f32 v60, 0x46800000, v52\n\t"
    "v_mul_f32 v61, 0x46800000, v53\n\t"
    "v_mul_f32 v62, 0x46800000, v54\n\t"
    "v_mul_f32 v63, 0x46800000, v55\n\t"
    "v_mov_b32 v86, 0x7400\n\t"
    "v_mov_b32 v87, 0x7400\n\t"
    "ds_write_b64 v4, v[86:87]\n\t"
    "v_cvt_pk_f16_f32 v80, v60, v61\n\t"
    "v_cvt_pk_f16_f32 v81, v62, v63\n\t"
    "ds_write_b64 v4, v[80:81] offset:528\n\t"
    "v_max3_f32 v88, |v64|, |v65|, |v66|\n\t"
    "v_pk_mul_f32 v[72:73], v[60:61], v[52:53] op_sel:[0,0] op_sel_hi:[0,1]\n\t"
    "v_pk_mul_f32 v[74:75], v[62:63], v[54:55] op_sel:[0,0] op_sel_hi:[0,1]\n\t"
    "v_pk_mul_f32 v[76:77], v[40:41], v[40:41] op_sel:[0,0] op_sel_hi:[0,1]\n\t"
    "v_pk_mul_f32 v[78:79], v[42:43], v[42:43] op_sel:[0,0] op_sel_hi:[0,1]\n\t"
    "v_pk_fma_f32 v[60:61], v[60:61], v[52:53], v[72:73] op_sel:[1,1,0] op_sel_hi:[1,0,1] neg_lo:[0,1,0]\n\t"
    "v_pk_fma_f32 v[62:63], v[62:63], v[54:55], v[74:75] op_sel:[1,1,0] op_sel_hi:[1,0,1] neg_lo:[0,1,0]\n\t"
    "v_pk_fma_f32 v[44:45], v[40:41], v[40:41], v[76:77] op_sel:[1,1,0] op_sel_hi:[1,0,1] neg_lo:[0,1,0]\n\t"
    "v_pk_fma_f32 v[46:47], v[42:43], v[42:43], v[78:79] op_sel:[1,1,0] op_sel_hi:[1,0,1] neg_lo:[0,1,0]\n\t"
    "v_cvt_pk_f16_f32 v82, v60, v61\n\t"
    "v_cvt_pk_f16_f32 v83, v62, v63\n\t"
    "ds_write_b64 v4, v[82:83] offset:1056\n\t"
    "v_max_f32_e64 v88, |v67|, v88\n\t"
    "v_pk_mul_f32 v[72:73], v[60:61], v[52:53] op_sel:[0,0] op_sel_hi:[0,1]\n\t"
    "buffer_load_dwordx2 v[24:25], v9, s[12:15], s17 offen offset:0\n\t"
    "v_pk_mul_f32 v[74:75], v[62:63], v[54:55] op_sel:[0,0] op_sel_hi:[0,1]\n\t"
    "v_pk_mul_f32 v[76:77], v[44:45], v[44:45] op_sel:[0,0] op_sel_hi:[0,1]\n\t"
    "v_pk_mul_f32 v[78:79], v[46:47], v[46:47] op_sel:[0,0] op_sel_hi:[0,1]\n\t"
    "v_pk_fma_f32 v[60:61], v[60:61], v[52:53], v[72:73] op_sel:[1,1,0] op_sel_hi:[1,0,1] neg_lo:[0,1,0]\n\t"
    "v_pk_fma_f32 v[62:63], v[62:63], v[54:55], v[74:75] op_sel:[1,1,0] op_sel_hi:[1,0,1] neg_lo:[0,1,0]\n\t"
    "v_pk_fma_f32 v[48:49], v[44:45], v[44:45], v[76:77] op_sel:[1,1,0] op_sel_hi:[1,0,1] neg_lo:[0,1,0]\n\t"
    "v_pk_fma_f32 v[50:51], v[46:47], v[46:47], v[78:79] op_sel:[1,1,0] op_sel_hi:[1,0,1] neg_lo:[0,1,0]\n\t"
    "v_cvt_pk_f16_f32 v84, v60, v61\n\t"
    "v_cvt_pk_f16_f32 v85, v62, v63\n\t"
    "ds_write_b64 v4, v[84:85] offset:1584\n\t"
    "v_max_f32_dpp v88, v88, v88 quad_perm:[1,0,3,2] row_mask:0xf bank_mask:0xf\n\t"
    "v_pk_mul_f32 v[72:73], v[60:61], v[52:53] op_sel:[0,0] op_sel_hi:[0,1]\n\t"
    "v_pk_mul_f32 v[74:75], v[62:63], v[54:55] op_sel:[0,0] op_sel_hi:[0,1]\n\t"
    "v_pk_fma_f32 v[60:61], v[60:61], v[52:53], v[72:73] op_sel:[1,1,0] op_sel_hi:[1,0,1] neg_lo:[0,1,0]\n\t"
    "v_pk_fma_f32 v[62:63], v[62:63], v[54:55], v[74:75] op_sel:[1,1,0] op_sel_hi:[1,0,1] neg_lo:[0,1,0]\n\t"
    "v_cvt_pk_f16_f32 v86, v60, v61\n\t"
    "v_cvt_pk_f16_f32 v87, v62, v63\n\t"
    "ds_write_b64 v4, v[86:87] offset:2112\n\t"
    "v_max_f32_dpp v88, v88, v88 quad_perm:[2,3,0,1] row_mask:0xf bank_mask:0xf\n\t"
    "v_pk_mul_f32 v[72:73], v[60:61], v[52:53] op_sel:[0,0] op_sel_hi:[0,1]\n\t"
    "v_pk_mul_f32 v[74:75], v[62:63], v[54:55] op_sel:[0,0] op_sel_hi:[0,1]\n\t"
    "v_pk_fma_f32 v[60:61], v[60:61], v[52:53], v[72:73] op_sel:[1,1,0] op_sel_hi:[1,0,1] neg_lo:[0,1,0]\n\t"
    "v_pk_fma_f32 v[62:63], v[62:63], v[54:55], v[74:75] op_sel:[1,1,0] op_sel_hi:[1,0,1] neg_lo:[0,1,0]\n\t"
    "v_cvt_pk_f16_f32 v80, v60, v61\n\t"
    "v_cvt_pk_f16_f32 v81, v62, v63\n\t"
    "ds_write_b64 v4, v[80:81] offset:2640\n\t"
    "v_max_f32_dpp v88, v88, v88 row_half_mirror row_mask:0xf bank_mask:0xf\n\t"
    "v_pk_mul_f32 v[72:73], v[60:61], v[52:53] op_sel:[0,0] op_sel_hi:[0,1]\n\t"
    "v_pk_mul_f32 v[74:75], v[62:63], v[54:55] op_sel:[0,0] op_sel_hi:[0,1]\n\t"
    "v_pk_fma_f32 v[60:61], v[60:61], v[52:53], v[72:73] op_sel:[1,1,0] op_sel_hi:[1,0,1] neg_lo:[0,1,0]\n\t"
    "v_pk_fma_f32 v[62:63], v[62:63], v[54:55], v[74:75] op_sel:[1,1,0] op_sel_hi:[1,0,1] neg_lo:[0,1,0]\n\t"
    "v_cvt_pk_f16_f32 v82, v60, v61\n\t"
    "v_cvt_pk_f16_f32 v83, v62, v63\n\t"
    "ds_write_b64 v4, v[82:83] offset:3168\n\t"
    "v_max_f32_dpp v88, v88, v88 row_mirror row_mask:0xf bank_mask:0xf\n\t"
    "v_pk_mul_f32 v[72:73], v[60:61], v[52:53] op_sel:[0,0] op_sel_hi:[0,1]\n\t"
    "v_pk_mul_f32 v[74:75], v[62:63], v[54:55] op_sel:[0,0] op_sel_hi:[0,1]\n\t"
    "v_pk_fma_f32 v[60:61], v[60:61], v[52:53], v[72:73] op_sel:[1,1,0] op_sel_hi:[1,0,1] neg_lo:[0,1,0]\n\t"
    "v_pk_fma_f32 v[62:63], v[62:63], v[54:55], v[74:75] op_sel:[1,1,0] op_sel_hi:[1,0,1] neg_lo:[0,1,0]\n\t"
    "v_cvt_pk_f16_f32 v84, v60, v61\n\t"
    "s_add_u32 s78, s36, 1179648\n\t"
    "s_addc_u32 s79, s37, 0\n\t"
    "global_store_dwordx4 v222, v[176:179], s[78:79] sc1\n\t"
    "v_cvt_pk_f16_f32 v85, v62, v63\n\t"
    "ds_write_b64 v4, v[84:85] offset:3696\n\t"
    "v_readlane_b32 s28, v88, 0\n\t"
    "v_readlane_b32 s29, v88, 16\n\t"
    "v_readlane_b32 s30, v88, 32\n\t"
    "v_readlane_b32 s31, v88, 48\n\t"
    "v_pk_mul_f32 v[72:73], v[60:61], v[52:53] op_sel:[0,0] op_sel_hi:[0,1]\n\t"
    "v_pk_mul_f32 v[74:75], v[62:63], v[54:55] op_sel:[0,0] op_sel_hi:[0,1]\n\t"
    "v_pk_fma_f32 v[60:61], v[60:61], v[52:53], v[72:73] op_sel:[1,1,0] op_sel_hi:[1,0,1] neg_lo:[0,1,0]\n\t"
    "v_pk_fma_f32 v[62:63], v[62:63], v[54:55], v[74:75] op_sel:[1,1,0] op_sel_hi:[1,0,1] neg_lo:[0,1,0]\n\t"
    "v_cvt_pk_f16_f32 v86, v60, v61\n\t"
    "v_cvt_pk_f16_f32 v87, v62, v63\n\t"
    "ds_write_b64 v4, v[86:87] offset:4224\n\t"
    "s_max_u32 s28, s28, s29\n\t"
    "s_max_u32 s30, s30, s31\n\t"
    "s_max_u32 s28, s28, s30\n\t"
    "s_lshr_b32 s28, s28, 23\n\t"
    "s_sub_u32 s33, 140, s28\n\t"
    "s_sub_u32 s34, s28, 154\n\t"
    "v_ldexp_f32 v64, v64, s33\n\t"
    "v_ldexp_f32 v65, v65, s33\n\t"
    "v_ldexp_f32 v66, v66, s33\n\t"
    "v_ldexp_f32 v67, v67, s33\n\t"
    "v_pk_mul_f32 v[72:73], v[60:61], v[52:53] op_sel:[0,0] op_sel_hi:[0,1]\n\t"
    "v_pk_mul_f32 v[74:75], v[62:63], v[54:55] op_sel:[0,0] op_sel_hi:[0,1]\n\t"
    "v_pk_fma_f32 v[60:61], v[60:61], v[52:53], v[72:73] op_sel:[1,1,0] op_sel_hi:[1,0,1] neg_lo:[0,1,0]\n\t"
    "v_pk_fma_f32 v[62:63], v[62:63], v[54:55], v[74:75] op_sel:[1,1,0] op_sel_hi:[1,0,1] neg_lo:[0,1,0]\n\t"
    "v_cvt_pk_f16_f32 v80, v60, v61\n\t"
    "v_cvt_pk_f16_f32 v81, v62, v63\n\t"
    "ds_write_b64 v4, v[80:81] offset:4752\n\t"
    "v_cvt_pk_f16_f32 v82, v64, -v65\n\t"
    "v_cvt_pk_f16_f32 v83, v66, -v67\n\t"
    "ds_write_b64 v4, v[82:83] offset:16896\n\t"
    "v_pk_mul_f32 v[72:73], v[60:61], v[52:53] op_sel:[0,0] op_sel_hi:[0,1]\n\t"
    "v_pk_mul_f32 v[74:75], v[62:63], v[54:55] op_sel:[0,0] op_sel_hi:[0,1]\n\t"
    "v_pk_mul_f32 v[76:77], v[64:65], v[40:41] op_sel:[0,0] op_sel_hi:[0,1]\n\t"
    "v_pk_mul_f32 v[78:79], v[66:67], v[42:43] op_sel:[0,0] op_sel_hi:[0,1]\n\t"
    "v_pk_fma_f32 v[60:61], v[60:61], v[52:53], v[72:73] op_sel:[1,1,0] op_sel_hi:[1,0,1] neg_lo:[0,1,0]\n\t"
    "v_pk_fma_f32 v[62:63], v[62:63], v[54:55], v[74:75] op_sel:[1,1,0] op_sel_hi:[1,0,1] neg_lo:[0,1,0]\n\t"
    "v_pk_fma_f32 v[68:69], v[64:65], v[40:41], v[76:77] op_sel:[1,1,0] op_sel_hi:[1,0,1] neg_lo:[0,1,0]\n\t"
    "s_add_u32 s78, s36, 1769472\n\t"
    "s_addc_u32 s79, s37, 0\n\t"
    "global_store_dwordx4 v222, v[176:179], s[78:79] sc1\n\t"
    "v_pk_fma_f32 v[70:71], v[66:67], v[42:43], v[78:79] op_sel:[1,1,0] op_sel_hi:[1,0,1] neg_lo:[0,1,0]\n\t"
    "v_cvt_pk_f16_f32 v84, v60, v61\n\t"
    "v_cvt_pk_f16_f32 v85, v62, v63\n\t"
    "v_cvt_pk_f16_f32 v86, v68, -v69\n\t"
    "v_cvt_pk_f16_f32 v87, v70, -v71\n\t"
    "ds_write_b64 v4, v[84:85] offset:5280\n\t"
    "ds_write_b64 v4, v[86:87] offset:17424\n\t"
    "v_pk_mul_f32 v[72:73], v[60:61], v[52:53] op_sel:[0,0] op_sel_hi:[0,1]\n\t"
    "v_pk_mul_f32 v[74:75], v[62:63], v[54:55] op_sel:[0,0] op_sel_hi:[0,1]\n\t"
    "v_pk_mul_f32 v[76:77], v[68:69], v[40:41] op_sel:[0,0] op_sel_hi:[0,1]\n\t"
    "v_pk_mul_f32 v[78:79], v[70:71], v[42:43] op_sel:[0,0] op_sel_hi:[0,1]\n\t"
    "v_pk_fma_f32 v[60:61], v[60:61], v[52:53], v[72:73] op_sel:[1,1,0] op_sel_hi:[1,0,1] neg_lo:[0,1,0]\n\t"
    "v_pk_fma_f32 v[62:63], v[62:63], v[54:55], v[74:75] op_sel:[1,1,0] op_sel_hi:[1,0,1] neg_lo:[0,1,0]\n\t"
    "v_pk_fma_f32 v[68:69], v[68:69], v[40:41], v[76:77] op_sel:[1,1,0] op_sel_hi:[1,0,1] neg_lo:[0,1,0]\n\t"
    "v_pk_fma_f32 v[70:71], v[70:71], v[42:43], v[78:79] op_sel:[1,1,0] op_sel_hi:[1,0,1] neg_lo:[0,1,0]\n\t"
    "v_cvt_pk_f16_f32 v80, v60, v61\n\t"
    "v_cvt_pk_f16_f32 v81, v62, v63\n\t"
    "v_cvt_pk_f16_f32 v82, v68, -v69\n\t"
    "v_cvt_pk_f16_f32 v83, v70, -v71\n\t"
    "ds_write_b64 v4, v[80:81] offset:5808\n\t"
    "ds_write_b64 v4, v[82:83] offset:17952\n\t"
    "v_pk_mul_f32 v[72:73], v[60:61], v[52:53] op_sel:[0,0] op_sel_hi:[0,1]\n\t"
    "v_pk_mul_f32 v[74:75], v[62:63], v[54:55] op_sel:[0,0] op_sel_hi:[0,1]\n\t"
    "v_pk_mul_f32 v[76:77], v[68:69], v[40:41] op_sel:[0,0] op_sel_hi:[0,1]\n\t"
    "v_pk_mul_f32 v[78:79], v[70:71], v[42:43] op_sel:[0,0] op_sel_hi:[0,1]\n\t"
    "v_pk_fma_f32 v[60:61], v[60:61], v[52:53], v[72:73] op_sel:[1,1,0] op_sel_hi:[1,0,1] neg_lo:[0,1,0]\n\t"
    "v_pk_fma_f32 v[62:63], v[62:63], v[54:55], v[74:75] op_sel:[1,1,0] op_sel_hi:[1,0,1] neg_lo:[0,1,0]\n\t"
    "v_pk_fma_f32 v[68:69], v[68:69], v[40:41], v[76:77] op_sel:[1,1,0] op_sel_hi:[1,0,1] neg_lo:[0,1,0]\n\t"
    "v_pk_fma_f32 v[70:71], v[70:71], v[42:43], v[78:79] op_sel:[1,1,0] op_sel_hi:[1,0,1] neg_lo:[0,1,0]\n\t"
    "v_cvt_pk_f16_f32 v84, v60, v61\n\t"
    "v_cvt_pk_f16_f32 v85, v62, v63\n\t"
    "v_cvt_pk_f16_f32 v86, v68, -v69\n\t"
    "v_cvt_pk_f16_f32 v87, v70, -v71\n\t"
    "ds_write_b64 v4, v[84:85] offset:6336\n\t"
    "ds_write_b64 v4, v[86:87] offset:18480\n\t"
    "v_pk_mul_f32 v[72:73], v[60:61], v[52:53] op_sel:[0,0] op_sel_hi:[0,1]\n\t"
    "v_pk_mul_f32 v[74:75], v[62:63], v[54:55] op_sel:[0,0] op_sel_hi:[0,1]\n\t"
    "v_pk_mul_f32 v[76:77], v[68:69], v[40:41] op_sel:[0,0] op_sel_hi:[0,1]\n\t"
    "v_pk_mul_f32 v[78:79], v[70:71], v[42:43] op_sel:[0,0] op_sel_hi:[0,1]\n\t"
    "v_pk_fma_f32 v[60:61], v[60:61], v[52:53], v[72:73] op_sel:[1,1,0] op_sel_hi:[1,0,1] neg_lo:[0,1,0]\n\t"
    "buffer_load_dwordx2 v[26:27], v9, s[12:15], s17 offen offset:1152\n\t"
    "v_pk_fma_f32 v[62:63], v[62:63], v[54:55], v[74:75] op_sel:[1,1,0] op_sel_hi:[1,0,1] neg_lo:[0,1,0]\n\t"
    "v_pk_fma_f32 v[68:69], v[68:69], v[40:41], v[76:77] op_sel:[1,1,0] op_sel_hi:[1,0,1] neg_lo:[0,1,0]\n\t"
    "v_pk_fma_f32 v[70:71], v[70:71], v[42:43], v[78:79] op_sel:[1,1,0] op_sel_hi:[1,0,1] neg_lo:[0,1,0]\n\t"
    "v_cvt_pk_f16_f32 v80, v60, v61\n\t"
    "v_cvt_pk_f16_f32 v81, v62, v63\n\t"
    "v_cvt_pk_f16_f32 v82, v68, -v69\n\t"
    "v_cvt_pk_f16_f32 v83, v70, -v71\n\t"
    "ds_write_b64 v4, v[80:81] offset:6864\n\t"
    "ds_write_b64 v4, v[82:83] offset:19008\n\t"
    "v_pk_mul_f32 v[72:73], v[60:61], v[52:53] op_sel:[0,0] op_sel_hi:[0,1]\n\t"
    "v_pk_mul_f32 v[74:75], v[62:63], v[54:55] op_sel:[0,0] op_sel_hi:[0,1]\n\t"
    "v_pk_mul_f32 v[76:77], v[68:69], v[40:41] op_sel:[0,0] op_sel_hi:[0,1]\n\t"
    "v_pk_mul_f32 v[78:79], v[70:71], v[42:43] op_sel:[0,0] op_sel_hi:[0,1]\n\t"
    "v_pk_fma_f32 v[60:61], v[60:61], v[52:53], v[72:73] op_sel:[1,1,0] op_sel_hi:[1,0,1] neg_lo:[0,1,0]\n\t"
    "v_pk_fma_f32 v[62:63], v[62:63], v[54:55], v[74:75] op_sel:[1,1,0] op_sel_hi:[1,0,1] neg_lo:[0,1,0]\n\t"
    "v_pk_fma_f32 v[68:69], v[68:69], v[40:41], v[76:77] op_sel:[1,1,0] op_sel_hi:[1,0,1] neg_lo:[0,1,0]\n\t"
    "v_pk_fma_f32 v[70:71], v[70:71], v[42:43], v[78:79] op_sel:[1,1,0] op_sel_hi:[1,0,1] neg_lo:[0,1,0]\n\t"
    "v_cvt_pk_f16_f32 v84, v60, v61\n\t"
    "v_cvt_pk_f16_f32 v85, v62, v63\n\t"
    "v_cvt_pk_f16_f32 v86, v68, -v69\n\t"
    "v_cvt_pk_f16_f32 v87, v70, -v71\n\t"
    "ds_write_b64 v4, v[84:85] offset:7392\n\t"
    "ds_write_b64 v4, v[86:87] offset:19536\n\t"
    "v_pk_mul_f32 v[72:73], v[60:61], v[52:53] op_sel:[0,0] op_sel_hi:[0,1]\n\t"
    "v_pk_mul_f32 v[74:75], v[62:63], v[54:55] op_sel:[0,0] op_sel_hi:[0,1]\n\t"
    "v_pk_mul_f32 v[76:77], v[68:69], v[40:41] op_sel:[0,0] op_sel_hi:[0,1]\n\t"
    "v_pk_mul_f32 v[78:79], v[70:71], v[42:43] op_sel:[0,0] op_sel_hi:[0,1]\n\t"
    "v_pk_fma_f32 v[60:61], v[60:61], v[52:53], v[72:73] op_sel:[1,1,0] op_sel_hi:[1,0,1] neg_lo:[0,1,0]\n\t"
    "v_pk_fma_f32 v[62:63], v[62:63], v[54:55], v[74:75] op_sel:[1,1,0] op_sel_hi:[1,0,1] neg_lo:[0,1,0]\n\t"
    "v_pk_fma_f32 v[68:69], v[68:69], v[40:41], v[76:77] op_sel:[1,1,0] op_sel_hi:[1,0,1] neg_lo:[0,1,0]\n\t"
    "v_pk_fma_f32 v[70:71], v[70:71], v[42:43], v[78:79] op_sel:[1,1,0] op_sel_hi:[1,0,1] neg_lo:[0,1,0]\n\t"
    "v_cvt_pk_f16_f32 v80, v60, v61\n\t"
    "v_cvt_pk_f16_f32 v81, v62, v63\n\t"
    "v_cvt_pk_f16_f32 v82, v68, -v69\n\t"
    "v_cvt_pk_f16_f32 v83, v70, -v71\n\t"
    "ds_write_b64 v4, v[80:81] offset:7920\n\t"
    "ds_write_b64 v4, v[82:83] offset:20064\n\t"
    "v_pk_mul_f32 v[72:73], v[60:61], v[52:53] op_sel:[0,0] op_sel_hi:[0,1]\n\t"
    "v_pk_mul_f32 v[74:75], v[62:63], v[54:55] op_sel:[0,0] op_sel_hi:[0,1]\n\t"
    "v_pk_mul_f32 v[76:77], v[68:69], v[40:41] op_sel:[0,0] op_sel_hi:[0,1]\n\t"
    "v_pk_mul_f32 v[78:79], v[70:71], v[42:43] op_sel:[0,0] op_sel_hi:[0,1]\n\t"
    "v_pk_fma_f32 v[60:61], v[60:61], v[52:53], v[72:73] op_sel:[1,1,0] op_sel_hi:[1,0,1] neg_lo:[0,1,0]\n\t"
    "v_pk_fma_f32 v[62:63], v[62:63], v[54:55], v[74:75] op_sel:[1,1,0] op_sel_hi:[1,0,1] neg_lo:[0,1,0]\n\t"
    "v_pk_fma_f32 v[68:69], v[68:69], v[40:41], v[76:77] op_sel:[1,1,0] op_sel_hi:[1,0,1] neg_lo:[0,1,0]\n\t"
    "v_pk_fma_f32 v[70:71], v[70:71], v[42:43], v[78:79] op_sel:[1,1,0] op_sel_hi:[1,0,1] neg_lo:[0,1,0]\n\t"
    "v_cvt_pk_f16_f32 v84, v60, v61\n\t"
    "v_cvt_pk_f16_f32 v85, v62, v63\n\t"
    "v_cvt_pk_f16_f32 v86, v68, -v69\n\t"
    "v_cvt_pk_f16_f32 v87, v70, -v71\n\t"
    "ds_write_b64 v4, v[84:85] offset:8448\n\t"
    "ds_write_b64 v4, v[86:87] offset:20592\n\t"
    "v_pk_mul_f32 v[72:73], v[60:61], v[52:53] op_sel:[0,0] op_sel_hi:[0,1]\n\t"
    "v_pk_mul_f32 v[74:75], v[62:63], v[54:55] op_sel:[0,0] op_sel_hi:[0,1]\n\t"
    "v_pk_mul_f32 v[76:77], v[64:65], v[56:57] op_sel:[0,0] op_sel_hi:[0,1]\n\t"
    "v_pk_mul_f32 v[78:79], v[66:67], v[58:59] op_sel:[0,0] op_sel_hi:[0,1]\n\t"
    "v_pk_fma_f32 v[60:61], v[60:61], v[52:53], v[72:73] op_sel:[1,1,0] op_sel_hi:[1,0,1] neg_lo:[0,1,0]\n\t"
    "v_pk_fma_f32 v[62:63], v[62:63], v[54:55], v[74:75] op_sel:[1,1,0] op_sel_hi:[1,0,1] neg_lo:[0,1,0]\n\t"
    "v_pk_fma_f32 v[64:65], v[64:65], v[56:57], v[76:77] op_sel:[1,1,0] op_sel_hi:[1,0,1] neg_lo:[0,1,0]\n\t"
    "v_pk_fma_f32 v[66:67], v[66:67], v[58:59], v[78:79] op_sel:[1,1,0] op_sel_hi:[1,0,1] neg_lo:[0,1,0]\n\t"
    "v_cvt_pk_f16_f32 v80, v60, v61\n\t"
    "v_cvt_pk_f16_f32 v81, v62, v63\n\t"
    "v_cvt_pk_f16_f32 v82, v64, -v65\n\t"
    "v_cvt_pk_f16_f32 v83, v66, -v67\n\t"
    "ds_write_b64 v4, v[80:81] offset:8976\n\t"
    "ds_write_b64 v4, v[82:83] offset:21120\n\t"
    "v_pk_mul_f32 v[72:73], v[60:61], v[52:53] op_sel:[0,0] op_sel_hi:[0,1]\n\t"
    "v_pk_mul_f32 v[74:75], v[62:63], v[54:55] op_sel:[0,0] op_sel_hi:[0,1]\n\t"
    "v_pk_mul_f32 v[76:77], v[64:65], v[44:45] op_sel:[0,0] op_sel_hi:[0,1]\n\t"
    "v_pk_mul_f32 v[78:79], v[66:67], v[46:47] op_sel:[0,0] op_sel_hi:[0,1]\n\t"
    "v_pk_fma_f32 v[60:61], v[60:61], v[52:53], v[72:73] op_sel:[1,1,0] op_sel_hi:[1,0,1] neg_lo:[0,1,0]\n\t"
    "v_pk_fma_f32 v[62:63], v[62:63], v[54:55], v[74:75] op_sel:[1,1,0] op_sel_hi:[1,0,1] neg_lo:[0,1,0]\n\t"
    "v_pk_fma_f32 v[68:69], v[64:65], v[44:45], v[76:77] op_sel:[1,1,0] op_sel_hi:[1,0,1] neg_lo:[0,1,0]\n\t"
    "v_pk_fma_f32 v[70:71], v[66:67], v[46:47], v[78:79] op_sel:[1,1,0] op_sel_hi:[1,0,1] neg_lo:[0,1,0]\n\t"
    "v_cvt_pk_f16_f32 v84, v60, v61\n\t"
    "v_cvt_pk_f16_f32 v85, v62, v63\n\t"
    "v_cvt_pk_f16_f32 v86, v68, -v69\n\t"
    "v_cvt_pk_f16_f32 v87, v70, -v71\n\t"
    "ds_write_b64 v4, v[84:85] offset:9504\n\t"
    "ds_write_b64 v4, v[86:87] offset:21648\n\t"
    "v_pk_mul_f32 v[72:73], v[60:61], v[52:53] op_sel:[0,0] op_sel_hi:[0,1]\n\t"
    "v_pk_mul_f32 v[74:75], v[62:63], v[54:55] op_sel:[0,0] op_sel_hi:[0,1]\n\t"
    "v_pk_mul_f32 v[76:77], v[68:69], v[44:45] op_sel:[0,0] op_sel_hi:[0,1]\n\t"
    "v_pk_mul_f32 v[78:79], v[70:71], v[46:47] op_sel:[0,0] op_sel_hi:[0,1]\n\t"
    "v_pk_fma_f32 v[60:61], v[60:61], v[52:53], v[72:73] op_sel:[1,1,0] op_sel_hi:[1,0,1] neg_lo:[0,1,0]\n\t"
    "v_pk_fma_f32 v[62:63], v[62:63], v[54:55], v[74:75] op_sel:[1,1,0] op_sel_hi:[1,0,1] neg_lo:[0,1,0]\n\t"
    "v_pk_fma_f32 v[68:69], v[68:69], v[44:45], v[76:77] op_sel:[1,1,0] op_sel_hi:[1,0,1] neg_lo:[0,1,0]\n\t"
    "v_pk_fma_f32 v[70:71], v[70:71], v[46:47], v[78:79] op_sel:[1,1,0] op_sel_hi:[1,0,1] neg_lo:[0,1,0]\n\t"
    "v_cvt_pk_f16_f32 v80, v60, v61\n\t"
    "v_cvt_pk_f16_f32 v81, v62, v63\n\t"
    "v_cvt_pk_f16_f32 v82, v68, -v69\n\t"
    "v_cvt_pk_f16_f32 v83, v70, -v71\n\t"
    "ds_write_b64 v4, v[80:81] offset:10032\n\t"
    "ds_write_b64 v4, v[82:83] offset:22176\n\t"
    "v_pk_mul_f32 v[72:73], v[60:61], v[52:53] op_sel:[0,0] op_sel_hi:[0,1]\n\t"
    "v_pk_mul_f32 v[74:75], v[62:63], v[54:55] op_sel:[0,0] op_sel_hi:[0,1]\n\t"
    "v_pk_mul_f32 v[76:77], v[68:69], v[44:45] op_sel:[0,0] op_sel_hi:[0,1]\n\t"
    "v_pk_mul_f32 v[78:79], v[70:71], v[46:47] op_sel:[0,0] op_sel_hi:[0,1]\n\t"
    "v_pk_fma_f32 v[60:61], v[60:61], v[52:53], v[72:73] op_sel:[1,1,0] op_sel_hi:[1,0,1] neg_lo:[0,1,0]\n\t"
    "v_pk_fma_f32 v[62:63], v[62:63], v[54:55], v[74:75] op_sel:[1,1,0] op_sel_hi:[1,0,1] neg_lo:[0,1,0]\n\t"
    "v_pk_fma_f32 v[68:69], v[68:69], v[44:45], v[76:77] op_sel:[1,1,0] op_sel_hi:[1,0,1] neg_lo:[0,1,0]\n\t"
    "v_pk_fma_f32 v[70:71], v[70:71], v[46:47], v[78:79] op_sel:[1,1,0] op_sel_hi:[1,0,1] neg_lo:[0,1,0]\n\t"
    "v_cvt_pk_f16_f32 v84, v60, v61\n\t"
    "v_cvt_pk_f16_f32 v85, v62, v63\n\t"
    "v_cvt_pk_f16_f32 v86, v68, -v69\n\t"
    "v_cvt_pk_f16_f32 v87, v70, -v71\n\t"
    "ds_write_b64 v4, v[84:85] offset:10560\n\t"
    "ds_write_b64 v4, v[86:87] offset:22704\n\t"
    "v_pk_mul_f32 v[72:73], v[60:61], v[52:53] op_sel:[0,0] op_sel_hi:[0,1]\n\t"
    "v_pk_mul_f32 v[74:75], v[62:63], v[54:55] op_sel:[0,0] op_sel_hi:[0,1]\n\t"
    "v_pk_mul_f32 v[76:77], v[64:65], v[56:57] op_sel:[0,0] op_sel_hi:[0,1]\n\t"
    "v_pk_mul_f32 v[78:79], v[66:67], v[58:59] op_sel:[0,0] op_sel_hi:[0,1]\n\t"
    "v_pk_fma_f32 v[60:61], v[60:61], v[52:53], v[72:73] op_sel:[1,1,0] op_sel_hi:[1,0,1] neg_lo:[0,1,0]\n\t"
    "v_pk_fma_f32 v[62:63], v[62:63], v[54:55], v[74:75] op_sel:[1,1,0] op_sel_hi:[1,0,1] neg_lo:[0,1,0]\n\t"
    "v_pk_fma_f32 v[64:65], v[64:65], v[56:57], v[76:77] op_sel:[1,1,0] op_sel_hi:[1,0,1] neg_lo:[0,1,0]\n\t"
    "v_pk_fma_f32 v[66:67], v[66:67], v[58:59], v[78:79] op_sel:[1,1,0] op_sel_hi:[1,0,1] neg_lo:[0,1,0]\n\t"
    "v_cvt_pk_f16_f32 v80, v60, v61\n\t"
    "v_cvt_pk_f16_f32 v81, v62, v63\n\t"
    "v_cvt_pk_f16_f32 v82, v64, -v65\n\t"
    "v_cvt_pk_f16_f32 v83, v66, -v67\n\t"
    "ds_write_b64 v4, v[80:81] offset:11088\n\t"
    "ds_write_b64 v4, v[82:83] offset:23232\n\t"
    "v_pk_mul_f32 v[72:73], v[60:61], v[52:53] op_sel:[0,0] op_sel_hi:[0,1]\n\t"
    "v_pk_mul_f32 v[74:75], v[62:63], v[54:55] op_sel:[0,0] op_sel_hi:[0,1]\n\t"
    "v_pk_mul_f32 v[76:77], v[64:65], v[48:49] op_sel:[0,0] op_sel_hi:[0,1]\n\t"
    "v_pk_mul_f32 v[78:79], v[66:67], v[50:51] op_sel:[0,0] op_sel_hi:[0,1]\n\t"
    "v_pk_fma_f32 v[60:61], v[60:61], v[52:53], v[72:73] op_sel:[1,1,0] op_sel_hi:[1,0,1] neg_lo:[0,1,0]\n\t"
    "v_pk_fma_f32 v[62:63], v[62:63], v[54:55], v[74:75] op_sel:[1,1,0] op_sel_hi:[1,0,1] neg_lo:[0,1,0]\n\t"
    "v_pk_fma_f32 v[68:69], v[64:65], v[48:49], v[76:77] op_sel:[1,1,0] op_sel_hi:[1,0,1] neg_lo:[0,1,0]\n\t"
    "v_pk_fma_f32 v[70:71], v[66:67], v[50:51], v[78:79] op_sel:[1,1,0] op_sel_hi:[1,0,1] neg_lo:[0,1,0]\n\t"
    "v_cvt_pk_f16_f32 v84, v60, v61\n\t"
    "v_cvt_pk_f16_f32 v85, v62, v63\n\t"
    "v_cvt_pk_f16_f32 v86, v68, -v69\n\t"
    "v_cvt_pk_f16_f32 v87, v70, -v71\n\t"
    "ds_write_b64 v4, v[84:85] offset:11616\n\t"
    "ds_write_b64 v4, v[86:87] offset:23760\n\t"
    "v_pk_mul_f32 v[72:73], v[60:61], v[52:53] op_sel:[0,0] op_sel_hi:[0,1]\n\t"
    "v_pk_mul_f32 v[74:75], v[62:63], v[54:55] op_sel:[0,0] op_sel_hi:[0,1]\n\t"
    "v_pk_mul_f32 v[76:77], v[64:65], v[56:57] op_sel:[0,0] op_sel_hi:[0,1]\n\t"
    "v_pk_mul_f32 v[78:79], v[66:67], v[58:59] op_sel:[0,0] op_sel_hi:[0,1]\n\t"
    "v_pk_fma_f32 v[60:61], v[60:61], v[52:53], v[72:73] op_sel:[1,1,0] op_sel_hi:[1,0,1] neg_lo:[0,1,0]\n\t"
    "v_pk_fma_f32 v[62:63], v[62:63], v[54:55], v[74:75] op_sel:[1,1,0] op_sel_hi:[1,0,1] neg_lo:[0,1,0]\n\t"
    "v_pk_fma_f32 v[64:65], v[64:65], v[56:57], v[76:77] op_sel:[1,1,0] op_sel_hi:[1,0,1] neg_lo:[0,1,0]\n\t"
    "v_pk_fma_f32 v[66:67], v[66:67], v[58:59], v[78:79] op_sel:[1,1,0] op_sel_hi:[1,0,1] neg_lo:[0,1,0]\n\t"
    "v_cvt_pk_f16_f32 v80, v60, v61\n\t"
    "v_cvt_pk_f16_f32 v81, v62, v63\n\t"
    "v_cvt_pk_f16_f32 v82, v64, -v65\n\t"
    "v_cvt_pk_f16_f32 v83, v66, -v67\n\t"
    "ds_write_b64 v4, v[80:81] offset:12144\n\t"
    "ds_write_b64 v4, v[82:83] offset:24288\n\t"
    "v_pk_mul_f32 v[72:73], v[60:61], v[52:53] op_sel:[0,0] op_sel_hi:[0,1]\n\t"
    "v_pk_mul_f32 v[74:75], v[62:63], v[54:55] op_sel:[0,0] op_sel_hi:[0,1]\n\t"
    "v_pk_mul_f32 v[76:77], v[64:65], v[48:49] op_sel:[0,0] op_sel_hi:[0,1]\n\t"
    "v_pk_mul_f32 v[78:79], v[66:67], v[50:51] op_sel:[0,0] op_sel_hi:[0,1]\n\t"
    "v_pk_fma_f32 v[60:61], v[60:61], v[52:53], v[72:73] op_sel:[1,1,0] op_sel_hi:[1,0,1] neg_lo:[0,1,0]\n\t"
    "v_pk_fma_f32 v[62:63], v[62:63], v[54:55], v[74:75] op_sel:[1,1,0] op_sel_hi:[1,0,1] neg_lo:[0,1,0]\n\t"
    "v_pk_fma_f32 v[68:69], v[64:65], v[48:49], v[76:77] op_sel:[1,1,0] op_sel_hi:[1,0,1] neg_lo:[0,1,0]\n\t"
    "v_pk_fma_f32 v[70:71], v[66:67], v[50:51], v[78:79] op_sel:[1,1,0] op_sel_hi:[1,0,1] neg_lo:[0,1,0]\n\t"
    "v_cvt_pk_f16_f32 v84, v60, v61\n\t"
    "v_cvt_pk_f16_f32 v85, v62, v63\n\t"
    "v_cvt_pk_f16_f32 v86, v68, -v69\n\t"
    "v_cvt_pk_f16_f32 v87, v70, -v71\n\t"
    "ds_write_b64 v4, v[84:85] offset:12672\n\t"
    "ds_write_b64 v4, v[86:87] offset:24816\n\t"
    "v_pk_mul_f32 v[72:73], v[60:61], v[52:53] op_sel:[0,0] op_sel_hi:[0,1]\n\t"
    "v_pk_mul_f32 v[74:75], v[62:63], v[54:55] op_sel:[0,0] op_sel_hi:[0,1]\n\t"
    "v_pk_mul_f32 v[76:77], v[64:65], v[56:57] op_sel:[0,0] op_sel_hi:[0,1]\n\t"
    "v_pk_mul_f32 v[78:79], v[66:67], v[58:59] op_sel:[0,0] op_sel_hi:[0,1]\n\t"
    "v_pk_fma_f32 v[60:61], v[60:61], v[52:53], v[72:73] op_sel:[1,1,0] op_sel_hi:[1,0,1] neg_lo:[0,1,0]\n\t"
    "v_pk_fma_f32 v[62:63], v[62:63], v[54:55], v[74:75] op_sel:[1,1,0] op_sel_hi:[1,0,1] neg_lo:[0,1,0]\n\t"
    "v_pk_fma_f32 v[64:65], v[64:65], v[56:57], v[76:77] op_sel:[1,1,0] op_sel_hi:[1,0,1] neg_lo:[0,1,0]\n\t"
    "v_pk_fma_f32 v[66:67], v[66:67], v[58:59], v[78:79] op_sel:[1,1,0] op_sel_hi:[1,0,1] neg_lo:[0,1,0]\n\t"
    "v_cvt_pk_f16_f32 v80, v60, v61\n\t"
    "v_cvt_pk_f16_f32 v81, v62, v63\n\t"
    "v_cvt_pk_f16_f32 v82, v64, -v65\n\t"
    "v_cvt_pk_f16_f32 v83, v66, -v67\n\t"
    "ds_write_b64 v4, v[80:81] offset:13200\n\t"
    "ds_write_b64 v4, v[82:83] offset:25344\n\t"
    "v_pk_mul_f32 v[72:73], v[60:61], v[52:53] op_sel:[0,0] op_sel_hi:[0,1]\n\t"
    "v_pk_mul_f32 v[74:75], v[62:63], v[54:55] op_sel:[0,0] op_sel_hi:[0,1]\n\t"
    "v_pk_mul_f32 v[76:77], v[64:65], v[56:57] op_sel:[0,0] op_sel_hi:[0,1]\n\t"
    "v_pk_mul_f32 v[78:79], v[66:67], v[58:59] op_sel:[0,0] op_sel_hi:[0,1]\n\t"
    "v_pk_fma_f32 v[60:61], v[60:61], v[52:53], v[72:73] op_sel:[1,1,0] op_sel_hi:[1,0,1] neg_lo:[0,1,0]\n\t"
    "v_pk_fma_f32 v[62:63], v[62:63], v[54:55], v[74:75] op_sel:[1,1,0] op_sel_hi:[1,0,1] neg_lo:[0,1,0]\n\t"
    "v_pk_fma_f32 v[64:65], v[64:65], v[56:57], v[76:77] op_sel:[1,1,0] op_sel_hi:[1,0,1] neg_lo:[0,1,0]\n\t"
    "v_pk_fma_f32 v[66:67], v[66:67], v[58:59], v[78:79] op_sel:[1,1,0] op_sel_hi:[1,0,1] neg_lo:[0,1,0]\n\t"
    "v_cvt_pk_f16_f32 v84, v60, v61\n\t"
    "v_cvt_pk_f16_f32 v85, v62, v63\n\t"
    "v_cvt_pk_f16_f32 v86, v64, -v65\n\t"
    "v_cvt_pk_f16_f32 v87, v66, -v67\n\t"
    "ds_write_b64 v4, v[84:85] offset:13728\n\t"
    "ds_write_b64 v4, v[86:87] offset:25872\n\t"
    "v_pk_mul_f32 v[72:73], v[60:61], v[52:53] op_sel:[0,0] op_sel_hi:[0,1]\n\t"
    "v_pk_mul_f32 v[74:75], v[62:63], v[54:55] op_sel:[0,0] op_sel_hi:[0,1]\n\t"
    "v_pk_mul_f32 v[76:77], v[64:65], v[56:57] op_sel:[0,0] op_sel_hi:[0,1]\n\t"
    "v_pk_mul_f32 v[78:79], v[66:67], v[58:59] op_sel:[0,0] op_sel_hi:[0,1]\n\t"
    "v_pk_fma_f32 v[60:61], v[60:61], v[52:53], v[72:73] op_sel:[1,1,0] op_sel_hi:[1,0,1] neg_lo:[0,1,0]\n\t"
    "v_pk_fma_f32 v[62:63], v[62:63], v[54:55], v[74:75] op_sel:[1,1,0] op_sel_hi:[1,0,1] neg_lo:[0,1,0]\n\t"
    "v_pk_fma_f32 v[64:65], v[64:65], v[56:57], v[76:77] op_sel:[1,1,0] op_sel_hi:[1,0,1] neg_lo:[0,1,0]\n\t"
    "v_pk_fma_f32 v[66:67], v[66:67], v[58:59], v[78:79] op_sel:[1,1,0] op_sel_hi:[1,0,1] neg_lo:[0,1,0]\n\t"
    "v_cvt_pk_f16_f32 v80, v60, v61\n\t"
    "v_cvt_pk_f16_f32 v81, v62, v63\n\t"
    "v_cvt_pk_f16_f32 v82, v64, -v65\n\t"
    "v_cvt_pk_f16_f32 v83, v66, -v67\n\t"
    "ds_write_b64 v4, v[80:81] offset:14256\n\t"
    "ds_write_b64 v4, v[82:83] offset:26400\n\t"
    "v_pk_mul_f32 v[72:73], v[60:61], v[52:53] op_sel:[0,0] op_sel_hi:[0,1]\n\t"
    "v_pk_mul_f32 v[74:75], v[62:63], v[54:55] op_sel:[0,0] op_sel_hi:[0,1]\n\t"
    "v_pk_mul_f32 v[76:77], v[64:65], v[56:57] op_sel:[0,0] op_sel_hi:[0,1]\n\t"
    "v_pk_mul_f32 v[78:79], v[66:67], v[58:59] op_sel:[0,0] op_sel_hi:[0,1]\n\t"
    "v_pk_fma_f32 v[60:61], v[60:61], v[52:53], v[72:73] op_sel:[1,1,0] op_sel_hi:[1,0,1] neg_lo:[0,1,0]\n\t"
    "v_pk_fma_f32 v[62:63], v[62:63], v[54:55], v[74:75] op_sel:[1,1,0] op_sel_hi:[1,0,1] neg_lo:[0,1,0]\n\t"
    "v_pk_fma_f32 v[64:65], v[64:65], v[56:57], v[76:77] op_sel:[1,1,0] op_sel_hi:[1,0,1] neg_lo:[0,1,0]\n\t"
    "v_pk_fma_f32 v[66:67], v[66:67], v[58:59], v[78:79] op_sel:[1,1,0] op_sel_hi:[1,0,1] neg_lo:[0,1,0]\n\t"
    "v_cvt_pk_f16_f32 v84, v60, v61\n\t"
    "v_cvt_pk_f16_f32 v85, v62, v63\n\t"
    "v_cvt_pk_f16_f32 v86, v64, -v65\n\t"
    "v_cvt_pk_f16_f32 v87, v66, -v67\n\t"
    "ds_write_b64 v4, v[84:85] offset:14784\n\t"
    "ds_write_b64 v4, v[86:87] offset:26928\n\t"
    "v_pk_mul_f32 v[72:73], v[60:61], v[52:53] op_sel:[0,0] op_sel_hi:[0,1]\n\t"
    "v_pk_mul_f32 v[74:75], v[62:63], v[54:55] op_sel:[0,0] op_sel_hi:[0,1]\n\t"
    "v_pk_mul_f32 v[76:77], v[64:65], v[56:57] op_sel:[0,0] op_sel_hi:[0,1]\n\t"
    "v_pk_mul_f32 v[78:79], v[66:67], v[58:59] op_sel:[0,0] op_sel_hi:[0,1]\n\t"
    "v_pk_fma_f32 v[60:61], v[60:61], v[52:53], v[72:73] op_sel:[1,1,0] op_sel_hi:[1,0,1] neg_lo:[0,1,0]\n\t"
    "v_pk_fma_f32 v[62:63], v[62:63], v[54:55], v[74:75] op_sel:[1,1,0] op_sel_hi:[1,0,1] neg_lo:[0,1,0]\n\t"
    "v_pk_fma_f32 v[64:65], v[64:65], v[56:57], v[76:77] op_sel:[1,1,0] op_sel_hi:[1,0,1] neg_lo:[0,1,0]\n\t"
    "v_pk_fma_f32 v[66:67], v[66:67], v[58:59], v[78:79] op_sel:[1,1,0] op_sel_hi:[1,0,1] neg_lo:[0,1,0]\n\t"
    "v_cvt_pk_f16_f32 v80, v60, v61\n\t"
    "v_cvt_pk_f16_f32 v81, v62, v63\n\t"
    "v_cvt_pk_f16_f32 v82, v64, -v65\n\t"
    "v_cvt_pk_f16_f32 v83, v66, -v67\n\t"
    "ds_write_b64 v4, v[80:81] offset:15312\n\t"
    "ds_write_b64 v4, v[82:83] offset:27456\n\t"
    "v_pk_mul_f32 v[72:73], v[60:61], v[52:53] op_sel:[0,0] op_sel_hi:[0,1]\n\t"
    "v_pk_mul_f32 v[74:75], v[62:63], v[54:55] op_sel:[0,0] op_sel_hi:[0,1]\n\t"
    "v_pk_fma_f32 v[60:61], v[60:61], v[52:53], v[72:73] op_sel:[1,1,0] op_sel_hi:[1,0,1] neg_lo:[0,1,0]\n\t"
    "v_pk_fma_f32 v[62:63], v[62:63], v[54:55], v[74:75] op_sel:[1,1,0] op_sel_hi:[1,0,1] neg_lo:[0,1,0]\n\t"
    "v_cvt_pk_f16_f32 v84, v60, v61\n\t"
    "v_cvt_pk_f16_f32 v85, v62, v63\n\t"
    "ds_write_b64 v4, v[84:85] offset:15840\n\t"
    "v_pk_mul_f32 v[72:73], v[60:61], v[52:53] op_sel:[0,0] op_sel_hi:[0,1]\n\t"
    "v_pk_mul_f32 v[74:75], v[62:63], v[54:55] op_sel:[0,0] op_sel_hi:[0,1]\n\t"
    "v_pk_fma_f32 v[60:61], v[60:61], v[52:53], v[72:73] op_sel:[1,1,0] op_sel_hi:[1,0,1] neg_lo:[0,1,0]\n\t"
    "v_pk_fma_f32 v[62:63], v[62:63], v[54:55], v[74:75] op_sel:[1,1,0] op_sel_hi:[1,0,1] neg_lo:[0,1,0]\n\t"
    "v_cvt_pk_f16_f32 v86, v60, v61\n\t"
    "v_cvt_pk_f16_f32 v87, v62, v63\n\t"
    "ds_write_b64 v4, v[86:87] offset:16368\n\t"
    "ds_read_b128 v[112:115], v7 offset:0\n\t"
    "ds_read_b128 v[116:119], v8 offset:0\n\t"
    "ds_read_b128 v[120:123], v7 offset:32\n\t"
    "ds_read_b128 v[124:127], v8 offset:32\n\t"
    "ds_read_b128 v[128:131], v7 offset:64\n\t"
    "ds_read_b128 v[132:135], v8 offset:64\n\t"
    "s_waitcnt lgkmcnt(4)\n\t"
    "v_mfma_f32_32x32x16_f16 v[96:111], v[112:115], v[116:119], 0\n\t"
    "ds_read_b128 v[136:139], v7 offset:96\n\t"
    "ds_read_b128 v[140:143], v8 offset:96\n\t"
    "v_lshlrev_b32 v91, 4, v0\n\t"
    "v_add_u32 v91, 53248, v91\n\t"
    "s_waitcnt lgkmcnt(4)\n\t"
    "v_mfma_f32_32x32x16_f16 v[96:111], v[120:123], v[124:127], v[96:111]\n\t"
    "ds_read_b128 v[112:115], v7 offset:128\n\t"
    "ds_read_b128 v[116:119], v8 offset:128\n\t"
    "v_add_u32 v92, 8192, v91\n\t"
    "v_lshrrev_b32 v88, 4, v1\n\t"
    "s_waitcnt lgkmcnt(4)\n\t"
    "v_mfma_f32_32x32x16_f16 v[96:111], v[128:131], v[132:135], v[96:111]\n\t"
    "ds_read_b128 v[120:123], v7 offset:160\n\t"
    "ds_read_b128 v[124:127], v8 offset:160\n\t"
    "s_lshl_b32 s28, s3, 3\n\t"
    "s_add_u32 s28, s28, 18432\n\t"
    "s_waitcnt lgkmcnt(4)\n\t"
    "v_mfma_f32_32x32x16_f16 v[96:111], v[136:139], v[140:143], v[96:111]\n\t"
    "ds_read_b128 v[128:131], v7 offset:192\n\t"
    "ds_read_b128 v[132:135], v8 offset:192\n\t"
    "v_lshl_add_u32 v89, v88, 3, v14\n\t"
    "v_subrev_u32 v78, s28, v89\n\t"
    "s_waitcnt lgkmcnt(4)\n\t"
    "v_mfma_f32_32x32x16_f16 v[96:111], v[112:115], v[116:119], v[96:111]\n\t"
    "ds_read_b128 v[136:139], v7 offset:224\n\t"
    "ds_read_b128 v[140:143], v8 offset:224\n\t"
    "v_add_u32 v79, 4608, v78\n\t"
    "v_add_u32 v80, 9216, v78\n\t"
    "s_waitcnt lgkmcnt(4)\n\t"
    "v_mfma_f32_32x32x16_f16 v[96:111], v[120:123], v[124:127], v[96:111]\n\t"
    "ds_read_b128 v[112:115], v7 offset:256\n\t"
    "ds_read_b128 v[116:119], v8 offset:256\n\t"
    "s_bitcmp1_b32 s3, 0\n\t"
    "s_cselect_b32 s72, s67, s66\n\t"
    "s_waitcnt lgkmcnt(4)\n\t"
    "v_mfma_f32_32x32x16_f16 v[96:111], v[128:131], v[132:135], v[96:111]\n\t"
    "ds_read_b128 v[120:123], v7 offset:288\n\t"
    "ds_read_b128 v[124:127], v8 offset:288\n\t"
    "v_mov_b32 v81, s66\n\t"
    "v_mov_b32 v88, s67\n\t"
    "s_waitcnt lgkmcnt(4)\n\t"
    "v_mfma_f32_32x32x16_f16 v[96:111], v[136:139], v[140:143], v[96:111]\n\t"
    "ds_read_b128 v[128:131], v7 offset:320\n\t"
    "ds_read_b128 v[132:135], v8 offset:320\n\t"
    "s_nop 1\n\t"
    "v_mov_b32_dpp v81, v88 quad_perm:[0,1,2,3] row_mask:0xa bank_mask:0xf\n\t"
    "s_waitcnt lgkmcnt(4)\n\t"
    "v_mfma_f32_32x32x16_f16 v[96:111], v[112:115], v[116:119], v[96:111]\n\t"
    "ds_read_b128 v[136:139], v7 offset:352\n\t"
    "ds_read_b128 v[140:143], v8 offset:352\n\t"
    "s_mov_b32 s24, 0x05040100\n\t"
    "s_mov_b32 s25, 0x07060302\n\t"
    "s_waitcnt lgkmcnt(4)\n\t"
    "v_mfma_f32_32x32x16_f16 v[96:111], v[120:123], v[124:127], v[96:111]\n\t"
    "ds_read_b128 v[112:115], v7 offset:384\n\t"
    "ds_read_b128 v[116:119], v8 offset:384\n\t"
    "s_mov_b32 s26, 0x0c04000c\n\t"
    "s_mov_b32 s27, 0x0402010c\n\t"
    "s_waitcnt lgkmcnt(4)\n\t"
    "v_mfma_f32_32x32x16_f16 v[96:111], v[128:131], v[132:135], v[96:111]\n\t"
    "ds_read_b128 v[120:123], v7 offset:416\n\t"
    "ds_read_b128 v[124:127], v8 offset:416\n\t"
    "s_waitcnt lgkmcnt(4)\n\t"
    "v_mfma_f32_32x32x16_f16 v[96:111], v[136:139], v[140:143], v[96:111]\n\t"
    "ds_read_b128 v[128:131], v7 offset:448\n\t"
    "ds_read_b128 v[132:135], v8 offset:448\n\t"
    "s_waitcnt lgkmcnt(4)\n\t"
    "v_mfma_f32_32x32x16_f16 v[96:111], v[112:115], v[116:119], v[96:111]\n\t"
    "ds_read_b128 v[136:139], v7 offset:480\n\t"
    "ds_read_b128 v[140:143], v8 offset:480\n\t"
    "s_waitcnt lgkmcnt(4)\n\t"
    "v_mfma_f32_32x32x16_f16 v[96:111], v[120:123], v[124:127], v[96:111]\n\t"
    "s_waitcnt lgkmcnt(2)\n\t"
    "v_mfma_f32_32x32x16_f16 v[96:111], v[128:131], v[132:135], v[96:111]\n\t"
    "s_waitcnt lgkmcnt(0)\n\t"
    "v_mfma_f32_32x32x16_f16 v[96:111], v[136:139], v[140:143], v[96:111]\n\t"
    "v_mov_b32 v84, 0\n\t"
    "v_mov_b32 v85, 0\n\t"
    "v_mov_b32 v86, 0\n\t"
    "v_mov_b32 v87, 0\n\t"
    "s_mul_i32 s28, s3, 11648\n\t"
    "s_add_u32 s28, s28, 44928\n\t"
    "v_lshrrev_b32 v88, 4, v1\n\t"
    "v_mul_u32_u24 v88, 264, v88\n\t"
    "v_and_b32 v89, 15, v1\n\t"
    "v_lshl_add_u32 v88, v89, 3, v88\n\t"
    "v_add_u32 v88, s28, v88\n\t"
    "s_mul_i32 s28, s3, 4624\n\t"
    "s_add_u32 s28, s28, 145344\n\t"
    "v_lshl_add_u32 v89, v1, 2, s28\n\t"
    "s_mov_b32 s28, 0x03ff03ff\n\t"
    "s_mov_b32 s29, s28\n\t"
    "s_barrier\n\t"
    "buffer_load_dwordx2 v[28:29], v9, s[12:15], s18 offen offset:0\n\t"
    "ds_write_b64 v88, v[84:85]\n\t"
    "v_ldexp_f32 v105, v105, s34\n\t"
    "ds_write_b64 v88, v[84:85] offset:1056\n\t"
    "v_ldexp_f32 v108, v108, s34\n\t"
    "s_mov_b64 exec, s[28:29]\n\t"
    "ds_write_b128 v195, v[84:87]\n\t"
    "s_mov_b64 exec, 0xfff\n\t"
    "ds_write_b32 v89, v84 offset:208\n\t"
    "s_mov_b64 exec, -1\n\t"
    "v_ldexp_f32 v110, v110, s34\n\t"
    "v_ldexp_f32 v96, v96, s34\n\t"
    "v_ldexp_f32 v97, v97, s34\n\t"
    "v_ldexp_f32 v98, v98, s34\n\t"
    "v_ldexp_f32 v99, v99, s34\n\t"
    "v_ldexp_f32 v100, v100, s34\n\t"
    "v_ldexp_f32 v101, v101, s34\n\t"
    "v_ldexp_f32 v102, v102, s34\n\t"
    "v_ldexp_f32 v103, v103, s34\n\t"
    "v_ldexp_f32 v106, v106, s34\n\t"
    "v_ldexp_f32 v109, v109, s34\n\t"
    "v_readlane_b32 s28, v105, 0\n\t"
    "v_readlane_b32 s29, v108, 0\n\t"
    "v_readlane_b32 s30, v110, 32\n\t"
    "v_mov_b32_dpp v104, v96 wave_shl:1 row_mask:0xf bank_mask:0xf\n\t"
    "v_mov_b32_dpp v107, v105 wave_shl:1 row_mask:0xf bank_mask:0xf\n\t"
    "v_add_f32 v96, v39, v96\n\t"
    "v_cvt_pk_bf16_f32 v82, v108, v108\n\t"
    "v_writelane_b32 v107, s29, 63\n\t"
    "v_writelane_b32 v104, s28, 63\n\t"
    "v_writelane_b32 v108, s30, 0\n\t"
    "v_readlane_b32 s31, v82, 0\n\t"
    "v_cvt_pk_bf16_f32 v119, v98, v97\n\t"
    "v_cvt_pk_bf16_f32 v115, v97, v96\n\t"
    "v_cvt_pk_bf16_f32 v135, v105, v105\n\t"
    "v_mov_b32_dpp v103, v104 quad_perm:[0,1,2,3] row_mask:0xc bank_mask:0xf\n\t"
    "v_cvt_pk_bf16_f32 v111, v108, v109\n\t"
    "v_mov_b32_dpp v120, v119 wave_shl:1 row_mask:0xf bank_mask:0xf\n\t"
    "v_mov_b32_dpp v124, v115 wave_shl:1 row_mask:0xf bank_mask:0xf\n\t"
    "v_readlane_b32 s28, v135, 0\n\t"
    "v_readlane_b32 s29, v119, 0\n\t"
    "v_readlane_b32 s30, v115, 0\n\t"
    "v_mov_b32 v82, s31\n\t"
    "v_cvt_pk_bf16_f32 v132, v107, v107\n\t"
    "ds_write_b16 v196, v111 offset:0\n\t"
    "v_cvt_pk_bf16_f32 v133, v107, v106\n\t"
    "ds_write_b16_d16_hi v197, v111 offset:0\n\t"
    "v_cvt_pk_bf16_f32 v134, v106, v106\n\t"
    "ds_write_b16 v196, v111 offset:266\n\t"
    "v_cvt_pk_bf16_f32 v140, v107, v107\n\t"
    "ds_write_b16_d16_hi v197, v111 offset:266\n\t"
    "v_cvt_pk_bf16_f32 v141, v107, v107\n\t"
    "ds_write_b16 v196, v111 offset:532\n\t"
    "v_cvt_pk_bf16_f32 v142, v106, v106\n\t"
    "ds_write_b16_d16_hi v197, v111 offset:532\n\t"
    "v_cvt_pk_bf16_f32 v143, v106, v105\n\t"
    "ds_write_b16 v196, v111 offset:798\n\t"
    "v_cvt_pk_bf16_f32 v136, v107, v107\n\t"
    "ds_write_b16_d16_hi v197, v111 offset:798\n\t"
    "v_cvt_pk_bf16_f32 v137, v107, v107\n\t"
    "v_cvt_pk_bf16_f32 v138, v107, v106\n\t"
    "v_cvt_pk_bf16_f32 v139, v106, v106\n\t"
    "v_cvt_pk_bf16_f32 v128, v107, v107\n\t"
    "v_cvt_pk_bf16_f32 v129, v106, v106\n\t"
    "v_cvt_pk_bf16_f32 v130, v106, v105\n\t"
    "v_cvt_pk_bf16_f32 v131, v105, v105\n\t"
    "global_load_dwordx4 v[40:43], v91, s[36:37] offset:-4096 sc1\n\t"
    "s_lshr_b32 s33, s30, 16\n\t"
    "s_and_b32 s34, s31, 0xffff0000\n\t"
    "v_writelane_b32 v120, s28, 63\n\t"
    "v_writelane_b32 v124, s28, 63\n\t"
    "v_cvt_pk_bf16_f32 v116, v104, v103\n\t"
    "ds_write_b128 v194, v[132:135] offset:0\n\t"
    "v_cvt_pk_bf16_f32 v117, v102, v101\n\t"
    "ds_write_b128 v194, v[140:143] offset:2368\n\t"
    "global_load_dwordx4 v[44:47], v91, s[36:37] offset:0 sc1\n\t"
    "v_cvt_pk_bf16_f32 v118, v100, v99\n\t"
    "ds_write_b128 v194, v[136:139] offset:4736\n\t"
    "v_cvt_pk_bf16_f32 v125, v103, v102\n\t"
    "ds_write_b64 v194, v[128:129] offset:7112\n\t"
    "v_cvt_pk_bf16_f32 v126, v101, v100\n\t"
    "ds_write_b64 v194, v[130:131] offset:7120\n\t"
    "v_cvt_pk_bf16_f32 v127, v99, v98\n\t"
    "v_cvt_pk_bf16_f32 v121, v104, v103\n\t"
    "global_load_dwordx4 v[48:51], v92, s[36:37] offset:-4096 sc1\n\t"
    "v_cvt_pk_bf16_f32 v122, v102, v101\n\t"
    "v_cvt_pk_bf16_f32 v123, v100, v99\n\t"
    "v_cvt_pk_bf16_f32 v112, v103, v102\n\t"
    "v_cvt_pk_bf16_f32 v113, v101, v100\n\t"
    "v_cvt_pk_bf16_f32 v114, v99, v98\n\t"
    "v_writelane_b32 v82, s33, 6\n\t"
    "ds_write_b128 v194, v[116:119] offset:1024\n\t"
    "v_writelane_b32 v82, s33, 7\n\t"
    "global_load_dwordx4 v[52:55], v92, s[36:37] offset:0 sc1\n\t"
    "ds_write_b128 v194, v[124:127] offset:3392\n\t"
    "v_writelane_b32 v82, s29, 8\n\t"
    "ds_write_b128 v194, v[120:123] offset:5760\n\t"
    "v_writelane_b32 v82, s30, 9\n\t"
    "ds_write_b64 v194, v[112:113] offset:8136\n\t"
    "v_writelane_b32 v82, s34, 10\n\t"
    "ds_write_b64 v194, v[114:115] offset:8144\n\t"
    "v_writelane_b32 v82, s34, 11\n\t"
    "global_load_dwordx2 v[72:73], v78, s[36:37] sc1\n\t"
    "s_mov_b64 exec, 0xfff\n\t"
    "ds_write_b32 v212, v82\n\t"
    "s_mov_b64 exec, -1\n\t"
    "global_load_dwordx2 v[74:75], v79, s[36:37] sc1\n\t"
    "global_load_dwordx2 v[76:77], v80, s[36:37] sc1\n\t"
    "buffer_load_dwordx2 v[30:31], v9, s[12:15], s18 offen offset:1152\n\t"
    "v_and_b32 v88, 3, v1\n\t"
    "v_mul_u32_u24 v88, 272, v88\n\t"
    "v_and_b32 v89, 12, v1\n\t"
    "v_lshl_add_u32 v88, v89, 2, v88\n\t"
    "v_lshrrev_b32 v89, 4, v1\n\t"
    "v_lshl_add_u32 v88, v89, 2, v88\n\t"
    "s_mul_i32 s28, s3, 4416\n\t"
    "v_add_u32 v194, s28, v88\n\t"
    "v_add_u32 v195, 17664, v194\n\t"
    "s_waitcnt vmcnt(17)\n\t"
    "v_perm_b32 v82, v18, v16, s24\n\t"
    "ds_write_b32 v10, v82 offset:0\n\t"
    "v_perm_b32 v83, v18, v16, s25\n\t"
    "ds_write_b32 v10, v83 offset:4416\n\t"
    "v_perm_b32 v84, v19, v17, s24\n\t"
    "ds_write_b32 v10, v84 offset:8832\n\t"
    "v_perm_b32 v85, v19, v17, s25\n\t"
    "ds_write_b32 v10, v85 offset:13248\n\t"
    "v_lshlrev_b32 v112, 16, v16\n\t"
    "v_and_b32 v113, 0xffff0000, v16\n\t"
    "v_lshlrev_b32 v114, 16, v17\n\t"
    "v_and_b32 v115, 0xffff0000, v17\n\t"
    "v_lshlrev_b32 v116, 16, v18\n\t"
    "v_and_b32 v117, 0xffff0000, v18\n\t"
    "v_lshlrev_b32 v118, 16, v19\n\t"
    "v_and_b32 v119, 0xffff0000, v19\n\t"
    "s_waitcnt vmcnt(14)\n\t"
    "v_perm_b32 v86, v22, v20, s24\n\t"
    "ds_write_b32 v10, v86 offset:1088\n\t"
    "v_perm_b32 v87, v22, v20, s25\n\t"
    "ds_write_b32 v10, v87 offset:5504\n\t"
    "v_perm_b32 v82, v23, v21, s24\n\t"
    "ds_write_b32 v10, v82 offset:9920\n\t"
    "v_perm_b32 v83, v23, v21, s25\n\t"
    "ds_write_b32 v10, v83 offset:14336\n\t"
    "v_lshlrev_b32 v120, 16, v20\n\t"
    "v_and_b32 v121, 0xffff0000, v20\n\t"
    "v_lshlrev_b32 v122, 16, v21\n\t"
    "v_and_b32 v123, 0xffff0000, v21\n\t"
    "v_lshlrev_b32 v124, 16, v22\n\t"
    "v_and_b32 v125, 0xffff0000, v22\n\t"
    "v_lshlrev_b32 v126, 16, v23\n\t"
    "v_and_b32 v127, 0xffff0000, v23\n\t"
    "s_waitcnt vmcnt(9)\n\t"
    "v_perm_b32 v84, v26, v24, s24\n\t"
    "ds_write_b32 v10, v84 offset:2176\n\t"
    "v_perm_b32 v85, v26, v24, s25\n\t"
    "ds_write_b32 v10, v85 offset:6592\n\t"
    "v_perm_b32 v86, v27, v25, s24\n\t"
    "ds_write_b32 v10, v86 offset:11008\n\t"
    "v_perm_b32 v87, v27, v25, s25\n\t"
    "ds_write_b32 v10, v87 offset:15424\n\t"
    "v_lshlrev_b32 v128, 16, v24\n\t"
    "v_and_b32 v129, 0xffff0000, v24\n\t"
    "v_lshlrev_b32 v130, 16, v25\n\t"
    "v_and_b32 v131, 0xffff0000, v25\n\t"
    "v_lshlrev_b32 v132, 16, v26\n\t"
    "v_and_b32 v133, 0xffff0000, v26\n\t"
    "v_lshlrev_b32 v134, 16, v27\n\t"
    "v_and_b32 v135, 0xffff0000, v27\n\t"
    "s_waitcnt vmcnt(1)\n\t"
    "v_cmpx_eq_u32 vcc, v81, v73\n\t"
    "v_cmpx_eq_u32 vcc, v81, v75\n\t"
    "v_cmpx_eq_u32 vcc, v81, v77\n\t"
    "s_cmp_eq_u64 exec, -1\n\t"
    "s_mov_b64 exec, -1\n\t"
    "s_cbranch_scc0 L_gsm_%=\n\t"
    "L_gbm_%=:\n\t"
    "v_cndmask_b32 v72, 0, v72, s[80:81]\n\t"
    "v_cndmask_b32 v74, 0, v74, s[80:81]\n\t"
    "v_cndmask_b32 v76, 0, v76, s[80:81]\n\t"
    "v_add_f32_dpp v72, v72, v72 quad_perm:[1,0,3,2] row_mask:0xf bank_mask:0xf\n\t"
    "v_add_f32_dpp v74, v74, v74 quad_perm:[1,0,3,2] row_mask:0xf bank_mask:0xf\n\t"
    "v_add_f32_dpp v76, v76, v76 quad_perm:[1,0,3,2] row_mask:0xf bank_mask:0xf\n\t"
    "v_add_f32_dpp v72, v72, v72 quad_perm:[2,3,0,1] row_mask:0xf bank_mask:0xf\n\t"
    "v_add_f32_dpp v74, v74, v74 quad_perm:[2,3,0,1] row_mask:0xf bank_mask:0xf\n\t"
    "v_add_f32_dpp v76, v76, v76 quad_perm:[2,3,0,1] row_mask:0xf bank_mask:0xf\n\t"
    "v_add_f32_dpp v72, v72, v72 row_half_mirror row_mask:0xf bank_mask:0xf\n\t"
    "v_add_f32_dpp v74, v74, v74 row_half_mirror row_mask:0xf bank_mask:0xf\n\t"
    "v_add_f32_dpp v76, v76, v76 row_half_mirror row_mask:0xf bank_mask:0xf\n\t"
    "v_add_f32_dpp v72, v72, v72 row_mirror row_mask:0xf bank_mask:0xf\n\t"
    "v_add_f32_dpp v74, v74, v74 row_mirror row_mask:0xf bank_mask:0xf\n\t"
    "v_add_f32_dpp v76, v76, v76 row_mirror row_mask:0xf bank_mask:0xf\n\t"
    "v_cvt_pk_bf16_f32 v72, v72, v72\n\t"
    "v_cvt_pk_bf16_f32 v74, v74, v74\n\t"
    "v_cvt_pk_bf16_f32 v76, v76, v76\n\t"
    "v_and_b32 v72, 0xffff0000, v72\n\t"
    "v_and_b32 v74, 0xffff0000, v74\n\t"
    "v_and_b32 v76, 0xffff0000, v76\n\t"
    "v_add_f32 v75, 1.0, v74\n\t"
    "v_mul_f32 v72, v76, v72\n\t"
    "v_cvt_pk_bf16_f32 v75, v75, v75\n\t"
    "v_and_b32 v75, 0xffff0000, v75\n\t"
    "v_mul_f32 v75, v76, v75\n\t"
    "v_readlane_b32 s33, v72, 0\n\t"
    "v_readlane_b32 s34, v72, 16\n\t"
    "v_readlane_b32 s35, v72, 32\n\t"
    "v_readlane_b32 vcc_lo, v72, 48\n\t"
    "v_readlane_b32 s28, v75, 0\n\t"
    "v_readlane_b32 s29, v75, 16\n\t"
    "v_readlane_b32 s30, v75, 32\n\t"
    "v_readlane_b32 s31, v75, 48\n\t"
    "v_mov_b32 v32, s33\n\t"
    "v_mov_b32 v33, s34\n\t"
    "v_mov_b32 v34, s35\n\t"
    "v_mov_b32 v35, vcc_lo\n\t"
    "v_cmpx_eq_u32 vcc, s66, v43\n\t"
    "v_cmpx_eq_u32 vcc, s66, v47\n\t"
    "v_cmpx_eq_u32 vcc, s66, v51\n\t"
    "v_cmpx_eq_u32 vcc, s66, v55\n\t"
    "s_cmp_eq_u64 exec, -1\n\t"
    "s_mov_b64 exec, -1\n\t"
    "s_cbranch_scc0 L_gss_%=\n\t"
    "L_gbs_%=:\n\t"
    "v_perm_b32 v43, v41, v40, s26\n\t"
    "v_perm_b32 v43, v42, v43, s27\n\t"
    "v_pk_fma_f32 v[112:113], v[112:113], v[42:43], v[40:41] op_sel:[0,1,0] op_sel_hi:[1,1,0] neg_lo:[0,0,1] neg_hi:[0,0,1]\n\t"
    "v_pk_fma_f32 v[114:115], v[114:115], v[42:43], v[40:41] op_sel:[0,1,0] op_sel_hi:[1,1,0] neg_lo:[0,0,1] neg_hi:[0,0,1]\n\t"
    "v_pk_fma_f32 v[116:117], v[116:117], v[42:43], v[40:41] op_sel:[0,0,1] op_sel_hi:[1,0,1] neg_lo:[0,0,1] neg_hi:[0,0,1]\n\t"
    "v_pk_fma_f32 v[118:119], v[118:119], v[42:43], v[40:41] op_sel:[0,0,1] op_sel_hi:[1,0,1] neg_lo:[0,0,1] neg_hi:[0,0,1]\n\t"
    "v_pk_fma_f32 v[112:113], v[112:113], s[28:29], v[32:33]\n\t"
    "v_pk_fma_f32 v[116:117], v[116:117], s[28:29], v[32:33]\n\t"
    "v_pk_fma_f32 v[114:115], v[114:115], s[30:31], v[34:35]\n\t"
    "v_pk_fma_f32 v[118:119], v[118:119], s[30:31], v[34:35]\n\t"
    "v_cvt_pk_bf16_f32 v112, v112, v116\n\t"
    "ds_write_b32 v6, v112 offset:0\n\t"
    "v_cvt_pk_bf16_f32 v113, v113, v117\n\t"
    "ds_write_b32 v6, v113 offset:4624\n\t"
    "v_cvt_pk_bf16_f32 v114, v114, v118\n\t"
    "ds_write_b32 v6, v114 offset:9248\n\t"
    "v_cvt_pk_bf16_f32 v115, v115, v119\n\t"
    "ds_write_b32 v6, v115 offset:13872\n\t"
    "v_perm_b32 v47, v45, v44, s26\n\t"
    "v_perm_b32 v47, v46, v47, s27\n\t"
    "v_pk_fma_f32 v[120:121], v[120:121], v[46:47], v[44:45] op_sel:[0,1,0] op_sel_hi:[1,1,0] neg_lo:[0,0,1] neg_hi:[0,0,1]\n\t"
    "v_pk_fma_f32 v[122:123], v[122:123], v[46:47], v[44:45] op_sel:[0,1,0] op_sel_hi:[1,1,0] neg_lo:[0,0,1] neg_hi:[0,0,1]\n\t"
    "v_pk_fma_f32 v[124:125], v[124:125], v[46:47], v[44:45] op_sel:[0,0,1] op_sel_hi:[1,0,1] neg_lo:[0,0,1] neg_hi:[0,0,1]\n\t"
    "v_pk_fma_f32 v[126:127], v[126:127], v[46:47], v[44:45] op_sel:[0,0,1] op_sel_hi:[1,0,1] neg_lo:[0,0,1] neg_hi:[0,0,1]\n\t"
    "v_pk_fma_f32 v[120:121], v[120:121], s[28:29], v[32:33]\n\t"
    "v_pk_fma_f32 v[124:125], v[124:125], s[28:29], v[32:33]\n\t"
    "v_pk_fma_f32 v[122:123], v[122:123], s[30:31], v[34:35]\n\t"
    "v_pk_fma_f32 v[126:127], v[126:127], s[30:31], v[34:35]\n\t"
    "v_cvt_pk_bf16_f32 v120, v120, v124\n\t"
    "ds_write_b32 v6, v120 offset:1088\n\t"
    "v_cvt_pk_bf16_f32 v121, v121, v125\n\t"
    "ds_write_b32 v6, v121 offset:5712\n\t"
    "v_cvt_pk_bf16_f32 v122, v122, v126\n\t"
    "ds_write_b32 v6, v122 offset:10336\n\t"
    "v_cvt_pk_bf16_f32 v123, v123, v127\n\t"
    "ds_write_b32 v6, v123 offset:14960\n\t"
    "v_perm_b32 v51, v49, v48, s26\n\t"
    "v_perm_b32 v51, v50, v51, s27\n\t"
    "v_pk_fma_f32 v[128:129], v[128:129], v[50:51], v[48:49] op_sel:[0,1,0] op_sel_hi:[1,1,0] neg_lo:[0,0,1] neg_hi:[0,0,1]\n\t"
    "v_pk_fma_f32 v[130:131], v[130:131], v[50:51], v[48:49] op_sel:[0,1,0] op_sel_hi:[1,1,0] neg_lo:[0,0,1] neg_hi:[0,0,1]\n\t"
    "v_pk_fma_f32 v[132:133], v[132:133], v[50:51], v[48:49] op_sel:[0,0,1] op_sel_hi:[1,0,1] neg_lo:[0,0,1] neg_hi:[0,0,1]\n\t"
    "v_pk_fma_f32 v[134:135], v[134:135], v[50:51], v[48:49] op_sel:[0,0,1] op_sel_hi:[1,0,1] neg_lo:[0,0,1] neg_hi:[0,0,1]\n\t"
    "v_pk_fma_f32 v[128:129], v[128:129], s[28:29], v[32:33]\n\t"
    "v_pk_fma_f32 v[132:133], v[132:133], s[28:29], v[32:33]\n\t"
    "v_pk_fma_f32 v[130:131], v[130:131], s[30:31], v[34:35]\n\t"
    "v_pk_fma_f32 v[134:135], v[134:135], s[30:31], v[34:35]\n\t"
    "v_cvt_pk_bf16_f32 v128, v128, v132\n\t"
    "ds_write_b32 v6, v128 offset:2176\n\t"
    "v_cvt_pk_bf16_f32 v129, v129, v133\n\t"
    "ds_write_b32 v6, v129 offset:6800\n\t"
    "v_cvt_pk_bf16_f32 v130, v130, v134\n\t"
    "ds_write_b32 v6, v130 offset:11424\n\t"
    "v_cvt_pk_bf16_f32 v131, v131, v135\n\t"
    "ds_write_b32 v6, v131 offset:16048\n\t"
    "s_waitcnt vmcnt(0)\n\t"
    "s_add_u32 m0, s0, 16384\n\t"
    "s_mov_b32 s1, 0\n\t"
    "buffer_load_dwordx4 v13, s[4:7], s1 offen lds\n\t"
    "buffer_load_dwordx4 v3, s[4:7], s1 offen offset:1024 lds\n\t"
    "buffer_load_dwordx4 v13, s[8:11], s1 offen offset:2048 lds\n\t"
    "buffer_load_dwordx4 v3, s[8:11], s1 offen offset:3072 lds\n\t"
    "s_add_u32 m0, s0, 32768\n\t"
    "s_mov_b32 s1, 294912\n\t"
    "buffer_load_dwordx4 v13, s[4:7], s1 offen lds\n\t"
    "buffer_load_dwordx4 v3, s[4:7], s1 offen offset:1024 lds\n\t"
    "buffer_load_dwordx4 v13, s[8:11], s1 offen offset:2048 lds\n\t"
    "buffer_load_dwordx4 v3, s[8:11], s1 offen offset:3072 lds\n\t"
    "s_add_u32 m0, s0, 0\n\t"
    "s_mov_b32 s1, 589824\n\t"
    "buffer_load_dwordx4 v13, s[4:7], s1 offen lds\n\t"
    "buffer_load_dwordx4 v3, s[4:7], s1 offen offset:1024 lds\n\t"
    "buffer_load_dwordx4 v13, s[8:11], s1 offen offset:2048 lds\n\t"
    "buffer_load_dwordx4 v3, s[8:11], s1 offen offset:3072 lds\n\t"
    "v_perm_b32 v82, v30, v28, s24\n\t"
    "ds_write_b32 v10, v82 offset:3264\n\t"
    "v_perm_b32 v83, v30, v28, s25\n\t"
    "ds_write_b32 v10, v83 offset:7680\n\t"
    "v_perm_b32 v84, v31, v29, s24\n\t"
    "ds_write_b32 v10, v84 offset:12096\n\t"
    "v_perm_b32 v85, v31, v29, s25\n\t"
    "ds_write_b32 v10, v85 offset:16512\n\t"
    "v_lshlrev_b32 v136, 16, v28\n\t"
    "v_and_b32 v137, 0xffff0000, v28\n\t"
    "v_lshlrev_b32 v138, 16, v29\n\t"
    "v_and_b32 v139, 0xffff0000, v29\n\t"
    "v_lshlrev_b32 v140, 16, v30\n\t"
    "v_and_b32 v141, 0xffff0000, v30\n\t"
    "v_lshlrev_b32 v142, 16, v31\n\t"
    "v_and_b32 v143, 0xffff0000, v31\n\t"
    "v_perm_b32 v55, v53, v52, s26\n\t"
    "v_perm_b32 v55, v54, v55, s27\n\t"
    "v_pk_fma_f32 v[136:137], v[136:137], v[54:55], v[52:53] op_sel:[0,1,0] op_sel_hi:[1,1,0] neg_lo:[0,0,1] neg_hi:[0,0,1]\n\t"
    "v_pk_fma_f32 v[138:139], v[138:139], v[54:55], v[52:53] op_sel:[0,1,0] op_sel_hi:[1,1,0] neg_lo:[0,0,1] neg_hi:[0,0,1]\n\t"
    "v_pk_fma_f32 v[140:141], v[140:141], v[54:55], v[52:53] op_sel:[0,0,1] op_sel_hi:[1,0,1] neg_lo:[0,0,1] neg_hi:[0,0,1]\n\t"
    "v_pk_fma_f32 v[142:143], v[142:143], v[54:55], v[52:53] op_sel:[0,0,1] op_sel_hi:[1,0,1] neg_lo:[0,0,1] neg_hi:[0,0,1]\n\t"
    "ds_read_b64 v[36:37], v192 offset:2112\n\t"
    "ds_read_b64 v[38:39], v192 offset:2120\n\t"
    "ds_read_b64 v[40:41], v192 offset:2080\n\t"
    "ds_read_b64 v[42:43], v192 offset:2088\n\t"
    "ds_read_b64 v[44:45], v192 offset:2048\n\t"
    "ds_read_b64 v[46:47], v192 offset:2056\n\t"
    "ds_read_b64 v[48:49], v192 offset:2016\n\t"
    "ds_read_b64 v[50:51], v192 offset:2024\n\t"
    "v_pk_fma_f32 v[136:137], v[136:137], s[28:29], v[32:33]\n\t"
    "v_pk_fma_f32 v[140:141], v[140:141], s[28:29], v[32:33]\n\t"
    "v_pk_fma_f32 v[138:139], v[138:139], s[30:31], v[34:35]\n\t"
    "v_pk_fma_f32 v[142:143], v[142:143], s[30:31], v[34:35]\n\t"
    "v_cvt_pk_bf16_f32 v136, v136, v140\n\t"
    "ds_write_b32 v6, v136 offset:3264\n\t"
    "v_cvt_pk_bf16_f32 v137, v137, v141\n\t"
    "ds_write_b32 v6, v137 offset:7888\n\t"
    "v_cvt_pk_bf16_f32 v138, v138, v142\n\t"
    "ds_write_b32 v6, v138 offset:12512\n\t"
    "v_cvt_pk_bf16_f32 v139, v139, v143\n\t"
    "ds_write_b32 v6, v139 offset:17136\n\t"
    "s_waitcnt lgkmcnt(0)\n\t"
    "s_barrier\n\t"
    "s_cmp_lg_u32 s3, 0\n\t"
    "s_cbranch_scc1 L_pfp_%=\n\t"
    "s_lshl_b32 s28, s92, 7\n\t"
    "s_add_u32 s28, s28, 41979912\n\t"
    "v_mov_b32 v88, s66\n\t"
    "v_mov_b32 v89, s67\n\t"
    "v_mov_b32 v90, s28\n\t"
    "s_mov_b64 exec, 1\n\t"
    "global_store_dwordx2 v90, v[88:89], s[36:37] sc1\n\t"
    "s_mov_b64 exec, -1\n\t"
    "L_pfp_%=:\n\t"
    "ds_read_b64 v[176:177], v193 offset:0\n\t"
    "ds_read_b64 v[180:181], v193 offset:64\n\t"
    "ds_read_b128 v[16:19], v213 offset:0\n\t"
    "ds_read_b64 v[184:185], v193 offset:128\n\t"
    "ds_read_b64 v[188:189], v193 offset:192\n\t"
    "ds_read_b128 v[20:23], v213 offset:64\n\t"
    "ds_read_b128 v[24:27], v213 offset:128\n\t"
    "ds_read_b128 v[28:31], v213 offset:192\n\t"
    "ds_read_b64 v[178:179], v193 offset:32\n\t"
    "ds_read_b64 v[182:183], v193 offset:96\n\t"
    "ds_read_b64 v[186:187], v193 offset:160\n\t"
    "ds_read_b64 v[190:191], v193 offset:224\n\t"
    "s_waitcnt lgkmcnt(10)\n\t"
    "v_lshlrev_b32 v144, 16, v176\n\t"
    "v_and_b32 v145, 0xffff0000, v176\n\t"
    "v_lshlrev_b32 v146, 16, v177\n\t"
    "v_and_b32 v147, 0xffff0000, v177\n\t"
    "v_lshlrev_b32 v152, 16, v180\n\t"
    "v_and_b32 v153, 0xffff0000, v180\n\t"
    "v_lshlrev_b32 v154, 16, v181\n\t"
    "v_and_b32 v155, 0xffff0000, v181\n\t"
    "s_waitcnt lgkmcnt(7)\n\t"
    "v_lshlrev_b32 v160, 16, v184\n\t"
    "v_and_b32 v161, 0xffff0000, v184\n\t"
    "v_lshlrev_b32 v162, 16, v185\n\t"
    "v_and_b32 v163, 0xffff0000, v185\n\t"
    "v_mfma_f32_16x16x32_bf16 v[144:147], v[36:39], v[16:19], v[144:147]\n\t"
    "s_waitcnt lgkmcnt(4)\n\t"
    "v_lshlrev_b32 v168, 16, v188\n\t"
    "v_and_b32 v169, 0xffff0000, v188\n\t"
    "v_lshlrev_b32 v170, 16, v189\n\t"
    "v_and_b32 v171, 0xffff0000, v189\n\t"
    "ds_read_b64 v[52:53], v192 offset:1984\n\t"
    "v_mfma_f32_16x16x32_bf16 v[152:155], v[36:39], v[20:23], v[152:155]\n\t"
    "s_waitcnt lgkmcnt(1)\n\t"
    "v_mfma_f32_16x16x32_bf16 v[160:163], v[36:39], v[24:27], v[160:163]\n\t"
    "v_lshlrev_b32 v148, 16, v178\n\t"
    "v_and_b32 v149, 0xffff0000, v178\n\t"
    "v_lshlrev_b32 v150, 16, v179\n\t"
    "v_and_b32 v151, 0xffff0000, v179\n\t"
    "ds_read_b64 v[54:55], v192 offset:1992\n\t"
    "v_mfma_f32_16x16x32_bf16 v[168:171], v[36:39], v[28:31], v[168:171]\n\t"
    "v_lshlrev_b32 v156, 16, v182\n\t"
    "v_and_b32 v157, 0xffff0000, v182\n\t"
    "v_lshlrev_b32 v158, 16, v183\n\t"
    "v_and_b32 v159, 0xffff0000, v183\n\t"
    "v_mfma_f32_16x16x32_bf16 v[148:151], v[40:43], v[16:19], v[148:151]\n\t"
    "v_lshlrev_b32 v164, 16, v186\n\t"
    "v_and_b32 v165, 0xffff0000, v186\n\t"
    "v_lshlrev_b32 v166, 16, v187\n\t"
    "v_and_b32 v167, 0xffff0000, v187\n\t"
    "ds_read_b64 v[36:37], v192 offset:1952\n\t"
    "global_load_dwordx2 v[94:95], v14, s[36:37] sc1\n\t"
    "v_mfma_f32_16x16x32_bf16 v[156:159], v[40:43], v[20:23], v[156:159]\n\t"
    "v_lshlrev_b32 v172, 16, v190\n\t"
    "v_and_b32 v173, 0xffff0000, v190\n\t"
    "v_lshlrev_b32 v174, 16, v191\n\t"
    "v_and_b32 v175, 0xffff0000, v191\n\t"
    "ds_read_b64 v[38:39], v192 offset:1960\n\t"
    "v_mfma_f32_16x16x32_bf16 v[164:167], v[40:43], v[24:27], v[164:167]\n\t"
    "ds_read_b128 v[56:59], v216 offset:0\n\t"
    "ds_read_b128 v[60:63], v216 offset:544\n\t"
    "v_mfma_f32_16x16x32_bf16 v[172:175], v[40:43], v[28:31], v[172:175]\n\t"
    "s_waitcnt lgkmcnt(15)\n\t"
    "v_mov_b32_dpp v28, v28 row_shr:1 row_mask:0xf bank_mask:0xf bound_ctrl:1\n\t"
    "v_mov_b32_dpp v29, v29 row_shr:1 row_mask:0xf bank_mask:0xf bound_ctrl:1\n\t"
    "v_mfma_f32_16x16x32_bf16 v[152:155], v[44:47], v[16:19], v[152:155]\n\t"
    "ds_read_b64 v[40:41], v192 offset:1920\n\t"
    "v_mov_b32_dpp v30, v30 row_shr:1 row_mask:0xf bank_mask:0xf bound_ctrl:1\n\t"
    "v_mov_b32_dpp v31, v31 row_shr:1 row_mask:0xf bank_mask:0xf bound_ctrl:1\n\t"
    "v_mfma_f32_16x16x32_bf16 v[160:163], v[44:47], v[20:23], v[160:163]\n\t"
    "ds_read_b128 v[64:67], v216 offset:1088\n\t"
    "ds_read_b128 v[68:71], v216 offset:1632\n\t"
    "v_mfma_f32_16x16x32_bf16 v[168:171], v[44:47], v[24:27], v[168:171]\n\t"
    "ds_read_b64 v[42:43], v192 offset:1928\n\t"
    "ds_read_b128 v[72:75], v217 offset:0\n\t"
    "v_mfma_f32_16x16x32_bf16 v[144:147], v[44:47], v[28:31], v[144:147]\n\t"
    "ds_read_b128 v[76:79], v217 offset:544\n\t"
    "ds_read_b128 v[80:83], v217 offset:1088\n\t"
    "v_mfma_f32_16x16x32_bf16 v[156:159], v[48:51], v[16:19], v[156:159]\n\t"
    "ds_read_b64 v[44:45], v192 offset:1888\n\t"
    "ds_read_b128 v[84:87], v217 offset:1632\n\t"
    "v_mfma_f32_16x16x32_bf16 v[164:167], v[48:51], v[20:23], v[164:167]\n\t"
    "ds_read_b64 v[46:47], v192 offset:1896\n\t"
    "v_mfma_f32_16x16x32_bf16 v[172:175], v[48:51], v[24:27], v[172:175]\n\t"
    "v_mov_b32_dpp v24, v24 row_shr:1 row_mask:0xf bank_mask:0xf bound_ctrl:1\n\t"
    "v_mov_b32_dpp v25, v25 row_shr:1 row_mask:0xf bank_mask:0xf bound_ctrl:1\n\t"
    "v_mfma_f32_16x16x32_bf16 v[148:151], v[48:51], v[28:31], v[148:151]\n\t"
    "s_waitcnt lgkmcnt(12)\n\t"
    "v_mov_b32_dpp v26, v26 row_shr:1 row_mask:0xf bank_mask:0xf bound_ctrl:1\n\t"
    "v_mov_b32_dpp v27, v27 row_shr:1 row_mask:0xf bank_mask:0xf bound_ctrl:1\n\t"
    "v_mfma_f32_16x16x32_bf16 v[160:163], v[52:55], v[16:19], v[160:163]\n\t"
    "ds_read_b64 v[48:49], v192 offset:1856\n\t"
    "v_mfma_f32_16x16x32_bf16 v[168:171], v[52:55], v[20:23], v[168:171]\n\t"
    "s_waitcnt vmcnt(9)\n\t"
    "s_barrier\n\t"
    "v_mfma_f32_16x16x32_bf16 v[144:147], v[52:55], v[24:27], v[144:147]\n\t"
    "ds_read_b64 v[50:51], v192 offset:1864\n\t"
    "ds_read_b64_tr_b16 a[0:1], v11 offset:16384\n\t"
    "v_mfma_f32_16x16x32_bf16 v[152:155], v[52:55], v[28:31], v[152:155]\n\t"
    "s_waitcnt lgkmcnt(4)\n\t"
    "ds_read_b64_tr_b16 a[2:3], v11 offset:17408\n\t"
    "ds_read_b64_tr_b16 a[4:5], v12 offset:16384\n\t"
    "v_mfma_f32_16x16x32_bf16 v[96:99], v[56:59], v[204:207], 0\n\t"
    "ds_read_b64_tr_b16 a[6:7], v12 offset:17408\n\t"
    "ds_read_b64_tr_b16 a[8:9], v11 offset:18432\n\t"
    "v_mfma_f32_16x16x32_bf16 v[100:103], v[60:63], v[204:207], 0\n\t"
    "ds_read_b64_tr_b16 a[10:11], v11 offset:19456\n\t"
    "ds_read_b64_tr_b16 a[12:13], v12 offset:18432\n\t"
    "v_mfma_f32_16x16x32_bf16 v[104:107], v[64:67], v[204:207], 0\n\t"
    "ds_read_b64_tr_b16 a[14:15], v12 offset:19456\n\t"
    "v_mfma_f32_16x16x32_bf16 v[108:111], v[68:71], v[204:207], 0\n\t"
    "v_mfma_f32_16x16x32_bf16 v[176:179], v[72:75], v[208:211], 0\n\t"
    "v_cvt_pk_bf16_f32 v112, v96, v97\n\t"
    "v_cvt_pk_bf16_f32 v113, v98, v99\n\t"
    "v_mfma_f32_16x16x32_bf16 v[180:183], v[76:79], v[208:211], 0\n\t"
    "v_cvt_pk_bf16_f32 v114, v100, v101\n\t"
    "v_cvt_pk_bf16_f32 v115, v102, v103\n\t"
    "v_mfma_f32_16x16x32_bf16 v[184:187], v[80:83], v[208:211], 0\n\t"
    "v_cvt_pk_bf16_f32 v116, v104, v105\n\t"
    "v_cvt_pk_bf16_f32 v117, v106, v107\n\t"
    "v_mfma_f32_16x16x32_bf16 v[188:191], v[84:87], v[208:211], 0\n\t"
    "v_cvt_pk_bf16_f32 v118, v108, v109\n\t"
    "v_cvt_pk_bf16_f32 v119, v110, v111\n\t"
    "v_mfma_f32_16x16x32_bf16 v[164:167], v[36:39], v[16:19], v[164:167]\n\t"
    "ds_read_b64 v[52:53], v192 offset:1824\n\t"
    "v_mfma_f32_16x16x32_bf16 v[172:175], v[36:39], v[20:23], v[172:175]\n\t"
    "ds_read_b64 v[54:55], v192 offset:1832\n\t"
    "v_mov_b32_dpp v20, v20 row_shr:1 row_mask:0xf bank_mask:0xf bound_ctrl:1\n\t"
    "v_mov_b32_dpp v21, v21 row_shr:1 row_mask:0xf bank_mask:0xf bound_ctrl:1\n\t"
    "v_mfma_f32_16x16x32_bf16 v[148:151], v[36:39], v[24:27], v[148:151]\n\t"
    "v_mov_b32_dpp v22, v22 row_shr:1 row_mask:0xf bank_mask:0xf bound_ctrl:1\n\t"
    "v_mov_b32_dpp v23, v23 row_shr:1 row_mask:0xf bank_mask:0xf bound_ctrl:1\n\t"
    "v_mfma_f32_16x16x32_bf16 v[156:159], v[36:39], v[28:31], v[156:159]\n\t"
    "s_waitcnt lgkmcnt(12)\n\t"
    "v_cvt_pk_bf16_f32 v120, v176, v177\n\t"
    "v_mfma_f32_16x16x32_bf16 v[168:171], v[40:43], v[16:19], v[168:171]\n\t"
    "ds_read_b64 v[36:37], v192 offset:1792\n\t"
    "v_cvt_pk_bf16_f32 v121, v178, v179\n\t"
    "v_mfma_f32_16x16x32_bf16 v[144:147], v[40:43], v[20:23], v[144:147]\n\t"
    "v_cvt_pk_bf16_f32 v122, v180, v181\n\t"
    "v_cvt_pk_bf16_f32 v123, v182, v183\n\t"
    "v_mfma_f32_16x16x32_bf16 v[152:155], v[40:43], v[24:27], v[152:155]\n\t"
    "ds_read_b64 v[38:39], v192 offset:1800\n\t"
    "v_cvt_pk_bf16_f32 v124, v184, v185\n\t"
    "v_mfma_f32_16x16x32_bf16 v[160:163], v[40:43], v[28:31], v[160:163]\n\t"
    "v_cvt_pk_bf16_f32 v125, v186, v187\n\t"
    "v_cvt_pk_bf16_f32 v126, v188, v189\n\t"
    "v_mfma_f32_16x16x32_bf16 v[172:175], v[44:47], v[16:19], v[172:175]\n\t"
    "ds_read_b64 v[40:41], v192 offset:1760\n\t"
    "v_mov_b32_dpp v16, v16 row_shr:1 row_mask:0xf bank_mask:0xf bound_ctrl:1\n\t"
    "v_mfma_f32_16x16x32_bf16 v[148:151], v[44:47], v[20:23], v[148:151]\n\t"
    "ds_read_b64 v[42:43], v192 offset:1768\n\t"
    "v_mov_b32_dpp v17, v17 row_shr:1 row_mask:0xf bank_mask:0xf bound_ctrl:1\n\t"
    "v_mov_b32_dpp v18, v18 row_shr:1 row_mask:0xf bank_mask:0xf bound_ctrl:1\n\t"
    "v_mfma_f32_16x16x32_bf16 v[156:159], v[44:47], v[24:27], v[156:159]\n\t"
    "v_mov_b32_dpp v19, v19 row_shr:1 row_mask:0xf bank_mask:0xf bound_ctrl:1\n\t"
    "v_cvt_pk_bf16_f32 v127, v190, v191\n\t"
    "v_mfma_f32_16x16x32_bf16 v[164:167], v[44:47], v[28:31], v[164:167]\n\t"
    "s_waitcnt lgkmcnt(4)\n\t"
    "v_mfma_f32_16x16x32_bf16 v[144:147], v[48:51], v[16:19], v[144:147]\n\t"
    "ds_read_b64 v[44:45], v192 offset:1728\n\t"
    "s_add_u32 s12, s36, 524288\n\t"
    "s_addc_u32 s13, s37, 0\n\t"
    "v_mfma_f32_16x16x32_bf16 v[152:155], v[48:51], v[20:23], v[152:155]\n\t"
    "s_waitcnt vmcnt(5) lgkmcnt(7)\n\t"
    "s_barrier\n\t"
    "s_mov_b32 s14, 2377728\n\t"
    "v_mfma_f32_16x16x32_bf16 v[160:163], v[48:51], v[24:27], v[160:163]\n\t"
    "ds_read_b64 v[46:47], v192 offset:1736\n\t"
    "s_add_u32 m0, s0, 16384\n\t"
    "s_mov_b32 s1, 884736\n\t"
    "buffer_load_dwordx4 v13, s[4:7], s1 offen lds\n\t"
    "v_mfma_f32_16x16x32_bf16 v[168:171], v[48:51], v[28:31], v[168:171]\n\t"
    "buffer_load_dwordx4 v3, s[4:7], s1 offen offset:1024 lds\n\t"
    "ds_read_b64_tr_b16 a[16:17], v11 offset:32768\n\t"
    "ds_read_b64_tr_b16 a[18:19], v11 offset:33792\n\t"
    "v_mfma_f32_16x16x32_bf16 v[148:151], v[52:55], v[16:19], v[148:151]\n\t"
    "ds_read_b64 v[48:49], v192 offset:1696\n\t"
    "buffer_load_dwordx4 v13, s[8:11], s1 offen offset:2048 lds\n\t"
    "ds_read_b64_tr_b16 a[20:21], v12 offset:32768\n\t"
    "v_mfma_f32_16x16x32_bf16 v[156:159], v[52:55], v[20:23], v[156:159]\n\t"
    "ds_read_b64 v[50:51], v192 offset:1704\n\t"
    "buffer_load_dwordx4 v3, s[8:11], s1 offen offset:3072 lds\n\t"
    "ds_read_b64_tr_b16 a[22:23], v12 offset:33792\n\t"
    "v_mfma_f32_16x16x32_bf16 v[164:167], v[52:55], v[24:27], v[164:167]\n\t"
    "s_add_u32 m0, s0, 49152\n\t"
    "s_mov_b32 s1, 1179648\n\t"
    "buffer_load_dwordx4 v13, s[4:7], s1 offen lds\n\t"
    "v_mfma_f32_16x16x32_bf16 v[172:175], v[52:55], v[28:31], v[172:175]\n\t"
    "s_waitcnt lgkmcnt(8)\n\t"
    "v_mov_b32_dpp v28, v28 row_shr:1 row_mask:0xf bank_mask:0xf bound_ctrl:1\n\t"
    "v_mov_b32_dpp v29, v29 row_shr:1 row_mask:0xf bank_mask:0xf bound_ctrl:1\n\t"
    "buffer_load_dwordx4 v3, s[4:7], s1 offen offset:1024 lds\n\t"
    "v_mfma_f32_16x16x32_bf16 v[152:155], v[36:39], v[16:19], v[152:155]\n\t"
    "ds_read_b64 v[52:53], v192 offset:1664\n\t"
    "v_mov_b32_dpp v30, v30 row_shr:1 row_mask:0xf bank_mask:0xf bound_ctrl:1\n\t"
    "v_mov_b32_dpp v31, v31 row_shr:1 row_mask:0xf bank_mask:0xf bound_ctrl:1\n\t"
    "buffer_load_dwordx4 v13, s[8:11], s1 offen offset:2048 lds\n\t"
    "v_mfma_f32_16x16x32_bf16 v[160:163], v[36:39], v[20:23], v[160:163]\n\t"
    "buffer_load_dwordx4 v3, s[8:11], s1 offen offset:3072 lds\n\t"
    "ds_read_b64_tr_b16 a[24:25], v11 offset:34816\n\t"
    "ds_read_b64_tr_b16 a[26:27], v11 offset:35840\n\t"
    "v_mfma_f32_16x16x32_bf16 v[168:171], v[36:39], v[24:27], v[168:171]\n\t"
    "ds_read_b64 v[54:55], v192 offset:1672\n\t"
    "ds_read_b64_tr_b16 a[28:29], v12 offset:34816\n\t"
    "ds_read_b64_tr_b16 a[30:31], v12 offset:35840\n\t"
    "v_mfma_f32_16x16x32_bf16 v[144:147], v[36:39], v[28:31], v[144:147]\n\t"
    "s_mov_b32 s15, 0x20000\n\t"
    "s_lshl_b32 s28, s20, 2\n\t"
    "s_add_u32 s28, s28, s3\n\t"
    "v_mfma_f32_16x16x32_bf16 v[156:159], v[40:43], v[16:19], v[156:159]\n\t"
    "ds_read_b64 v[36:37], v192 offset:1632\n\t"
    "s_lshl_b32 s28, s28, 10\n\t"
    "s_sub_u32 s18, 19021824, s28\n\t"
    "v_mfma_f32_16x16x32_bf16 v[164:167], v[40:43], v[20:23], v[164:167]\n\t"
    "ds_read_b64 v[38:39], v192 offset:1640\n\t"
    "s_waitcnt vmcnt(9) lgkmcnt(2)\n\t"
    "s_barrier\n\t"
    "v_mfma_f32_16x16x32_bf16 v[172:175], v[40:43], v[24:27], v[172:175]\n\t"
    "v_mov_b32_dpp v24, v24 row_shr:1 row_mask:0xf bank_mask:0xf bound_ctrl:1\n\t"
    "v_mov_b32_dpp v25, v25 row_shr:1 row_mask:0xf bank_mask:0xf bound_ctrl:1\n\t"
    "s_add_u32 m0, s0, 32768\n\t"
    "s_mov_b32 s1, 1474560\n\t"
    "buffer_load_dwordx4 v13, s[4:7], s1 offen lds\n\t"
    "v_mfma_f32_16x16x32_bf16 v[148:151], v[40:43], v[28:31], v[148:151]\n\t"
    "s_waitcnt lgkmcnt(9)\n\t"
    "v_mov_b32_dpp v26, v26 row_shr:1 row_mask:0xf bank_mask:0xf bound_ctrl:1\n\t"
    "v_mov_b32_dpp v27, v27 row_shr:1 row_mask:0xf bank_mask:0xf bound_ctrl:1\n\t"
    "v_mfma_f32_16x16x32_bf16 v[160:163], v[44:47], v[16:19], v[160:163]\n\t"
    "ds_read_b64 v[40:41], v192 offset:1600\n\t"
    "buffer_load_dwordx4 v3, s[4:7], s1 offen offset:1024 lds\n\t"
    "ds_read_b64_tr_b16 a[32:33], v11 offset:0\n\t"
    "v_mfma_f32_16x16x32_bf16 v[168:171], v[44:47], v[20:23], v[168:171]\n\t"
    "ds_read_b64_tr_b16 a[34:35], v11 offset:1024\n\t"
    "ds_read_b64_tr_b16 a[36:37], v12 offset:0\n\t"
    "ds_read_b64_tr_b16 a[38:39], v12 offset:1024\n\t"
    "v_mfma_f32_16x16x32_bf16 v[144:147], v[44:47], v[24:27], v[144:147]\n\t"
    "ds_read_b64 v[42:43], v192 offset:1608\n\t"
    "buffer_load_dwordx4 v13, s[8:11], s1 offen offset:2048 lds\n\t"
    "ds_read_b64_tr_b16 a[40:41], v11 offset:2048\n\t"
    "v_mfma_f32_16x16x32_bf16 v[152:155], v[44:47], v[28:31], v[152:155]\n\t"
    "ds_read_b64_tr_b16 a[42:43], v11 offset:3072\n\t"
    "ds_read_b64_tr_b16 a[44:45], v12 offset:2048\n\t"
    "ds_read_b64_tr_b16 a[46:47], v12 offset:3072\n\t"
    "v_mfma_f32_16x16x32_bf16 v[164:167], v[48:51], v[16:19], v[164:167]\n\t"
    "ds_read_b64 v[44:45], v192 offset:1568\n\t"
    "buffer_load_dwordx4 v3, s[8:11], s1 offen offset:3072 lds\n\t"
    "s_add_u32 s28, s28, 5373952\n\t"
    "v_mfma_f32_16x16x32_bf16 v[172:175], v[48:51], v[20:23], v[172:175]\n\t"
    "ds_read_b64 v[46:47], v192 offset:1576\n\t"
    "v_mov_b32_dpp v20, v20 row_shr:1 row_mask:0xf bank_mask:0xf bound_ctrl:1\n\t"
    "v_mov_b32_dpp v21, v21 row_shr:1 row_mask:0xf bank_mask:0xf bound_ctrl:1\n\t"
    "s_waitcnt vmcnt(12)\n\t"
    "v_cmp_ne_u32 vcc, s72, v95\n\t"
    "s_cbranch_vccnz L_gsc_%=\n\t"
    "L_gbc_%=:\n\t"
    "v_mfma_f32_16x16x32_bf16 v[148:151], v[48:51], v[24:27], v[148:151]\n\t"
    "v_mov_b32_dpp v22, v22 row_shr:1 row_mask:0xf bank_mask:0xf bound_ctrl:1\n\t"
    "v_mov_b32_dpp v23, v23 row_shr:1 row_mask:0xf bank_mask:0xf bound_ctrl:1\n\t"
    "s_add_u32 s16, s36, s28\n\t"
    "s_addc_u32 s17, s37, 0\n\t"
    "v_mfma_f32_16x16x32_bf16 v[156:159], v[48:51], v[28:31], v[156:159]\n\t"
    "s_waitcnt lgkmcnt(12)\n\t"
    "v_cndmask_b32 v94, 0, v94, s[80:81]\n\t"
    "s_mov_b32 s19, 0x20000\n\t"
    "v_mfma_f32_16x16x32_bf16 v[168:171], v[52:55], v[16:19], v[168:171]\n\t"
    "ds_read_b64 v[48:49], v192 offset:1536\n\t"
    "v_add_f32_dpp v94, v94, v94 quad_perm:[1,0,3,2] row_mask:0xf bank_mask:0xf\n\t"
    "v_lshlrev_b32 v2, 3, v1\n\t"
    "v_mfma_f32_16x16x32_bf16 v[144:147], v[52:55], v[20:23], v[144:147]\n\t"
    "v_add_f32_dpp v94, v94, v94 quad_perm:[2,3,0,1] row_mask:0xf bank_mask:0xf\n\t"
    "v_and_b32 v5, 0xf0, v15\n\t"
    "s_lshl_b32 s28, s21, 8\n\t"
    "v_mfma_f32_16x16x32_bf16 v[152:155], v[52:55], v[24:27], v[152:155]\n\t"
    "ds_read_b64 v[50:51], v192 offset:1544\n\t"
    "v_add_f32_dpp v94, v94, v94 row_half_mirror row_mask:0xf bank_mask:0xf\n\t"
    "s_add_u32 s28, s28, 16384\n\t"
    "v_mfma_f32_16x16x32_bf16 v[160:163], v[52:55], v[28:31], v[160:163]\n\t"
    "v_add_f32_dpp v94, v94, v94 row_mirror row_mask:0xf bank_mask:0xf\n\t"
    "v_add_u32 v5, s28, v5\n\t"
    "s_mov_b32 s78, 0x21800000\n\t"
    "v_mfma_f32_16x16x32_bf16 v[172:175], v[36:39], v[16:19], v[172:175]\n\t"
    "ds_read_b64 v[52:53], v192 offset:1504\n\t"
    "v_mov_b32_dpp v16, v16 row_shr:1 row_mask:0xf bank_mask:0xf bound_ctrl:1\n\t"
    "s_mov_b32 s79, 0x5d800000\n\t"
    "v_mfma_f32_16x16x32_bf16 v[148:151], v[36:39], v[20:23], v[148:151]\n\t"
    "ds_read_b64 v[54:55], v192 offset:1512\n\t"
    "v_mov_b32_dpp v17, v17 row_shr:1 row_mask:0xf bank_mask:0xf bound_ctrl:1\n\t"
    "v_mov_b32_dpp v18, v18 row_shr:1 row_mask:0xf bank_mask:0xf bound_ctrl:1\n\t"
    "v_cvt_pk_bf16_f32 v94, v94, v94\n\t"
    "s_waitcnt vmcnt(8) lgkmcnt(6)\n\t"
    "s_barrier\n\t"
    "v_mfma_f32_16x16x32_bf16 v[156:159], v[36:39], v[24:27], v[156:159]\n\t"
    "v_mov_b32_dpp v19, v19 row_shr:1 row_mask:0xf bank_mask:0xf bound_ctrl:1\n\t"
    "v_and_b32 v94, 0xffff0000, v94\n\t"
    "s_add_u32 m0, s0, 0\n\t"
    "s_mov_b32 s1, 1769472\n\t"
    "buffer_load_dwordx4 v13, s[4:7], s1 offen lds\n\t"
    "v_mfma_f32_16x16x32_bf16 v[164:167], v[36:39], v[28:31], v[164:167]\n\t"
    "s_waitcnt lgkmcnt(4)\n\t"
    "v_add_f32 v94, 1.0, v94\n\t"
    "ds_read_b64_tr_b16 a[48:49], v11 offset:16384\n\t"
    "v_mfma_f32_16x16x32_bf16 v[144:147], v[40:43], v[16:19], v[144:147]\n\t"
    "ds_read_b64 v[36:37], v192 offset:1472\n\t"
    "v_cvt_pk_bf16_f32 v94, v94, v94\n\t"
    "buffer_load_dwordx4 v3, s[4:7], s1 offen offset:1024 lds\n\t"
    "v_mfma_f32_16x16x32_bf16 v[152:155], v[40:43], v[20:23], v[152:155]\n\t"
    "v_and_b32 v94, 0xffff0000, v94\n\t"
    "ds_read_b64_tr_b16 a[50:51], v11 offset:17408\n\t"
    "ds_read_b64_tr_b16 a[52:53], v12 offset:16384\n\t"
    "v_mfma_f32_16x16x32_bf16 v[160:163], v[40:43], v[24:27], v[160:163]\n\t"
    "ds_read_b64 v[38:39], v192 offset:1480\n\t"
    "v_readlane_b32 s94, v94, 0\n\t"
    "buffer_load_dwordx4 v13, s[8:11], s1 offen offset:2048 lds\n\t"
    "v_mfma_f32_16x16x32_bf16 v[168:171], v[40:43], v[28:31], v[168:171]\n\t"
    "ds_read_b64_tr_b16 a[54:55], v12 offset:17408\n\t"
    "ds_read_b64_tr_b16 a[56:57], v11 offset:18432\n\t"
    "ds_read_b64_tr_b16 a[58:59], v11 offset:19456\n\t"
    "v_mfma_f32_16x16x32_bf16 v[148:151], v[44:47], v[16:19], v[148:151]\n\t"
    "ds_read_b64 v[40:41], v192 offset:1440\n\t"
    "buffer_load_dwordx4 v3, s[8:11], s1 offen offset:3072 lds\n\t"
    "ds_read_b64_tr_b16 a[60:61], v12 offset:18432\n\t"
    "v_mfma_f32_16x16x32_bf16 v[156:159], v[44:47], v[20:23], v[156:159]\n\t"
    "ds_read_b64 v[42:43], v192 offset:1448\n\t"
    "ds_read_b64_tr_b16 a[62:63], v12 offset:19456\n\t"
    "s_mov_b32 s23, 0\n\t"
    "v_mfma_f32_16x16x32_bf16 v[164:167], v[44:47], v[24:27], v[164:167]\n\t"
    "s_mov_b32 s26, 145408\n\t"
    "s_mul_i32 s38, s21, 147456\n\t"
    "s_mov_b32 s27, 18874368\n\t"
    "v_mfma_f32_16x16x32_bf16 v[172:175], v[44:47], v[28:31], v[172:175]\n\t"
    "s_waitcnt lgkmcnt(12)\n\t"
    "v_mov_b32_dpp v28, v28 row_shr:1 row_mask:0xf bank_mask:0xf bound_ctrl:1\n\t"
    "v_mov_b32_dpp v29, v29 row_shr:1 row_mask:0xf bank_mask:0xf bound_ctrl:1\n\t"
    "v_mfma_f32_16x16x32_bf16 v[152:155], v[48:51], v[16:19], v[152:155]\n\t"
    "ds_read_b64 v[44:45], v192 offset:1408\n\t"
    "v_mov_b32_dpp v30, v30 row_shr:1 row_mask:0xf bank_mask:0xf bound_ctrl:1\n\t"
    "v_mov_b32_dpp v31, v31 row_shr:1 row_mask:0xf bank_mask:0xf bound_ctrl:1\n\t"
    "v_mfma_f32_16x16x32_bf16 v[160:163], v[48:51], v[20:23], v[160:163]\n\t"
    "s_waitcnt vmcnt(8) lgkmcnt(1)\n\t"
    "s_barrier\n\t"
    "s_mul_i32 s29, s3, 4608\n\t"
    "v_mfma_f32_16x16x32_bf16 v[168:171], v[48:51], v[24:27], v[168:171]\n\t"
    "ds_read_b64 v[46:47], v192 offset:1416\n\t"
    "s_add_u32 m0, s0, 16384\n\t"
    "s_mov_b32 s1, 2064384\n\t"
    "buffer_load_dwordx4 v13, s[4:7], s1 offen lds\n\t"
    "v_mfma_f32_16x16x32_bf16 v[144:147], v[48:51], v[28:31], v[144:147]\n\t"
    "ds_read_b64_tr_b16 a[64:65], v11 offset:49152\n\t"
    "ds_read_b64_tr_b16 a[66:67], v11 offset:50176\n\t"
    "s_mul_i32 s24, s21, 18432\n\t"
    "v_mfma_f32_16x16x32_bf16 v[156:159], v[52:55], v[16:19], v[156:159]\n\t"
    "ds_read_b64 v[48:49], v192 offset:1376\n\t"
    "buffer_load_dwordx4 v3, s[4:7], s1 offen offset:1024 lds\n\t"
    "s_add_u32 s24, s24, s29\n\t"
    "v_mfma_f32_16x16x32_bf16 v[164:167], v[52:55], v[20:23], v[164:167]\n\t"
    "ds_read_b64 v[50:51], v192 offset:1384\n\t"
    "ds_read_b64_tr_b16 a[68:69], v12 offset:49152\n\t"
    "s_add_u32 s25, s29, 145408\n\t"
    "v_mfma_f32_16x16x32_bf16 v[172:175], v[52:55], v[24:27], v[172:175]\n\t"
    "v_mov_b32_dpp v24, v24 row_shr:1 row_mask:0xf bank_mask:0xf bound_ctrl:1\n\t"
    "v_mov_b32_dpp v25, v25 row_shr:1 row_mask:0xf bank_mask:0xf bound_ctrl:1\n\t"
    "buffer_load_dwordx4 v13, s[8:11], s1 offen offset:2048 lds\n\t"
    "v_mfma_f32_16x16x32_bf16 v[148:151], v[52:55], v[28:31], v[148:151]\n\t"
    "s_waitcnt lgkmcnt(8)\n\t"
    "v_mov_b32_dpp v26, v26 row_shr:1 row_mask:0xf bank_mask:0xf bound_ctrl:1\n\t"
    "v_mov_b32_dpp v27, v27 row_shr:1 row_mask:0xf bank_mask:0xf bound_ctrl:1\n\t"
    "v_mfma_f32_16x16x32_bf16 v[160:163], v[36:39], v[16:19], v[160:163]\n\t"
    "ds_read_b64 v[52:53], v192 offset:1344\n\t"
    "buffer_load_dwordx4 v3, s[8:11], s1 offen offset:3072 lds\n\t"
    "v_mfma_f32_16x16x32_bf16 v[168:171], v[36:39], v[20:23], v[168:171]\n\t"
    "ds_read_b64_tr_b16 a[70:71], v12 offset:50176\n\t"
    "ds_read_b64_tr_b16 a[72:73], v11 offset:51200\n\t"
    "v_mfma_f32_16x16x32_bf16 v[144:147], v[36:39], v[24:27], v[144:147]\n\t"
    "ds_read_b64 v[54:55], v192 offset:1352\n\t"
    "ds_read_b64_tr_b16 a[74:75], v11 offset:52224\n\t"
    "v_mfma_f32_16x16x32_bf16 v[152:155], v[36:39], v[28:31], v[152:155]\n\t"
    "ds_read_b64_tr_b16 a[76:77], v12 offset:51200\n\t"
    "ds_read_b64_tr_b16 a[78:79], v12 offset:52224\n\t"
    "v_mfma_f32_16x16x32_bf16 v[164:167], v[40:43], v[16:19], v[164:167]\n\t"
    "ds_read_b64 v[36:37], v192 offset:1312\n\t"
    "v_mfma_f32_16x16x32_bf16 v[172:175], v[40:43], v[20:23], v[172:175]\n\t"
    "ds_read_b64 v[38:39], v192 offset:1320\n\t"
    "v_mov_b32_dpp v20, v20 row_shr:1 row_mask:0xf bank_mask:0xf bound_ctrl:1\n\t"
    "v_mov_b32_dpp v21, v21 row_shr:1 row_mask:0xf bank_mask:0xf bound_ctrl:1\n\t"
    "v_mfma_f32_16x16x32_bf16 v[148:151], v[40:43], v[24:27], v[148:151]\n\t"
    "v_mov_b32_dpp v22, v22 row_shr:1 row_mask:0xf bank_mask:0xf bound_ctrl:1\n\t"
    "v_mov_b32_dpp v23, v23 row_shr:1 row_mask:0xf bank_mask:0xf bound_ctrl:1\n\t"
    "v_mfma_f32_16x16x32_bf16 v[156:159], v[40:43], v[28:31], v[156:159]\n\t"
    "s_waitcnt lgkmcnt(10)\n\t"
    "v_mfma_f32_16x16x32_bf16 v[168:171], v[44:47], v[16:19], v[168:171]\n\t"
    "ds_read_b64 v[40:41], v192 offset:1280\n\t"
    "v_mfma_f32_16x16x32_bf16 v[144:147], v[44:47], v[20:23], v[144:147]\n\t"
    "s_waitcnt vmcnt(8) lgkmcnt(3)\n\t"
    "s_barrier\n\t"
    "v_mfma_f32_16x16x32_bf16 v[152:155], v[44:47], v[24:27], v[152:155]\n\t"
    "ds_read_b64 v[42:43], v192 offset:1288\n\t"
    "s_add_u32 m0, s0, 49152\n\t"
    "s_mov_b32 s1, 2359296\n\t"
    "buffer_load_dwordx4 v13, s[4:7], s1 offen lds\n\t"
    "v_mfma_f32_16x16x32_bf16 v[160:163], v[44:47], v[28:31], v[160:163]\n\t"
    "ds_read_b64_tr_b16 a[80:81], v11 offset:32768\n\t"
    "ds_read_b64_tr_b16 a[82:83], v11 offset:33792\n\t"
    "ds_read_b64_tr_b16 a[84:85], v12 offset:32768\n\t"
    "v_mfma_f32_16x16x32_bf16 v[172:175], v[48:51], v[16:19], v[172:175]\n\t"
    "ds_read_b64 v[44:45], v192 offset:1248\n\t"
    "v_mov_b32_dpp v16, v16 row_shr:1 row_mask:0xf bank_mask:0xf bound_ctrl:1\n\t"
    "buffer_load_dwordx4 v3, s[4:7], s1 offen offset:1024 lds\n\t"
    "v_mfma_f32_16x16x32_bf16 v[148:151], v[48:51], v[20:23], v[148:151]\n\t"
    "ds_read_b64 v[46:47], v192 offset:1256\n\t"
    "v_mov_b32_dpp v17, v17 row_shr:1 row_mask:0xf bank_mask:0xf bound_ctrl:1\n\t"
    "v_mov_b32_dpp v18, v18 row_shr:1 row_mask:0xf bank_mask:0xf bound_ctrl:1\n\t"
    "v_mfma_f32_16x16x32_bf16 v[156:159], v[48:51], v[24:27], v[156:159]\n\t"
    "v_mov_b32_dpp v19, v19 row_shr:1 row_mask:0xf bank_mask:0xf bound_ctrl:1\n\t"
    "buffer_load_dwordx4 v13, s[8:11], s1 offen offset:2048 lds\n\t"
    "ds_read_b64_tr_b16 a[86:87], v12 offset:33792\n\t"
    "v_mfma_f32_16x16x32_bf16 v[164:167], v[48:51], v[28:31], v[164:167]\n\t"
    "s_waitcnt lgkmcnt(8)\n\t"
    "ds_read_b64_tr_b16 a[88:89], v11 offset:34816\n\t"
    "ds_read_b64_tr_b16 a[90:91], v11 offset:35840\n\t"
    "v_mfma_f32_16x16x32_bf16 v[144:147], v[52:55], v[16:19], v[144:147]\n\t"
    "ds_read_b64 v[48:49], v192 offset:1216\n\t"
    "buffer_load_dwordx4 v3, s[8:11], s1 offen offset:3072 lds\n\t"
    "ds_read_b64_tr_b16 a[92:93], v12 offset:34816\n\t"
    "v_mfma_f32_16x16x32_bf16 v[152:155], v[52:55], v[20:23], v[152:155]\n\t"
    "ds_read_b64_tr_b16 a[94:95], v12 offset:35840\n\t"
    "v_mfma_f32_16x16x32_bf16 v[160:163], v[52:55], v[24:27], v[160:163]\n\t"
    "ds_read_b64 v[50:51], v192 offset:1224\n\t"
    "v_mfma_f32_16x16x32_bf16 v[168:171], v[52:55], v[28:31], v[168:171]\n\t"
    "v_mov_b32 v60, v144\n\t"
    "v_mov_b32 v61, v145\n\t"
    "v_mov_b32 v62, v146\n\t"
    "v_mfma_f32_16x16x32_bf16 v[148:151], v[36:39], v[16:19], v[148:151]\n\t"
    "ds_read_b64 v[52:53], v192 offset:1184\n\t"
    "v_mov_b32 v63, v147\n\t"
    "v_mfma_f32_16x16x32_bf16 v[156:159], v[36:39], v[20:23], v[156:159]\n\t"
    "ds_read_b64 v[54:55], v192 offset:1192\n\t"
    "s_waitcnt vmcnt(8) lgkmcnt(3)\n\t"
    "s_barrier\n\t"
    "v_mfma_f32_16x16x32_bf16 v[164:167], v[36:39], v[24:27], v[164:167]\n\t"
    "s_add_u32 m0, s0, 32768\n\t"
    "s_mov_b32 s1, 2654208\n\t"
    "buffer_load_dwordx4 v13, s[4:7], s1 offen lds\n\t"
    "ds_read_b64_tr_b16 a[96:97], v11 offset:0\n\t"
    "v_mfma_f32_16x16x32_bf16 v[172:175], v[36:39], v[28:31], v[172:175]\n\t"
    "s_waitcnt lgkmcnt(10)\n\t"
    "v_mov_b32_dpp v28, v28 row_shr:1 row_mask:0xf bank_mask:0xf bound_ctrl:1\n\t"
    "v_mov_b32_dpp v29, v29 row_shr:1 row_mask:0xf bank_mask:0xf bound_ctrl:1\n\t"
    "ds_read_b64_tr_b16 a[98:99], v11 offset:1024\n\t"
    "v_mfma_f32_16x16x32_bf16 v[152:155], v[40:43], v[16:19], v[152:155]\n\t"
    "ds_read_b64 v[36:37], v192 offset:1152\n\t"
    "v_mov_b32_dpp v30, v30 row_shr:1 row_mask:0xf bank_mask:0xf bound_ctrl:1\n\t"
    "v_mov_b32_dpp v31, v31 row_shr:1 row_mask:0xf bank_mask:0xf bound_ctrl:1\n\t"
    "buffer_load_dwordx4 v3, s[4:7], s1 offen offset:1024 lds\n\t"
    "v_mfma_f32_16x16x32_bf16 v[160:163], v[40:43], v[20:23], v[160:163]\n\t"
    "v_mov_b32_dpp v60, v148 row_shr:4 row_mask:0xf bank_mask:2\n\t"
    "v_mov_b32_dpp v61, v149 row_shr:4 row_mask:0xf bank_mask:2\n\t"
    "v_mov_b32_dpp v62, v150 row_shr:4 row_mask:0xf bank_mask:2\n\t"
    "ds_read_b64_tr_b16 a[100:101], v12 offset:0\n\t"
    "v_mfma_f32_16x16x32_bf16 v[168:171], v[40:43], v[24:27], v[168:171]\n\t"
    "ds_read_b64 v[38:39], v192 offset:1160\n\t"
    "v_mov_b32_dpp v63, v151 row_shr:4 row_mask:0xf bank_mask:2\n\t"
    "buffer_load_dwordx4 v13, s[8:11], s1 offen offset:2048 lds\n\t"
    "ds_read_b64_tr_b16 a[102:103], v12 offset:1024\n\t"
    "v_mfma_f32_16x16x32_bf16 v[144:147], v[40:43], v[28:31], v[144:147]\n\t"
    "v_mov_b32_dpp v60, v152 row_shr:8 row_mask:0xf bank_mask:4\n\t"
    "v_mov_b32_dpp v61, v153 row_shr:8 row_mask:0xf bank_mask:4\n\t"
    "v_mov_b32_dpp v62, v154 row_shr:8 row_mask:0xf bank_mask:4\n\t"
    "ds_read_b64_tr_b16 a[104:105], v11 offset:2048\n\t"
    "v_mfma_f32_16x16x32_bf16 v[156:159], v[44:47], v[16:19], v[156:159]\n\t"
    "ds_read_b64 v[40:41], v192 offset:1120\n\t"
    "v_mov_b32_dpp v63, v155 row_shr:8 row_mask:0xf bank_mask:4\n\t"
    "buffer_load_dwordx4 v3, s[8:11], s1 offen offset:3072 lds\n\t"
    "ds_read_b64_tr_b16 a[106:107], v11 offset:3072\n\t"
    "v_mfma_f32_16x16x32_bf16 v[164:167], v[44:47], v[20:23], v[164:167]\n\t"
    "ds_read_b64 v[42:43], v192 offset:1128\n\t"
    "ds_read_b64_tr_b16 a[108:109], v12 offset:2048\n\t"
    "ds_read_b64_tr_b16 a[110:111], v12 offset:3072\n\t"
    "v_mfma_f32_16x16x32_bf16 v[172:175], v[44:47], v[24:27], v[172:175]\n\t"
    "v_mov_b32_dpp v24, v24 row_shr:1 row_mask:0xf bank_mask:0xf bound_ctrl:1\n\t"
    "v_mov_b32_dpp v25, v25 row_shr:1 row_mask:0xf bank_mask:0xf bound_ctrl:1\n\t"
    "v_mfma_f32_16x16x32_bf16 v[148:151], v[44:47], v[28:31], v[148:151]\n\t"
    "s_waitcnt lgkmcnt(12)\n\t"
    "v_mov_b32_dpp v26, v26 row_shr:1 row_mask:0xf bank_mask:0xf bound_ctrl:1\n\t"
    "v_mov_b32_dpp v27, v27 row_shr:1 row_mask:0xf bank_mask:0xf bound_ctrl:1\n\t"
    "v_mfma_f32_16x16x32_bf16 v[160:163], v[48:51], v[16:19], v[160:163]\n\t"
    "ds_read_b64 v[44:45], v192 offset:1088\n\t"
    "v_mov_b32_dpp v60, v156 row_shr:12 row_mask:0xf bank_mask:8\n\t"
    "v_mov_b32_dpp v61, v157 row_shr:12 row_mask:0xf bank_mask:8\n\t"
    "v_mfma_f32_16x16x32_bf16 v[168:171], v[48:51], v[20:23], v[168:171]\n\t"
    "v_mov_b32_dpp v62, v158 row_shr:12 row_mask:0xf bank_mask:8\n\t"
    "v_mov_b32_dpp v63, v159 row_shr:12 row_mask:0xf bank_mask:8\n\t"
    "v_cvt_pk_bf16_f32 v68, v60, v62\n\t"
    "v_mfma_f32_16x16x32_bf16 v[144:147], v[48:51], v[24:27], v[144:147]\n\t"
    "ds_read_b64 v[46:47], v192 offset:1096\n\t"
    "v_cvt_pk_bf16_f32 v69, v61, v63\n\t"
    "v_mul_f32 v64, s94, v60\n\t"
    "v_mfma_f32_16x16x32_bf16 v[152:155], v[48:51], v[28:31], v[152:155]\n\t"
    "v_mul_f32 v65, s94, v61\n\t"
    "v_mul_f32 v66, s94, v62\n\t"
    "v_mul_f32 v67, s94, v63\n\t"
    "v_mfma_f32_16x16x32_bf16 v[164:167], v[52:55], v[16:19], v[164:167]\n\t"
    "ds_read_b64 v[48:49], v192 offset:1056\n\t"
    "ds_write2_b32 v194, v68, v69 offset0:0 offset1:32\n\t"
    "v_cvt_pk_bf16_f32 v70, v64, v66\n\t"
    "v_mfma_f32_16x16x32_bf16 v[172:175], v[52:55], v[20:23], v[172:175]\n\t"
    "ds_read_b64 v[50:51], v192 offset:1064\n\t"
    "v_mov_b32_dpp v20, v20 row_shr:1 row_mask:0xf bank_mask:0xf bound_ctrl:1\n\t"
    "v_mov_b32_dpp v21, v21 row_shr:1 row_mask:0xf bank_mask:0xf bound_ctrl:1\n\t"
    "s_waitcnt vmcnt(8) lgkmcnt(5)\n\t"
    "s_barrier\n\t"
    "v_mfma_f32_16x16x32_bf16 v[148:151], v[52:55], v[24:27], v[148:151]\n\t"
    "v_mov_b32_dpp v22, v22 row_shr:1 row_mask:0xf bank_mask:0xf bound_ctrl:1\n\t"
    "v_mov_b32_dpp v23, v23 row_shr:1 row_mask:0xf bank_mask:0xf bound_ctrl:1\n\t"
    "v_cvt_pk_bf16_f32 v71, v65, v67\n\t"
    "s_add_u32 m0, s0, 0\n\t"
    "s_mov_b32 s1, 2949120\n\t"
    "buffer_load_dwordx4 v13, s[4:7], s1 offen lds\n\t"
    "v_mfma_f32_16x16x32_bf16 v[156:159], v[52:55], v[28:31], v[156:159]\n\t"
    "s_waitcnt lgkmcnt(7)\n\t"
    "ds_write2_b32 v195, v70, v71 offset0:0 offset1:32\n\t"
    "v_mov_b32 v72, v160\n\t"
    "ds_read_b64_tr_b16 a[112:113], v11 offset:16384\n\t"
    "v_mfma_f32_16x16x32_bf16 v[168:171], v[36:39], v[16:19], v[168:171]\n\t"
    "ds_read_b64 v[52:53], v192 offset:1024\n\t"
    "v_mov_b32 v73, v161\n\t"
    "v_mov_b32 v74, v162\n\t"
    "buffer_load_dwordx4 v3, s[4:7], s1 offen offset:1024 lds\n\t"
    "v_mfma_f32_16x16x32_bf16 v[144:147], v[36:39], v[20:23], v[144:147]\n\t"
    "v_mov_b32 v75, v163\n\t"
    "v_mov_b32_dpp v72, v164 row_shr:4 row_mask:0xf bank_mask:2\n\t"
    "v_mov_b32_dpp v73, v165 row_shr:4 row_mask:0xf bank_mask:2\n\t"
    "ds_read_b64_tr_b16 a[114:115], v11 offset:17408\n\t"
    "v_mfma_f32_16x16x32_bf16 v[152:155], v[36:39], v[24:27], v[152:155]\n\t"
    "ds_read_b64 v[54:55], v192 offset:1032\n\t"
    "v_mov_b32_dpp v74, v166 row_shr:4 row_mask:0xf bank_mask:2\n\t"
    "v_mov_b32_dpp v75, v167 row_shr:4 row_mask:0xf bank_mask:2\n\t"
    "buffer_load_dwordx4 v13, s[8:11], s1 offen offset:2048 lds\n\t"
    "v_mfma_f32_16x16x32_bf16 v[160:163], v[36:39], v[28:31], v[160:163]\n\t"
    "v_mov_b32_dpp v72, v168 row_shr:8 row_mask:0xf bank_mask:4\n\t"
    "v_mov_b32_dpp v73, v169 row_shr:8 row_mask:0xf bank_mask:4\n\t"
    "v_mov_b32_dpp v74, v170 row_shr:8 row_mask:0xf bank_mask:4\n\t"
    "ds_read_b64_tr_b16 a[116:117], v12 offset:16384\n\t"
    "v_mfma_f32_16x16x32_bf16 v[172:175], v[40:43], v[16:19], v[172:175]\n\t"
    "ds_read_b64 v[36:37], v192 offset:992\n\t"
    "v_mov_b32_dpp v16, v16 row_shr:1 row_mask:0xf bank_mask:0xf bound_ctrl:1\n\t"
    "v_mov_b32_dpp v75, v171 row_shr:8 row_mask:0xf bank_mask:4\n\t"
    "buffer_load_dwordx4 v3, s[8:11], s1 offen offset:3072 lds\n\t"
    "v_mfma_f32_16x16x32_bf16 v[148:151], v[40:43], v[20:23], v[148:151]\n\t"
    "ds_read_b64 v[38:39], v192 offset:1000\n\t"
    "v_mov_b32_dpp v17, v17 row_shr:1 row_mask:0xf bank_mask:0xf bound_ctrl:1\n\t"
    "v_mov_b32_dpp v18, v18 row_shr:1 row_mask:0xf bank_mask:0xf bound_ctrl:1\n\t"
    "ds_read_b64_tr_b16 a[118:119], v12 offset:17408\n\t"
    "v_mfma_f32_16x16x32_bf16 v[156:159], v[40:43], v[24:27], v[156:159]\n\t"
    "v_mov_b32_dpp v19, v19 row_shr:1 row_mask:0xf bank_mask:0xf bound_ctrl:1\n\t"
    "ds_read_b64_tr_b16 a[120:121], v11 offset:18432\n\t"
    "ds_read_b64_tr_b16 a[122:123], v11 offset:19456\n\t"
    "ds_read_b64_tr_b16 a[124:125], v12 offset:18432\n\t"
    "v_mfma_f32_16x16x32_bf16 v[164:167], v[40:43], v[28:31], v[164:167]\n\t"
    "s_waitcnt lgkmcnt(12)\n\t"
    "v_mov_b32_dpp v72, v172 row_shr:12 row_mask:0xf bank_mask:8\n\t"
    "v_mov_b32_dpp v73, v173 row_shr:12 row_mask:0xf bank_mask:8\n\t"
    "ds_read_b64_tr_b16 a[126:127], v12 offset:19456\n\t"
    "v_mfma_f32_16x16x32_bf16 v[144:147], v[44:47], v[16:19], v[144:147]\n\t"
    "ds_read_b64 v[40:41], v192 offset:960\n\t"
    "v_mov_b32_dpp v74, v174 row_shr:12 row_mask:0xf bank_mask:8\n\t"
    "v_mov_b32_dpp v75, v175 row_shr:12 row_mask:0xf bank_mask:8\n\t"
    "v_mfma_f32_16x16x32_bf16 v[152:155], v[44:47], v[20:23], v[152:155]\n\t"
    "v_cvt_pk_bf16_f32 v80, v72, v74\n\t"
    "v_cvt_pk_bf16_f32 v81, v73, v75\n\t"
    "v_mul_f32 v76, s94, v72\n\t"
    "s_waitcnt vmcnt(8) lgkmcnt(1)\n\t"
    "s_barrier\n\t"
    "v_mfma_f32_16x16x32_bf16 v[160:163], v[44:47], v[24:27], v[160:163]\n\t"
    "ds_read_b64 v[42:43], v192 offset:968\n\t"
    "v_mul_f32 v77, s94, v73\n\t"
    "v_mul_f32 v78, s94, v74\n\t"
    "s_add_u32 m0, s0, 16384\n\t"
    "s_mov_b32 s1, 3244032\n\t"
    "buffer_load_dwordx4 v13, s[4:7], s1 offen lds\n\t"
    "v_mfma_f32_16x16x32_bf16 v[168:171], v[44:47], v[28:31], v[168:171]\n\t"
    "v_mul_f32 v79, s94, v75\n\t"
    "ds_write2_b32 v194, v80, v81 offset0:16 offset1:48\n\t"
    "v_cvt_pk_bf16_f32 v82, v76, v78\n\t"
    "v_mfma_f32_16x16x32_bf16 v[148:151], v[48:51], v[16:19], v[148:151]\n\t"
    "ds_read_b64 v[44:45], v192 offset:928\n\t"
    "v_cvt_pk_bf16_f32 v83, v77, v79\n\t"
    "ds_write2_b32 v195, v82, v83 offset0:16 offset1:48\n\t"
    "buffer_load_dwordx4 v3, s[4:7], s1 offen offset:1024 lds\n\t"
    "v_mfma_f32_16x16x32_bf16 v[156:159], v[48:51], v[20:23], v[156:159]\n\t"
    "ds_read_b64 v[46:47], v192 offset:936\n\t"
    "ds_read_b64_tr_b16 a[128:129], v11 offset:49152\n\t"
    "ds_read_b64_tr_b16 a[130:131], v11 offset:50176\n\t"
    "v_mfma_f32_16x16x32_bf16 v[164:167], v[48:51], v[24:27], v[164:167]\n\t"
    "buffer_load_dwordx4 v13, s[8:11], s1 offen offset:2048 lds\n\t"
    "ds_read_b64_tr_b16 a[132:133], v12 offset:49152\n\t"
    "ds_read_b64_tr_b16 a[134:135], v12 offset:50176\n\t"
    "v_mfma_f32_16x16x32_bf16 v[172:175], v[48:51], v[28:31], v[172:175]\n\t"
    "s_waitcnt lgkmcnt(15)\n\t"
    "v_mov_b32_dpp v28, v28 row_shr:1 row_mask:0xf bank_mask:0xf bound_ctrl:1\n\t"
    "v_mov_b32_dpp v29, v29 row_shr:1 row_mask:0xf bank_mask:0xf bound_ctrl:1\n\t"
    "s_waitcnt lgkmcnt(5)\n\t"
    "s_barrier\n\t"
    "v_mfma_f32_16x16x32_bf16 v[152:155], v[52:55], v[16:19], v[152:155]\n\t"
    "ds_read_b64 v[48:49], v192 offset:896\n\t"
    "v_mov_b32_dpp v30, v30 row_shr:1 row_mask:0xf bank_mask:0xf bound_ctrl:1\n\t"
    "v_mov_b32_dpp v31, v31 row_shr:1 row_mask:0xf bank_mask:0xf bound_ctrl:1\n\t"
    "buffer_load_dwordx4 v3, s[8:11], s1 offen offset:3072 lds\n\t"
    "v_mfma_f32_16x16x32_bf16 v[160:163], v[52:55], v[20:23], v[160:163]\n\t"
    "ds_read_b64_tr_b16 v[88:89], v221 offset:17664\n\t"
    "ds_read_b64_tr_b16 v[90:91], v221 offset:17792\n\t"
    "ds_read_b64_tr_b16 a[136:137], v11 offset:51200\n\t"
    "v_mfma_f32_16x16x32_bf16 v[168:171], v[52:55], v[24:27], v[168:171]\n\t"
    "ds_read_b64 v[50:51], v192 offset:904\n\t"
    "ds_read_b64_tr_b16 v[92:93], v221 offset:0\n\t"
    "ds_read_b64_tr_b16 a[138:139], v11 offset:52224\n\t"
    "v_mfma_f32_16x16x32_bf16 v[144:147], v[52:55], v[28:31], v[144:147]\n\t"
    "ds_read_b64_tr_b16 v[94:95], v221 offset:128\n\t"
    "ds_read_b64_tr_b16 a[140:141], v12 offset:51200\n\t"
    "ds_read_b64_tr_b16 a[142:143], v12 offset:52224\n\t"
    "v_mfma_f32_16x16x32_bf16 v[156:159], v[36:39], v[16:19], v[156:159]\n\t"
    "ds_read_b64 v[52:53], v192 offset:864\n\t"
    "v_mfma_f32_16x16x32_bf16 v[164:167], v[36:39], v[20:23], v[164:167]\n\t"
    "ds_read_b64 v[54:55], v192 offset:872\n\t"
    "v_mfma_f32_16x16x32_bf16 v[172:175], v[36:39], v[24:27], v[172:175]\n\t"
    "v_mov_b32_dpp v24, v24 row_shr:1 row_mask:0xf bank_mask:0xf bound_ctrl:1\n\t"
    "v_mov_b32_dpp v25, v25 row_shr:1 row_mask:0xf bank_mask:0xf bound_ctrl:1\n\t"
    "v_mfma_f32_16x16x32_bf16 v[148:151], v[36:39], v[28:31], v[148:151]\n\t"
    "s_waitcnt lgkmcnt(15)\n\t"
    "v_mov_b32_dpp v26, v26 row_shr:1 row_mask:0xf bank_mask:0xf bound_ctrl:1\n\t"
    "v_mov_b32_dpp v27, v27 row_shr:1 row_mask:0xf bank_mask:0xf bound_ctrl:1\n\t"
    "v_mfma_f32_16x16x32_bf16 v[160:163], v[40:43], v[16:19], v[160:163]\n\t"
    "ds_read_b64 v[36:37], v192 offset:832\n\t"
    "v_mfma_f32_16x16x32_bf16 v[168:171], v[40:43], v[20:23], v[168:171]\n\t"
    "s_waitcnt lgkmcnt(5)\n\t"
    "s_waitcnt vmcnt(8) lgkmcnt(3)\n\t"
    "s_barrier\n\t"
    "v_mfma_f32_16x16x32_bf16 v[144:147], v[40:43], v[24:27], v[144:147]\n\t"
    "ds_read_b64 v[38:39], v192 offset:840\n\t"
    "global_store_dwordx4 v223, v[88:91], s[36:37] sc1\n\t"
    "s_add_u32 m0, s0, 49152\n\t"
    "s_mov_b32 s1, 3538944\n\t"
    "buffer_load_dwordx4 v13, s[4:7], s1 offen lds\n\t"
    "v_mfma_f32_16x16x32_bf16 v[152:155], v[40:43], v[28:31], v[152:155]\n\t"
    "global_store_dwordx4 v222, v[92:95], s[36:37] sc1\n\t"
    "v_add_u32 v223, 589824, v223\n\t"
    "ds_read_b64_tr_b16 a[144:145], v11 offset:32768\n\t"
    "v_mfma_f32_16x16x32_bf16 v[164:167], v[44:47], v[16:19], v[164:167]\n\t"
    "ds_read_b64 v[40:41], v192 offset:800\n\t"
    "v_add_u32 v222, 589824, v222\n\t"
    "buffer_load_dwordx4 v3, s[4:7], s1 offen offset:1024 lds\n\t"
    "v_mfma_f32_16x16x32_bf16 v[172:175], v[44:47], v[20:23], v[172:175]\n\t"
    "ds_read_b64 v[42:43], v192 offset:808\n\t"
    "v_mov_b32_dpp v20, v20 row_shr:1 row_mask:0xf bank_mask:0xf bound_ctrl:1\n\t"
    "v_mov_b32_dpp v21, v21 row_shr:1 row_mask:0xf bank_mask:0xf bound_ctrl:1\n\t"
    "v_mfma_f32_16x16x32_bf16 v[148:151], v[44:47], v[24:27], v[148:151]\n\t"
    "v_mov_b32_dpp v22, v22 row_shr:1 row_mask:0xf bank_mask:0xf bound_ctrl:1\n\t"
    "v_mov_b32_dpp v23, v23 row_shr:1 row_mask:0xf bank_mask:0xf bound_ctrl:1\n\t"
    "buffer_load_dwordx4 v13, s[8:11], s1 offen offset:2048 lds\n\t"
    "v_mfma_f32_16x16x32_bf16 v[156:159], v[44:47], v[28:31], v[156:159]\n\t"
    "s_waitcnt lgkmcnt(5)\n\t"
    "ds_read_b64_tr_b16 a[146:147], v11 offset:33792\n\t"
    "ds_read_b64_tr_b16 a[148:149], v12 offset:32768\n\t"
    "v_mfma_f32_16x16x32_bf16 v[168:171], v[48:51], v[16:19], v[168:171]\n\t"
    "ds_read_b64 v[44:45], v192 offset:768\n\t"
    "buffer_load_dwordx4 v3, s[8:11], s1 offen offset:3072 lds\n\t"
    "ds_read_b64_tr_b16 a[150:151], v12 offset:33792\n\t"
    "v_mfma_f32_16x16x32_bf16 v[144:147], v[48:51], v[20:23], v[144:147]\n\t"
    "ds_read_b64_tr_b16 a[152:153], v11 offset:34816\n\t"
    "ds_read_b64_tr_b16 a[154:155], v11 offset:35840\n\t"
    "ds_read_b64_tr_b16 a[156:157], v12 offset:34816\n\t"
    "v_mfma_f32_16x16x32_bf16 v[152:155], v[48:51], v[24:27], v[152:155]\n\t"
    "ds_read_b64 v[46:47], v192 offset:776\n\t"
    "ds_read_b64_tr_b16 a[158:159], v12 offset:35840\n\t"
    "v_mfma_f32_16x16x32_bf16 v[160:163], v[48:51], v[28:31], v[160:163]\n\t"
    "v_mfma_f32_16x16x32_bf16 v[172:175], v[52:55], v[16:19], v[172:175]\n\t"
    "ds_read_b64 v[48:49], v192 offset:736\n\t"
    "v_mov_b32_dpp v16, v16 row_shr:1 row_mask:0xf bank_mask:0xf bound_ctrl:1\n\t"
    "v_mfma_f32_16x16x32_bf16 v[148:151], v[52:55], v[20:23], v[148:151]\n\t"
    "ds_read_b64 v[50:51], v192 offset:744\n\t"
    "v_mov_b32_dpp v17, v17 row_shr:1 row_mask:0xf bank_mask:0xf bound_ctrl:1\n\t"
    "v_mov_b32_dpp v18, v18 row_shr:1 row_mask:0xf bank_mask:0xf bound_ctrl:1\n\t"
    "s_waitcnt vmcnt(10) lgkmcnt(2)\n\t"
    "s_barrier\n\t"
    "v_mfma_f32_16x16x32_bf16 v[156:159], v[52:55], v[24:27], v[156:159]\n\t"
    "v_mov_b32_dpp v19, v19 row_shr:1 row_mask:0xf bank_mask:0xf bound_ctrl:1\n\t"
    "s_add_u32 m0, s0, 32768\n\t"
    "s_mov_b32 s1, 3833856\n\t"
    "buffer_load_dwordx4 v13, s[4:7], s1 offen lds\n\t"
    "v_mfma_f32_16x16x32_bf16 v[164:167], v[52:55], v[28:31], v[164:167]\n\t"
    "s_waitcnt lgkmcnt(11)\n\t"
    "ds_read_b64_tr_b16 a[160:161], v11 offset:0\n\t"
    "v_mfma_f32_16x16x32_bf16 v[144:147], v[36:39], v[16:19], v[144:147]\n\t"
    "ds_read_b64 v[52:53], v192 offset:704\n\t"
    "buffer_load_dwordx4 v3, s[4:7], s1 offen offset:1024 lds\n\t"
    "v_mfma_f32_16x16x32_bf16 v[152:155], v[36:39], v[20:23], v[152:155]\n\t"
    "ds_read_b64_tr_b16 a[162:163], v11 offset:1024\n\t"
    "ds_read_b64_tr_b16 a[164:165], v12 offset:0\n\t"
    "v_mfma_f32_16x16x32_bf16 v[160:163], v[36:39], v[24:27], v[160:163]\n\t"
    "ds_read_b64 v[54:55], v192 offset:712\n\t"
    "buffer_load_dwordx4 v13, s[8:11], s1 offen offset:2048 lds\n\t"
    "v_mfma_f32_16x16x32_bf16 v[168:171], v[36:39], v[28:31], v[168:171]\n\t"
    "ds_read_b64_tr_b16 a[166:167], v12 offset:1024\n\t"
    "ds_read_b64_tr_b16 a[168:169], v11 offset:2048\n\t"
    "v_mfma_f32_16x16x32_bf16 v[148:151], v[40:43], v[16:19], v[148:151]\n\t"
    "ds_read_b64 v[36:37], v192 offset:672\n\t"
    "buffer_load_dwordx4 v3, s[8:11], s1 offen offset:3072 lds\n\t"
    "v_mfma_f32_16x16x32_bf16 v[156:159], v[40:43], v[20:23], v[156:159]\n\t"
    "ds_read_b64 v[38:39], v192 offset:680\n\t"
    "ds_read_b64_tr_b16 a[170:171], v11 offset:3072\n\t"
    "v_mfma_f32_16x16x32_bf16 v[164:167], v[40:43], v[24:27], v[164:167]\n\t"
    "ds_read_b64_tr_b16 a[172:173], v12 offset:2048\n\t"
    "ds_read_b64_tr_b16 a[174:175], v12 offset:3072\n\t"
    "v_mfma_f32_16x16x32_bf16 v[172:175], v[40:43], v[28:31], v[172:175]\n\t"
    "s_waitcnt lgkmcnt(12)\n\t"
    "v_mov_b32_dpp v28, v28 row_shr:1 row_mask:0xf bank_mask:0xf bound_ctrl:1\n\t"
    "v_mov_b32_dpp v29, v29 row_shr:1 row_mask:0xf bank_mask:0xf bound_ctrl:1\n\t"
    "v_mfma_f32_16x16x32_bf16 v[152:155], v[44:47], v[16:19], v[152:155]\n\t"
    "ds_read_b64 v[40:41], v192 offset:640\n\t"
    "v_mov_b32_dpp v30, v30 row_shr:1 row_mask:0xf bank_mask:0xf bound_ctrl:1\n\t"
    "v_mov_b32_dpp v31, v31 row_shr:1 row_mask:0xf bank_mask:0xf bound_ctrl:1\n\t"
    "v_mfma_f32_16x16x32_bf16 v[160:163], v[44:47], v[20:23], v[160:163]\n\t"
    "v_mfma_f32_16x16x32_bf16 v[168:171], v[44:47], v[24:27], v[168:171]\n\t"
    "ds_read_b64 v[42:43], v192 offset:648\n\t"
    "v_mfma_f32_16x16x32_bf16 v[144:147], v[44:47], v[28:31], v[144:147]\n\t"
    "v_mfma_f32_16x16x32_bf16 v[156:159], v[48:51], v[16:19], v[156:159]\n\t"
    "ds_read_b64 v[44:45], v192 offset:608\n\t"
    "v_mfma_f32_16x16x32_bf16 v[164:167], v[48:51], v[20:23], v[164:167]\n\t"
    "ds_read_b64 v[46:47], v192 offset:616\n\t"
    "s_waitcnt vmcnt(7)\n\t"
    "s_waitcnt vmcnt(10) lgkmcnt(4)\n\t"
    "s_barrier\n\t"
    "s_cmp_lg_u32 s3, 0\n\t"
    "s_cbranch_scc1 L_pfe_%=\n\t"
    "s_lshl_b32 s28, s92, 7\n\t"
    "s_add_u32 s28, s28, 41979904\n\t"
    "v_mov_b32 v88, s66\n\t"
    "v_mov_b32 v89, s67\n\t"
    "v_mov_b32 v90, s28\n\t"
    "s_mov_b64 exec, 1\n\t"
    "global_store_dwordx2 v90, v[88:89], s[36:37] sc1\n\t"
    "s_mov_b64 exec, -1\n\t"
    "L_pfe_%=:\n\t"
    "v_mfma_f32_16x16x32_bf16 v[172:175], v[48:51], v[24:27], v[172:175]\n\t"
    "v_mov_b32_dpp v24, v24 row_shr:1 row_mask:0xf bank_mask:0xf bound_ctrl:1\n\t"
    "v_mov_b32_dpp v25, v25 row_shr:1 row_mask:0xf bank_mask:0xf bound_ctrl:1\n\t"
    "s_add_u32 m0, s0, 0\n\t"
    "s_mov_b32 s1, 4128768\n\t"
    "buffer_load_dwordx4 v13, s[4:7], s1 offen lds\n\t"
    "v_mfma_f32_16x16x32_bf16 v[148:151], v[48:51], v[28:31], v[148:151]\n\t"
    "s_waitcnt lgkmcnt(7)\n\t"
    "v_mov_b32_dpp v26, v26 row_shr:1 row_mask:0xf bank_mask:0xf bound_ctrl:1\n\t"
    "v_mov_b32_dpp v27, v27 row_shr:1 row_mask:0xf bank_mask:0xf bound_ctrl:1\n\t"
    "v_mfma_f32_16x16x32_bf16 v[160:163], v[52:55], v[16:19], v[160:163]\n\t"
    "ds_read_b64 v[48:49], v192 offset:576\n\t"
    "buffer_load_dwordx4 v3, s[4:7], s1 offen offset:1024 lds\n\t"
    "ds_read_b64_tr_b16 a[176:177], v11 offset:16384\n\t"
    "v_mfma_f32_16x16x32_bf16 v[168:171], v[52:55], v[20:23], v[168:171]\n\t"
    "ds_read_b64_tr_b16 a[178:179], v11 offset:17408\n\t"
    "ds_read_b64_tr_b16 a[180:181], v12 offset:16384\n\t"
    "ds_read_b64_tr_b16 a[182:183], v12 offset:17408\n\t"
    "v_mfma_f32_16x16x32_bf16 v[144:147], v[52:55], v[24:27], v[144:147]\n\t"
    "ds_read_b64 v[50:51], v192 offset:584\n\t"
    "buffer_load_dwordx4 v13, s[8:11], s1 offen offset:2048 lds\n\t"
    "ds_read_b64_tr_b16 a[184:185], v11 offset:18432\n\t"
    "v_mfma_f32_16x16x32_bf16 v[152:155], v[52:55], v[28:31], v[152:155]\n\t"
    "ds_read_b64_tr_b16 a[186:187], v11 offset:19456\n\t"
    "ds_read_b64_tr_b16 a[188:189], v12 offset:18432\n\t"
    "ds_read_b64_tr_b16 a[190:191], v12 offset:19456\n\t"
    "v_mfma_f32_16x16x32_bf16 v[164:167], v[36:39], v[16:19], v[164:167]\n\t"
    "ds_read_b64 v[52:53], v192 offset:544\n\t"
    "buffer_load_dwordx4 v3, s[8:11], s1 offen offset:3072 lds\n\t"
    "v_min_u32 v128, 35, v1\n\t"
    "s_mul_i32 s28, s3, 4608\n\t"
    "s_add_u32 s28, s28, 41979904\n\t"
    "v_lshl_add_u32 v128, v128, 7, s28\n\t"
    "v_mfma_f32_16x16x32_bf16 v[172:175], v[36:39], v[20:23], v[172:175]\n\t"
    "ds_read_b64 v[54:55], v192 offset:552\n\t"
    "v_mov_b32_dpp v20, v20 row_shr:1 row_mask:0xf bank_mask:0xf bound_ctrl:1\n\t"
    "v_mov_b32_dpp v21, v21 row_shr:1 row_mask:0xf bank_mask:0xf bound_ctrl:1\n\t"
    "v_mfma_f32_16x16x32_bf16 v[148:151], v[36:39], v[24:27], v[148:151]\n\t"
    "v_mov_b32_dpp v22, v22 row_shr:1 row_mask:0xf bank_mask:0xf bound_ctrl:1\n\t"
    "v_mov_b32_dpp v23, v23 row_shr:1 row_mask:0xf bank_mask:0xf bound_ctrl:1\n\t"
    "v_mfma_f32_16x16x32_bf16 v[156:159], v[36:39], v[28:31], v[156:159]\n\t"
    "s_waitcnt lgkmcnt(12)\n\t"
    "v_mfma_f32_16x16x32_bf16 v[168:171], v[40:43], v[16:19], v[168:171]\n\t"
    "ds_read_b64 v[36:37], v192 offset:512\n\t"
    "v_mfma_f32_16x16x32_bf16 v[144:147], v[40:43], v[20:23], v[144:147]\n\t"
    "s_waitcnt vmcnt(8) lgkmcnt(3)\n\t"
    "s_barrier\n\t"
    "v_mfma_f32_16x16x32_bf16 v[152:155], v[40:43], v[24:27], v[152:155]\n\t"
    "ds_read_b64 v[38:39], v192 offset:520\n\t"
    "s_add_u32 m0, s0, 16384\n\t"
    "s_mov_b32 s1, 4423680\n\t"
    "buffer_load_dwordx4 v13, s[4:7], s1 offen lds\n\t"
    "v_mfma_f32_16x16x32_bf16 v[160:163], v[40:43], v[28:31], v[160:163]\n\t"
    "ds_read_b64_tr_b16 a[192:193], v11 offset:49152\n\t"
    "ds_read_b64_tr_b16 a[194:195], v11 offset:50176\n\t"
    "v_mfma_f32_16x16x32_bf16 v[172:175], v[44:47], v[16:19], v[172:175]\n\t"
    "ds_read_b64 v[40:41], v192 offset:480\n\t"
    "v_mov_b32_dpp v16, v16 row_shr:1 row_mask:0xf bank_mask:0xf bound_ctrl:1\n\t"
    "buffer_load_dwordx4 v3, s[4:7], s1 offen offset:1024 lds\n\t"
    "v_mfma_f32_16x16x32_bf16 v[148:151], v[44:47], v[20:23], v[148:151]\n\t"
    "ds_read_b64 v[42:43], v192 offset:488\n\t"
    "v_mov_b32_dpp v17, v17 row_shr:1 row_mask:0xf bank_mask:0xf bound_ctrl:1\n\t"
    "v_mov_b32_dpp v18, v18 row_shr:1 row_mask:0xf bank_mask:0xf bound_ctrl:1\n\t"
    "v_mfma_f32_16x16x32_bf16 v[156:159], v[44:47], v[24:27], v[156:159]\n\t"
    "v_mov_b32_dpp v19, v19 row_shr:1 row_mask:0xf bank_mask:0xf bound_ctrl:1\n\t"
    "buffer_load_dwordx4 v13, s[8:11], s1 offen offset:2048 lds\n\t"
    "v_mfma_f32_16x16x32_bf16 v[164:167], v[44:47], v[28:31], v[164:167]\n\t"
    "s_waitcnt lgkmcnt(6)\n\t"
    "ds_read_b64_tr_b16 a[196:197], v12 offset:49152\n\t"
    "v_mfma_f32_16x16x32_bf16 v[144:147], v[48:51], v[16:19], v[144:147]\n\t"
    "ds_read_b64 v[44:45], v192 offset:448\n\t"
    "buffer_load_dwordx4 v3, s[8:11], s1 offen offset:3072 lds\n\t"
    "v_mfma_f32_16x16x32_bf16 v[152:155], v[48:51], v[20:23], v[152:155]\n\t"
    "ds_read_b64_tr_b16 a[198:199], v12 offset:50176\n\t"
    "ds_read_b64_tr_b16 a[200:201], v11 offset:51200\n\t"
    "v_mfma_f32_16x16x32_bf16 v[160:163], v[48:51], v[24:27], v[160:163]\n\t"
    "ds_read_b64 v[46:47], v192 offset:456\n\t"
    "ds_read_b64_tr_b16 a[202:203], v11 offset:52224\n\t"
    "v_mfma_f32_16x16x32_bf16 v[168:171], v[48:51], v[28:31], v[168:171]\n\t"
    "ds_read_b64_tr_b16 a[204:205], v12 offset:51200\n\t"
    "ds_read_b64_tr_b16 a[206:207], v12 offset:52224\n\t"
    "v_mfma_f32_16x16x32_bf16 v[148:151], v[52:55], v[16:19], v[148:151]\n\t"
    "ds_read_b64 v[48:49], v192 offset:416\n\t"
    "v_mfma_f32_16x16x32_bf16 v[156:159], v[52:55], v[20:23], v[156:159]\n\t"
    "ds_read_b64 v[50:51], v192 offset:424\n\t"
    "v_mfma_f32_16x16x32_bf16 v[164:167], v[52:55], v[24:27], v[164:167]\n\t"
    "v_mfma_f32_16x16x32_bf16 v[172:175], v[52:55], v[28:31], v[172:175]\n\t"
    "s_waitcnt lgkmcnt(10)\n\t"
    "v_mov_b32_dpp v28, v28 row_shr:1 row_mask:0xf bank_mask:0xf bound_ctrl:1\n\t"
    "v_mov_b32_dpp v29, v29 row_shr:1 row_mask:0xf bank_mask:0xf bound_ctrl:1\n\t"
    "v_mfma_f32_16x16x32_bf16 v[152:155], v[36:39], v[16:19], v[152:155]\n\t"
    "ds_read_b64 v[52:53], v192 offset:384\n\t"
    "v_mov_b32_dpp v30, v30 row_shr:1 row_mask:0xf bank_mask:0xf bound_ctrl:1\n\t"
    "v_mov_b32_dpp v31, v31 row_shr:1 row_mask:0xf bank_mask:0xf bound_ctrl:1\n\t"
    "v_mfma_f32_16x16x32_bf16 v[160:163], v[36:39], v[20:23], v[160:163]\n\t"
    "s_waitcnt vmcnt(8) lgkmcnt(3)\n\t"
    "s_barrier\n\t"
    "v_mfma_f32_16x16x32_bf16 v[168:171], v[36:39], v[24:27], v[168:171]\n\t"
    "ds_read_b64 v[54:55], v192 offset:392\n\t"
    "s_add_u32 m0, s0, 49152\n\t"
    "s_mov_b32 s1, 4718592\n\t"
    "buffer_load_dwordx4 v13, s[4:7], s1 offen lds\n\t"
    "v_mfma_f32_16x16x32_bf16 v[144:147], v[36:39], v[28:31], v[144:147]\n\t"
    "ds_read_b64_tr_b16 a[208:209], v11 offset:32768\n\t"
    "ds_read_b64_tr_b16 a[210:211], v11 offset:33792\n\t"
    "ds_read_b64_tr_b16 a[212:213], v12 offset:32768\n\t"
    "v_mfma_f32_16x16x32_bf16 v[156:159], v[40:43], v[16:19], v[156:159]\n\t"
    "ds_read_b64 v[36:37], v192 offset:352\n\t"
    "ds_read2_b64 v[56:59], v203 offset0:1 offset1:5\n\t"
    "buffer_load_dwordx4 v3, s[4:7], s1 offen offset:1024 lds\n\t"
    "v_mfma_f32_16x16x32_bf16 v[164:167], v[40:43], v[20:23], v[164:167]\n\t"
    "ds_read_b64 v[38:39], v192 offset:360\n\t"
    "ds_read2_b64 v[60:63], v203 offset0:9 offset1:13\n\t"
    "ds_read_b64_tr_b16 a[214:215], v12 offset:33792\n\t"
    "v_mfma_f32_16x16x32_bf16 v[172:175], v[40:43], v[24:27], v[172:175]\n\t"
    "v_mov_b32_dpp v24, v24 row_shr:1 row_mask:0xf bank_mask:0xf bound_ctrl:1\n\t"
    "v_mov_b32_dpp v25, v25 row_shr:1 row_mask:0xf bank_mask:0xf bound_ctrl:1\n\t"
    "buffer_load_dwordx4 v13, s[8:11], s1 offen offset:2048 lds\n\t"
    "v_mfma_f32_16x16x32_bf16 v[148:151], v[40:43], v[28:31], v[148:151]\n\t"
    "s_waitcnt lgkmcnt(10)\n\t"
    "v_mov_b32_dpp v26, v26 row_shr:1 row_mask:0xf bank_mask:0xf bound_ctrl:1\n\t"
    "v_mov_b32_dpp v27, v27 row_shr:1 row_mask:0xf bank_mask:0xf bound_ctrl:1\n\t"
    "v_mfma_f32_16x16x32_bf16 v[160:163], v[44:47], v[16:19], v[160:163]\n\t"
    "ds_read_b64 v[40:41], v192 offset:320\n\t"
    "ds_read2_b64 v[64:67], v203 offset0:133 offset1:137\n\t"
    "buffer_load_dwordx4 v3, s[8:11], s1 offen offset:3072 lds\n\t"
    "v_mfma_f32_16x16x32_bf16 v[168:171], v[44:47], v[20:23], v[168:171]\n\t"
    "ds_read2_b64 v[68:71], v203 offset0:141 offset1:145\n\t"
    "ds_read2_b64 v[72:75], v203 offset0:67 offset1:71\n\t"
    "ds_read_b64_tr_b16 a[216:217], v11 offset:34816\n\t"
    "v_mfma_f32_16x16x32_bf16 v[144:147], v[44:47], v[24:27], v[144:147]\n\t"
    "ds_read_b64 v[42:43], v192 offset:328\n\t"
    "ds_read2_b64 v[76:79], v203 offset0:75 offset1:79\n\t"
    "ds_read_b64_tr_b16 a[218:219], v11 offset:35840\n\t"
    "v_mfma_f32_16x16x32_bf16 v[152:155], v[44:47], v[28:31], v[152:155]\n\t"
    "ds_read2_b64 v[80:83], v203 offset0:199 offset1:203\n\t"
    "ds_read2_b64 v[84:87], v203 offset0:207 offset1:211\n\t"
    "ds_read_b64_tr_b16 a[220:221], v12 offset:34816\n\t"
    "v_mfma_f32_16x16x32_bf16 v[164:167], v[48:51], v[16:19], v[164:167]\n\t"
    "ds_read_b64 v[44:45], v192 offset:288\n\t"
    "ds_read_b64_tr_b16 a[222:223], v12 offset:35840\n\t"
    "v_mfma_f32_16x16x32_bf16 v[172:175], v[48:51], v[20:23], v[172:175]\n\t"
    "ds_read_b64 v[46:47], v192 offset:296\n\t"
    "v_mov_b32_dpp v20, v20 row_shr:1 row_mask:0xf bank_mask:0xf bound_ctrl:1\n\t"
    "v_mov_b32_dpp v21, v21 row_shr:1 row_mask:0xf bank_mask:0xf bound_ctrl:1\n\t"
    "s_waitcnt vmcnt(8) lgkmcnt(1)\n\t"
    "s_barrier\n\t"
    "v_mfma_f32_16x16x32_bf16 v[148:151], v[48:51], v[24:27], v[148:151]\n\t"
    "v_mov_b32_dpp v22, v22 row_shr:1 row_mask:0xf bank_mask:0xf bound_ctrl:1\n\t"
    "v_mov_b32_dpp v23, v23 row_shr:1 row_mask:0xf bank_mask:0xf bound_ctrl:1\n\t"
    "s_add_u32 m0, s0, 32768\n\t"
    "s_mov_b32 s1, 5013504\n\t"
    "buffer_load_dwordx4 v13, s[4:7], s1 offen lds\n\t"
    "v_mfma_f32_16x16x32_bf16 v[156:159], v[48:51], v[28:31], v[156:159]\n\t"
    "s_waitcnt lgkmcnt(11)\n\t"
    "ds_read_b64_tr_b16 a[224:225], v11 offset:0\n\t"
    "ds_read_b64_tr_b16 a[226:227], v11 offset:1024\n\t"
    "v_mfma_f32_16x16x32_bf16 v[176:179], v[112:115], v[56:59], 0\n\t"
    "buffer_load_dwordx4 v3, s[4:7], s1 offen offset:1024 lds\n\t"
    "ds_read_b64_tr_b16 a[228:229], v12 offset:0\n\t"
    "v_mfma_f32_16x16x32_bf16 v[176:179], v[116:119], v[60:63], v[176:179]\n\t"
    "ds_read_b64_tr_b16 a[230:231], v12 offset:1024\n\t"
    "ds_read_b64_tr_b16 a[232:233], v11 offset:2048\n\t"
    "v_mfma_f32_16x16x32_bf16 v[176:179], v[120:123], v[64:67], v[176:179]\n\t"
    "buffer_load_dwordx4 v13, s[8:11], s1 offen offset:2048 lds\n\t"
    "ds_read_b64_tr_b16 a[234:235], v11 offset:3072\n\t"
    "v_mfma_f32_16x16x32_bf16 v[176:179], v[124:127], v[68:71], v[176:179]\n\t"
    "s_waitcnt lgkmcnt(15)\n\t"
    "ds_read2_b64 v[56:59], v203 offset0:0 offset1:4\n\t"
    "v_mfma_f32_16x16x32_bf16 v[168:171], v[52:55], v[16:19], v[168:171]\n\t"
    "ds_read_b64 v[48:49], v192 offset:256\n\t"
    "ds_read2_b64 v[60:63], v203 offset0:8 offset1:12\n\t"
    "buffer_load_dwordx4 v3, s[8:11], s1 offen offset:3072 lds\n\t"
    "v_mfma_f32_16x16x32_bf16 v[144:147], v[52:55], v[20:23], v[144:147]\n\t"
    "ds_read2_b64 v[64:67], v203 offset0:132 offset1:136\n\t"
    "ds_read2_b64 v[68:71], v203 offset0:140 offset1:144\n\t"
    "v_mfma_f32_16x16x32_bf16 v[152:155], v[52:55], v[24:27], v[152:155]\n\t"
    "ds_read_b64 v[50:51], v192 offset:264\n\t"
    "ds_read_b64_tr_b16 a[236:237], v12 offset:2048\n\t"
    "v_mfma_f32_16x16x32_bf16 v[160:163], v[52:55], v[28:31], v[160:163]\n\t"
    "s_waitcnt lgkmcnt(15)\n\t"
    "ds_read_b64_tr_b16 a[238:239], v12 offset:3072\n\t"
    "v_mfma_f32_16x16x32_bf16 v[180:183], v[112:115], v[72:75], 0\n\t"
    "v_mfma_f32_16x16x32_bf16 v[180:183], v[116:119], v[76:79], v[180:183]\n\t"
    "v_mfma_f32_16x16x32_bf16 v[180:183], v[120:123], v[80:83], v[180:183]\n\t"
    "v_mfma_f32_16x16x32_bf16 v[180:183], v[124:127], v[84:87], v[180:183]\n\t"
    "ds_read2_b64 v[72:75], v203 offset0:66 offset1:70\n\t"
    "ds_read2_b64 v[76:79], v203 offset0:74 offset1:78\n\t"
    "v_mfma_f32_16x16x32_bf16 v[172:175], v[36:39], v[16:19], v[172:175]\n\t"
    "ds_read_b64 v[52:53], v192 offset:224\n\t"
    "v_mov_b32_dpp v16, v16 row_shr:1 row_mask:0xf bank_mask:0xf bound_ctrl:1\n\t"
    "v_mfma_f32_16x16x32_bf16 v[148:151], v[36:39], v[20:23], v[148:151]\n\t"
    "ds_read_b64 v[54:55], v192 offset:232\n\t"
    "v_mov_b32_dpp v17, v17 row_shr:1 row_mask:0xf bank_mask:0xf bound_ctrl:1\n\t"
    "v_mov_b32_dpp v18, v18 row_shr:1 row_mask:0xf bank_mask:0xf bound_ctrl:1\n\t"
    "v_mfma_f32_16x16x32_bf16 v[156:159], v[36:39], v[24:27], v[156:159]\n\t"
    "v_mov_b32_dpp v19, v19 row_shr:1 row_mask:0xf bank_mask:0xf bound_ctrl:1\n\t"
    "ds_read2_b64 v[80:83], v203 offset0:198 offset1:202\n\t"
    "v_mfma_f32_16x16x32_bf16 v[164:167], v[36:39], v[28:31], v[164:167]\n\t"
    "ds_read2_b64 v[84:87], v203 offset0:206 offset1:210\n\t"
    "s_waitcnt lgkmcnt(9)\n\t"
    "v_mfma_f32_16x16x32_bf16 v[184:187], v[112:115], v[56:59], 0\n\t"
    "v_mfma_f32_16x16x32_bf16 v[184:187], v[116:119], v[60:63], v[184:187]\n\t"
    "v_mfma_f32_16x16x32_bf16 v[184:187], v[120:123], v[64:67], v[184:187]\n\t"
    "v_mfma_f32_16x16x32_bf16 v[184:187], v[124:127], v[68:71], v[184:187]\n\t"
    "s_waitcnt lgkmcnt(15)\n\t"
    "v_mfma_f32_16x16x32_bf16 v[144:147], v[40:43], v[16:19], v[144:147]\n\t"
    "ds_read_b64 v[36:37], v192 offset:192\n\t"
    "v_mfma_f32_16x16x32_bf16 v[152:155], v[40:43], v[20:23], v[152:155]\n\t"
    "v_mfma_f32_16x16x32_bf16 v[160:163], v[40:43], v[24:27], v[160:163]\n\t"
    "ds_read_b64 v[38:39], v192 offset:200\n\t"
    "v_mfma_f32_16x16x32_bf16 v[168:171], v[40:43], v[28:31], v[168:171]\n\t"
    "s_waitcnt lgkmcnt(2)\n\t"
    "v_mfma_f32_16x16x32_bf16 v[188:191], v[112:115], v[72:75], 0\n\t"
    "v_mfma_f32_16x16x32_bf16 v[188:191], v[116:119], v[76:79], v[188:191]\n\t"
    "v_mfma_f32_16x16x32_bf16 v[188:191], v[120:123], v[80:83], v[188:191]\n\t"
    "v_mfma_f32_16x16x32_bf16 v[188:191], v[124:127], v[84:87], v[188:191]\n\t"
    "v_mfma_f32_16x16x32_bf16 v[148:151], v[44:47], v[16:19], v[148:151]\n\t"
    "ds_read_b64 v[40:41], v192 offset:160\n\t"
    "v_mfma_f32_16x16x32_bf16 v[156:159], v[44:47], v[20:23], v[156:159]\n\t"
    "ds_read_b64 v[42:43], v192 offset:168\n\t"
    "s_waitcnt vmcnt(8) lgkmcnt(10)\n\t"
    "s_barrier\n\t"
    "v_mfma_f32_16x16x32_bf16 v[164:167], v[44:47], v[24:27], v[164:167]\n\t"
    "ds_read_b64_tr_b16 a[240:241], v11 offset:16384\n\t"
    "ds_read_b64_tr_b16 a[242:243], v11 offset:17408\n\t"
    "v_mfma_f32_16x16x32_bf16 v[172:175], v[44:47], v[28:31], v[172:175]\n\t"
    "s_waitcnt lgkmcnt(8)\n\t"
    "v_mov_b32_dpp v28, v28 row_shr:1 row_mask:0xf bank_mask:0xf bound_ctrl:1\n\t"
    "v_mov_b32_dpp v29, v29 row_shr:1 row_mask:0xf bank_mask:0xf bound_ctrl:1\n\t"
    "v_mfma_f32_16x16x32_bf16 v[152:155], v[48:51], v[16:19], v[152:155]\n\t"
    "ds_read_b64 v[44:45], v192 offset:128\n\t"
    "v_mov_b32_dpp v30, v30 row_shr:1 row_mask:0xf bank_mask:0xf bound_ctrl:1\n\t"
    "v_mov_b32_dpp v31, v31 row_shr:1 row_mask:0xf bank_mask:0xf bound_ctrl:1\n\t"
    "v_mfma_f32_16x16x32_bf16 v[160:163], v[48:51], v[20:23], v[160:163]\n\t"
    "ds_read_b64_tr_b16 a[244:245], v12 offset:16384\n\t"
    "ds_read_b64_tr_b16 a[246:247], v12 offset:17408\n\t"
    "v_mfma_f32_16x16x32_bf16 v[168:171], v[48:51], v[24:27], v[168:171]\n\t"
    "ds_read_b64 v[46:47], v192 offset:136\n\t"
    "ds_read_b64_tr_b16 a[248:249], v11 offset:18432\n\t"
    "v_mfma_f32_16x16x32_bf16 v[144:147], v[48:51], v[28:31], v[144:147]\n\t"
    "ds_read_b64_tr_b16 a[250:251], v11 offset:19456\n\t"
    "ds_read_b64_tr_b16 a[252:253], v12 offset:18432\n\t"
    "v_mfma_f32_16x16x32_bf16 v[156:159], v[52:55], v[16:19], v[156:159]\n\t"
    "ds_read_b64 v[48:49], v192 offset:96\n\t"
    "ds_read_b64_tr_b16 a[254:255], v12 offset:19456\n\t"
    "s_lshl_b32 s28, s20, 2\n\t"
    "s_add_u32 s28, s28, s3\n\t"
    "s_lshl_b32 s28, s28, 10\n\t"
    "s_add_u32 s28, s28, 48234496\n\t"
    "v_lshrrev_b32 v9, 4, v1\n\t"
    "v_lshl_add_u32 v9, v9, 8, s28\n\t"
    "v_mfma_f32_16x16x32_bf16 v[164:167], v[52:55], v[20:23], v[164:167]\n\t"
    "ds_read_b64 v[50:51], v192 offset:104\n\t"
    "v_mfma_f32_16x16x32_bf16 v[172:175], v[52:55], v[24:27], v[172:175]\n\t"
    "v_mov_b32_dpp v24, v24 row_shr:1 row_mask:0xf bank_mask:0xf bound_ctrl:1\n\t"
    "v_mov_b32_dpp v25, v25 row_shr:1 row_mask:0xf bank_mask:0xf bound_ctrl:1\n\t"
    "v_mfma_f32_16x16x32_bf16 v[148:151], v[52:55], v[28:31], v[148:151]\n\t"
    "s_waitcnt lgkmcnt(12)\n\t"
    "v_mov_b32_dpp v26, v26 row_shr:1 row_mask:0xf bank_mask:0xf bound_ctrl:1\n\t"
    "v_mov_b32_dpp v27, v27 row_shr:1 row_mask:0xf bank_mask:0xf bound_ctrl:1\n\t"
    "v_mfma_f32_16x16x32_bf16 v[160:163], v[36:39], v[16:19], v[160:163]\n\t"
    "ds_read_b64 v[52:53], v192 offset:64\n\t"
    "v_mfma_f32_16x16x32_bf16 v[168:171], v[36:39], v[20:23], v[168:171]\n\t"
    "s_waitcnt vmcnt(4) lgkmcnt(2)\n\t"
    "s_barrier\n\t"
    "v_mfma_f32_16x16x32_bf16 v[144:147], v[36:39], v[24:27], v[144:147]\n\t"
    "ds_read_b64 v[54:55], v192 offset:72\n\t"
    "ds_read_b64_tr_b16 v[224:225], v11 offset:49152\n\t"
    "v_mfma_f32_16x16x32_bf16 v[152:155], v[36:39], v[28:31], v[152:155]\n\t"
    "ds_read_b64_tr_b16 v[226:227], v11 offset:50176\n\t"
    "ds_read_b64_tr_b16 v[228:229], v12 offset:49152\n\t"
    "v_mfma_f32_16x16x32_bf16 v[164:167], v[40:43], v[16:19], v[164:167]\n\t"
    "ds_read_b64 v[36:37], v192 offset:32\n\t"
    "ds_read_b64_tr_b16 v[230:231], v12 offset:50176\n\t"
    "v_mfma_f32_16x16x32_bf16 v[172:175], v[40:43], v[20:23], v[172:175]\n\t"
    "ds_read_b64 v[38:39], v192 offset:40\n\t"
    "v_mov_b32_dpp v20, v20 row_shr:1 row_mask:0xf bank_mask:0xf bound_ctrl:1\n\t"
    "v_mov_b32_dpp v21, v21 row_shr:1 row_mask:0xf bank_mask:0xf bound_ctrl:1\n\t"
    "v_mfma_f32_16x16x32_bf16 v[148:151], v[40:43], v[24:27], v[148:151]\n\t"
    "v_mov_b32_dpp v22, v22 row_shr:1 row_mask:0xf bank_mask:0xf bound_ctrl:1\n\t"
    "v_mov_b32_dpp v23, v23 row_shr:1 row_mask:0xf bank_mask:0xf bound_ctrl:1\n\t"
    "v_mfma_f32_16x16x32_bf16 v[156:159], v[40:43], v[28:31], v[156:159]\n\t"
    "s_waitcnt lgkmcnt(8)\n\t"
    "ds_read_b64_tr_b16 v[232:233], v11 offset:51200\n\t"
    "v_mfma_f32_16x16x32_bf16 v[168:171], v[44:47], v[16:19], v[168:171]\n\t"
    "ds_read_b64 v[40:41], v192 offset:0\n\t"
    "ds_read_b64_tr_b16 v[234:235], v11 offset:52224\n\t"
    "v_mfma_f32_16x16x32_bf16 v[144:147], v[44:47], v[20:23], v[144:147]\n\t"
    "ds_read_b64_tr_b16 v[236:237], v12 offset:51200\n\t"
    "ds_read_b64_tr_b16 v[238:239], v12 offset:52224\n\t"
    "v_mfma_f32_16x16x32_bf16 v[152:155], v[44:47], v[24:27], v[152:155]\n\t"
    "ds_read_b64 v[42:43], v192 offset:8\n\t"
    "v_mfma_f32_16x16x32_bf16 v[160:163], v[44:47], v[28:31], v[160:163]\n\t"
    "v_mfma_f32_16x16x32_bf16 v[172:175], v[48:51], v[16:19], v[172:175]\n\t"
    "v_mov_b32_dpp v16, v16 row_shr:1 row_mask:0xf bank_mask:0xf bound_ctrl:1\n\t"
    "v_mfma_f32_16x16x32_bf16 v[148:151], v[48:51], v[20:23], v[148:151]\n\t"
    "v_mov_b32_dpp v17, v17 row_shr:1 row_mask:0xf bank_mask:0xf bound_ctrl:1\n\t"
    "v_mov_b32_dpp v18, v18 row_shr:1 row_mask:0xf bank_mask:0xf bound_ctrl:1\n\t"
    "v_mfma_f32_16x16x32_bf16 v[156:159], v[48:51], v[24:27], v[156:159]\n\t"
    "v_mov_b32_dpp v19, v19 row_shr:1 row_mask:0xf bank_mask:0xf bound_ctrl:1\n\t"
    "v_mfma_f32_16x16x32_bf16 v[164:167], v[48:51], v[28:31], v[164:167]\n\t"
    "s_waitcnt lgkmcnt(6)\n\t"
    "v_mfma_f32_16x16x32_bf16 v[144:147], v[52:55], v[16:19], v[144:147]\n\t"
    "v_mfma_f32_16x16x32_bf16 v[152:155], v[52:55], v[20:23], v[152:155]\n\t"
    "s_waitcnt vmcnt(0) lgkmcnt(1)\n\t"
    "s_barrier\n\t"
    "v_mfma_f32_16x16x32_bf16 v[160:163], v[52:55], v[24:27], v[160:163]\n\t"
    "ds_read_b64_tr_b16 v[240:241], v11 offset:32768\n\t"
    "ds_read_b64_tr_b16 v[242:243], v11 offset:33792\n\t"
    "v_mfma_f32_16x16x32_bf16 v[168:171], v[52:55], v[28:31], v[168:171]\n\t"
    "ds_read_b64_tr_b16 v[244:245], v12 offset:32768\n\t"
    "ds_read_b64_tr_b16 v[246:247], v12 offset:33792\n\t"
    "v_mfma_f32_16x16x32_bf16 v[148:151], v[36:39], v[16:19], v[148:151]\n\t"
    "ds_read_b64_tr_b16 v[248:249], v11 offset:34816\n\t"
    "ds_read_b64_tr_b16 v[250:251], v11 offset:35840\n\t"
    "v_mfma_f32_16x16x32_bf16 v[156:159], v[36:39], v[20:23], v[156:159]\n\t"
    "ds_read_b64_tr_b16 v[252:253], v12 offset:34816\n\t"
    "ds_read_b64_tr_b16 v[254:255], v12 offset:35840\n\t"
    "v_mfma_f32_16x16x32_bf16 v[164:167], v[36:39], v[24:27], v[164:167]\n\t"
    "v_mfma_f32_16x16x32_bf16 v[172:175], v[36:39], v[28:31], v[172:175]\n\t"
    "s_waitcnt lgkmcnt(8)\n\t"
    "v_mov_b32_dpp v28, v28 row_shr:1 row_mask:0xf bank_mask:0xf bound_ctrl:1\n\t"
    "v_mov_b32_dpp v29, v29 row_shr:1 row_mask:0xf bank_mask:0xf bound_ctrl:1\n\t"
    "v_mfma_f32_16x16x32_bf16 v[152:155], v[40:43], v[16:19], v[152:155]\n\t"
    "v_mov_b32_dpp v30, v30 row_shr:1 row_mask:0xf bank_mask:0xf bound_ctrl:1\n\t"
    "v_mov_b32_dpp v31, v31 row_shr:1 row_mask:0xf bank_mask:0xf bound_ctrl:1\n\t"
    "v_mfma_f32_16x16x32_bf16 v[160:163], v[40:43], v[20:23], v[160:163]\n\t"
    "v_mfma_f32_16x16x32_bf16 v[168:171], v[40:43], v[24:27], v[168:171]\n\t"
    "v_mfma_f32_16x16x32_bf16 v[144:147], v[40:43], v[28:31], v[144:147]\n\t"
    "v_add_f32_dpp v148, v176, v148 quad_perm:[0,1,2,3] row_mask:0xf bank_mask:0xc\n\t"
    "v_add_f32_dpp v149, v177, v149 quad_perm:[0,1,2,3] row_mask:0xf bank_mask:0xc\n\t"
    "v_add_f32_dpp v150, v178, v150 quad_perm:[0,1,2,3] row_mask:0xf bank_mask:0xc\n\t"
    "v_add_f32_dpp v151, v179, v151 quad_perm:[0,1,2,3] row_mask:0xf bank_mask:0xc\n\t"
    "v_mul_f32 v24, s94, v148\n\t"
    "v_mul_f32 v25, s94, v149\n\t"
    "v_mul_f32 v26, s94, v150\n\t"
    "v_mul_f32 v27, s94, v151\n\t"
    "v_cvt_pk_bf16_f32 v28, v148, v150\n\t"
    "v_cvt_pk_bf16_f32 v29, v149, v151\n\t"
    "ds_write2_b32 v218, v28, v29 offset0:4 offset1:36\n\t"
    "v_cvt_pk_bf16_f32 v30, v24, v26\n\t"
    "v_cvt_pk_bf16_f32 v31, v25, v27\n\t"
    "ds_write2_b32 v220, v30, v31 offset0:4 offset1:36\n\t"
    "s_lshl_b32 s28, s92, 7\n\t"
    "s_add_u32 s28, s28, 42000384\n\t"
    "v_mov_b32 v63, s28\n\t"
    "s_cmp_eq_u32 s3, 0\n\t"
    "s_cselect_b64 s[80:81], 1, 0\n\t"
    "v_add_f32_dpp v156, v180, v156 quad_perm:[0,1,2,3] row_mask:0xf bank_mask:0xc\n\t"
    "v_add_f32_dpp v157, v181, v157 quad_perm:[0,1,2,3] row_mask:0xf bank_mask:0xc\n\t"
    "v_add_f32_dpp v158, v182, v158 quad_perm:[0,1,2,3] row_mask:0xf bank_mask:0xc\n\t"
    "v_add_f32_dpp v159, v183, v159 quad_perm:[0,1,2,3] row_mask:0xf bank_mask:0xc\n\t"
    "v_mul_f32 v24, s94, v156\n\t"
    "v_mul_f32 v25, s94, v157\n\t"
    "v_mul_f32 v26, s94, v158\n\t"
    "v_mul_f32 v27, s94, v159\n\t"
    "v_cvt_pk_bf16_f32 v28, v156, v158\n\t"
    "v_cvt_pk_bf16_f32 v29, v157, v159\n\t"
    "ds_write2_b32 v218, v28, v29 offset0:12 offset1:44\n\t"
    "v_cvt_pk_bf16_f32 v30, v24, v26\n\t"
    "v_cvt_pk_bf16_f32 v31, v25, v27\n\t"
    "ds_write2_b32 v220, v30, v31 offset0:12 offset1:44\n\t"
    "v_mov_b32 v116, -1\n\t"
    "v_mov_b32 v117, -1\n\t"
    "v_mov_b32 v118, -1\n\t"
    "v_mov_b32 v119, -1\n\t"
    "v_mov_b32 v70, s66\n\t"
    "v_mov_b32 v71, s67\n\t"
    "v_add_f32_dpp v164, v184, v164 quad_perm:[0,1,2,3] row_mask:0xf bank_mask:0xc\n\t"
    "v_add_f32_dpp v165, v185, v165 quad_perm:[0,1,2,3] row_mask:0xf bank_mask:0xc\n\t"
    "v_add_f32_dpp v166, v186, v166 quad_perm:[0,1,2,3] row_mask:0xf bank_mask:0xc\n\t"
    "v_add_f32_dpp v167, v187, v167 quad_perm:[0,1,2,3] row_mask:0xf bank_mask:0xc\n\t"
    "v_mul_f32 v24, s94, v164\n\t"
    "v_mul_f32 v25, s94, v165\n\t"
    "v_mul_f32 v26, s94, v166\n\t"
    "v_mul_f32 v27, s94, v167\n\t"
    "v_cvt_pk_bf16_f32 v28, v164, v166\n\t"
    "v_cvt_pk_bf16_f32 v29, v165, v167\n\t"
    "ds_write2_b32 v218, v28, v29 offset0:20 offset1:52\n\t"
    "v_cvt_pk_bf16_f32 v30, v24, v26\n\t"
    "v_cvt_pk_bf16_f32 v31, v25, v27\n\t"
    "ds_write2_b32 v220, v30, v31 offset0:20 offset1:52\n\t"
    "s_movk_i32 s39, 512\n\t"
    "v_add_f32_dpp v172, v188, v172 quad_perm:[0,1,2,3] row_mask:0xf bank_mask:0xc\n\t"
    "v_add_f32_dpp v173, v189, v173 quad_perm:[0,1,2,3] row_mask:0xf bank_mask:0xc\n\t"
    "v_add_f32_dpp v174, v190, v174 quad_perm:[0,1,2,3] row_mask:0xf bank_mask:0xc\n\t"
    "v_add_f32_dpp v175, v191, v175 quad_perm:[0,1,2,3] row_mask:0xf bank_mask:0xc\n\t"
    "v_mul_f32 v24, s94, v172\n\t"
    "v_mul_f32 v25, s94, v173\n\t"
    "v_mul_f32 v26, s94, v174\n\t"
    "v_mul_f32 v27, s94, v175\n\t"
    "v_cvt_pk_bf16_f32 v28, v172, v174\n\t"
    "v_cvt_pk_bf16_f32 v29, v173, v175\n\t"
    "ds_write2_b32 v218, v28, v29 offset0:28 offset1:60\n\t"
    "v_cvt_pk_bf16_f32 v30, v24, v26\n\t"
    "v_cvt_pk_bf16_f32 v31, v25, v27\n\t"
    "ds_write2_b32 v220, v30, v31 offset0:28 offset1:60\n\t"
    "v_add_f32_dpp v144, v176, v144 row_shr:8 row_mask:0xf bank_mask:0xc\n\t"
    "v_add_f32_dpp v145, v177, v145 row_shr:8 row_mask:0xf bank_mask:0xc\n\t"
    "v_add_f32_dpp v146, v178, v146 row_shr:8 row_mask:0xf bank_mask:0xc\n\t"
    "v_add_f32_dpp v147, v179, v147 row_shr:8 row_mask:0xf bank_mask:0xc\n\t"
    "v_mul_f32 v16, s94, v144\n\t"
    "v_mul_f32 v17, s94, v145\n\t"
    "v_mul_f32 v18, s94, v146\n\t"
    "v_mul_f32 v19, s94, v147\n\t"
    "v_cvt_pk_bf16_f32 v20, v144, v146\n\t"
    "v_cvt_pk_bf16_f32 v21, v145, v147\n\t"
    "ds_write2_b32 v218, v20, v21 offset0:0 offset1:32\n\t"
    "v_cvt_pk_bf16_f32 v22, v16, v18\n\t"
    "v_cvt_pk_bf16_f32 v23, v17, v19\n\t"
    "ds_write2_b32 v220, v22, v23 offset0:0 offset1:32\n\t"
    "v_add_f32_dpp v152, v180, v152 row_shr:8 row_mask:0xf bank_mask:0xc\n\t"
    "v_add_f32_dpp v153, v181, v153 row_shr:8 row_mask:0xf bank_mask:0xc\n\t"
    "v_add_f32_dpp v154, v182, v154 row_shr:8 row_mask:0xf bank_mask:0xc\n\t"
    "v_add_f32_dpp v155, v183, v155 row_shr:8 row_mask:0xf bank_mask:0xc\n\t"
    "v_mul_f32 v16, s94, v152\n\t"
    "v_mul_f32 v17, s94, v153\n\t"
    "v_mul_f32 v18, s94, v154\n\t"
    "v_mul_f32 v19, s94, v155\n\t"
    "v_cvt_pk_bf16_f32 v20, v152, v154\n\t"
    "v_cvt_pk_bf16_f32 v21, v153, v155\n\t"
    "ds_write2_b32 v218, v20, v21 offset0:8 offset1:40\n\t"
    "v_cvt_pk_bf16_f32 v22, v16, v18\n\t"
    "v_cvt_pk_bf16_f32 v23, v17, v19\n\t"
    "ds_write2_b32 v220, v22, v23 offset0:8 offset1:40\n\t"
    "v_add_f32_dpp v160, v184, v160 row_shr:8 row_mask:0xf bank_mask:0xc\n\t"
    "v_add_f32_dpp v161, v185, v161 row_shr:8 row_mask:0xf bank_mask:0xc\n\t"
    "v_add_f32_dpp v162, v186, v162 row_shr:8 row_mask:0xf bank_mask:0xc\n\t"
    "v_add_f32_dpp v163, v187, v163 row_shr:8 row_mask:0xf bank_mask:0xc\n\t"
    "v_mul_f32 v16, s94, v160\n\t"
    "v_mul_f32 v17, s94, v161\n\t"
    "v_mul_f32 v18, s94, v162\n\t"
    "v_mul_f32 v19, s94, v163\n\t"
    "v_cvt_pk_bf16_f32 v20, v160, v162\n\t"
    "v_cvt_pk_bf16_f32 v21, v161, v163\n\t"
    "ds_write2_b32 v218, v20, v21 offset0:16 offset1:48\n\t"
    "v_cvt_pk_bf16_f32 v22, v16, v18\n\t"
    "v_cvt_pk_bf16_f32 v23, v17, v19\n\t"
    "ds_write2_b32 v220, v22, v23 offset0:16 offset1:48\n\t"
    "v_add_f32_dpp v168, v188, v168 row_shr:8 row_mask:0xf bank_mask:0xc\n\t"
    "v_add_f32_dpp v169, v189, v169 row_shr:8 row_mask:0xf bank_mask:0xc\n\t"
    "v_add_f32_dpp v170, v190, v170 row_shr:8 row_mask:0xf bank_mask:0xc\n\t"
    "v_add_f32_dpp v171, v191, v171 row_shr:8 row_mask:0xf bank_mask:0xc\n\t"
    "v_mul_f32 v16, s94, v168\n\t"
    "v_mul_f32 v17, s94, v169\n\t"
    "v_mul_f32 v18, s94, v170\n\t"
    "v_mul_f32 v19, s94, v171\n\t"
    "v_cvt_pk_bf16_f32 v20, v168, v170\n\t"
    "v_cvt_pk_bf16_f32 v21, v169, v171\n\t"
    "ds_write2_b32 v218, v20, v21 offset0:24 offset1:56\n\t"
    "v_cvt_pk_bf16_f32 v22, v16, v18\n\t"
    "v_cvt_pk_bf16_f32 v23, v17, v19\n\t"
    "ds_write2_b32 v220, v22, v23 offset0:24 offset1:56\n\t"
    "s_lshl_b32 s28, s92, 2\n\t"
    "s_add_u32 s28, s28, s3\n\t"
    "s_mul_i32 s28, s28, 9216\n\t"
    "v_min_u32 v190, 23, v1\n\t"
    "v_lshl_add_u32 v190, v190, 7, s28\n\t"
    "s_waitcnt lgkmcnt(0)\n\t"
    "s_barrier\n\t"
    "ds_read_b64_tr_b16 v[32:33], v221 offset:18752\n\t"
    "ds_read_b64_tr_b16 v[34:35], v221 offset:18880\n\t"
    "ds_read_b64_tr_b16 v[36:37], v221 offset:1088\n\t"
    "ds_read_b64_tr_b16 v[38:39], v221 offset:1216\n\t"
    "ds_read_b64_tr_b16 v[40:41], v221 offset:19840\n\t"
    "ds_read_b64_tr_b16 v[42:43], v221 offset:19968\n\t"
    "ds_read_b64_tr_b16 v[44:45], v221 offset:2176\n\t"
    "ds_read_b64_tr_b16 v[46:47], v221 offset:2304\n\t"
    "ds_read_b64_tr_b16 v[120:121], v221 offset:20928\n\t"
    "ds_read_b64_tr_b16 v[122:123], v221 offset:21056\n\t"
    "ds_read_b64_tr_b16 v[124:125], v221 offset:3264\n\t"
    "ds_read_b64_tr_b16 v[126:127], v221 offset:3392\n\t"
    "global_load_dwordx2 v[130:131], v128, s[36:37] sc1\n\t"
    "s_waitcnt lgkmcnt(8)\n\t"
    "global_store_dwordx4 v223, v[32:35], s[36:37] sc1\n\t"
    "global_store_dwordx4 v222, v[36:39], s[36:37] sc1\n\t"
    "v_add_u32 v223, 589824, v223\n\t"
    "v_add_u32 v222, 589824, v222\n\t"
    "s_waitcnt lgkmcnt(4)\n\t"
    "global_store_dwordx4 v223, v[40:43], s[36:37] sc1\n\t"
    "global_store_dwordx4 v222, v[44:47], s[36:37] sc1\n\t"
    "v_add_u32 v223, 589824, v223\n\t"
    "v_add_u32 v222, 589824, v222\n\t"
    "s_waitcnt lgkmcnt(0)\n\t"
    "global_store_dwordx4 v223, v[120:123], s[36:37] sc1\n\t"
    "global_store_dwordx4 v222, v[124:127], s[36:37] sc1\n\t"
    "global_load_dwordx4 v[72:75], v9, s[36:37] offset:0 sc1\n\t"
    "global_load_dwordx4 v[76:79], v9, s[36:37] offset:16 sc1\n\t"
    "global_load_dwordx4 v[80:83], v9, s[36:37] offset:32 sc1\n\t"
    "global_load_dwordx4 v[84:87], v9, s[36:37] offset:48 sc1\n\t"
    "global_load_dwordx4 v[88:91], v9, s[36:37] offset:64 sc1\n\t"
    "global_load_dwordx4 v[92:95], v9, s[36:37] offset:80 sc1\n\t"
    "global_load_dwordx4 v[96:99], v9, s[36:37] offset:96 sc1\n\t"
    "global_load_dwordx4 v[100:103], v9, s[36:37] offset:112 sc1\n\t"
    "global_load_dwordx4 v[104:107], v9, s[36:37] offset:128 sc1\n\t"
    "global_load_dwordx4 v[108:111], v9, s[36:37] offset:144 sc1\n\t"
    "global_load_dwordx4 v[112:115], v9, s[36:37] offset:160 sc1\n\t"
    "s_waitcnt vmcnt(17)\n\t"
    "v_cmp_ne_u64 vcc, s[66:67], v[130:131]\n\t"
    "s_cbranch_vccnz L_pse_%=\n\t"
    "L_pbe_%=:\n\t"
    "s_mov_b32 m0, s25\n\t"
    "s_nop 0\n\t"
    "buffer_load_dwordx4 v15, s[12:15], s24 offen offset:0 sc1 lds\n\t"
    "buffer_load_dwordx4 v15, s[12:15], s24 offen offset:1024 sc1 lds\n\t"
    "buffer_load_dwordx4 v15, s[12:15], s24 offen offset:2048 sc1 lds\n\t"
    "buffer_load_dwordx4 v15, s[12:15], s24 offen offset:3072 sc1 lds\n\t"
    "buffer_load_dwordx4 v15, s[12:15], s24 offen offset:3584 sc1 lds\n\t"
    "s_add_u32 s24, s24, 129024\n\t"
    "s_add_u32 s25, s25, 18432\n\t"
    "s_cmp_ge_u32 s25, 163840\n\t"
    "s_cselect_b32 s28, 92160, 0\n\t"
    "s_sub_u32 s25, s25, s28\n\t"
    "s_waitcnt vmcnt(16)\n\t"
    "s_barrier\n\t"
    "s_mov_b64 exec, s[80:81]\n\t"
    "global_store_dwordx2 v63, v[70:71], s[36:37] sc1\n\t"
    "s_mov_b64 exec, -1\n\t"
    "global_load_dwordx4 v[52:55], v5, s[36:37] offset:0 sc1\n\t"
    "v_add_u32 v5, 1792, v5\n\t"
    "v_add_u32 v3, s26, v2\n\t"
    "s_mov_b32 m0, s25\n\t"
    "s_nop 0\n\t"
    "buffer_load_dwordx4 v15, s[12:15], s24 offen offset:0 sc1 lds\n\t"
    "buffer_load_dwordx4 v15, s[12:15], s24 offen offset:1024 sc1 lds\n\t"
    "buffer_load_dwordx4 v15, s[12:15], s24 offen offset:2048 sc1 lds\n\t"
    "buffer_load_dwordx4 v15, s[12:15], s24 offen offset:3072 sc1 lds\n\t"
    "buffer_load_dwordx4 v15, s[12:15], s24 offen offset:3584 sc1 lds\n\t"
    "s_add_u32 s24, s24, 129024\n\t"
    "s_add_u32 s25, s25, 18432\n\t"
    "s_cmp_ge_u32 s25, 163840\n\t"
    "s_cselect_b32 s28, 92160, 0\n\t"
    "s_sub_u32 s25, s25, s28\n\t"
    "s_mov_b32 m0, s25\n\t"
    "s_nop 0\n\t"
    "buffer_load_dwordx4 v15, s[12:15], s24 offen offset:0 sc1 lds\n\t"
    "buffer_load_dwordx4 v15, s[12:15], s24 offen offset:1024 sc1 lds\n\t"
    "buffer_load_dwordx4 v15, s[12:15], s24 offen offset:2048 sc1 lds\n\t"
    "buffer_load_dwordx4 v15, s[12:15], s24 offen offset:3072 sc1 lds\n\t"
    "buffer_load_dwordx4 v15, s[12:15], s24 offen offset:3584 sc1 lds\n\t"
    "s_add_u32 s24, s24, 129024\n\t"
    "s_add_u32 s25, s25, 18432\n\t"
    "s_cmp_ge_u32 s25, 163840\n\t"
    "s_cselect_b32 s28, 92160, 0\n\t"
    "s_sub_u32 s25, s25, s28\n\t"
    "s_waitcnt vmcnt(17)\n\t"
    "v_cmpx_eq_u32 vcc, s66, v75\n\t"
    "v_cmpx_eq_u32 vcc, s66, v79\n\t"
    "v_cmpx_eq_u32 vcc, s66, v83\n\t"
    "v_cmpx_eq_u32 vcc, s66, v87\n\t"
    "v_cmpx_eq_u32 vcc, s66, v91\n\t"
    "v_cmpx_eq_u32 vcc, s66, v95\n\t"
    "v_cmpx_eq_u32 vcc, s66, v99\n\t"
    "v_cmpx_eq_u32 vcc, s66, v103\n\t"
    "v_cmpx_eq_u32 vcc, s66, v107\n\t"
    "v_cmpx_eq_u32 vcc, s66, v111\n\t"
    "v_cmpx_eq_u32 vcc, s66, v115\n\t"
    "s_cmp_eq_u64 exec, -1\n\t"
    "s_mov_b64 exec, -1\n\t"
    "s_cbranch_scc0 L_gsv_%=\n\t"
    "L_gbv_%=:\n\t"
    "s_waitcnt vmcnt(12)\n\t"
    "s_barrier\n\t"
    "s_branch L_V1_%=\n\t"
    "L_aux_%=:\n\t"
    "s_mul_i32 s93, s21, 36\n\t"
    "s_add_u32 s93, s93, s20\n\t"
    "s_lshl_b32 s28, s93, 1\n\t"
    "s_add_u32 s28, s28, s3\n\t"
    "s_add_u32 s29, s93, 216\n\t"
    "s_cmp_lt_u32 s3, 2\n\t"
    "s_cselect_b32 s27, s28, s29\n\t"
    "s_add_u32 s29, s93, 68\n\t"
    "s_cmp_eq_u32 s3, 3\n\t"
    "s_cselect_b32 s30, s93, s29\n\t"
    "s_cselect_b32 s27, 512, s27\n\t"
    "s_add_u32 s28, s27, 108\n\t"
    "s_cmp_lt_u32 s28, 168\n\t"
    "s_cselect_b32 s28, 512, s28\n\t"
    "s_movk_i32 s29, 168\n\t"
    "s_movk_i32 s31, 512\n\t"
    "s_cmp_lt_u32 s27, 256\n\t"
    "s_cselect_b32 s28, s28, s30\n\t"
    "s_cselect_b32 s29, 324, s29\n\t"
    "s_cselect_b32 s30, s31, s30\n\t"
    "s_add_u32 s94, s30, 432\n\t"
    "s_cmp_lt_u32 s28, s29\n\t"
    "s_cselect_b32 s26, 1, 0\n\t"
    "s_cselect_b32 s28, s28, 0\n\t"
    "s_mul_i32 s34, s28, 5462\n\t"
    "s_lshr_b32 s34, s34, 16\n\t"
    "s_mul_i32 s29, s34, 12\n\t"
    "s_sub_u32 s33, s28, s29\n\t"
    "v_and_b32 v104, 15, v1\n\t"
    "v_lshrrev_b32 v105, 4, v1\n\t"
    "s_mul_i32 s28, s33, 48\n\t"
    "v_add_u32 v106, s28, v105\n\t"
    "v_min_u32 v107, 11, v104\n\t"
    "v_lshl_add_u32 v107, v107, 2, v106\n\t"
    "v_lshlrev_b32 v9, 1, v107\n\t"
    "v_mul_u32_u24 v106, 6912, v106\n\t"
    "v_lshl_add_u32 v106, v104, 4, v106\n\t"
    "s_lshl_b32 s28, s34, 8\n\t"
    "v_add_u32 v10, s28, v106\n\t"
    "v_and_b32 v104, 3, v1\n\t"
    "v_lshrrev_b32 v105, 5, v1\n\t"
    "v_lshl_add_u32 v106, v104, 1, v105\n\t"
    "v_mul_u32_u24 v107, 1152, v106\n\t"
    "v_bfe_u32 v108, v1, 2, 3\n\t"
    "v_lshl_add_u32 v107, v108, 4, v107\n\t"
    "s_mul_i32 s28, s27, 9216\n\t"
    "v_add_u32 v14, s28, v107\n\t"
    "s_waitcnt lgkmcnt(0)\n\t"
    "s_mov_b64 s[36:37], s[66:67]\n\t"
    "s_cmp_ge_u32 s27, 256\n\t"
    "s_cbranch_scc1 L_nl1_%=\n\t"
    "v_mov_b32 v4, 41943040\n\t"
    "global_load_dwordx2 v[2:3], v4, s[36:37] sc1\n\t"
    "global_load_dwordx4 v[64:67], v14, s[76:77] offset:0\n\t"
    "global_load_dwordx4 v[68:71], v14, s[76:77] offset:128\n\t"
    "global_load_dwordx4 v[72:75], v14, s[76:77] offset:256\n\t"
    "global_load_dwordx4 v[76:79], v14, s[76:77] offset:384\n\t"
    "global_load_dwordx4 v[80:83], v14, s[76:77] offset:512\n\t"
    "global_load_dwordx4 v[84:87], v14, s[76:77] offset:640\n\t"
    "global_load_dwordx4 v[88:91], v14, s[76:77] offset:768\n\t"
    "global_load_dwordx4 v[92:95], v14, s[76:77] offset:896\n\t"
    "global_load_dwordx4 v[96:99], v14, s[76:77] offset:1024\n\t"
    "v_and_b32 v104, 15, v1\n\t"
    "v_lshrrev_b32 v105, 4, v1\n\t"
    "v_lshl_add_u32 v106, v104, 2, v105\n\t"
    "s_mul_i32 s28, s33, 27648\n\t"
    "s_lshl_b32 s29, s34, 10\n\t"
    "s_add_u32 s28, s28, s29\n\t"
    "s_add_u32 s28, s28, 81920\n\t"
    "v_lshl_add_u32 v113, v106, 4, s28\n\t"
    "s_waitcnt vmcnt(9)\n\t"
    "s_barrier\n\t"
    "v_readfirstlane_b32 s66, v2\n\t"
    "v_readfirstlane_b32 s67, v3\n\t"
    "s_nop 0\n\t"
    "s_add_u32 s66, s66, 1\n\t"
    "s_add_u32 s67, s67, 1\n\t"
    "s_waitcnt vmcnt(8)\n\t"
    "v_permlane32_swap_b32 v64, v66\n\t"
    "v_permlane32_swap_b32 v65, v67\n\t"
    "s_waitcnt vmcnt(7)\n\t"
    "v_permlane32_swap_b32 v68, v70\n\t"
    "v_permlane32_swap_b32 v69, v71\n\t"
    "s_waitcnt vmcnt(6)\n\t"
    "v_permlane32_swap_b32 v72, v74\n\t"
    "v_permlane32_swap_b32 v73, v75\n\t"
    "s_waitcnt vmcnt(5)\n\t"
    "v_permlane32_swap_b32 v76, v78\n\t"
    "v_permlane32_swap_b32 v77, v79\n\t"
    "s_waitcnt vmcnt(4)\n\t"
    "v_permlane32_swap_b32 v80, v82\n\t"
    "v_permlane32_swap_b32 v81, v83\n\t"
    "s_waitcnt vmcnt(3)\n\t"
    "v_permlane32_swap_b32 v84, v86\n\t"
    "v_permlane32_swap_b32 v85, v87\n\t"
    "s_waitcnt vmcnt(2)\n\t"
    "v_permlane32_swap_b32 v88, v90\n\t"
    "v_permlane32_swap_b32 v89, v91\n\t"
    "s_waitcnt vmcnt(1)\n\t"
    "v_permlane32_swap_b32 v92, v94\n\t"
    "v_permlane32_swap_b32 v93, v95\n\t"
    "s_waitcnt vmcnt(0)\n\t"
    "v_permlane32_swap_b32 v96, v98\n\t"
    "v_permlane32_swap_b32 v97, v99\n\t"
    "global_load_ushort v112, v9, s[58:59]\n\t"
    "global_load_dwordx4 v[16:19], v10, s[60:61]\n\t"
    "v_add_u32 v10, 27648, v10\n\t"
    "global_load_dwordx4 v[20:23], v10, s[60:61]\n\t"
    "v_add_u32 v10, 27648, v10\n\t"
    "global_load_dwordx4 v[24:27], v10, s[60:61]\n\t"
    "v_add_u32 v10, 27648, v10\n\t"
    "global_load_dwordx4 v[28:31], v10, s[60:61]\n\t"
    "v_add_u32 v10, 27648, v10\n\t"
    "global_load_dwordx4 v[32:35], v10, s[60:61]\n\t"
    "v_add_u32 v10, 27648, v10\n\t"
    "global_load_dwordx4 v[36:39], v10, s[60:61]\n\t"
    "v_add_u32 v10, 27648, v10\n\t"
    "global_load_dwordx4 v[40:43], v10, s[60:61]\n\t"
    "v_add_u32 v10, 27648, v10\n\t"
    "global_load_dwordx4 v[44:47], v10, s[60:61]\n\t"
    "v_add_u32 v10, 27648, v10\n\t"
    "global_load_dwordx4 v[48:51], v10, s[60:61]\n\t"
    "v_add_u32 v10, 27648, v10\n\t"
    "global_load_dwordx4 v[52:55], v10, s[60:61]\n\t"
    "v_add_u32 v10, 27648, v10\n\t"
    "global_load_dwordx4 v[56:59], v10, s[60:61]\n\t"
    "v_add_u32 v10, 27648, v10\n\t"
    "global_load_dwordx4 v[60:63], v10, s[60:61]\n\t"
    "v_mov_b32 v100, 0\n\t"
    "v_mov_b32 v101, 0\n\t"
    "v_mov_b32 v102, 0\n\t"
    "v_mov_b32 v103, 0\n\t"
    "s_mov_b32 s29, 0x3f803f80\n\t"
    "v_dot2c_f32_bf16 v100, s29, v64\n\t"
    "v_dot2c_f32_bf16 v101, v64, v64\n\t"
    "v_dot2c_f32_bf16 v102, s29, v66\n\t"
    "v_dot2c_f32_bf16 v103, v66, v66\n\t"
    "v_dot2c_f32_bf16 v100, s29, v65\n\t"
    "v_dot2c_f32_bf16 v101, v65, v65\n\t"
    "v_dot2c_f32_bf16 v102, s29, v67\n\t"
    "v_dot2c_f32_bf16 v103, v67, v67\n\t"
    "v_dot2c_f32_bf16 v100, s29, v68\n\t"
    "v_dot2c_f32_bf16 v101, v68, v68\n\t"
    "v_dot2c_f32_bf16 v102, s29, v70\n\t"
    "v_dot2c_f32_bf16 v103, v70, v70\n\t"
    "v_dot2c_f32_bf16 v100, s29, v69\n\t"
    "v_dot2c_f32_bf16 v101, v69, v69\n\t"
    "v_dot2c_f32_bf16 v102, s29, v71\n\t"
    "v_dot2c_f32_bf16 v103, v71, v71\n\t"
    "v_dot2c_f32_bf16 v100, s29, v72\n\t"
    "v_dot2c_f32_bf16 v101, v72, v72\n\t"
    "v_dot2c_f32_bf16 v102, s29, v74\n\t"
    "v_dot2c_f32_bf16 v103, v74, v74\n\t"
    "v_dot2c_f32_bf16 v100, s29, v73\n\t"
    "v_dot2c_f32_bf16 v101, v73, v73\n\t"
    "v_dot2c_f32_bf16 v102, s29, v75\n\t"
    "v_dot2c_f32_bf16 v103, v75, v75\n\t"
    "v_dot2c_f32_bf16 v100, s29, v76\n\t"
    "v_dot2c_f32_bf16 v101, v76, v76\n\t"
    "v_dot2c_f32_bf16 v102, s29, v78\n\t"
    "v_dot2c_f32_bf16 v103, v78, v78\n\t"
    "v_dot2c_f32_bf16 v100, s29, v77\n\t"
    "v_dot2c_f32_bf16 v101, v77, v77\n\t"
    "v_dot2c_f32_bf16 v102, s29, v79\n\t"
    "v_dot2c_f32_bf16 v103, v79, v79\n\t"
    "v_dot2c_f32_bf16 v100, s29, v80\n\t"
    "v_dot2c_f32_bf16 v101, v80, v80\n\t"
    "v_dot2c_f32_bf16 v102, s29, v82\n\t"
    "v_dot2c_f32_bf16 v103, v82, v82\n\t"
    "v_dot2c_f32_bf16 v100, s29, v81\n\t"
    "v_dot2c_f32_bf16 v101, v81, v81\n\t"
    "v_dot2c_f32_bf16 v102, s29, v83\n\t"
    "v_dot2c_f32_bf16 v103, v83, v83\n\t"
    "v_dot2c_f32_bf16 v100, s29, v84\n\t"
    "v_dot2c_f32_bf16 v101, v84, v84\n\t"
    "v_dot2c_f32_bf16 v102, s29, v86\n\t"
    "v_dot2c_f32_bf16 v103, v86, v86\n\t"
    "v_dot2c_f32_bf16 v100, s29, v85\n\t"
    "v_dot2c_f32_bf16 v101, v85, v85\n\t"
    "v_dot2c_f32_bf16 v102, s29, v87\n\t"
    "v_dot2c_f32_bf16 v103, v87, v87\n\t"
    "v_dot2c_f32_bf16 v100, s29, v88\n\t"
    "v_dot2c_f32_bf16 v101, v88, v88\n\t"
    "v_dot2c_f32_bf16 v102, s29, v90\n\t"
    "v_dot2c_f32_bf16 v103, v90, v90\n\t"
    "v_dot2c_f32_bf16 v100, s29, v89\n\t"
    "v_dot2c_f32_bf16 v101, v89, v89\n\t"
    "v_dot2c_f32_bf16 v102, s29, v91\n\t"
    "v_dot2c_f32_bf16 v103, v91, v91\n\t"
    "v_dot2c_f32_bf16 v100, s29, v92\n\t"
    "v_dot2c_f32_bf16 v101, v92, v92\n\t"
    "v_dot2c_f32_bf16 v102, s29, v94\n\t"
    "v_dot2c_f32_bf16 v103, v94, v94\n\t"
    "v_dot2c_f32_bf16 v100, s29, v93\n\t"
    "v_dot2c_f32_bf16 v101, v93, v93\n\t"
    "v_dot2c_f32_bf16 v102, s29, v95\n\t"
    "v_dot2c_f32_bf16 v103, v95, v95\n\t"
    "v_dot2c_f32_bf16 v100, s29, v96\n\t"
    "v_dot2c_f32_bf16 v101, v96, v96\n\t"
    "v_dot2c_f32_bf16 v102, s29, v98\n\t"
    "v_dot2c_f32_bf16 v103, v98, v98\n\t"
    "v_dot2c_f32_bf16 v100, s29, v97\n\t"
    "v_dot2c_f32_bf16 v101, v97, v97\n\t"
    "v_dot2c_f32_bf16 v102, s29, v99\n\t"
    "v_dot2c_f32_bf16 v103, v99, v99\n\t"
    "s_nop 1\n\t"
    "v_permlane32_swap_b32 v100, v102\n\t"
    "v_permlane32_swap_b32 v101, v103\n\t"
    "v_add_f32 v100, v100, v102\n\t"
    "v_add_f32 v101, v101, v103\n\t"
    "s_nop 1\n\t"
    "v_permlane16_swap_b32 v100, v101\n\t"
    "v_add_f32 v100, v100, v101\n\t"
    "s_nop 1\n\t"
    "v_add_f32_dpp v100, v100, v100 row_ror:8 row_mask:0xf bank_mask:0xf\n\t"
    "s_nop 1\n\t"
    "v_add_f32_dpp v100, v100, v100 row_ror:4 row_mask:0xf bank_mask:0xf\n\t"
    "v_mov_b32 v101, v100\n\t"
    "s_nop 1\n\t"
    "v_permlane16_swap_b32 v100, v101\n\t"
    "v_mul_f32 v100, 0x3ae38e39, v100\n\t"
    "v_mul_f32 v104, v100, v100\n\t"
    "s_mov_b32 s28, 0x3ae38e39\n\t"
    "v_fma_f32 v101, v101, s28, -v104\n\t"
    "v_max_f32 v101, 0, v101\n\t"
    "v_add_f32 v101, 0x3727c5ac, v101\n\t"
    "v_rsq_f32 v106, v101\n\t"
    "s_nop 0\n\t"
    "v_mul_f32 v104, v106, v100\n\t"
    "s_lshl_b32 s28, s27, 6\n\t"
    "s_add_u32 s28, s28, 49152\n\t"
    "v_add_u32 v102, s28, v15\n\t"
    "v_mov_b32 v101, v106\n\t"
    "v_mov_b32 v105, v104\n\t"
    "v_mov_b32 v107, s66\n\t"
    "v_permlane32_swap_b32 v101, v106\n\t"
    "v_permlane32_swap_b32 v104, v105\n\t"
    "s_mov_b32 s28, 117835009\n\t"
    "v_perm_b32 v104, v104, v101, s28\n\t"
    "s_mov_b32 s28, 117835010\n\t"
    "v_perm_b32 v105, v105, v101, s28\n\t"
    "s_mov_b32 s28, 117835011\n\t"
    "v_perm_b32 v106, v106, v101, s28\n\t"
    "s_mov_b64 exec, 15\n\t"
    "global_store_dwordx4 v102, v[104:107], s[36:37] sc1\n\t"
    "s_mov_b64 exec, -1\n\t"
    "s_cmp_eq_u32 s26, 0\n\t"
    "s_cbranch_scc1 L_mdone_%=\n\t"
    "s_waitcnt vmcnt(13)\n\t"
    "v_lshlrev_b32 v112, 16, v112\n\t"
    "v_mul_f32 v108, 0xbfb8aa3b, v112\n\t"
    "v_exp_f32 v108, v108\n\t"
    "s_nop 0\n\t"
    "v_add_f32 v108, 1.0, v108\n\t"
    "v_rcp_f32 v108, v108\n\t"
    "s_nop 0\n\t"
    "v_mul_f32 v112, v108, v112\n\t"
    "v_cvt_pk_bf16_f32 v112, v112, v112\n\t"
    "v_and_b32 v112, 0xffff0000, v112\n\t"
    "v_mov_b32 v116, 0\n\t"
    "v_mov_b32 v117, 0\n\t"
    "v_mov_b32 v118, 0\n\t"
    "v_mov_b32 v119, 0\n\t"
    "v_mov_b32 v120, 0\n\t"
    "v_mov_b32 v121, 0\n\t"
    "v_mov_b32 v122, 0\n\t"
    "v_mov_b32 v123, 0\n\t"
    "s_waitcnt vmcnt(12)\n\t"
    "v_lshlrev_b32 v104, 16, v16\n\t"
    "v_and_b32 v105, 0xffff0000, v16\n\t"
    "v_lshlrev_b32 v106, 16, v17\n\t"
    "v_and_b32 v107, 0xffff0000, v17\n\t"
    "v_lshlrev_b32 v108, 16, v18\n\t"
    "v_and_b32 v109, 0xffff0000, v18\n\t"
    "v_lshlrev_b32 v110, 16, v19\n\t"
    "v_and_b32 v111, 0xffff0000, v19\n\t"
    "v_fmac_f32_dpp v116, v112, v104 row_newbcast:0 row_mask:0xf bank_mask:0xf\n\t"
    "v_fmac_f32_dpp v117, v112, v105 row_newbcast:0 row_mask:0xf bank_mask:0xf\n\t"
    "v_fmac_f32_dpp v118, v112, v106 row_newbcast:0 row_mask:0xf bank_mask:0xf\n\t"
    "v_fmac_f32_dpp v119, v112, v107 row_newbcast:0 row_mask:0xf bank_mask:0xf\n\t"
    "v_fmac_f32_dpp v120, v112, v108 row_newbcast:0 row_mask:0xf bank_mask:0xf\n\t"
    "v_fmac_f32_dpp v121, v112, v109 row_newbcast:0 row_mask:0xf bank_mask:0xf\n\t"
    "v_fmac_f32_dpp v122, v112, v110 row_newbcast:0 row_mask:0xf bank_mask:0xf\n\t"
    "v_fmac_f32_dpp v123, v112, v111 row_newbcast:0 row_mask:0xf bank_mask:0xf\n\t"
    "s_waitcnt vmcnt(11)\n\t"
    "v_lshlrev_b32 v104, 16, v20\n\t"
    "v_and_b32 v105, 0xffff0000, v20\n\t"
    "v_lshlrev_b32 v106, 16, v21\n\t"
    "v_and_b32 v107, 0xffff0000, v21\n\t"
    "v_lshlrev_b32 v108, 16, v22\n\t"
    "v_and_b32 v109, 0xffff0000, v22\n\t"
    "v_lshlrev_b32 v110, 16, v23\n\t"
    "v_and_b32 v111, 0xffff0000, v23\n\t"
    "v_fmac_f32_dpp v116, v112, v104 row_newbcast:1 row_mask:0xf bank_mask:0xf\n\t"
    "v_fmac_f32_dpp v117, v112, v105 row_newbcast:1 row_mask:0xf bank_mask:0xf\n\t"
    "v_fmac_f32_dpp v118, v112, v106 row_newbcast:1 row_mask:0xf bank_mask:0xf\n\t"
    "v_fmac_f32_dpp v119, v112, v107 row_newbcast:1 row_mask:0xf bank_mask:0xf\n\t"
    "v_fmac_f32_dpp v120, v112, v108 row_newbcast:1 row_mask:0xf bank_mask:0xf\n\t"
    "v_fmac_f32_dpp v121, v112, v109 row_newbcast:1 row_mask:0xf bank_mask:0xf\n\t"
    "v_fmac_f32_dpp v122, v112, v110 row_newbcast:1 row_mask:0xf bank_mask:0xf\n\t"
    "v_fmac_f32_dpp v123, v112, v111 row_newbcast:1 row_mask:0xf bank_mask:0xf\n\t"
    "s_waitcnt vmcnt(10)\n\t"
    "v_lshlrev_b32 v104, 16, v24\n\t"
    "v_and_b32 v105, 0xffff0000, v24\n\t"
    "v_lshlrev_b32 v106, 16, v25\n\t"
    "v_and_b32 v107, 0xffff0000, v25\n\t"
    "v_lshlrev_b32 v108, 16, v26\n\t"
    "v_and_b32 v109, 0xffff0000, v26\n\t"
    "v_lshlrev_b32 v110, 16, v27\n\t"
    "v_and_b32 v111, 0xffff0000, v27\n\t"
    "v_fmac_f32_dpp v116, v112, v104 row_newbcast:2 row_mask:0xf bank_mask:0xf\n\t"
    "v_fmac_f32_dpp v117, v112, v105 row_newbcast:2 row_mask:0xf bank_mask:0xf\n\t"
    "v_fmac_f32_dpp v118, v112, v106 row_newbcast:2 row_mask:0xf bank_mask:0xf\n\t"
    "v_fmac_f32_dpp v119, v112, v107 row_newbcast:2 row_mask:0xf bank_mask:0xf\n\t"
    "v_fmac_f32_dpp v120, v112, v108 row_newbcast:2 row_mask:0xf bank_mask:0xf\n\t"
    "v_fmac_f32_dpp v121, v112, v109 row_newbcast:2 row_mask:0xf bank_mask:0xf\n\t"
    "v_fmac_f32_dpp v122, v112, v110 row_newbcast:2 row_mask:0xf bank_mask:0xf\n\t"
    "v_fmac_f32_dpp v123, v112, v111 row_newbcast:2 row_mask:0xf bank_mask:0xf\n\t"
    "s_waitcnt vmcnt(9)\n\t"
    "v_lshlrev_b32 v104, 16, v28\n\t"
    "v_and_b32 v105, 0xffff0000, v28\n\t"
    "v_lshlrev_b32 v106, 16, v29\n\t"
    "v_and_b32 v107, 0xffff0000, v29\n\t"
    "v_lshlrev_b32 v108, 16, v30\n\t"
    "v_and_b32 v109, 0xffff0000, v30\n\t"
    "v_lshlrev_b32 v110, 16, v31\n\t"
    "v_and_b32 v111, 0xffff0000, v31\n\t"
    "v_fmac_f32_dpp v116, v112, v104 row_newbcast:3 row_mask:0xf bank_mask:0xf\n\t"
    "v_fmac_f32_dpp v117, v112, v105 row_newbcast:3 row_mask:0xf bank_mask:0xf\n\t"
    "v_fmac_f32_dpp v118, v112, v106 row_newbcast:3 row_mask:0xf bank_mask:0xf\n\t"
    "v_fmac_f32_dpp v119, v112, v107 row_newbcast:3 row_mask:0xf bank_mask:0xf\n\t"
    "v_fmac_f32_dpp v120, v112, v108 row_newbcast:3 row_mask:0xf bank_mask:0xf\n\t"
    "v_fmac_f32_dpp v121, v112, v109 row_newbcast:3 row_mask:0xf bank_mask:0xf\n\t"
    "v_fmac_f32_dpp v122, v112, v110 row_newbcast:3 row_mask:0xf bank_mask:0xf\n\t"
    "v_fmac_f32_dpp v123, v112, v111 row_newbcast:3 row_mask:0xf bank_mask:0xf\n\t"
    "s_waitcnt vmcnt(8)\n\t"
    "v_lshlrev_b32 v104, 16, v32\n\t"
    "v_and_b32 v105, 0xffff0000, v32\n\t"
    "v_lshlrev_b32 v106, 16, v33\n\t"
    "v_and_b32 v107, 0xffff0000, v33\n\t"
    "v_lshlrev_b32 v108, 16, v34\n\t"
    "v_and_b32 v109, 0xffff0000, v34\n\t"
    "v_lshlrev_b32 v110, 16, v35\n\t"
    "v_and_b32 v111, 0xffff0000, v35\n\t"
    "v_fmac_f32_dpp v116, v112, v104 row_newbcast:4 row_mask:0xf bank_mask:0xf\n\t"
    "v_fmac_f32_dpp v117, v112, v105 row_newbcast:4 row_mask:0xf bank_mask:0xf\n\t"
    "v_fmac_f32_dpp v118, v112, v106 row_newbcast:4 row_mask:0xf bank_mask:0xf\n\t"
    "v_fmac_f32_dpp v119, v112, v107 row_newbcast:4 row_mask:0xf bank_mask:0xf\n\t"
    "v_fmac_f32_dpp v120, v112, v108 row_newbcast:4 row_mask:0xf bank_mask:0xf\n\t"
    "v_fmac_f32_dpp v121, v112, v109 row_newbcast:4 row_mask:0xf bank_mask:0xf\n\t"
    "v_fmac_f32_dpp v122, v112, v110 row_newbcast:4 row_mask:0xf bank_mask:0xf\n\t"
    "v_fmac_f32_dpp v123, v112, v111 row_newbcast:4 row_mask:0xf bank_mask:0xf\n\t"
    "s_waitcnt vmcnt(7)\n\t"
    "v_lshlrev_b32 v104, 16, v36\n\t"
    "v_and_b32 v105, 0xffff0000, v36\n\t"
    "v_lshlrev_b32 v106, 16, v37\n\t"
    "v_and_b32 v107, 0xffff0000, v37\n\t"
    "v_lshlrev_b32 v108, 16, v38\n\t"
    "v_and_b32 v109, 0xffff0000, v38\n\t"
    "v_lshlrev_b32 v110, 16, v39\n\t"
    "v_and_b32 v111, 0xffff0000, v39\n\t"
    "v_fmac_f32_dpp v116, v112, v104 row_newbcast:5 row_mask:0xf bank_mask:0xf\n\t"
    "v_fmac_f32_dpp v117, v112, v105 row_newbcast:5 row_mask:0xf bank_mask:0xf\n\t"
    "v_fmac_f32_dpp v118, v112, v106 row_newbcast:5 row_mask:0xf bank_mask:0xf\n\t"
    "v_fmac_f32_dpp v119, v112, v107 row_newbcast:5 row_mask:0xf bank_mask:0xf\n\t"
    "v_fmac_f32_dpp v120, v112, v108 row_newbcast:5 row_mask:0xf bank_mask:0xf\n\t"
    "v_fmac_f32_dpp v121, v112, v109 row_newbcast:5 row_mask:0xf bank_mask:0xf\n\t"
    "v_fmac_f32_dpp v122, v112, v110 row_newbcast:5 row_mask:0xf bank_mask:0xf\n\t"
    "v_fmac_f32_dpp v123, v112, v111 row_newbcast:5 row_mask:0xf bank_mask:0xf\n\t"
    "s_waitcnt vmcnt(6)\n\t"
    "v_lshlrev_b32 v104, 16, v40\n\t"
    "v_and_b32 v105, 0xffff0000, v40\n\t"
    "v_lshlrev_b32 v106, 16, v41\n\t"
    "v_and_b32 v107, 0xffff0000, v41\n\t"
    "v_lshlrev_b32 v108, 16, v42\n\t"
    "v_and_b32 v109, 0xffff0000, v42\n\t"
    "v_lshlrev_b32 v110, 16, v43\n\t"
    "v_and_b32 v111, 0xffff0000, v43\n\t"
    "v_fmac_f32_dpp v116, v112, v104 row_newbcast:6 row_mask:0xf bank_mask:0xf\n\t"
    "v_fmac_f32_dpp v117, v112, v105 row_newbcast:6 row_mask:0xf bank_mask:0xf\n\t"
    "v_fmac_f32_dpp v118, v112, v106 row_newbcast:6 row_mask:0xf bank_mask:0xf\n\t"
    "v_fmac_f32_dpp v119, v112, v107 row_newbcast:6 row_mask:0xf bank_mask:0xf\n\t"
    "v_fmac_f32_dpp v120, v112, v108 row_newbcast:6 row_mask:0xf bank_mask:0xf\n\t"
    "v_fmac_f32_dpp v121, v112, v109 row_newbcast:6 row_mask:0xf bank_mask:0xf\n\t"
    "v_fmac_f32_dpp v122, v112, v110 row_newbcast:6 row_mask:0xf bank_mask:0xf\n\t"
    "v_fmac_f32_dpp v123, v112, v111 row_newbcast:6 row_mask:0xf bank_mask:0xf\n\t"
    "s_waitcnt vmcnt(5)\n\t"
    "v_lshlrev_b32 v104, 16, v44\n\t"
    "v_and_b32 v105, 0xffff0000, v44\n\t"
    "v_lshlrev_b32 v106, 16, v45\n\t"
    "v_and_b32 v107, 0xffff0000, v45\n\t"
    "v_lshlrev_b32 v108, 16, v46\n\t"
    "v_and_b32 v109, 0xffff0000, v46\n\t"
    "v_lshlrev_b32 v110, 16, v47\n\t"
    "v_and_b32 v111, 0xffff0000, v47\n\t"
    "v_fmac_f32_dpp v116, v112, v104 row_newbcast:7 row_mask:0xf bank_mask:0xf\n\t"
    "v_fmac_f32_dpp v117, v112, v105 row_newbcast:7 row_mask:0xf bank_mask:0xf\n\t"
    "v_fmac_f32_dpp v118, v112, v106 row_newbcast:7 row_mask:0xf bank_mask:0xf\n\t"
    "v_fmac_f32_dpp v119, v112, v107 row_newbcast:7 row_mask:0xf bank_mask:0xf\n\t"
    "v_fmac_f32_dpp v120, v112, v108 row_newbcast:7 row_mask:0xf bank_mask:0xf\n\t"
    "v_fmac_f32_dpp v121, v112, v109 row_newbcast:7 row_mask:0xf bank_mask:0xf\n\t"
    "v_fmac_f32_dpp v122, v112, v110 row_newbcast:7 row_mask:0xf bank_mask:0xf\n\t"
    "v_fmac_f32_dpp v123, v112, v111 row_newbcast:7 row_mask:0xf bank_mask:0xf\n\t"
    "s_waitcnt vmcnt(4)\n\t"
    "v_lshlrev_b32 v104, 16, v48\n\t"
    "v_and_b32 v105, 0xffff0000, v48\n\t"
    "v_lshlrev_b32 v106, 16, v49\n\t"
    "v_and_b32 v107, 0xffff0000, v49\n\t"
    "v_lshlrev_b32 v108, 16, v50\n\t"
    "v_and_b32 v109, 0xffff0000, v50\n\t"
    "v_lshlrev_b32 v110, 16, v51\n\t"
    "v_and_b32 v111, 0xffff0000, v51\n\t"
    "v_fmac_f32_dpp v116, v112, v104 row_newbcast:8 row_mask:0xf bank_mask:0xf\n\t"
    "v_fmac_f32_dpp v117, v112, v105 row_newbcast:8 row_mask:0xf bank_mask:0xf\n\t"
    "v_fmac_f32_dpp v118, v112, v106 row_newbcast:8 row_mask:0xf bank_mask:0xf\n\t"
    "v_fmac_f32_dpp v119, v112, v107 row_newbcast:8 row_mask:0xf bank_mask:0xf\n\t"
    "v_fmac_f32_dpp v120, v112, v108 row_newbcast:8 row_mask:0xf bank_mask:0xf\n\t"
    "v_fmac_f32_dpp v121, v112, v109 row_newbcast:8 row_mask:0xf bank_mask:0xf\n\t"
    "v_fmac_f32_dpp v122, v112, v110 row_newbcast:8 row_mask:0xf bank_mask:0xf\n\t"
    "v_fmac_f32_dpp v123, v112, v111 row_newbcast:8 row_mask:0xf bank_mask:0xf\n\t"
    "s_waitcnt vmcnt(3)\n\t"
    "v_lshlrev_b32 v104, 16, v52\n\t"
    "v_and_b32 v105, 0xffff0000, v52\n\t"
    "v_lshlrev_b32 v106, 16, v53\n\t"
    "v_and_b32 v107, 0xffff0000, v53\n\t"
    "v_lshlrev_b32 v108, 16, v54\n\t"
    "v_and_b32 v109, 0xffff0000, v54\n\t"
    "v_lshlrev_b32 v110, 16, v55\n\t"
    "v_and_b32 v111, 0xffff0000, v55\n\t"
    "v_fmac_f32_dpp v116, v112, v104 row_newbcast:9 row_mask:0xf bank_mask:0xf\n\t"
    "v_fmac_f32_dpp v117, v112, v105 row_newbcast:9 row_mask:0xf bank_mask:0xf\n\t"
    "v_fmac_f32_dpp v118, v112, v106 row_newbcast:9 row_mask:0xf bank_mask:0xf\n\t"
    "v_fmac_f32_dpp v119, v112, v107 row_newbcast:9 row_mask:0xf bank_mask:0xf\n\t"
    "v_fmac_f32_dpp v120, v112, v108 row_newbcast:9 row_mask:0xf bank_mask:0xf\n\t"
    "v_fmac_f32_dpp v121, v112, v109 row_newbcast:9 row_mask:0xf bank_mask:0xf\n\t"
    "v_fmac_f32_dpp v122, v112, v110 row_newbcast:9 row_mask:0xf bank_mask:0xf\n\t"
    "v_fmac_f32_dpp v123, v112, v111 row_newbcast:9 row_mask:0xf bank_mask:0xf\n\t"
    "s_waitcnt vmcnt(2)\n\t"
    "v_lshlrev_b32 v104, 16, v56\n\t"
    "v_and_b32 v105, 0xffff0000, v56\n\t"
    "v_lshlrev_b32 v106, 16, v57\n\t"
    "v_and_b32 v107, 0xffff0000, v57\n\t"
    "v_lshlrev_b32 v108, 16, v58\n\t"
    "v_and_b32 v109, 0xffff0000, v58\n\t"
    "v_lshlrev_b32 v110, 16, v59\n\t"
    "v_and_b32 v111, 0xffff0000, v59\n\t"
    "v_fmac_f32_dpp v116, v112, v104 row_newbcast:10 row_mask:0xf bank_mask:0xf\n\t"
    "v_fmac_f32_dpp v117, v112, v105 row_newbcast:10 row_mask:0xf bank_mask:0xf\n\t"
    "v_fmac_f32_dpp v118, v112, v106 row_newbcast:10 row_mask:0xf bank_mask:0xf\n\t"
    "v_fmac_f32_dpp v119, v112, v107 row_newbcast:10 row_mask:0xf bank_mask:0xf\n\t"
    "v_fmac_f32_dpp v120, v112, v108 row_newbcast:10 row_mask:0xf bank_mask:0xf\n\t"
    "v_fmac_f32_dpp v121, v112, v109 row_newbcast:10 row_mask:0xf bank_mask:0xf\n\t"
    "v_fmac_f32_dpp v122, v112, v110 row_newbcast:10 row_mask:0xf bank_mask:0xf\n\t"
    "v_fmac_f32_dpp v123, v112, v111 row_newbcast:10 row_mask:0xf bank_mask:0xf\n\t"
    "s_waitcnt vmcnt(1)\n\t"
    "v_lshlrev_b32 v104, 16, v60\n\t"
    "v_and_b32 v105, 0xffff0000, v60\n\t"
    "v_lshlrev_b32 v106, 16, v61\n\t"
    "v_and_b32 v107, 0xffff0000, v61\n\t"
    "v_lshlrev_b32 v108, 16, v62\n\t"
    "v_and_b32 v109, 0xffff0000, v62\n\t"
    "v_lshlrev_b32 v110, 16, v63\n\t"
    "v_and_b32 v111, 0xffff0000, v63\n\t"
    "v_fmac_f32_dpp v116, v112, v104 row_newbcast:11 row_mask:0xf bank_mask:0xf\n\t"
    "v_fmac_f32_dpp v117, v112, v105 row_newbcast:11 row_mask:0xf bank_mask:0xf\n\t"
    "v_fmac_f32_dpp v118, v112, v106 row_newbcast:11 row_mask:0xf bank_mask:0xf\n\t"
    "v_fmac_f32_dpp v119, v112, v107 row_newbcast:11 row_mask:0xf bank_mask:0xf\n\t"
    "v_fmac_f32_dpp v120, v112, v108 row_newbcast:11 row_mask:0xf bank_mask:0xf\n\t"
    "v_fmac_f32_dpp v121, v112, v109 row_newbcast:11 row_mask:0xf bank_mask:0xf\n\t"
    "v_fmac_f32_dpp v122, v112, v110 row_newbcast:11 row_mask:0xf bank_mask:0xf\n\t"
    "v_fmac_f32_dpp v123, v112, v111 row_newbcast:11 row_mask:0xf bank_mask:0xf\n\t"
    "v_permlane32_swap_b32 v116, v120\n\t"
    "v_permlane32_swap_b32 v117, v121\n\t"
    "v_permlane32_swap_b32 v118, v122\n\t"
    "v_permlane32_swap_b32 v119, v123\n\t"
    "v_add_f32 v116, v116, v120\n\t"
    "v_add_f32 v117, v117, v121\n\t"
    "v_add_f32 v118, v118, v122\n\t"
    "v_add_f32 v119, v119, v123\n\t"
    "s_nop 0\n\t"
    "v_permlane16_swap_b32 v116, v118\n\t"
    "v_permlane16_swap_b32 v117, v119\n\t"
    "v_add_f32 v104, v116, v118\n\t"
    "v_add_f32 v106, v117, v119\n\t"
    "v_mov_b32 v105, s66\n\t"
    "v_mov_b32 v107, s67\n\t"
    "global_store_dwordx4 v113, v[104:107], s[36:37] sc1\n\t"
    "s_branch L_mdone_%=\n\t"
    "L_nl1_%=:\n\t"
    "v_mov_b32 v4, 41943040\n\t"
    "global_load_dwordx2 v[2:3], v4, s[36:37] sc1\n\t"
    "global_load_ushort v112, v9, s[58:59]\n\t"
    "global_load_dwordx4 v[16:19], v10, s[60:61]\n\t"
    "v_add_u32 v10, 27648, v10\n\t"
    "global_load_dwordx4 v[20:23], v10, s[60:61]\n\t"
    "v_add_u32 v10, 27648, v10\n\t"
    "global_load_dwordx4 v[24:27], v10, s[60:61]\n\t"
    "v_add_u32 v10, 27648, v10\n\t"
    "global_load_dwordx4 v[28:31], v10, s[60:61]\n\t"
    "v_add_u32 v10, 27648, v10\n\t"
    "global_load_dwordx4 v[32:35], v10, s[60:61]\n\t"
    "v_add_u32 v10, 27648, v10\n\t"
    "global_load_dwordx4 v[36:39], v10, s[60:61]\n\t"
    "v_add_u32 v10, 27648, v10\n\t"
    "global_load_dwordx4 v[40:43], v10, s[60:61]\n\t"
    "v_add_u32 v10, 27648, v10\n\t"
    "global_load_dwordx4 v[44:47], v10, s[60:61]\n\t"
    "v_add_u32 v10, 27648, v10\n\t"
    "global_load_dwordx4 v[48:51], v10, s[60:61]\n\t"
    "v_add_u32 v10, 27648, v10\n\t"
    "global_load_dwordx4 v[52:55], v10, s[60:61]\n\t"
    "v_add_u32 v10, 27648, v10\n\t"
    "global_load_dwordx4 v[56:59], v10, s[60:61]\n\t"
    "v_add_u32 v10, 27648, v10\n\t"
    "global_load_dwordx4 v[60:63], v10, s[60:61]\n\t"
    "v_and_b32 v104, 15, v1\n\t"
    "v_lshrrev_b32 v105, 4, v1\n\t"
    "v_lshl_add_u32 v106, v104, 2, v105\n\t"
    "s_mul_i32 s28, s33, 27648\n\t"
    "s_lshl_b32 s29, s34, 10\n\t"
    "s_add_u32 s28, s28, s29\n\t"
    "s_add_u32 s28, s28, 81920\n\t"
    "v_lshl_add_u32 v113, v106, 4, s28\n\t"
    "s_waitcnt vmcnt(13)\n\t"
    "s_barrier\n\t"
    "v_readfirstlane_b32 s66, v2\n\t"
    "v_readfirstlane_b32 s67, v3\n\t"
    "s_nop 0\n\t"
    "s_add_u32 s66, s66, 1\n\t"
    "s_add_u32 s67, s67, 1\n\t"
    "s_waitcnt vmcnt(12)\n\t"
    "s_cmp_eq_u32 s26, 0\n\t"
    "s_cbranch_scc1 L_mdone_%=\n\t"
    "v_lshlrev_b32 v112, 16, v112\n\t"
    "v_mul_f32 v108, 0xbfb8aa3b, v112\n\t"
    "v_exp_f32 v108, v108\n\t"
    "s_nop 0\n\t"
    "v_add_f32 v108, 1.0, v108\n\t"
    "v_rcp_f32 v108, v108\n\t"
    "s_nop 0\n\t"
    "v_mul_f32 v112, v108, v112\n\t"
    "v_cvt_pk_bf16_f32 v112, v112, v112\n\t"
    "v_and_b32 v112, 0xffff0000, v112\n\t"
    "v_mov_b32 v116, 0\n\t"
    "v_mov_b32 v117, 0\n\t"
    "v_mov_b32 v118, 0\n\t"
    "v_mov_b32 v119, 0\n\t"
    "v_mov_b32 v120, 0\n\t"
    "v_mov_b32 v121, 0\n\t"
    "v_mov_b32 v122, 0\n\t"
    "v_mov_b32 v123, 0\n\t"
    "s_waitcnt vmcnt(11)\n\t"
    "v_lshlrev_b32 v104, 16, v16\n\t"
    "v_and_b32 v105, 0xffff0000, v16\n\t"
    "v_lshlrev_b32 v106, 16, v17\n\t"
    "v_and_b32 v107, 0xffff0000, v17\n\t"
    "v_lshlrev_b32 v108, 16, v18\n\t"
    "v_and_b32 v109, 0xffff0000, v18\n\t"
    "v_lshlrev_b32 v110, 16, v19\n\t"
    "v_and_b32 v111, 0xffff0000, v19\n\t"
    "v_fmac_f32_dpp v116, v112, v104 row_newbcast:0 row_mask:0xf bank_mask:0xf\n\t"
    "v_fmac_f32_dpp v117, v112, v105 row_newbcast:0 row_mask:0xf bank_mask:0xf\n\t"
    "v_fmac_f32_dpp v118, v112, v106 row_newbcast:0 row_mask:0xf bank_mask:0xf\n\t"
    "v_fmac_f32_dpp v119, v112, v107 row_newbcast:0 row_mask:0xf bank_mask:0xf\n\t"
    "v_fmac_f32_dpp v120, v112, v108 row_newbcast:0 row_mask:0xf bank_mask:0xf\n\t"
    "v_fmac_f32_dpp v121, v112, v109 row_newbcast:0 row_mask:0xf bank_mask:0xf\n\t"
    "v_fmac_f32_dpp v122, v112, v110 row_newbcast:0 row_mask:0xf bank_mask:0xf\n\t"
    "v_fmac_f32_dpp v123, v112, v111 row_newbcast:0 row_mask:0xf bank_mask:0xf\n\t"
    "s_waitcnt vmcnt(10)\n\t"
    "v_lshlrev_b32 v104, 16, v20\n\t"
    "v_and_b32 v105, 0xffff0000, v20\n\t"
    "v_lshlrev_b32 v106, 16, v21\n\t"
    "v_and_b32 v107, 0xffff0000, v21\n\t"
    "v_lshlrev_b32 v108, 16, v22\n\t"
    "v_and_b32 v109, 0xffff0000, v22\n\t"
    "v_lshlrev_b32 v110, 16, v23\n\t"
    "v_and_b32 v111, 0xffff0000, v23\n\t"
    "v_fmac_f32_dpp v116, v112, v104 row_newbcast:1 row_mask:0xf bank_mask:0xf\n\t"
    "v_fmac_f32_dpp v117, v112, v105 row_newbcast:1 row_mask:0xf bank_mask:0xf\n\t"
    "v_fmac_f32_dpp v118, v112, v106 row_newbcast:1 row_mask:0xf bank_mask:0xf\n\t"
    "v_fmac_f32_dpp v119, v112, v107 row_newbcast:1 row_mask:0xf bank_mask:0xf\n\t"
    "v_fmac_f32_dpp v120, v112, v108 row_newbcast:1 row_mask:0xf bank_mask:0xf\n\t"
    "v_fmac_f32_dpp v121, v112, v109 row_newbcast:1 row_mask:0xf bank_mask:0xf\n\t"
    "v_fmac_f32_dpp v122, v112, v110 row_newbcast:1 row_mask:0xf bank_mask:0xf\n\t"
    "v_fmac_f32_dpp v123, v112, v111 row_newbcast:1 row_mask:0xf bank_mask:0xf\n\t"
    "s_waitcnt vmcnt(9)\n\t"
    "v_lshlrev_b32 v104, 16, v24\n\t"
    "v_and_b32 v105, 0xffff0000, v24\n\t"
    "v_lshlrev_b32 v106, 16, v25\n\t"
    "v_and_b32 v107, 0xffff0000, v25\n\t"
    "v_lshlrev_b32 v108, 16, v26\n\t"
    "v_and_b32 v109, 0xffff0000, v26\n\t"
    "v_lshlrev_b32 v110, 16, v27\n\t"
    "v_and_b32 v111, 0xffff0000, v27\n\t"
    "v_fmac_f32_dpp v116, v112, v104 row_newbcast:2 row_mask:0xf bank_mask:0xf\n\t"
    "v_fmac_f32_dpp v117, v112, v105 row_newbcast:2 row_mask:0xf bank_mask:0xf\n\t"
    "v_fmac_f32_dpp v118, v112, v106 row_newbcast:2 row_mask:0xf bank_mask:0xf\n\t"
    "v_fmac_f32_dpp v119, v112, v107 row_newbcast:2 row_mask:0xf bank_mask:0xf\n\t"
    "v_fmac_f32_dpp v120, v112, v108 row_newbcast:2 row_mask:0xf bank_mask:0xf\n\t"
    "v_fmac_f32_dpp v121, v112, v109 row_newbcast:2 row_mask:0xf bank_mask:0xf\n\t"
    "v_fmac_f32_dpp v122, v112, v110 row_newbcast:2 row_mask:0xf bank_mask:0xf\n\t"
    "v_fmac_f32_dpp v123, v112, v111 row_newbcast:2 row_mask:0xf bank_mask:0xf\n\t"
    "s_waitcnt vmcnt(8)\n\t"
    "v_lshlrev_b32 v104, 16, v28\n\t"
    "v_and_b32 v105, 0xffff0000, v28\n\t"
    "v_lshlrev_b32 v106, 16, v29\n\t"
    "v_and_b32 v107, 0xffff0000, v29\n\t"
    "v_lshlrev_b32 v108, 16, v30\n\t"
    "v_and_b32 v109, 0xffff0000, v30\n\t"
    "v_lshlrev_b32 v110, 16, v31\n\t"
    "v_and_b32 v111, 0xffff0000, v31\n\t"
    "v_fmac_f32_dpp v116, v112, v104 row_newbcast:3 row_mask:0xf bank_mask:0xf\n\t"
    "v_fmac_f32_dpp v117, v112, v105 row_newbcast:3 row_mask:0xf bank_mask:0xf\n\t"
    "v_fmac_f32_dpp v118, v112, v106 row_newbcast:3 row_mask:0xf bank_mask:0xf\n\t"
    "v_fmac_f32_dpp v119, v112, v107 row_newbcast:3 row_mask:0xf bank_mask:0xf\n\t"
    "v_fmac_f32_dpp v120, v112, v108 row_newbcast:3 row_mask:0xf bank_mask:0xf\n\t"
    "v_fmac_f32_dpp v121, v112, v109 row_newbcast:3 row_mask:0xf bank_mask:0xf\n\t"
    "v_fmac_f32_dpp v122, v112, v110 row_newbcast:3 row_mask:0xf bank_mask:0xf\n\t"
    "v_fmac_f32_dpp v123, v112, v111 row_newbcast:3 row_mask:0xf bank_mask:0xf\n\t"
    "s_waitcnt vmcnt(7)\n\t"
    "v_lshlrev_b32 v104, 16, v32\n\t"
    "v_and_b32 v105, 0xffff0000, v32\n\t"
    "v_lshlrev_b32 v106, 16, v33\n\t"
    "v_and_b32 v107, 0xffff0000, v33\n\t"
    "v_lshlrev_b32 v108, 16, v34\n\t"
    "v_and_b32 v109, 0xffff0000, v34\n\t"
    "v_lshlrev_b32 v110, 16, v35\n\t"
    "v_and_b32 v111, 0xffff0000, v35\n\t"
    "v_fmac_f32_dpp v116, v112, v104 row_newbcast:4 row_mask:0xf bank_mask:0xf\n\t"
    "v_fmac_f32_dpp v117, v112, v105 row_newbcast:4 row_mask:0xf bank_mask:0xf\n\t"
    "v_fmac_f32_dpp v118, v112, v106 row_newbcast:4 row_mask:0xf bank_mask:0xf\n\t"
    "v_fmac_f32_dpp v119, v112, v107 row_newbcast:4 row_mask:0xf bank_mask:0xf\n\t"
    "v_fmac_f32_dpp v120, v112, v108 row_newbcast:4 row_mask:0xf bank_mask:0xf\n\t"
    "v_fmac_f32_dpp v121, v112, v109 row_newbcast:4 row_mask:0xf bank_mask:0xf\n\t"
    "v_fmac_f32_dpp v122, v112, v110 row_newbcast:4 row_mask:0xf bank_mask:0xf\n\t"
    "v_fmac_f32_dpp v123, v112, v111 row_newbcast:4 row_mask:0xf bank_mask:0xf\n\t"
    "s_waitcnt vmcnt(6)\n\t"
    "v_lshlrev_b32 v104, 16, v36\n\t"
    "v_and_b32 v105, 0xffff0000, v36\n\t"
    "v_lshlrev_b32 v106, 16, v37\n\t"
    "v_and_b32 v107, 0xffff0000, v37\n\t"
    "v_lshlrev_b32 v108, 16, v38\n\t"
    "v_and_b32 v109, 0xffff0000, v38\n\t"
    "v_lshlrev_b32 v110, 16, v39\n\t"
    "v_and_b32 v111, 0xffff0000, v39\n\t"
    "v_fmac_f32_dpp v116, v112, v104 row_newbcast:5 row_mask:0xf bank_mask:0xf\n\t"
    "v_fmac_f32_dpp v117, v112, v105 row_newbcast:5 row_mask:0xf bank_mask:0xf\n\t"
    "v_fmac_f32_dpp v118, v112, v106 row_newbcast:5 row_mask:0xf bank_mask:0xf\n\t"
    "v_fmac_f32_dpp v119, v112, v107 row_newbcast:5 row_mask:0xf bank_mask:0xf\n\t"
    "v_fmac_f32_dpp v120, v112, v108 row_newbcast:5 row_mask:0xf bank_mask:0xf\n\t"
    "v_fmac_f32_dpp v121, v112, v109 row_newbcast:5 row_mask:0xf bank_mask:0xf\n\t"
    "v_fmac_f32_dpp v122, v112, v110 row_newbcast:5 row_mask:0xf bank_mask:0xf\n\t"
    "v_fmac_f32_dpp v123, v112, v111 row_newbcast:5 row_mask:0xf bank_mask:0xf\n\t"
    "s_waitcnt vmcnt(5)\n\t"
    "v_lshlrev_b32 v104, 16, v40\n\t"
    "v_and_b32 v105, 0xffff0000, v40\n\t"
    "v_lshlrev_b32 v106, 16, v41\n\t"
    "v_and_b32 v107, 0xffff0000, v41\n\t"
    "v_lshlrev_b32 v108, 16, v42\n\t"
    "v_and_b32 v109, 0xffff0000, v42\n\t"
    "v_lshlrev_b32 v110, 16, v43\n\t"
    "v_and_b32 v111, 0xffff0000, v43\n\t"
    "v_fmac_f32_dpp v116, v112, v104 row_newbcast:6 row_mask:0xf bank_mask:0xf\n\t"
    "v_fmac_f32_dpp v117, v112, v105 row_newbcast:6 row_mask:0xf bank_mask:0xf\n\t"
    "v_fmac_f32_dpp v118, v112, v106 row_newbcast:6 row_mask:0xf bank_mask:0xf\n\t"
    "v_fmac_f32_dpp v119, v112, v107 row_newbcast:6 row_mask:0xf bank_mask:0xf\n\t"
    "v_fmac_f32_dpp v120, v112, v108 row_newbcast:6 row_mask:0xf bank_mask:0xf\n\t"
    "v_fmac_f32_dpp v121, v112, v109 row_newbcast:6 row_mask:0xf bank_mask:0xf\n\t"
    "v_fmac_f32_dpp v122, v112, v110 row_newbcast:6 row_mask:0xf bank_mask:0xf\n\t"
    "v_fmac_f32_dpp v123, v112, v111 row_newbcast:6 row_mask:0xf bank_mask:0xf\n\t"
    "s_waitcnt vmcnt(4)\n\t"
    "v_lshlrev_b32 v104, 16, v44\n\t"
    "v_and_b32 v105, 0xffff0000, v44\n\t"
    "v_lshlrev_b32 v106, 16, v45\n\t"
    "v_and_b32 v107, 0xffff0000, v45\n\t"
    "v_lshlrev_b32 v108, 16, v46\n\t"
    "v_and_b32 v109, 0xffff0000, v46\n\t"
    "v_lshlrev_b32 v110, 16, v47\n\t"
    "v_and_b32 v111, 0xffff0000, v47\n\t"
    "v_fmac_f32_dpp v116, v112, v104 row_newbcast:7 row_mask:0xf bank_mask:0xf\n\t"
    "v_fmac_f32_dpp v117, v112, v105 row_newbcast:7 row_mask:0xf bank_mask:0xf\n\t"
    "v_fmac_f32_dpp v118, v112, v106 row_newbcast:7 row_mask:0xf bank_mask:0xf\n\t"
    "v_fmac_f32_dpp v119, v112, v107 row_newbcast:7 row_mask:0xf bank_mask:0xf\n\t"
    "v_fmac_f32_dpp v120, v112, v108 row_newbcast:7 row_mask:0xf bank_mask:0xf\n\t"
    "v_fmac_f32_dpp v121, v112, v109 row_newbcast:7 row_mask:0xf bank_mask:0xf\n\t"
    "v_fmac_f32_dpp v122, v112, v110 row_newbcast:7 row_mask:0xf bank_mask:0xf\n\t"
    "v_fmac_f32_dpp v123, v112, v111 row_newbcast:7 row_mask:0xf bank_mask:0xf\n\t"
    "s_waitcnt vmcnt(3)\n\t"
    "v_lshlrev_b32 v104, 16, v48\n\t"
    "v_and_b32 v105, 0xffff0000, v48\n\t"
    "v_lshlrev_b32 v106, 16, v49\n\t"
    "v_and_b32 v107, 0xffff0000, v49\n\t"
    "v_lshlrev_b32 v108, 16, v50\n\t"
    "v_and_b32 v109, 0xffff0000, v50\n\t"
    "v_lshlrev_b32 v110, 16, v51\n\t"
    "v_and_b32 v111, 0xffff0000, v51\n\t"
    "v_fmac_f32_dpp v116, v112, v104 row_newbcast:8 row_mask:0xf bank_mask:0xf\n\t"
    "v_fmac_f32_dpp v117, v112, v105 row_newbcast:8 row_mask:0xf bank_mask:0xf\n\t"
    "v_fmac_f32_dpp v118, v112, v106 row_newbcast:8 row_mask:0xf bank_mask:0xf\n\t"
    "v_fmac_f32_dpp v119, v112, v107 row_newbcast:8 row_mask:0xf bank_mask:0xf\n\t"
    "v_fmac_f32_dpp v120, v112, v108 row_newbcast:8 row_mask:0xf bank_mask:0xf\n\t"
    "v_fmac_f32_dpp v121, v112, v109 row_newbcast:8 row_mask:0xf bank_mask:0xf\n\t"
    "v_fmac_f32_dpp v122, v112, v110 row_newbcast:8 row_mask:0xf bank_mask:0xf\n\t"
    "v_fmac_f32_dpp v123, v112, v111 row_newbcast:8 row_mask:0xf bank_mask:0xf\n\t"
    "s_waitcnt vmcnt(2)\n\t"
    "v_lshlrev_b32 v104, 16, v52\n\t"
    "v_and_b32 v105, 0xffff0000, v52\n\t"
    "v_lshlrev_b32 v106, 16, v53\n\t"
    "v_and_b32 v107, 0xffff0000, v53\n\t"
    "v_lshlrev_b32 v108, 16, v54\n\t"
    "v_and_b32 v109, 0xffff0000, v54\n\t"
    "v_lshlrev_b32 v110, 16, v55\n\t"
    "v_and_b32 v111, 0xffff0000, v55\n\t"
    "v_fmac_f32_dpp v116, v112, v104 row_newbcast:9 row_mask:0xf bank_mask:0xf\n\t"
    "v_fmac_f32_dpp v117, v112, v105 row_newbcast:9 row_mask:0xf bank_mask:0xf\n\t"
    "v_fmac_f32_dpp v118, v112, v106 row_newbcast:9 row_mask:0xf bank_mask:0xf\n\t"
    "v_fmac_f32_dpp v119, v112, v107 row_newbcast:9 row_mask:0xf bank_mask:0xf\n\t"
    "v_fmac_f32_dpp v120, v112, v108 row_newbcast:9 row_mask:0xf bank_mask:0xf\n\t"
    "v_fmac_f32_dpp v121, v112, v109 row_newbcast:9 row_mask:0xf bank_mask:0xf\n\t"
    "v_fmac_f32_dpp v122, v112, v110 row_newbcast:9 row_mask:0xf bank_mask:0xf\n\t"
    "v_fmac_f32_dpp v123, v112, v111 row_newbcast:9 row_mask:0xf bank_mask:0xf\n\t"
    "s_waitcnt vmcnt(1)\n\t"
    "v_lshlrev_b32 v104, 16, v56\n\t"
    "v_and_b32 v105, 0xffff0000, v56\n\t"
    "v_lshlrev_b32 v106, 16, v57\n\t"
    "v_and_b32 v107, 0xffff0000, v57\n\t"
    "v_lshlrev_b32 v108, 16, v58\n\t"
    "v_and_b32 v109, 0xffff0000, v58\n\t"
    "v_lshlrev_b32 v110, 16, v59\n\t"
    "v_and_b32 v111, 0xffff0000, v59\n\t"
    "v_fmac_f32_dpp v116, v112, v104 row_newbcast:10 row_mask:0xf bank_mask:0xf\n\t"
    "v_fmac_f32_dpp v117, v112, v105 row_newbcast:10 row_mask:0xf bank_mask:0xf\n\t"
    "v_fmac_f32_dpp v118, v112, v106 row_newbcast:10 row_mask:0xf bank_mask:0xf\n\t"
    "v_fmac_f32_dpp v119, v112, v107 row_newbcast:10 row_mask:0xf bank_mask:0xf\n\t"
    "v_fmac_f32_dpp v120, v112, v108 row_newbcast:10 row_mask:0xf bank_mask:0xf\n\t"
    "v_fmac_f32_dpp v121, v112, v109 row_newbcast:10 row_mask:0xf bank_mask:0xf\n\t"
    "v_fmac_f32_dpp v122, v112, v110 row_newbcast:10 row_mask:0xf bank_mask:0xf\n\t"
    "v_fmac_f32_dpp v123, v112, v111 row_newbcast:10 row_mask:0xf bank_mask:0xf\n\t"
    "s_waitcnt vmcnt(0)\n\t"
    "v_lshlrev_b32 v104, 16, v60\n\t"
    "v_and_b32 v105, 0xffff0000, v60\n\t"
    "v_lshlrev_b32 v106, 16, v61\n\t"
    "v_and_b32 v107, 0xffff0000, v61\n\t"
    "v_lshlrev_b32 v108, 16, v62\n\t"
    "v_and_b32 v109, 0xffff0000, v62\n\t"
    "v_lshlrev_b32 v110, 16, v63\n\t"
    "v_and_b32 v111, 0xffff0000, v63\n\t"
    "v_fmac_f32_dpp v116, v112, v104 row_newbcast:11 row_mask:0xf bank_mask:0xf\n\t"
    "v_fmac_f32_dpp v117, v112, v105 row_newbcast:11 row_mask:0xf bank_mask:0xf\n\t"
    "v_fmac_f32_dpp v118, v112, v106 row_newbcast:11 row_mask:0xf bank_mask:0xf\n\t"
    "v_fmac_f32_dpp v119, v112, v107 row_newbcast:11 row_mask:0xf bank_mask:0xf\n\t"
    "v_fmac_f32_dpp v120, v112, v108 row_newbcast:11 row_mask:0xf bank_mask:0xf\n\t"
    "v_fmac_f32_dpp v121, v112, v109 row_newbcast:11 row_mask:0xf bank_mask:0xf\n\t"
    "v_fmac_f32_dpp v122, v112, v110 row_newbcast:11 row_mask:0xf bank_mask:0xf\n\t"
    "v_fmac_f32_dpp v123, v112, v111 row_newbcast:11 row_mask:0xf bank_mask:0xf\n\t"
    "v_permlane32_swap_b32 v116, v120\n\t"
    "v_permlane32_swap_b32 v117, v121\n\t"
    "v_permlane32_swap_b32 v118, v122\n\t"
    "v_permlane32_swap_b32 v119, v123\n\t"
    "v_add_f32 v116, v116, v120\n\t"
    "v_add_f32 v117, v117, v121\n\t"
    "v_add_f32 v118, v118, v122\n\t"
    "v_add_f32 v119, v119, v123\n\t"
    "s_nop 0\n\t"
    "v_permlane16_swap_b32 v116, v118\n\t"
    "v_permlane16_swap_b32 v117, v119\n\t"
    "v_add_f32 v104, v116, v118\n\t"
    "v_add_f32 v106, v117, v119\n\t"
    "v_mov_b32 v105, s66\n\t"
    "v_mov_b32 v107, s67\n\t"
    "global_store_dwordx4 v113, v[104:107], s[36:37] sc1\n\t"
    "L_mdone_%=:\n\t"
    "s_lshl_b32 s28, s20, 8\n\t"
    "s_sub_u32 s29, s28, 2048\n\t"
    "s_subb_u32 s30, 0, 0\n\t"
    "s_add_u32 s8, s6, s29\n\t"
    "s_addc_u32 s9, s7, s30\n\t"
    "s_add_u32 s4, s4, s28\n\t"
    "s_addc_u32 s5, s5, 0\n\t"
    "s_sub_u32 s6, 5308416, s28\n\t"
    "s_add_u32 s10, s6, 2048\n\t"
    "s_mov_b32 s7, 0x20000\n\t"
    "s_mov_b32 s11, 0x20000\n\t"
    "s_lshl_b32 s0, s3, 12\n\t"
    "s_add_u32 s0, s0, 0\n\t"
    "v_lshrrev_b32 v105, 4, v1\n\t"
    "s_lshl_b32 s28, s3, 2\n\t"
    "v_add_u32 v106, s28, v105\n\t"
    "v_mul_u32_u24 v13, 9216, v106\n\t"
    "v_lshlrev_b32 v106, 5, v106\n\t"
    "v_xor_b32 v106, v106, v15\n\t"
    "v_and_b32 v106, 0xf0, v106\n\t"
    "v_add_u32 v13, v13, v106\n\t"
    "v_add_u32 v3, 146432, v13\n\t"
    "v_lshrrev_b32 v104, 2, v1\n\t"
    "v_and_b32 v106, 7, v104\n\t"
    "v_bfe_u32 v11, v1, 1, 1\n\t"
    "v_add_u32 v11, s28, v11\n\t"
    "v_lshlrev_b32 v106, 1, v106\n\t"
    "v_xor_b32 v106, v106, v11\n\t"
    "v_and_b32 v104, 3, v104\n\t"
    "v_lshlrev_b32 v104, 8, v104\n\t"
    "v_lshl_add_u32 v104, v105, 12, v104\n\t"
    "v_lshl_add_u32 v104, v106, 4, v104\n\t"
    "v_and_b32 v106, 1, v1\n\t"
    "v_lshl_add_u32 v104, v106, 3, v104\n\t"
    "v_add_u32 v12, 0, v104\n\t"
    "v_xor_b32 v104, 32, v104\n\t"
    "v_mov_b32 v11, v12\n\t"
    "v_add_u32 v12, 0, v104\n\t"
    "s_mul_i32 s33, s21, 294912\n\t"
    "s_add_u32 s33, s33, 0\n\t"
    "s_add_u32 m0, s0, 0\n\t"
    "s_add_u32 s1, s33, 0\n\t"
    "buffer_load_dwordx4 v13, s[4:7], s1 offen lds\n\t"
    "buffer_load_dwordx4 v3, s[4:7], s1 offen offset:1024 lds\n\t"
    "buffer_load_dwordx4 v13, s[8:11], s1 offen offset:2048 lds\n\t"
    "buffer_load_dwordx4 v3, s[8:11], s1 offen offset:3072 lds\n\t"
    "s_add_u32 m0, s0, 16384\n\t"
    "s_add_u32 s1, s33, 884736\n\t"
    "buffer_load_dwordx4 v13, s[4:7], s1 offen lds\n\t"
    "buffer_load_dwordx4 v3, s[4:7], s1 offen offset:1024 lds\n\t"
    "buffer_load_dwordx4 v13, s[8:11], s1 offen offset:2048 lds\n\t"
    "buffer_load_dwordx4 v3, s[8:11], s1 offen offset:3072 lds\n\t"
    "s_add_u32 m0, s0, 32768\n\t"
    "s_add_u32 s1, s33, 1769472\n\t"
    "buffer_load_dwordx4 v13, s[4:7], s1 offen lds\n\t"
    "buffer_load_dwordx4 v3, s[4:7], s1 offen offset:1024 lds\n\t"
    "buffer_load_dwordx4 v13, s[8:11], s1 offen offset:2048 lds\n\t"
    "buffer_load_dwordx4 v3, s[8:11], s1 offen offset:3072 lds\n\t"
    "s_add_u32 m0, s0, 49152\n\t"
    "s_add_u32 s1, s33, 2654208\n\t"
    "buffer_load_dwordx4 v13, s[4:7], s1 offen lds\n\t"
    "buffer_load_dwordx4 v3, s[4:7], s1 offen offset:1024 lds\n\t"
    "buffer_load_dwordx4 v13, s[8:11], s1 offen offset:2048 lds\n\t"
    "buffer_load_dwordx4 v3, s[8:11], s1 offen offset:3072 lds\n\t"
    "s_add_u32 s12, s36, 524288\n\t"
    "s_addc_u32 s13, s37, 0\n\t"
    "s_mov_b32 s14, 2377728\n\t"
    "s_mov_b32 s15, 0x20000\n\t"
    "s_lshl_b32 s28, s20, 2\n\t"
    "s_add_u32 s28, s28, s3\n\t"
    "s_lshl_b32 s28, s28, 10\n\t"
    "s_sub_u32 s18, 19021824, s28\n\t"
    "s_add_u32 s28, s28, 5373952\n\t"
    "s_add_u32 s16, s36, s28\n\t"
    "s_addc_u32 s17, s37, 0\n\t"
    "s_mov_b32 s19, 0x20000\n\t"
    "v_lshlrev_b32 v2, 3, v1\n\t"
    "v_and_b32 v5, 0xf0, v15\n\t"
    "s_lshl_b32 s28, s21, 8\n\t"
    "s_add_u32 s28, s28, 16384\n\t"
    "v_add_u32 v5, s28, v5\n\t"
    "s_mov_b32 s78, 0x21800000\n\t"
    "s_mov_b32 s79, 0x5d800000\n\t"
    "s_mov_b32 s23, 0\n\t"
    "s_mov_b32 s26, 126976\n\t"
    "s_mul_i32 s38, s21, 147456\n\t"
    "s_mov_b32 s27, 18874368\n\t"
    "s_cmp_lt_u32 s21, 2\n\t"
    "s_cselect_b32 s26, 108544, s26\n\t"
    "v_mov_b32 v116, -1\n\t"
    "v_mov_b32 v117, -1\n\t"
    "v_mov_b32 v118, -1\n\t"
    "v_mov_b32 v119, -1\n\t"
    "v_mov_b32 v70, s66\n\t"
    "v_mov_b32 v71, s67\n\t"
    "s_sub_u32 s28, 2, s21\n\t"
    "s_mul_i32 s28, s28, 36\n\t"
    "s_add_u32 s28, s28, s20\n\t"
    "s_lshl_b32 s28, s28, 1\n\t"
    "s_add_u32 s39, s28, 64\n\t"
    "v_and_b32 v126, 3, v1\n\t"
    "v_lshlrev_b32 v126, 7, v126\n\t"
    "v_add_u32 v126, 42020864, v126\n\t"
    "s_cmp_eq_u32 s21, 1\n\t"
    "s_cbranch_scc1 L_wr1_%=\n\t"
    "s_cmp_eq_u32 s21, 2\n\t"
    "s_cbranch_scc1 L_wr2_%=\n\t"
    "s_waitcnt vmcnt(12)\n\t"
    "s_barrier\n\t"
    "ds_read_b64_tr_b16 a[0:1], v11 offset:0\n\t"
    "ds_read_b64_tr_b16 a[2:3], v11 offset:1024\n\t"
    "ds_read_b64_tr_b16 a[4:5], v12 offset:0\n\t"
    "ds_read_b64_tr_b16 a[6:7], v12 offset:1024\n\t"
    "ds_read_b64_tr_b16 a[8:9], v11 offset:2048\n\t"
    "ds_read_b64_tr_b16 a[10:11], v11 offset:3072\n\t"
    "ds_read_b64_tr_b16 a[12:13], v12 offset:2048\n\t"
    "ds_read_b64_tr_b16 a[14:15], v12 offset:3072\n\t"
    "s_waitcnt vmcnt(8) lgkmcnt(0)\n\t"
    "s_barrier\n\t"
    "ds_read_b64_tr_b16 a[48:49], v11 offset:16384\n\t"
    "s_add_u32 m0, s0, 0\n\t"
    "s_mov_b32 s1, 3538944\n\t"
    "buffer_load_dwordx4 v13, s[4:7], s1 offen lds\n\t"
    "ds_read_b64_tr_b16 a[50:51], v11 offset:17408\n\t"
    "buffer_load_dwordx4 v3, s[4:7], s1 offen offset:1024 lds\n\t"
    "ds_read_b64_tr_b16 a[52:53], v12 offset:16384\n\t"
    "buffer_load_dwordx4 v13, s[8:11], s1 offen offset:2048 lds\n\t"
    "ds_read_b64_tr_b16 a[54:55], v12 offset:17408\n\t"
    "buffer_load_dwordx4 v3, s[8:11], s1 offen offset:3072 lds\n\t"
    "ds_read_b64_tr_b16 a[56:57], v11 offset:18432\n\t"
    "ds_read_b64_tr_b16 a[58:59], v11 offset:19456\n\t"
    "ds_read_b64_tr_b16 a[60:61], v12 offset:18432\n\t"
    "ds_read_b64_tr_b16 a[62:63], v12 offset:19456\n\t"
    "s_waitcnt vmcnt(8) lgkmcnt(0)\n\t"
    "s_barrier\n\t"
    "ds_read_b64_tr_b16 a[96:97], v11 offset:32768\n\t"
    "s_add_u32 m0, s0, 16384\n\t"
    "s_mov_b32 s1, 4423680\n\t"
    "buffer_load_dwordx4 v13, s[4:7], s1 offen lds\n\t"
    "ds_read_b64_tr_b16 a[98:99], v11 offset:33792\n\t"
    "buffer_load_dwordx4 v3, s[4:7], s1 offen offset:1024 lds\n\t"
    "ds_read_b64_tr_b16 a[100:101], v12 offset:32768\n\t"
    "buffer_load_dwordx4 v13, s[8:11], s1 offen offset:2048 lds\n\t"
    "ds_read_b64_tr_b16 a[102:103], v12 offset:33792\n\t"
    "buffer_load_dwordx4 v3, s[8:11], s1 offen offset:3072 lds\n\t"
    "ds_read_b64_tr_b16 a[104:105], v11 offset:34816\n\t"
    "ds_read_b64_tr_b16 a[106:107], v11 offset:35840\n\t"
    "ds_read_b64_tr_b16 a[108:109], v12 offset:34816\n\t"
    "ds_read_b64_tr_b16 a[110:111], v12 offset:35840\n\t"
    "s_waitcnt vmcnt(8) lgkmcnt(0)\n\t"
    "s_barrier\n\t"
    "ds_read_b64_tr_b16 a[144:145], v11 offset:49152\n\t"
    "s_add_u32 m0, s0, 32768\n\t"
    "s_mov_b32 s1, 294912\n\t"
    "buffer_load_dwordx4 v13, s[4:7], s1 offen lds\n\t"
    "ds_read_b64_tr_b16 a[146:147], v11 offset:50176\n\t"
    "buffer_load_dwordx4 v3, s[4:7], s1 offen offset:1024 lds\n\t"
    "ds_read_b64_tr_b16 a[148:149], v12 offset:49152\n\t"
    "buffer_load_dwordx4 v13, s[8:11], s1 offen offset:2048 lds\n\t"
    "ds_read_b64_tr_b16 a[150:151], v12 offset:50176\n\t"
    "buffer_load_dwordx4 v3, s[8:11], s1 offen offset:3072 lds\n\t"
    "ds_read_b64_tr_b16 a[152:153], v11 offset:51200\n\t"
    "ds_read_b64_tr_b16 a[154:155], v11 offset:52224\n\t"
    "ds_read_b64_tr_b16 a[156:157], v12 offset:51200\n\t"
    "ds_read_b64_tr_b16 a[158:159], v12 offset:52224\n\t"
    "s_waitcnt vmcnt(8) lgkmcnt(0)\n\t"
    "s_barrier\n\t"
    "ds_read_b64_tr_b16 a[192:193], v11 offset:0\n\t"
    "s_add_u32 m0, s0, 49152\n\t"
    "s_mov_b32 s1, 589824\n\t"
    "buffer_load_dwordx4 v13, s[4:7], s1 offen lds\n\t"
    "ds_read_b64_tr_b16 a[194:195], v11 offset:1024\n\t"
    "buffer_load_dwordx4 v3, s[4:7], s1 offen offset:1024 lds\n\t"
    "ds_read_b64_tr_b16 a[196:197], v12 offset:0\n\t"
    "buffer_load_dwordx4 v13, s[8:11], s1 offen offset:2048 lds\n\t"
    "ds_read_b64_tr_b16 a[198:199], v12 offset:1024\n\t"
    "buffer_load_dwordx4 v3, s[8:11], s1 offen offset:3072 lds\n\t"
    "ds_read_b64_tr_b16 a[200:201], v11 offset:2048\n\t"
    "ds_read_b64_tr_b16 a[202:203], v11 offset:3072\n\t"
    "ds_read_b64_tr_b16 a[204:205], v12 offset:2048\n\t"
    "ds_read_b64_tr_b16 a[206:207], v12 offset:3072\n\t"
    "s_waitcnt vmcnt(8) lgkmcnt(0)\n\t"
    "s_barrier\n\t"
    "ds_read_b64_tr_b16 a[240:241], v11 offset:16384\n\t"
    "s_add_u32 m0, s0, 0\n\t"
    "s_mov_b32 s1, 1179648\n\t"
    "buffer_load_dwordx4 v13, s[4:7], s1 offen lds\n\t"
    "ds_read_b64_tr_b16 a[242:243], v11 offset:17408\n\t"
    "buffer_load_dwordx4 v3, s[4:7], s1 offen offset:1024 lds\n\t"
    "ds_read_b64_tr_b16 a[244:245], v12 offset:16384\n\t"
    "buffer_load_dwordx4 v13, s[8:11], s1 offen offset:2048 lds\n\t"
    "ds_read_b64_tr_b16 a[246:247], v12 offset:17408\n\t"
    "buffer_load_dwordx4 v3, s[8:11], s1 offen offset:3072 lds\n\t"
    "ds_read_b64_tr_b16 a[248:249], v11 offset:18432\n\t"
    "ds_read_b64_tr_b16 a[250:251], v11 offset:19456\n\t"
    "ds_read_b64_tr_b16 a[252:253], v12 offset:18432\n\t"
    "ds_read_b64_tr_b16 a[254:255], v12 offset:19456\n\t"
    "s_waitcnt vmcnt(8) lgkmcnt(0)\n\t"
    "s_barrier\n\t"
    "ds_read_b64_tr_b16 a[16:17], v11 offset:32768\n\t"
    "s_add_u32 m0, s0, 16384\n\t"
    "s_mov_b32 s1, 1474560\n\t"
    "buffer_load_dwordx4 v13, s[4:7], s1 offen lds\n\t"
    "ds_read_b64_tr_b16 a[18:19], v11 offset:33792\n\t"
    "buffer_load_dwordx4 v3, s[4:7], s1 offen offset:1024 lds\n\t"
    "ds_read_b64_tr_b16 a[20:21], v12 offset:32768\n\t"
    "buffer_load_dwordx4 v13, s[8:11], s1 offen offset:2048 lds\n\t"
    "ds_read_b64_tr_b16 a[22:23], v12 offset:33792\n\t"
    "buffer_load_dwordx4 v3, s[8:11], s1 offen offset:3072 lds\n\t"
    "ds_read_b64_tr_b16 a[24:25], v11 offset:34816\n\t"
    "ds_read_b64_tr_b16 a[26:27], v11 offset:35840\n\t"
    "ds_read_b64_tr_b16 a[28:29], v12 offset:34816\n\t"
    "ds_read_b64_tr_b16 a[30:31], v12 offset:35840\n\t"
    "s_waitcnt vmcnt(8) lgkmcnt(0)\n\t"
    "s_barrier\n\t"
    "ds_read_b64_tr_b16 a[32:33], v11 offset:49152\n\t"
    "s_add_u32 m0, s0, 32768\n\t"
    "s_mov_b32 s1, 2064384\n\t"
    "buffer_load_dwordx4 v13, s[4:7], s1 offen lds\n\t"
    "ds_read_b64_tr_b16 a[34:35], v11 offset:50176\n\t"
    "buffer_load_dwordx4 v3, s[4:7], s1 offen offset:1024 lds\n\t"
    "ds_read_b64_tr_b16 a[36:37], v12 offset:49152\n\t"
    "buffer_load_dwordx4 v13, s[8:11], s1 offen offset:2048 lds\n\t"
    "ds_read_b64_tr_b16 a[38:39], v12 offset:50176\n\t"
    "buffer_load_dwordx4 v3, s[8:11], s1 offen offset:3072 lds\n\t"
    "ds_read_b64_tr_b16 a[40:41], v11 offset:51200\n\t"
    "ds_read_b64_tr_b16 a[42:43], v11 offset:52224\n\t"
    "ds_read_b64_tr_b16 a[44:45], v12 offset:51200\n\t"
    "ds_read_b64_tr_b16 a[46:47], v12 offset:52224\n\t"
    "s_waitcnt vmcnt(8) lgkmcnt(0)\n\t"
    "s_barrier\n\t"
    "ds_read_b64_tr_b16 a[64:65], v11 offset:0\n\t"
    "s_add_u32 m0, s0, 49152\n\t"
    "s_mov_b32 s1, 2359296\n\t"
    "buffer_load_dwordx4 v13, s[4:7], s1 offen lds\n\t"
    "ds_read_b64_tr_b16 a[66:67], v11 offset:1024\n\t"
    "buffer_load_dwordx4 v3, s[4:7], s1 offen offset:1024 lds\n\t"
    "ds_read_b64_tr_b16 a[68:69], v12 offset:0\n\t"
    "buffer_load_dwordx4 v13, s[8:11], s1 offen offset:2048 lds\n\t"
    "ds_read_b64_tr_b16 a[70:71], v12 offset:1024\n\t"
    "buffer_load_dwordx4 v3, s[8:11], s1 offen offset:3072 lds\n\t"
    "ds_read_b64_tr_b16 a[72:73], v11 offset:2048\n\t"
    "ds_read_b64_tr_b16 a[74:75], v11 offset:3072\n\t"
    "ds_read_b64_tr_b16 a[76:77], v12 offset:2048\n\t"
    "ds_read_b64_tr_b16 a[78:79], v12 offset:3072\n\t"
    "s_waitcnt vmcnt(8) lgkmcnt(0)\n\t"
    "s_barrier\n\t"
    "ds_read_b64_tr_b16 a[80:81], v11 offset:16384\n\t"
    "s_add_u32 m0, s0, 0\n\t"
    "s_mov_b32 s1, 2949120\n\t"
    "buffer_load_dwordx4 v13, s[4:7], s1 offen lds\n\t"
    "ds_read_b64_tr_b16 a[82:83], v11 offset:17408\n\t"
    "buffer_load_dwordx4 v3, s[4:7], s1 offen offset:1024 lds\n\t"
    "ds_read_b64_tr_b16 a[84:85], v12 offset:16384\n\t"
    "buffer_load_dwordx4 v13, s[8:11], s1 offen offset:2048 lds\n\t"
    "ds_read_b64_tr_b16 a[86:87], v12 offset:17408\n\t"
    "buffer_load_dwordx4 v3, s[8:11], s1 offen offset:3072 lds\n\t"
    "ds_read_b64_tr_b16 a[88:89], v11 offset:18432\n\t"
    "ds_read_b64_tr_b16 a[90:91], v11 offset:19456\n\t"
    "ds_read_b64_tr_b16 a[92:93], v12 offset:18432\n\t"
    "ds_read_b64_tr_b16 a[94:95], v12 offset:19456\n\t"
    "s_waitcnt vmcnt(8) lgkmcnt(0)\n\t"
    "s_barrier\n\t"
    "ds_read_b64_tr_b16 a[112:113], v11 offset:32768\n\t"
    "s_add_u32 m0, s0, 16384\n\t"
    "s_mov_b32 s1, 3244032\n\t"
    "buffer_load_dwordx4 v13, s[4:7], s1 offen lds\n\t"
    "ds_read_b64_tr_b16 a[114:115], v11 offset:33792\n\t"
    "buffer_load_dwordx4 v3, s[4:7], s1 offen offset:1024 lds\n\t"
    "ds_read_b64_tr_b16 a[116:117], v12 offset:32768\n\t"
    "buffer_load_dwordx4 v13, s[8:11], s1 offen offset:2048 lds\n\t"
    "ds_read_b64_tr_b16 a[118:119], v12 offset:33792\n\t"
    "buffer_load_dwordx4 v3, s[8:11], s1 offen offset:3072 lds\n\t"
    "ds_read_b64_tr_b16 a[120:121], v11 offset:34816\n\t"
    "ds_read_b64_tr_b16 a[122:123], v11 offset:35840\n\t"
    "ds_read_b64_tr_b16 a[124:125], v12 offset:34816\n\t"
    "ds_read_b64_tr_b16 a[126:127], v12 offset:35840\n\t"
    "s_waitcnt vmcnt(8) lgkmcnt(0)\n\t"
    "s_barrier\n\t"
    "ds_read_b64_tr_b16 a[128:129], v11 offset:49152\n\t"
    "s_add_u32 m0, s0, 32768\n\t"
    "s_mov_b32 s1, 3833856\n\t"
    "buffer_load_dwordx4 v13, s[4:7], s1 offen lds\n\t"
    "ds_read_b64_tr_b16 a[130:131], v11 offset:50176\n\t"
    "buffer_load_dwordx4 v3, s[4:7], s1 offen offset:1024 lds\n\t"
    "ds_read_b64_tr_b16 a[132:133], v12 offset:49152\n\t"
    "buffer_load_dwordx4 v13, s[8:11], s1 offen offset:2048 lds\n\t"
    "ds_read_b64_tr_b16 a[134:135], v12 offset:50176\n\t"
    "buffer_load_dwordx4 v3, s[8:11], s1 offen offset:3072 lds\n\t"
    "ds_read_b64_tr_b16 a[136:137], v11 offset:51200\n\t"
    "ds_read_b64_tr_b16 a[138:139], v11 offset:52224\n\t"
    "ds_read_b64_tr_b16 a[140:141], v12 offset:51200\n\t"
    "ds_read_b64_tr_b16 a[142:143], v12 offset:52224\n\t"
    "s_waitcnt vmcnt(8) lgkmcnt(0)\n\t"
    "s_barrier\n\t"
    "ds_read_b64_tr_b16 a[160:161], v11 offset:0\n\t"
    "s_add_u32 m0, s0, 49152\n\t"
    "s_mov_b32 s1, 4128768\n\t"
    "buffer_load_dwordx4 v13, s[4:7], s1 offen lds\n\t"
    "ds_read_b64_tr_b16 a[162:163], v11 offset:1024\n\t"
    "buffer_load_dwordx4 v3, s[4:7], s1 offen offset:1024 lds\n\t"
    "ds_read_b64_tr_b16 a[164:165], v12 offset:0\n\t"
    "buffer_load_dwordx4 v13, s[8:11], s1 offen offset:2048 lds\n\t"
    "ds_read_b64_tr_b16 a[166:167], v12 offset:1024\n\t"
    "buffer_load_dwordx4 v3, s[8:11], s1 offen offset:3072 lds\n\t"
    "ds_read_b64_tr_b16 a[168:169], v11 offset:2048\n\t"
    "ds_read_b64_tr_b16 a[170:171], v11 offset:3072\n\t"
    "ds_read_b64_tr_b16 a[172:173], v12 offset:2048\n\t"
    "ds_read_b64_tr_b16 a[174:175], v12 offset:3072\n\t"
    "s_waitcnt vmcnt(8) lgkmcnt(0)\n\t"
    "s_barrier\n\t"
    "ds_read_b64_tr_b16 a[176:177], v11 offset:16384\n\t"
    "s_add_u32 m0, s0, 0\n\t"
    "s_mov_b32 s1, 4718592\n\t"
    "buffer_load_dwordx4 v13, s[4:7], s1 offen lds\n\t"
    "ds_read_b64_tr_b16 a[178:179], v11 offset:17408\n\t"
    "buffer_load_dwordx4 v3, s[4:7], s1 offen offset:1024 lds\n\t"
    "ds_read_b64_tr_b16 a[180:181], v12 offset:16384\n\t"
    "buffer_load_dwordx4 v13, s[8:11], s1 offen offset:2048 lds\n\t"
    "ds_read_b64_tr_b16 a[182:183], v12 offset:17408\n\t"
    "buffer_load_dwordx4 v3, s[8:11], s1 offen offset:3072 lds\n\t"
    "ds_read_b64_tr_b16 a[184:185], v11 offset:18432\n\t"
    "ds_read_b64_tr_b16 a[186:187], v11 offset:19456\n\t"
    "ds_read_b64_tr_b16 a[188:189], v12 offset:18432\n\t"
    "ds_read_b64_tr_b16 a[190:191], v12 offset:19456\n\t"
    "s_waitcnt vmcnt(8) lgkmcnt(0)\n\t"
    "s_barrier\n\t"
    "ds_read_b64_tr_b16 a[208:209], v11 offset:32768\n\t"
    "s_add_u32 m0, s0, 16384\n\t"
    "s_mov_b32 s1, 5013504\n\t"
    "buffer_load_dwordx4 v13, s[4:7], s1 offen lds\n\t"
    "ds_read_b64_tr_b16 a[210:211], v11 offset:33792\n\t"
    "buffer_load_dwordx4 v3, s[4:7], s1 offen offset:1024 lds\n\t"
    "ds_read_b64_tr_b16 a[212:213], v12 offset:32768\n\t"
    "buffer_load_dwordx4 v13, s[8:11], s1 offen offset:2048 lds\n\t"
    "ds_read_b64_tr_b16 a[214:215], v12 offset:33792\n\t"
    "buffer_load_dwordx4 v3, s[8:11], s1 offen offset:3072 lds\n\t"
    "ds_read_b64_tr_b16 a[216:217], v11 offset:34816\n\t"
    "ds_read_b64_tr_b16 a[218:219], v11 offset:35840\n\t"
    "ds_read_b64_tr_b16 a[220:221], v12 offset:34816\n\t"
    "ds_read_b64_tr_b16 a[222:223], v12 offset:35840\n\t"
    "s_waitcnt vmcnt(8) lgkmcnt(0)\n\t"
    "s_barrier\n\t"
    "ds_read_b64_tr_b16 a[224:225], v11 offset:49152\n\t"
    "ds_read_b64_tr_b16 a[226:227], v11 offset:50176\n\t"
    "ds_read_b64_tr_b16 a[228:229], v12 offset:49152\n\t"
    "ds_read_b64_tr_b16 a[230:231], v12 offset:50176\n\t"
    "ds_read_b64_tr_b16 a[232:233], v11 offset:51200\n\t"
    "ds_read_b64_tr_b16 a[234:235], v11 offset:52224\n\t"
    "ds_read_b64_tr_b16 a[236:237], v12 offset:51200\n\t"
    "ds_read_b64_tr_b16 a[238:239], v12 offset:52224\n\t"
    "global_load_dwordx2 v[124:125], v126, s[36:37] sc1\n\t"
    "s_waitcnt vmcnt(5) lgkmcnt(0)\n\t"
    "s_barrier\n\t"
    "ds_read_b64_tr_b16 v[224:225], v11 offset:0\n\t"
    "ds_read_b64_tr_b16 v[226:227], v11 offset:1024\n\t"
    "ds_read_b64_tr_b16 v[228:229], v12 offset:0\n\t"
    "ds_read_b64_tr_b16 v[230:231], v12 offset:1024\n\t"
    "ds_read_b64_tr_b16 v[232:233], v11 offset:2048\n\t"
    "ds_read_b64_tr_b16 v[234:235], v11 offset:3072\n\t"
    "ds_read_b64_tr_b16 v[236:237], v12 offset:2048\n\t"
    "ds_read_b64_tr_b16 v[238:239], v12 offset:3072\n\t"
    "s_waitcnt vmcnt(1) lgkmcnt(0)\n\t"
    "s_barrier\n\t"
    "ds_read_b64_tr_b16 v[240:241], v11 offset:16384\n\t"
    "ds_read_b64_tr_b16 v[242:243], v11 offset:17408\n\t"
    "ds_read_b64_tr_b16 v[244:245], v12 offset:16384\n\t"
    "ds_read_b64_tr_b16 v[246:247], v12 offset:17408\n\t"
    "ds_read_b64_tr_b16 v[248:249], v11 offset:18432\n\t"
    "ds_read_b64_tr_b16 v[250:251], v11 offset:19456\n\t"
    "ds_read_b64_tr_b16 v[252:253], v12 offset:18432\n\t"
    "ds_read_b64_tr_b16 v[254:255], v12 offset:19456\n\t"
    "L_wrj_%=:\n\t"
    "v_mov_b32 v63, 41945088\n\t"
    "s_mov_b64 s[80:81], 0\n\t"
    "s_waitcnt vmcnt(0)\n\t"
    "v_cmp_ne_u64 vcc, s[66:67], v[124:125]\n\t"
    "s_cbranch_vccnz L_psm_%=\n\t"
    "L_pbm_%=:\n\t"
    "s_mul_i32 s29, s3, 4608\n\t"
    "s_add_u32 s24, s29, 2359296\n\t"
    "s_add_u32 s25, s29, s26\n\t"
    "s_mov_b32 m0, s25\n\t"
    "s_nop 0\n\t"
    "buffer_load_dwordx4 v15, s[12:15], s24 offen offset:0 sc1 lds\n\t"
    "buffer_load_dwordx4 v15, s[12:15], s24 offen offset:1024 sc1 lds\n\t"
    "buffer_load_dwordx4 v15, s[12:15], s24 offen offset:2048 sc1 lds\n\t"
    "buffer_load_dwordx4 v15, s[12:15], s24 offen offset:3072 sc1 lds\n\t"
    "buffer_load_dwordx4 v15, s[12:15], s24 offen offset:3584 sc1 lds\n\t"
    "s_mul_i32 s24, s21, 18432\n\t"
    "s_add_u32 s24, s24, s29\n\t"
    "s_add_u32 s25, s25, 18432\n\t"
    "v_add_u32 v3, s26, v2\n\t"
    "s_waitcnt vmcnt(0) lgkmcnt(0)\n\t"
    "s_barrier\n\t"
    "ds_read2st64_b64 v[128:131], v3 offset0:0 offset1:1\n\t"
    "ds_read2st64_b64 v[132:135], v3 offset0:2 offset1:3\n\t"
    "s_waitcnt lgkmcnt(0)\n\t"
    "v_mfma_f32_16x16x32_bf16 v[16:19], a[0:3], v[128:131], 0\n\t"
    "ds_read2st64_b64 v[136:139], v3 offset0:4 offset1:5\n\t"
    "v_mfma_f32_16x16x32_bf16 v[16:19], a[16:19], v[132:135], v[16:19]\n\t"
    "v_mfma_f32_16x16x32_bf16 v[20:23], a[4:7], v[128:131], 0\n\t"
    "ds_read2st64_b64 v[140:143], v3 offset0:6 offset1:7\n\t"
    "v_mfma_f32_16x16x32_bf16 v[20:23], a[20:23], v[132:135], v[20:23]\n\t"
    "v_mfma_f32_16x16x32_bf16 v[24:27], a[8:11], v[128:131], 0\n\t"
    "v_mfma_f32_16x16x32_bf16 v[24:27], a[24:27], v[132:135], v[24:27]\n\t"
    "v_mfma_f32_16x16x32_bf16 v[28:31], a[12:15], v[128:131], 0\n\t"
    "v_mfma_f32_16x16x32_bf16 v[28:31], a[28:31], v[132:135], v[28:31]\n\t"
    "s_waitcnt lgkmcnt(0)\n\t"
    "v_mfma_f32_16x16x32_bf16 v[16:19], a[32:35], v[136:139], v[16:19]\n\t"
    "ds_read2st64_b64 v[128:131], v3 offset0:8 offset1:9\n\t"
    "v_mfma_f32_16x16x32_bf16 v[16:19], a[48:51], v[140:143], v[16:19]\n\t"
    "v_mfma_f32_16x16x32_bf16 v[20:23], a[36:39], v[136:139], v[20:23]\n\t"
    "ds_read2st64_b64 v[132:135], v3 offset0:10 offset1:11\n\t"
    "v_mfma_f32_16x16x32_bf16 v[20:23], a[52:55], v[140:143], v[20:23]\n\t"
    "v_mfma_f32_16x16x32_bf16 v[24:27], a[40:43], v[136:139], v[24:27]\n\t"
    "v_mfma_f32_16x16x32_bf16 v[24:27], a[56:59], v[140:143], v[24:27]\n\t"
    "v_mfma_f32_16x16x32_bf16 v[28:31], a[44:47], v[136:139], v[28:31]\n\t"
    "v_mfma_f32_16x16x32_bf16 v[28:31], a[60:63], v[140:143], v[28:31]\n\t"
    "s_waitcnt lgkmcnt(0)\n\t"
    "v_mfma_f32_16x16x32_bf16 v[16:19], a[64:67], v[128:131], v[16:19]\n\t"
    "ds_read2st64_b64 v[136:139], v3 offset0:12 offset1:13\n\t"
    "v_mfma_f32_16x16x32_bf16 v[16:19], a[80:83], v[132:135], v[16:19]\n\t"
    "v_mfma_f32_16x16x32_bf16 v[20:23], a[68:71], v[128:131], v[20:23]\n\t"
    "ds_read2st64_b64 v[140:143], v3 offset0:14 offset1:15\n\t"
    "v_mfma_f32_16x16x32_bf16 v[20:23], a[84:87], v[132:135], v[20:23]\n\t"
    "v_mfma_f32_16x16x32_bf16 v[24:27], a[72:75], v[128:131], v[24:27]\n\t"
    "v_mfma_f32_16x16x32_bf16 v[24:27], a[88:91], v[132:135], v[24:27]\n\t"
    "v_mfma_f32_16x16x32_bf16 v[28:31], a[76:79], v[128:131], v[28:31]\n\t"
    "v_mfma_f32_16x16x32_bf16 v[28:31], a[92:95], v[132:135], v[28:31]\n\t"
    "s_waitcnt lgkmcnt(0)\n\t"
    "v_mfma_f32_16x16x32_bf16 v[16:19], a[96:99], v[136:139], v[16:19]\n\t"
    "ds_read2st64_b64 v[128:131], v3 offset0:16 offset1:17\n\t"
    "v_mfma_f32_16x16x32_bf16 v[16:19], a[112:115], v[140:143], v[16:19]\n\t"
    "v_mfma_f32_16x16x32_bf16 v[20:23], a[100:103], v[136:139], v[20:23]\n\t"
    "ds_read2st64_b64 v[132:135], v3 offset0:18 offset1:19\n\t"
    "v_mfma_f32_16x16x32_bf16 v[20:23], a[116:119], v[140:143], v[20:23]\n\t"
    "v_mfma_f32_16x16x32_bf16 v[24:27], a[104:107], v[136:139], v[24:27]\n\t"
    "v_mfma_f32_16x16x32_bf16 v[24:27], a[120:123], v[140:143], v[24:27]\n\t"
    "v_mfma_f32_16x16x32_bf16 v[28:31], a[108:111], v[136:139], v[28:31]\n\t"
    "v_mfma_f32_16x16x32_bf16 v[28:31], a[124:127], v[140:143], v[28:31]\n\t"
    "s_waitcnt lgkmcnt(0)\n\t"
    "v_mfma_f32_16x16x32_bf16 v[16:19], a[128:131], v[128:131], v[16:19]\n\t"
    "ds_read2st64_b64 v[136:139], v3 offset0:20 offset1:21\n\t"
    "v_mfma_f32_16x16x32_bf16 v[16:19], a[144:147], v[132:135], v[16:19]\n\t"
    "v_mfma_f32_16x16x32_bf16 v[20:23], a[132:135], v[128:131], v[20:23]\n\t"
    "ds_read2st64_b64 v[140:143], v3 offset0:22 offset1:23\n\t"
    "v_mfma_f32_16x16x32_bf16 v[20:23], a[148:151], v[132:135], v[20:23]\n\t"
    "v_mfma_f32_16x16x32_bf16 v[24:27], a[136:139], v[128:131], v[24:27]\n\t"
    "v_mfma_f32_16x16x32_bf16 v[24:27], a[152:155], v[132:135], v[24:27]\n\t"
    "v_mfma_f32_16x16x32_bf16 v[28:31], a[140:143], v[128:131], v[28:31]\n\t"
    "v_mfma_f32_16x16x32_bf16 v[28:31], a[156:159], v[132:135], v[28:31]\n\t"
    "s_waitcnt lgkmcnt(0)\n\t"
    "v_mfma_f32_16x16x32_bf16 v[16:19], a[160:163], v[136:139], v[16:19]\n\t"
    "ds_read2st64_b64 v[128:131], v3 offset0:24 offset1:25\n\t"
    "v_mfma_f32_16x16x32_bf16 v[16:19], a[176:179], v[140:143], v[16:19]\n\t"
    "v_mfma_f32_16x16x32_bf16 v[20:23], a[164:167], v[136:139], v[20:23]\n\t"
    "ds_read2st64_b64 v[132:135], v3 offset0:26 offset1:27\n\t"
    "v_mfma_f32_16x16x32_bf16 v[20:23], a[180:183], v[140:143], v[20:23]\n\t"
    "v_mfma_f32_16x16x32_bf16 v[24:27], a[168:171], v[136:139], v[24:27]\n\t"
    "v_mfma_f32_16x16x32_bf16 v[24:27], a[184:187], v[140:143], v[24:27]\n\t"
    "v_mfma_f32_16x16x32_bf16 v[28:31], a[172:175], v[136:139], v[28:31]\n\t"
    "v_mfma_f32_16x16x32_bf16 v[28:31], a[188:191], v[140:143], v[28:31]\n\t"
    "s_waitcnt lgkmcnt(0)\n\t"
    "v_mfma_f32_16x16x32_bf16 v[16:19], a[192:195], v[128:131], v[16:19]\n\t"
    "ds_read2st64_b64 v[136:139], v3 offset0:28 offset1:29\n\t"
    "v_mfma_f32_16x16x32_bf16 v[16:19], a[208:211], v[132:135], v[16:19]\n\t"
    "v_mfma_f32_16x16x32_bf16 v[20:23], a[196:199], v[128:131], v[20:23]\n\t"
    "ds_read2st64_b64 v[140:143], v3 offset0:30 offset1:31\n\t"
    "v_mfma_f32_16x16x32_bf16 v[20:23], a[212:215], v[132:135], v[20:23]\n\t"
    "v_mfma_f32_16x16x32_bf16 v[24:27], a[200:203], v[128:131], v[24:27]\n\t"
    "v_mfma_f32_16x16x32_bf16 v[24:27], a[216:219], v[132:135], v[24:27]\n\t"
    "v_mfma_f32_16x16x32_bf16 v[28:31], a[204:207], v[128:131], v[28:31]\n\t"
    "v_mfma_f32_16x16x32_bf16 v[28:31], a[220:223], v[132:135], v[28:31]\n\t"
    "s_waitcnt lgkmcnt(0)\n\t"
    "v_mfma_f32_16x16x32_bf16 v[16:19], a[224:227], v[136:139], v[16:19]\n\t"
    "ds_read2st64_b64 v[128:131], v3 offset0:32 offset1:33\n\t"
    "s_add_u32 s26, s26, 18432\n\t"
    "v_mfma_f32_16x16x32_bf16 v[16:19], a[240:243], v[140:143], v[16:19]\n\t"
    "s_cmp_ge_u32 s26, 163840\n\t"
    "v_mfma_f32_16x16x32_bf16 v[20:23], a[228:231], v[136:139], v[20:23]\n\t"
    "ds_read2st64_b64 v[132:135], v3 offset0:34 offset1:35\n\t"
    "s_cselect_b32 s26, 71680, s26\n\t"
    "v_mfma_f32_16x16x32_bf16 v[20:23], a[244:247], v[140:143], v[20:23]\n\t"
    "v_mfma_f32_16x16x32_bf16 v[24:27], a[232:235], v[136:139], v[24:27]\n\t"
    "v_mfma_f32_16x16x32_bf16 v[24:27], a[248:251], v[140:143], v[24:27]\n\t"
    "v_add_u32 v3, s26, v2\n\t"
    "v_mfma_f32_16x16x32_bf16 v[28:31], a[236:239], v[136:139], v[28:31]\n\t"
    "v_mfma_f32_16x16x32_bf16 v[28:31], a[252:255], v[140:143], v[28:31]\n\t"
    "s_waitcnt lgkmcnt(0)\n\t"
    "v_mfma_f32_16x16x32_bf16 v[16:19], v[224:227], v[128:131], v[16:19]\n\t"
    "v_mfma_f32_16x16x32_bf16 v[16:19], v[240:243], v[132:135], v[16:19]\n\t"
    "v_mfma_f32_16x16x32_bf16 v[20:23], v[228:231], v[128:131], v[20:23]\n\t"
    "v_mfma_f32_16x16x32_bf16 v[20:23], v[244:247], v[132:135], v[20:23]\n\t"
    "v_mfma_f32_16x16x32_bf16 v[24:27], v[232:235], v[128:131], v[24:27]\n\t"
    "v_mfma_f32_16x16x32_bf16 v[24:27], v[248:251], v[132:135], v[24:27]\n\t"
    "v_mfma_f32_16x16x32_bf16 v[28:31], v[236:239], v[128:131], v[28:31]\n\t"
    "v_mfma_f32_16x16x32_bf16 v[28:31], v[252:255], v[132:135], v[28:31]\n\t"
    "v_min_u32 v126, 35, v1\n\t"
    "s_mul_i32 s28, s3, 4608\n\t"
    "s_add_u32 s28, s28, 41979904\n\t"
    "v_lshl_add_u32 v126, v126, 7, s28\n\t"
    "v_mov_b32 v64, s78\n\t"
    "v_mov_b32 v65, s79\n\t"
    "s_nop 1\n\t"
    "v_mul_f32_dpp v72, v16, v64 row_newbcast:0 row_mask:0xf bank_mask:0xf\n\t"
    "v_mul_f32_dpp v82, v16, v64 row_newbcast:1 row_mask:0xf bank_mask:0xf\n\t"
    "v_mul_f32_dpp v93, v24, v65 row_newbcast:0 row_mask:0xf bank_mask:0xf\n\t"
    "v_mul_f32_dpp v104, v24, v65 row_newbcast:1 row_mask:0xf bank_mask:0xf\n\t"
    "v_mul_f32_dpp v73, v17, v64 row_newbcast:0 row_mask:0xf bank_mask:0xf\n\t"
    "v_mul_f32_dpp v84, v17, v64 row_newbcast:1 row_mask:0xf bank_mask:0xf\n\t"
    "v_mul_f32_dpp v94, v25, v65 row_newbcast:0 row_mask:0xf bank_mask:0xf\n\t"
    "v_mul_f32_dpp v105, v25, v65 row_newbcast:1 row_mask:0xf bank_mask:0xf\n\t"
    "v_mul_f32_dpp v74, v18, v64 row_newbcast:0 row_mask:0xf bank_mask:0xf\n\t"
    "v_mul_f32_dpp v85, v18, v64 row_newbcast:1 row_mask:0xf bank_mask:0xf\n\t"
    "v_mul_f32_dpp v96, v26, v65 row_newbcast:0 row_mask:0xf bank_mask:0xf\n\t"
    "v_mul_f32_dpp v106, v26, v65 row_newbcast:1 row_mask:0xf bank_mask:0xf\n\t"
    "v_mul_f32_dpp v76, v19, v64 row_newbcast:0 row_mask:0xf bank_mask:0xf\n\t"
    "v_mul_f32_dpp v86, v19, v64 row_newbcast:1 row_mask:0xf bank_mask:0xf\n\t"
    "v_mul_f32_dpp v97, v27, v65 row_newbcast:0 row_mask:0xf bank_mask:0xf\n\t"
    "v_mul_f32_dpp v108, v27, v65 row_newbcast:1 row_mask:0xf bank_mask:0xf\n\t"
    "v_mul_f32_dpp v77, v20, v64 row_newbcast:0 row_mask:0xf bank_mask:0xf\n\t"
    "v_mul_f32_dpp v88, v20, v64 row_newbcast:1 row_mask:0xf bank_mask:0xf\n\t"
    "v_mul_f32_dpp v98, v28, v65 row_newbcast:0 row_mask:0xf bank_mask:0xf\n\t"
    "v_mul_f32_dpp v109, v28, v65 row_newbcast:1 row_mask:0xf bank_mask:0xf\n\t"
    "v_mul_f32_dpp v78, v21, v64 row_newbcast:0 row_mask:0xf bank_mask:0xf\n\t"
    "v_mul_f32_dpp v89, v21, v64 row_newbcast:1 row_mask:0xf bank_mask:0xf\n\t"
    "v_mul_f32_dpp v100, v29, v65 row_newbcast:0 row_mask:0xf bank_mask:0xf\n\t"
    "v_mul_f32_dpp v110, v29, v65 row_newbcast:1 row_mask:0xf bank_mask:0xf\n\t"
    "v_mul_f32_dpp v80, v22, v64 row_newbcast:0 row_mask:0xf bank_mask:0xf\n\t"
    "v_mul_f32_dpp v90, v22, v64 row_newbcast:1 row_mask:0xf bank_mask:0xf\n\t"
    "v_mul_f32_dpp v101, v30, v65 row_newbcast:0 row_mask:0xf bank_mask:0xf\n\t"
    "v_mul_f32_dpp v112, v30, v65 row_newbcast:1 row_mask:0xf bank_mask:0xf\n\t"
    "v_mul_f32_dpp v81, v23, v64 row_newbcast:0 row_mask:0xf bank_mask:0xf\n\t"
    "v_mul_f32_dpp v92, v23, v64 row_newbcast:1 row_mask:0xf bank_mask:0xf\n\t"
    "v_mul_f32_dpp v102, v31, v65 row_newbcast:0 row_mask:0xf bank_mask:0xf\n\t"
    "v_mul_f32_dpp v113, v31, v65 row_newbcast:1 row_mask:0xf bank_mask:0xf\n\t"
    "s_cmp_lg_u32 s21, 0\n\t"
    "s_cbranch_scc1 L_ngv_%=\n\t"
    "s_lshl_b32 s28, s20, 2\n\t"
    "s_add_u32 s28, s28, s3\n\t"
    "s_lshl_b32 s28, s28, 10\n\t"
    "s_add_u32 s28, s28, 48234496\n\t"
    "v_lshrrev_b32 v14, 4, v1\n\t"
    "v_lshl_add_u32 v14, v14, 8, s28\n\t"
    "v_mov_b32 v75, s66\n\t"
    "v_mov_b32 v79, s66\n\t"
    "v_mov_b32 v83, s66\n\t"
    "v_mov_b32 v87, s66\n\t"
    "v_mov_b32 v91, s66\n\t"
    "v_mov_b32 v95, s66\n\t"
    "v_mov_b32 v99, s66\n\t"
    "v_mov_b32 v103, s66\n\t"
    "v_mov_b32 v107, s66\n\t"
    "v_mov_b32 v111, s66\n\t"
    "v_mov_b32 v115, s66\n\t"
    "s_mov_b32 exec_lo, 0x00010001\n\t"
    "s_mov_b32 exec_hi, 0x00010001\n\t"
    "global_store_dwordx4 v14, v[72:75], s[36:37] offset:0 sc1\n\t"
    "global_store_dwordx4 v14, v[76:79], s[36:37] offset:16 sc1\n\t"
    "global_store_dwordx4 v14, v[80:83], s[36:37] offset:32 sc1\n\t"
    "global_store_dwordx4 v14, v[84:87], s[36:37] offset:48 sc1\n\t"
    "global_store_dwordx4 v14, v[88:91], s[36:37] offset:64 sc1\n\t"
    "global_store_dwordx4 v14, v[92:95], s[36:37] offset:80 sc1\n\t"
    "global_store_dwordx4 v14, v[96:99], s[36:37] offset:96 sc1\n\t"
    "global_store_dwordx4 v14, v[100:103], s[36:37] offset:112 sc1\n\t"
    "global_store_dwordx4 v14, v[104:107], s[36:37] offset:128 sc1\n\t"
    "global_store_dwordx4 v14, v[108:111], s[36:37] offset:144 sc1\n\t"
    "global_store_dwordx4 v14, v[112:115], s[36:37] offset:160 sc1\n\t"
    "s_mov_b64 exec, -1\n\t"
    "L_ngv_%=:\n\t"
    "s_lshl_b32 s28, s93, 8\n\t"
    "v_add_u32 v190, s28, v0\n\t"
    "v_mul_u32_u24 v191, 21846, v190\n\t"
    "v_lshrrev_b32 v191, 20, v191\n\t"
    "v_mul_u32_u24 v192, 24, v191\n\t"
    "v_add3_u32 v190, v190, v192, 24\n\t"
    "v_lshlrev_b32 v190, 7, v190\n\t"
    "s_cmp_lg_u32 s21, 2\n\t"
    "s_cbranch_scc1 L_ne2_%=\n\t"
    "s_mov_b32 s35, 0\n\t"
    "L_fqp_%=:\n\t"
    "global_load_dwordx2 v[124:125], v126, s[36:37] offset:8 sc1\n\t"
    "s_add_u32 s35, s35, 1\n\t"
    "s_waitcnt vmcnt(0)\n\t"
    "v_cmp_ne_u64 vcc, s[66:67], v[124:125]\n\t"
    "s_cbranch_vccz L_fbp_%=\n\t"
    "s_sleep 4\n\t"
    "s_cmp_lt_u32 s35, 0x10000\n\t"
    "s_cbranch_scc1 L_fqp_%=\n\t"
    "L_fbp_%=:\n\t"
    "s_barrier\n\t"
    "s_mul_i32 s34, s3, 36\n\t"
    "s_add_u32 s34, s34, s20\n\t"
    "s_cmp_ge_u32 s34, 64\n\t"
    "s_cbranch_scc1 L_ne2_%=\n\t"
    "v_lshrrev_b32 v6, 2, v1\n\t"
    "v_lshlrev_b32 v6, 7, v6\n\t"
    "v_and_b32 v7, 3, v1\n\t"
    "v_lshl_add_u32 v6, v7, 4, v6\n\t"
    "v_lshrrev_b32 v8, 5, v1\n\t"
    "v_lshl_add_u32 v7, v7, 1, v8\n\t"
    "v_lshlrev_b32 v7, 4, v7\n\t"
    "s_lshr_b32 s28, s34, 1\n\t"
    "s_mul_i32 s28, s28, 18432\n\t"
    "s_and_b32 s29, s34, 1\n\t"
    "s_lshl_b32 s29, s29, 6\n\t"
    "s_add_u32 s28, s28, s29\n\t"
    "s_add_u32 s28, s28, 2953216\n\t"
    "s_lshl_b32 s29, s20, 9\n\t"
    "s_add_u32 s28, s28, s29\n\t"
    "s_sub_u32 s28, s28, 4096\n\t"
    "v_mov_b32 v14, s28\n\t"
    "s_mov_b32 s35, 0\n\t"
    "L_tql_%=:\n\t"
    "global_load_dword v8, v14, s[36:37] sc1\n\t"
    "s_add_u32 s35, s35, 1\n\t"
    "s_waitcnt vmcnt(0)\n\t"
    "v_cmp_eq_u32 vcc, -1, v8\n\t"
    "s_cbranch_vccz L_tbl_%=\n\t"
    "s_sleep 4\n\t"
    "s_cmp_lt_u32 s35, 0x8000\n\t"
    "s_cbranch_scc1 L_tql_%=\n\t"
    "L_tbl_%=:\n\t"
    "s_mov_b32 s35, 0\n\t"
    "L_e2_%=:\n\t"
    "s_lshr_b32 s28, s34, 1\n\t"
    "s_mul_i32 s28, s28, 18432\n\t"
    "s_and_b32 s29, s34, 1\n\t"
    "s_lshl_b32 s29, s29, 6\n\t"
    "s_add_u32 s28, s28, s29\n\t"
    "s_add_u32 s28, s28, 2953216\n\t"
    "v_add_u32 v14, s28, v6\n\t"
    "global_load_dwordx4 v[144:147], v14, s[36:37] offset:-4096 sc1\n\t"
    "global_load_dwordx4 v[148:151], v14, s[36:37] offset:-2048 sc1\n\t"
    "global_load_dwordx4 v[152:155], v14, s[36:37] offset:0 sc1\n\t"
    "global_load_dwordx4 v[156:159], v14, s[36:37] offset:2048 sc1\n\t"
    "v_add_u32 v14, 8192, v14\n\t"
    "global_load_dwordx4 v[160:163], v14, s[36:37] offset:-4096 sc1\n\t"
    "global_load_dwordx4 v[164:167], v14, s[36:37] offset:-2048 sc1\n\t"
    "global_load_dwordx4 v[168:171], v14, s[36:37] offset:0 sc1\n\t"
    "global_load_dwordx4 v[172:175], v14, s[36:37] offset:2048 sc1\n\t"
    "v_add_u32 v14, 8192, v14\n\t"
    "global_load_dwordx4 v[176:179], v14, s[36:37] offset:-4096 sc1\n\t"
    "s_add_u32 s35, s35, 1\n\t"
    "s_waitcnt vmcnt(0)\n\t"
    "v_max3_u32 v9, v146, v150, v154\n\t"
    "v_max3_u32 v9, v158, v162, v9\n\t"
    "v_max3_u32 v9, v166, v170, v9\n\t"
    "v_max3_u32 v9, v174, v178, v9\n\t"
    "v_max3_u32 v8, v144, v148, v152\n\t"
    "v_max3_u32 v8, v156, v160, v8\n\t"
    "v_max3_u32 v8, v164, v168, v8\n\t"
    "v_max3_u32 v8, v172, v176, v8\n\t"
    "v_max_u32 v8, v8, v9\n\t"
    "v_cmp_eq_u32 vcc, -1, v8\n\t"
    "s_cbranch_vccz L_e3_%=\n\t"
    "s_sleep 4\n\t"
    "s_cmp_lt_u32 s35, 0x8000\n\t"
    "s_cbranch_scc1 L_e2_%=\n\t"
    "L_e3_%=:\n\t"
    "v_mov_b32 v180, 0\n\t"
    "v_mov_b32 v181, 0\n\t"
    "v_mov_b32 v182, 0\n\t"
    "v_mov_b32 v183, 0\n\t"
    "s_mov_b32 s29, 0x3f803f80\n\t"
    "v_dot2c_f32_bf16 v180, s29, v144\n\t"
    "v_dot2c_f32_bf16 v181, v144, v144\n\t"
    "v_dot2c_f32_bf16 v182, s29, v146\n\t"
    "v_dot2c_f32_bf16 v183, v146, v146\n\t"
    "v_dot2c_f32_bf16 v180, s29, v145\n\t"
    "v_dot2c_f32_bf16 v181, v145, v145\n\t"
    "v_dot2c_f32_bf16 v182, s29, v147\n\t"
    "v_dot2c_f32_bf16 v183, v147, v147\n\t"
    "v_dot2c_f32_bf16 v180, s29, v148\n\t"
    "v_dot2c_f32_bf16 v181, v148, v148\n\t"
    "v_dot2c_f32_bf16 v182, s29, v150\n\t"
    "v_dot2c_f32_bf16 v183, v150, v150\n\t"
    "v_dot2c_f32_bf16 v180, s29, v149\n\t"
    "v_dot2c_f32_bf16 v181, v149, v149\n\t"
    "v_dot2c_f32_bf16 v182, s29, v151\n\t"
    "v_dot2c_f32_bf16 v183, v151, v151\n\t"
    "v_dot2c_f32_bf16 v180, s29, v152\n\t"
    "v_dot2c_f32_bf16 v181, v152, v152\n\t"
    "v_dot2c_f32_bf16 v182, s29, v154\n\t"
    "v_dot2c_f32_bf16 v183, v154, v154\n\t"
    "v_dot2c_f32_bf16 v180, s29, v153\n\t"
    "v_dot2c_f32_bf16 v181, v153, v153\n\t"
    "v_dot2c_f32_bf16 v182, s29, v155\n\t"
    "v_dot2c_f32_bf16 v183, v155, v155\n\t"
    "v_dot2c_f32_bf16 v180, s29, v156\n\t"
    "v_dot2c_f32_bf16 v181, v156, v156\n\t"
    "v_dot2c_f32_bf16 v182, s29, v158\n\t"
    "v_dot2c_f32_bf16 v183, v158, v158\n\t"
    "v_dot2c_f32_bf16 v180, s29, v157\n\t"
    "v_dot2c_f32_bf16 v181, v157, v157\n\t"
    "v_dot2c_f32_bf16 v182, s29, v159\n\t"
    "v_dot2c_f32_bf16 v183, v159, v159\n\t"
    "v_dot2c_f32_bf16 v180, s29, v160\n\t"
    "v_dot2c_f32_bf16 v181, v160, v160\n\t"
    "v_dot2c_f32_bf16 v182, s29, v162\n\t"
    "v_dot2c_f32_bf16 v183, v162, v162\n\t"
    "v_dot2c_f32_bf16 v180, s29, v161\n\t"
    "v_dot2c_f32_bf16 v181, v161, v161\n\t"
    "v_dot2c_f32_bf16 v182, s29, v163\n\t"
    "v_dot2c_f32_bf16 v183, v163, v163\n\t"
    "v_dot2c_f32_bf16 v180, s29, v164\n\t"
    "v_dot2c_f32_bf16 v181, v164, v164\n\t"
    "v_dot2c_f32_bf16 v182, s29, v166\n\t"
    "v_dot2c_f32_bf16 v183, v166, v166\n\t"
    "v_dot2c_f32_bf16 v180, s29, v165\n\t"
    "v_dot2c_f32_bf16 v181, v165, v165\n\t"
    "v_dot2c_f32_bf16 v182, s29, v167\n\t"
    "v_dot2c_f32_bf16 v183, v167, v167\n\t"
    "v_dot2c_f32_bf16 v180, s29, v168\n\t"
    "v_dot2c_f32_bf16 v181, v168, v168\n\t"
    "v_dot2c_f32_bf16 v182, s29, v170\n\t"
    "v_dot2c_f32_bf16 v183, v170, v170\n\t"
    "v_dot2c_f32_bf16 v180, s29, v169\n\t"
    "v_dot2c_f32_bf16 v181, v169, v169\n\t"
    "v_dot2c_f32_bf16 v182, s29, v171\n\t"
    "v_dot2c_f32_bf16 v183, v171, v171\n\t"
    "v_dot2c_f32_bf16 v180, s29, v172\n\t"
    "v_dot2c_f32_bf16 v181, v172, v172\n\t"
    "v_dot2c_f32_bf16 v182, s29, v174\n\t"
    "v_dot2c_f32_bf16 v183, v174, v174\n\t"
    "v_dot2c_f32_bf16 v180, s29, v173\n\t"
    "v_dot2c_f32_bf16 v181, v173, v173\n\t"
    "v_dot2c_f32_bf16 v182, s29, v175\n\t"
    "v_dot2c_f32_bf16 v183, v175, v175\n\t"
    "v_dot2c_f32_bf16 v180, s29, v176\n\t"
    "v_dot2c_f32_bf16 v181, v176, v176\n\t"
    "v_dot2c_f32_bf16 v182, s29, v178\n\t"
    "v_dot2c_f32_bf16 v183, v178, v178\n\t"
    "v_dot2c_f32_bf16 v180, s29, v177\n\t"
    "v_dot2c_f32_bf16 v181, v177, v177\n\t"
    "v_dot2c_f32_bf16 v182, s29, v179\n\t"
    "v_dot2c_f32_bf16 v183, v179, v179\n\t"
    "s_nop 1\n\t"
    "v_permlane32_swap_b32 v180, v182\n\t"
    "v_permlane32_swap_b32 v181, v183\n\t"
    "v_add_f32 v180, v180, v182\n\t"
    "v_add_f32 v181, v181, v183\n\t"
    "s_nop 1\n\t"
    "v_permlane16_swap_b32 v180, v181\n\t"
    "v_add_f32 v180, v180, v181\n\t"
    "s_nop 1\n\t"
    "v_add_f32_dpp v180, v180, v180 row_ror:8 row_mask:0xf bank_mask:0xf\n\t"
    "s_nop 1\n\t"
    "v_add_f32_dpp v180, v180, v180 row_ror:4 row_mask:0xf bank_mask:0xf\n\t"
    "v_mov_b32 v181, v180\n\t"
    "s_nop 1\n\t"
    "v_permlane16_swap_b32 v180, v181\n\t"
    "v_mul_f32 v180, 0x3ae38e39, v180\n\t"
    "v_mul_f32 v184, v180, v180\n\t"
    "s_mov_b32 s28, 0x3ae38e39\n\t"
    "v_fma_f32 v181, v181, s28, -v184\n\t"
    "v_max_f32 v181, 0, v181\n\t"
    "v_add_f32 v181, 0x3727c5ac, v181\n\t"
    "v_rsq_f32 v186, v181\n\t"
    "v_mov_b32 v185, s66\n\t"
    "v_mov_b32 v187, s67\n\t"
    "v_mul_f32 v184, v186, v180\n\t"
    "s_lshl_b32 s28, s34, 7\n\t"
    "s_add_u32 s28, s28, 16384\n\t"
    "v_add_u32 v182, s28, v7\n\t"
    "s_mov_b32 exec_lo, 15\n\t"
    "s_mov_b32 exec_hi, 15\n\t"
    "global_store_dwordx4 v182, v[184:187], s[36:37] sc1\n\t"
    "s_mov_b64 exec, -1\n\t"
    "L_ne2_%=:\n\t"
    "s_cmp_lg_u32 s21, 0\n\t"
    "s_cbranch_scc1 L_ngt_%=\n\t"
    "v_and_b32 v8, 1, v1\n\t"
    "v_lshl_add_u32 v14, v8, 7, v14\n\t"
    "s_waitcnt vmcnt(0)\n\t"
    "global_load_dword v219, v14, s[36:37] sc1\n\t"
    "L_ngt_%=:\n\t"
    "s_lshl_b32 s28, s20, 9\n\t"
    "s_add_u32 s28, s28, 41979904\n\t"
    "v_mov_b32 v14, s28\n\t"
    "s_mov_b32 s35, 0\n\t"
    "L_tqe_%=:\n\t"
    "global_load_dword v8, v14, s[36:37] sc1\n\t"
    "s_add_u32 s35, s35, 1\n\t"
    "s_waitcnt vmcnt(0)\n\t"
    "v_cmp_ne_u32 vcc, s66, v8\n\t"
    "s_cbranch_vccz L_tbe_%=\n\t"
    "s_sleep 4\n\t"
    "s_cmp_lt_u32 s35, 0x8000\n\t"
    "s_cbranch_scc1 L_tqe_%=\n\t"
    "L_tbe_%=:\n\t"
    "s_mov_b32 s35, 0\n\t"
    "L_fqe_%=:\n\t"
    "global_load_dwordx2 v[124:125], v126, s[36:37] offset:0 sc1\n\t"
    "s_add_u32 s35, s35, 1\n\t"
    "s_waitcnt vmcnt(0)\n\t"
    "v_cmp_ne_u64 vcc, s[66:67], v[124:125]\n\t"
    "s_cbranch_vccz L_fbe_%=\n\t"
    "s_sleep 12\n\t"
    "s_cmp_lt_u32 s35, 0x10000\n\t"
    "s_cbranch_scc1 L_fqe_%=\n\t"
    "L_fbe_%=:\n\t"
    "s_barrier\n\t"
    "L_nl2_%=:\n\t"
    "s_mov_b32 m0, s25\n\t"
    "s_nop 0\n\t"
    "buffer_load_dwordx4 v15, s[12:15], s24 offen offset:0 sc1 lds\n\t"
    "buffer_load_dwordx4 v15, s[12:15], s24 offen offset:1024 sc1 lds\n\t"
    "buffer_load_dwordx4 v15, s[12:15], s24 offen offset:2048 sc1 lds\n\t"
    "buffer_load_dwordx4 v15, s[12:15], s24 offen offset:3072 sc1 lds\n\t"
    "buffer_load_dwordx4 v15, s[12:15], s24 offen offset:3584 sc1 lds\n\t"
    "s_add_u32 s24, s24, 129024\n\t"
    "s_add_u32 s25, s25, 18432\n\t"
    "s_cmp_ge_u32 s25, 163840\n\t"
    "s_cselect_b32 s28, 92160, 0\n\t"
    "s_sub_u32 s25, s25, s28\n\t"
    "s_mov_b32 m0, s25\n\t"
    "s_nop 0\n\t"
    "buffer_load_dwordx4 v15, s[12:15], s24 offen offset:0 sc1 lds\n\t"
    "buffer_load_dwordx4 v15, s[12:15], s24 offen offset:1024 sc1 lds\n\t"
    "buffer_load_dwordx4 v15, s[12:15], s24 offen offset:2048 sc1 lds\n\t"
    "buffer_load_dwordx4 v15, s[12:15], s24 offen offset:3072 sc1 lds\n\t"
    "buffer_load_dwordx4 v15, s[12:15], s24 offen offset:3584 sc1 lds\n\t"
    "s_add_u32 s24, s24, 129024\n\t"
    "s_add_u32 s25, s25, 18432\n\t"
    "s_cmp_ge_u32 s25, 163840\n\t"
    "s_cselect_b32 s28, 92160, 0\n\t"
    "s_sub_u32 s25, s25, s28\n\t"
    "s_mov_b32 m0, s25\n\t"
    "s_nop 0\n\t"
    "buffer_load_dwordx4 v15, s[12:15], s24 offen offset:0 sc1 lds\n\t"
    "buffer_load_dwordx4 v15, s[12:15], s24 offen offset:1024 sc1 lds\n\t"
    "buffer_load_dwordx4 v15, s[12:15], s24 offen offset:2048 sc1 lds\n\t"
    "buffer_load_dwordx4 v15, s[12:15], s24 offen offset:3072 sc1 lds\n\t"
    "buffer_load_dwordx4 v15, s[12:15], s24 offen offset:3584 sc1 lds\n\t"
    "s_add_u32 s24, s24, 129024\n\t"
    "s_add_u32 s25, s25, 18432\n\t"
    "s_cmp_ge_u32 s25, 163840\n\t"
    "s_cselect_b32 s28, 92160, 0\n\t"
    "s_sub_u32 s25, s25, s28\n\t"
    "L_l2d_%=:\n\t"
    "global_load_dwordx4 v[48:51], v5, s[36:37] offset:0 sc1\n\t"
    "global_load_dwordx4 v[52:55], v5, s[36:37] offset:0 sc1\n\t"
    "v_add_u32 v5, 1792, v5\n\t"
    "s_waitcnt vmcnt(12)\n\t"
    "s_barrier\n\t"
    "s_cmp_lg_u32 s21, 2\n\t"
    "s_cbranch_scc1 L_ua_%=\n\t"
    "s_waitcnt vmcnt(0)\n\t"
    "s_branch L_V1_%=\n\t"
    "L_ua_%=:\n\t"
    "ds_read2st64_b64 v[136:139], v3 offset0:0 offset1:1\n\t"
    "ds_read2st64_b64 v[140:143], v3 offset0:2 offset1:3\n\t"
    "s_waitcnt lgkmcnt(0)\n\t"
    "v_mfma_f32_16x16x32_bf16 v[32:35], a[0:3], v[136:139], 0\n\t"
    "ds_read2st64_b64 v[128:131], v3 offset0:4 offset1:5\n\t"
    "v_mfma_f32_16x16x32_bf16 v[32:35], a[16:19], v[140:143], v[32:35]\n\t"
    "global_load_dwordx4 v[52:55], v5, s[36:37] offset:0 sc1\n\t"
    "v_mfma_f32_16x16x32_bf16 v[36:39], a[4:7], v[136:139], 0\n\t"
    "ds_read2st64_b64 v[132:135], v3 offset0:6 offset1:7\n\t"
    "v_mfma_f32_16x16x32_bf16 v[36:39], a[20:23], v[140:143], v[36:39]\n\t"
    "s_mov_b32 m0, s25\n\t"
    "s_add_u32 s25, s25, 18432\n\t"
    "buffer_load_dwordx4 v15, s[12:15], s24 offen offset:0 sc1 lds\n\t"
    "s_cmp_ge_u32 s25, 163840\n\t"
    "s_cselect_b32 s28, 92160, 0\n\t"
    "s_sub_u32 s25, s25, s28\n\t"
    "v_mfma_f32_16x16x32_bf16 v[40:43], a[8:11], v[136:139], 0\n\t"
    "v_add_u32 v5, 1792, v5\n\t"
    "v_mfma_f32_16x16x32_bf16 v[40:43], a[24:27], v[140:143], v[40:43]\n\t"
    "v_mfma_f32_16x16x32_bf16 v[44:47], a[12:15], v[136:139], 0\n\t"
    "v_mfma_f32_16x16x32_bf16 v[44:47], a[28:31], v[140:143], v[44:47]\n\t"
    "s_waitcnt lgkmcnt(0)\n\t"
    "v_mfma_f32_16x16x32_bf16 v[32:35], a[32:35], v[128:131], v[32:35]\n\t"
    "ds_read2st64_b64 v[136:139], v3 offset0:8 offset1:9\n\t"
    "v_mfma_f32_16x16x32_bf16 v[32:35], a[48:51], v[132:135], v[32:35]\n\t"
    "v_mfma_f32_16x16x32_bf16 v[36:39], a[36:39], v[128:131], v[36:39]\n\t"
    "ds_read2st64_b64 v[140:143], v3 offset0:10 offset1:11\n\t"
    "v_mfma_f32_16x16x32_bf16 v[36:39], a[52:55], v[132:135], v[36:39]\n\t"
    "v_mfma_f32_16x16x32_bf16 v[40:43], a[40:43], v[128:131], v[40:43]\n\t"
    "v_mfma_f32_16x16x32_bf16 v[40:43], a[56:59], v[132:135], v[40:43]\n\t"
    "buffer_load_dwordx4 v15, s[12:15], s24 offen offset:1024 sc1 lds\n\t"
    "v_mfma_f32_16x16x32_bf16 v[44:47], a[44:47], v[128:131], v[44:47]\n\t"
    "v_mfma_f32_16x16x32_bf16 v[44:47], a[60:63], v[132:135], v[44:47]\n\t"
    "s_waitcnt lgkmcnt(0)\n\t"
    "v_mfma_f32_16x16x32_bf16 v[32:35], a[64:67], v[136:139], v[32:35]\n\t"
    "ds_read2st64_b64 v[128:131], v3 offset0:12 offset1:13\n\t"
    "v_mfma_f32_16x16x32_bf16 v[32:35], a[80:83], v[140:143], v[32:35]\n\t"
    "v_mfma_f32_16x16x32_bf16 v[36:39], a[68:71], v[136:139], v[36:39]\n\t"
    "ds_read2st64_b64 v[132:135], v3 offset0:14 offset1:15\n\t"
    "v_mfma_f32_16x16x32_bf16 v[36:39], a[84:87], v[140:143], v[36:39]\n\t"
    "v_mfma_f32_16x16x32_bf16 v[40:43], a[72:75], v[136:139], v[40:43]\n\t"
    "v_mfma_f32_16x16x32_bf16 v[40:43], a[88:91], v[140:143], v[40:43]\n\t"
    "v_mfma_f32_16x16x32_bf16 v[44:47], a[76:79], v[136:139], v[44:47]\n\t"
    "v_mfma_f32_16x16x32_bf16 v[44:47], a[92:95], v[140:143], v[44:47]\n\t"
    "s_waitcnt lgkmcnt(0)\n\t"
    "v_mfma_f32_16x16x32_bf16 v[32:35], a[96:99], v[128:131], v[32:35]\n\t"
    "ds_read2st64_b64 v[136:139], v3 offset0:16 offset1:17\n\t"
    "v_mfma_f32_16x16x32_bf16 v[32:35], a[112:115], v[132:135], v[32:35]\n\t"
    "v_mfma_f32_16x16x32_bf16 v[36:39], a[100:103], v[128:131], v[36:39]\n\t"
    "ds_read2st64_b64 v[140:143], v3 offset0:18 offset1:19\n\t"
    "v_mfma_f32_16x16x32_bf16 v[36:39], a[116:119], v[132:135], v[36:39]\n\t"
    "v_mfma_f32_16x16x32_bf16 v[40:43], a[104:107], v[128:131], v[40:43]\n\t"
    "buffer_load_dwordx4 v15, s[12:15], s24 offen offset:2048 sc1 lds\n\t"
    "v_mfma_f32_16x16x32_bf16 v[40:43], a[120:123], v[132:135], v[40:43]\n\t"
    "v_mfma_f32_16x16x32_bf16 v[44:47], a[108:111], v[128:131], v[44:47]\n\t"
    "v_mfma_f32_16x16x32_bf16 v[44:47], a[124:127], v[132:135], v[44:47]\n\t"
    "s_waitcnt lgkmcnt(0)\n\t"
    "v_mfma_f32_16x16x32_bf16 v[32:35], a[128:131], v[136:139], v[32:35]\n\t"
    "ds_read2st64_b64 v[128:131], v3 offset0:20 offset1:21\n\t"
    "v_mfma_f32_16x16x32_bf16 v[32:35], a[144:147], v[140:143], v[32:35]\n\t"
    "v_mfma_f32_16x16x32_bf16 v[36:39], a[132:135], v[136:139], v[36:39]\n\t"
    "ds_read2st64_b64 v[132:135], v3 offset0:22 offset1:23\n\t"
    "v_mfma_f32_16x16x32_bf16 v[36:39], a[148:151], v[140:143], v[36:39]\n\t"
    "v_mfma_f32_16x16x32_bf16 v[40:43], a[136:139], v[136:139], v[40:43]\n\t"
    "v_mfma_f32_16x16x32_bf16 v[40:43], a[152:155], v[140:143], v[40:43]\n\t"
    "v_mfma_f32_16x16x32_bf16 v[44:47], a[140:143], v[136:139], v[44:47]\n\t"
    "v_mfma_f32_16x16x32_bf16 v[44:47], a[156:159], v[140:143], v[44:47]\n\t"
    "s_waitcnt lgkmcnt(0)\n\t"
    "v_mfma_f32_16x16x32_bf16 v[32:35], a[160:163], v[128:131], v[32:35]\n\t"
    "ds_read2st64_b64 v[136:139], v3 offset0:24 offset1:25\n\t"
    "v_mfma_f32_16x16x32_bf16 v[32:35], a[176:179], v[132:135], v[32:35]\n\t"
    "v_mfma_f32_16x16x32_bf16 v[36:39], a[164:167], v[128:131], v[36:39]\n\t"
    "ds_read2st64_b64 v[140:143], v3 offset0:26 offset1:27\n\t"
    "v_mfma_f32_16x16x32_bf16 v[36:39], a[180:183], v[132:135], v[36:39]\n\t"
    "v_mfma_f32_16x16x32_bf16 v[40:43], a[168:171], v[128:131], v[40:43]\n\t"
    "buffer_load_dwordx4 v15, s[12:15], s24 offen offset:3072 sc1 lds\n\t"
    "v_mfma_f32_16x16x32_bf16 v[40:43], a[184:187], v[132:135], v[40:43]\n\t"
    "v_mfma_f32_16x16x32_bf16 v[44:47], a[172:175], v[128:131], v[44:47]\n\t"
    "v_mfma_f32_16x16x32_bf16 v[44:47], a[188:191], v[132:135], v[44:47]\n\t"
    "s_waitcnt lgkmcnt(0)\n\t"
    "v_mfma_f32_16x16x32_bf16 v[32:35], a[192:195], v[136:139], v[32:35]\n\t"
    "ds_read2st64_b64 v[128:131], v3 offset0:28 offset1:29\n\t"
    "v_mfma_f32_16x16x32_bf16 v[32:35], a[208:211], v[140:143], v[32:35]\n\t"
    "v_mfma_f32_16x16x32_bf16 v[36:39], a[196:199], v[136:139], v[36:39]\n\t"
    "ds_read2st64_b64 v[132:135], v3 offset0:30 offset1:31\n\t"
    "v_mfma_f32_16x16x32_bf16 v[36:39], a[212:215], v[140:143], v[36:39]\n\t"
    "v_mfma_f32_16x16x32_bf16 v[40:43], a[200:203], v[136:139], v[40:43]\n\t"
    "buffer_load_dwordx4 v15, s[12:15], s24 offen offset:3584 sc1 lds\n\t"
    "s_add_u32 s24, s24, 129024\n\t"
    "v_mfma_f32_16x16x32_bf16 v[40:43], a[216:219], v[140:143], v[40:43]\n\t"
    "v_mfma_f32_16x16x32_bf16 v[44:47], a[204:207], v[136:139], v[44:47]\n\t"
    "v_mfma_f32_16x16x32_bf16 v[44:47], a[220:223], v[140:143], v[44:47]\n\t"
    "s_waitcnt lgkmcnt(0)\n\t"
    "v_mfma_f32_16x16x32_bf16 v[32:35], a[224:227], v[128:131], v[32:35]\n\t"
    "ds_read2st64_b64 v[136:139], v3 offset0:32 offset1:33\n\t"
    "s_add_u32 s26, s26, 18432\n\t"
    "v_mfma_f32_16x16x32_bf16 v[32:35], a[240:243], v[132:135], v[32:35]\n\t"
    "s_cmp_ge_u32 s26, 163840\n\t"
    "v_mfma_f32_16x16x32_bf16 v[36:39], a[228:231], v[128:131], v[36:39]\n\t"
    "ds_read2st64_b64 v[140:143], v3 offset0:34 offset1:35\n\t"
    "s_cselect_b32 s26, 71680, s26\n\t"
    "v_mfma_f32_16x16x32_bf16 v[36:39], a[244:247], v[132:135], v[36:39]\n\t"
    "v_mfma_f32_16x16x32_bf16 v[40:43], a[232:235], v[128:131], v[40:43]\n\t"
    "v_mfma_f32_16x16x32_bf16 v[40:43], a[248:251], v[132:135], v[40:43]\n\t"
    "v_add_u32 v3, s26, v2\n\t"
    "v_mfma_f32_16x16x32_bf16 v[44:47], a[236:239], v[128:131], v[44:47]\n\t"
    "v_mfma_f32_16x16x32_bf16 v[44:47], a[252:255], v[132:135], v[44:47]\n\t"
    "s_waitcnt lgkmcnt(0)\n\t"
    "s_waitcnt vmcnt(6)\n\t"
    "s_barrier\n\t"
    "v_mfma_f32_16x16x32_bf16 v[32:35], v[224:227], v[136:139], v[32:35]\n\t"
    "ds_read2st64_b64 v[128:131], v3 offset0:0 offset1:1\n\t"
    "v_mfma_f32_16x16x32_bf16 v[32:35], v[240:243], v[140:143], v[32:35]\n\t"
    "v_cmp_ne_u32 vcc, s66, v49\n\t"
    "v_cmp_ne_u32 s[30:31], s67, v51\n\t"
    "v_mfma_f32_16x16x32_bf16 v[36:39], v[228:231], v[136:139], v[36:39]\n\t"
    "ds_read2st64_b64 v[132:135], v3 offset0:2 offset1:3\n\t"
    "v_mfma_f32_16x16x32_bf16 v[36:39], v[244:247], v[140:143], v[36:39]\n\t"
    "s_or_b64 vcc, vcc, s[30:31]\n\t"
    "s_cbranch_vccnz L_ss1a_%=\n\t"
    "L_sr1a_%=:\n\t"
    "v_mov_b32 v62, v48\n\t"
    "v_mfma_f32_16x16x32_bf16 v[40:43], v[232:235], v[136:139], v[40:43]\n\t"
    "v_mul_f32 v60, s78, v50\n\t"
    "v_mfma_f32_16x16x32_bf16 v[40:43], v[248:251], v[140:143], v[40:43]\n\t"
    "v_mul_f32 v61, s79, v50\n\t"
    "v_mfma_f32_16x16x32_bf16 v[44:47], v[236:239], v[136:139], v[44:47]\n\t"
    "s_mov_b32 s27, s38\n\t"
    "s_add_u32 s38, s38, 1032192\n\t"
    "v_mfma_f32_16x16x32_bf16 v[44:47], v[252:255], v[140:143], v[44:47]\n\t"
    "s_mov_b32 s23, 1\n\t"
    "s_waitcnt vmcnt(5)\n\t"
    "s_branch L_V1b_%=\n\t"
    "L_wr1_%=:\n\t"
    "s_waitcnt vmcnt(12)\n\t"
    "s_barrier\n\t"
    "ds_read_b64_tr_b16 a[16:17], v11 offset:0\n\t"
    "ds_read_b64_tr_b16 a[18:19], v11 offset:1024\n\t"
    "ds_read_b64_tr_b16 a[20:21], v12 offset:0\n\t"
    "ds_read_b64_tr_b16 a[22:23], v12 offset:1024\n\t"
    "ds_read_b64_tr_b16 a[24:25], v11 offset:2048\n\t"
    "ds_read_b64_tr_b16 a[26:27], v11 offset:3072\n\t"
    "ds_read_b64_tr_b16 a[28:29], v12 offset:2048\n\t"
    "ds_read_b64_tr_b16 a[30:31], v12 offset:3072\n\t"
    "s_waitcnt vmcnt(8) lgkmcnt(0)\n\t"
    "s_barrier\n\t"
    "ds_read_b64_tr_b16 a[64:65], v11 offset:16384\n\t"
    "s_add_u32 m0, s0, 0\n\t"
    "s_mov_b32 s1, 3833856\n\t"
    "buffer_load_dwordx4 v13, s[4:7], s1 offen lds\n\t"
    "ds_read_b64_tr_b16 a[66:67], v11 offset:17408\n\t"
    "buffer_load_dwordx4 v3, s[4:7], s1 offen offset:1024 lds\n\t"
    "ds_read_b64_tr_b16 a[68:69], v12 offset:16384\n\t"
    "buffer_load_dwordx4 v13, s[8:11], s1 offen offset:2048 lds\n\t"
    "ds_read_b64_tr_b16 a[70:71], v12 offset:17408\n\t"
    "buffer_load_dwordx4 v3, s[8:11], s1 offen offset:3072 lds\n\t"
    "ds_read_b64_tr_b16 a[72:73], v11 offset:18432\n\t"
    "ds_read_b64_tr_b16 a[74:75], v11 offset:19456\n\t"
    "ds_read_b64_tr_b16 a[76:77], v12 offset:18432\n\t"
    "ds_read_b64_tr_b16 a[78:79], v12 offset:19456\n\t"
    "s_waitcnt vmcnt(8) lgkmcnt(0)\n\t"
    "s_barrier\n\t"
    "ds_read_b64_tr_b16 a[112:113], v11 offset:32768\n\t"
    "s_add_u32 m0, s0, 16384\n\t"
    "s_mov_b32 s1, 4718592\n\t"
    "buffer_load_dwordx4 v13, s[4:7], s1 offen lds\n\t"
    "ds_read_b64_tr_b16 a[114:115], v11 offset:33792\n\t"
    "buffer_load_dwordx4 v3, s[4:7], s1 offen offset:1024 lds\n\t"
    "ds_read_b64_tr_b16 a[116:117], v12 offset:32768\n\t"
    "buffer_load_dwordx4 v13, s[8:11], s1 offen offset:2048 lds\n\t"
    "ds_read_b64_tr_b16 a[118:119], v12 offset:33792\n\t"
    "buffer_load_dwordx4 v3, s[8:11], s1 offen offset:3072 lds\n\t"
    "ds_read_b64_tr_b16 a[120:121], v11 offset:34816\n\t"
    "ds_read_b64_tr_b16 a[122:123], v11 offset:35840\n\t"
    "ds_read_b64_tr_b16 a[124:125], v12 offset:34816\n\t"
    "ds_read_b64_tr_b16 a[126:127], v12 offset:35840\n\t"
    "s_waitcnt vmcnt(8) lgkmcnt(0)\n\t"
    "s_barrier\n\t"
    "ds_read_b64_tr_b16 a[160:161], v11 offset:49152\n\t"
    "s_add_u32 m0, s0, 32768\n\t"
    "s_mov_b32 s1, 0\n\t"
    "buffer_load_dwordx4 v13, s[4:7], s1 offen lds\n\t"
    "ds_read_b64_tr_b16 a[162:163], v11 offset:50176\n\t"
    "buffer_load_dwordx4 v3, s[4:7], s1 offen offset:1024 lds\n\t"
    "ds_read_b64_tr_b16 a[164:165], v12 offset:49152\n\t"
    "buffer_load_dwordx4 v13, s[8:11], s1 offen offset:2048 lds\n\t"
    "ds_read_b64_tr_b16 a[166:167], v12 offset:50176\n\t"
    "buffer_load_dwordx4 v3, s[8:11], s1 offen offset:3072 lds\n\t"
    "ds_read_b64_tr_b16 a[168:169], v11 offset:51200\n\t"
    "ds_read_b64_tr_b16 a[170:171], v11 offset:52224\n\t"
    "ds_read_b64_tr_b16 a[172:173], v12 offset:51200\n\t"
    "ds_read_b64_tr_b16 a[174:175], v12 offset:52224\n\t"
    "s_waitcnt vmcnt(8) lgkmcnt(0)\n\t"
    "s_barrier\n\t"
    "ds_read_b64_tr_b16 a[208:209], v11 offset:0\n\t"
    "s_add_u32 m0, s0, 49152\n\t"
    "s_mov_b32 s1, 589824\n\t"
    "buffer_load_dwordx4 v13, s[4:7], s1 offen lds\n\t"
    "ds_read_b64_tr_b16 a[210:211], v11 offset:1024\n\t"
    "buffer_load_dwordx4 v3, s[4:7], s1 offen offset:1024 lds\n\t"
    "ds_read_b64_tr_b16 a[212:213], v12 offset:0\n\t"
    "buffer_load_dwordx4 v13, s[8:11], s1 offen offset:2048 lds\n\t"
    "ds_read_b64_tr_b16 a[214:215], v12 offset:1024\n\t"
    "buffer_load_dwordx4 v3, s[8:11], s1 offen offset:3072 lds\n\t"
    "ds_read_b64_tr_b16 a[216:217], v11 offset:2048\n\t"
    "ds_read_b64_tr_b16 a[218:219], v11 offset:3072\n\t"
    "ds_read_b64_tr_b16 a[220:221], v12 offset:2048\n\t"
    "ds_read_b64_tr_b16 a[222:223], v12 offset:3072\n\t"
    "s_waitcnt vmcnt(8) lgkmcnt(0)\n\t"
    "s_barrier\n\t"
    "ds_read_b64_tr_b16 v[224:225], v11 offset:16384\n\t"
    "s_add_u32 m0, s0, 0\n\t"
    "s_mov_b32 s1, 884736\n\t"
    "buffer_load_dwordx4 v13, s[4:7], s1 offen lds\n\t"
    "ds_read_b64_tr_b16 v[226:227], v11 offset:17408\n\t"
    "buffer_load_dwordx4 v3, s[4:7], s1 offen offset:1024 lds\n\t"
    "ds_read_b64_tr_b16 v[228:229], v12 offset:16384\n\t"
    "buffer_load_dwordx4 v13, s[8:11], s1 offen offset:2048 lds\n\t"
    "ds_read_b64_tr_b16 v[230:231], v12 offset:17408\n\t"
    "buffer_load_dwordx4 v3, s[8:11], s1 offen offset:3072 lds\n\t"
    "ds_read_b64_tr_b16 v[232:233], v11 offset:18432\n\t"
    "ds_read_b64_tr_b16 v[234:235], v11 offset:19456\n\t"
    "ds_read_b64_tr_b16 v[236:237], v12 offset:18432\n\t"
    "ds_read_b64_tr_b16 v[238:239], v12 offset:19456\n\t"
    "s_waitcnt vmcnt(8) lgkmcnt(0)\n\t"
    "s_barrier\n\t"
    "ds_read_b64_tr_b16 a[0:1], v11 offset:32768\n\t"
    "s_add_u32 m0, s0, 16384\n\t"
    "s_mov_b32 s1, 1474560\n\t"
    "buffer_load_dwordx4 v13, s[4:7], s1 offen lds\n\t"
    "ds_read_b64_tr_b16 a[2:3], v11 offset:33792\n\t"
    "buffer_load_dwordx4 v3, s[4:7], s1 offen offset:1024 lds\n\t"
    "ds_read_b64_tr_b16 a[4:5], v12 offset:32768\n\t"
    "buffer_load_dwordx4 v13, s[8:11], s1 offen offset:2048 lds\n\t"
    "ds_read_b64_tr_b16 a[6:7], v12 offset:33792\n\t"
    "buffer_load_dwordx4 v3, s[8:11], s1 offen offset:3072 lds\n\t"
    "ds_read_b64_tr_b16 a[8:9], v11 offset:34816\n\t"
    "ds_read_b64_tr_b16 a[10:11], v11 offset:35840\n\t"
    "ds_read_b64_tr_b16 a[12:13], v12 offset:34816\n\t"
    "ds_read_b64_tr_b16 a[14:15], v12 offset:35840\n\t"
    "s_waitcnt vmcnt(8) lgkmcnt(0)\n\t"
    "s_barrier\n\t"
    "ds_read_b64_tr_b16 a[32:33], v11 offset:49152\n\t"
    "s_add_u32 m0, s0, 32768\n\t"
    "s_mov_b32 s1, 1769472\n\t"
    "buffer_load_dwordx4 v13, s[4:7], s1 offen lds\n\t"
    "ds_read_b64_tr_b16 a[34:35], v11 offset:50176\n\t"
    "buffer_load_dwordx4 v3, s[4:7], s1 offen offset:1024 lds\n\t"
    "ds_read_b64_tr_b16 a[36:37], v12 offset:49152\n\t"
    "buffer_load_dwordx4 v13, s[8:11], s1 offen offset:2048 lds\n\t"
    "ds_read_b64_tr_b16 a[38:39], v12 offset:50176\n\t"
    "buffer_load_dwordx4 v3, s[8:11], s1 offen offset:3072 lds\n\t"
    "ds_read_b64_tr_b16 a[40:41], v11 offset:51200\n\t"
    "ds_read_b64_tr_b16 a[42:43], v11 offset:52224\n\t"
    "ds_read_b64_tr_b16 a[44:45], v12 offset:51200\n\t"
    "ds_read_b64_tr_b16 a[46:47], v12 offset:52224\n\t"
    "s_waitcnt vmcnt(8) lgkmcnt(0)\n\t"
    "s_barrier\n\t"
    "ds_read_b64_tr_b16 a[48:49], v11 offset:0\n\t"
    "s_add_u32 m0, s0, 49152\n\t"
    "s_mov_b32 s1, 2359296\n\t"
    "buffer_load_dwordx4 v13, s[4:7], s1 offen lds\n\t"
    "ds_read_b64_tr_b16 a[50:51], v11 offset:1024\n\t"
    "buffer_load_dwordx4 v3, s[4:7], s1 offen offset:1024 lds\n\t"
    "ds_read_b64_tr_b16 a[52:53], v12 offset:0\n\t"
    "buffer_load_dwordx4 v13, s[8:11], s1 offen offset:2048 lds\n\t"
    "ds_read_b64_tr_b16 a[54:55], v12 offset:1024\n\t"
    "buffer_load_dwordx4 v3, s[8:11], s1 offen offset:3072 lds\n\t"
    "ds_read_b64_tr_b16 a[56:57], v11 offset:2048\n\t"
    "ds_read_b64_tr_b16 a[58:59], v11 offset:3072\n\t"
    "ds_read_b64_tr_b16 a[60:61], v12 offset:2048\n\t"
    "ds_read_b64_tr_b16 a[62:63], v12 offset:3072\n\t"
    "s_waitcnt vmcnt(8) lgkmcnt(0)\n\t"
    "s_barrier\n\t"
    "ds_read_b64_tr_b16 a[80:81], v11 offset:16384\n\t"
    "s_add_u32 m0, s0, 0\n\t"
    "s_mov_b32 s1, 2654208\n\t"
    "buffer_load_dwordx4 v13, s[4:7], s1 offen lds\n\t"
    "ds_read_b64_tr_b16 a[82:83], v11 offset:17408\n\t"
    "buffer_load_dwordx4 v3, s[4:7], s1 offen offset:1024 lds\n\t"
    "ds_read_b64_tr_b16 a[84:85], v12 offset:16384\n\t"
    "buffer_load_dwordx4 v13, s[8:11], s1 offen offset:2048 lds\n\t"
    "ds_read_b64_tr_b16 a[86:87], v12 offset:17408\n\t"
    "buffer_load_dwordx4 v3, s[8:11], s1 offen offset:3072 lds\n\t"
    "ds_read_b64_tr_b16 a[88:89], v11 offset:18432\n\t"
    "ds_read_b64_tr_b16 a[90:91], v11 offset:19456\n\t"
    "ds_read_b64_tr_b16 a[92:93], v12 offset:18432\n\t"
    "ds_read_b64_tr_b16 a[94:95], v12 offset:19456\n\t"
    "s_waitcnt vmcnt(8) lgkmcnt(0)\n\t"
    "s_barrier\n\t"
    "ds_read_b64_tr_b16 a[96:97], v11 offset:32768\n\t"
    "s_add_u32 m0, s0, 16384\n\t"
    "s_mov_b32 s1, 3244032\n\t"
    "buffer_load_dwordx4 v13, s[4:7], s1 offen lds\n\t"
    "ds_read_b64_tr_b16 a[98:99], v11 offset:33792\n\t"
    "buffer_load_dwordx4 v3, s[4:7], s1 offen offset:1024 lds\n\t"
    "ds_read_b64_tr_b16 a[100:101], v12 offset:32768\n\t"
    "buffer_load_dwordx4 v13, s[8:11], s1 offen offset:2048 lds\n\t"
    "ds_read_b64_tr_b16 a[102:103], v12 offset:33792\n\t"
    "buffer_load_dwordx4 v3, s[8:11], s1 offen offset:3072 lds\n\t"
    "ds_read_b64_tr_b16 a[104:105], v11 offset:34816\n\t"
    "ds_read_b64_tr_b16 a[106:107], v11 offset:35840\n\t"
    "ds_read_b64_tr_b16 a[108:109], v12 offset:34816\n\t"
    "ds_read_b64_tr_b16 a[110:111], v12 offset:35840\n\t"
    "s_waitcnt vmcnt(8) lgkmcnt(0)\n\t"
    "s_barrier\n\t"
    "ds_read_b64_tr_b16 a[128:129], v11 offset:49152\n\t"
    "s_add_u32 m0, s0, 32768\n\t"
    "s_mov_b32 s1, 3538944\n\t"
    "buffer_load_dwordx4 v13, s[4:7], s1 offen lds\n\t"
    "ds_read_b64_tr_b16 a[130:131], v11 offset:50176\n\t"
    "buffer_load_dwordx4 v3, s[4:7], s1 offen offset:1024 lds\n\t"
    "ds_read_b64_tr_b16 a[132:133], v12 offset:49152\n\t"
    "buffer_load_dwordx4 v13, s[8:11], s1 offen offset:2048 lds\n\t"
    "ds_read_b64_tr_b16 a[134:135], v12 offset:50176\n\t"
    "buffer_load_dwordx4 v3, s[8:11], s1 offen offset:3072 lds\n\t"
    "ds_read_b64_tr_b16 a[136:137], v11 offset:51200\n\t"
    "ds_read_b64_tr_b16 a[138:139], v11 offset:52224\n\t"
    "ds_read_b64_tr_b16 a[140:141], v12 offset:51200\n\t"
    "ds_read_b64_tr_b16 a[142:143], v12 offset:52224\n\t"
    "s_waitcnt vmcnt(8) lgkmcnt(0)\n\t"
    "s_barrier\n\t"
    "ds_read_b64_tr_b16 a[144:145], v11 offset:0\n\t"
    "s_add_u32 m0, s0, 49152\n\t"
    "s_mov_b32 s1, 4128768\n\t"
    "buffer_load_dwordx4 v13, s[4:7], s1 offen lds\n\t"
    "ds_read_b64_tr_b16 a[146:147], v11 offset:1024\n\t"
    "buffer_load_dwordx4 v3, s[4:7], s1 offen offset:1024 lds\n\t"
    "ds_read_b64_tr_b16 a[148:149], v12 offset:0\n\t"
    "buffer_load_dwordx4 v13, s[8:11], s1 offen offset:2048 lds\n\t"
    "ds_read_b64_tr_b16 a[150:151], v12 offset:1024\n\t"
    "buffer_load_dwordx4 v3, s[8:11], s1 offen offset:3072 lds\n\t"
    "ds_read_b64_tr_b16 a[152:153], v11 offset:2048\n\t"
    "ds_read_b64_tr_b16 a[154:155], v11 offset:3072\n\t"
    "ds_read_b64_tr_b16 a[156:157], v12 offset:2048\n\t"
    "ds_read_b64_tr_b16 a[158:159], v12 offset:3072\n\t"
    "s_waitcnt vmcnt(8) lgkmcnt(0)\n\t"
    "s_barrier\n\t"
    "ds_read_b64_tr_b16 a[176:177], v11 offset:16384\n\t"
    "s_add_u32 m0, s0, 0\n\t"
    "s_mov_b32 s1, 4423680\n\t"
    "buffer_load_dwordx4 v13, s[4:7], s1 offen lds\n\t"
    "ds_read_b64_tr_b16 a[178:179], v11 offset:17408\n\t"
    "buffer_load_dwordx4 v3, s[4:7], s1 offen offset:1024 lds\n\t"
    "ds_read_b64_tr_b16 a[180:181], v12 offset:16384\n\t"
    "buffer_load_dwordx4 v13, s[8:11], s1 offen offset:2048 lds\n\t"
    "ds_read_b64_tr_b16 a[182:183], v12 offset:17408\n\t"
    "buffer_load_dwordx4 v3, s[8:11], s1 offen offset:3072 lds\n\t"
    "ds_read_b64_tr_b16 a[184:185], v11 offset:18432\n\t"
    "ds_read_b64_tr_b16 a[186:187], v11 offset:19456\n\t"
    "ds_read_b64_tr_b16 a[188:189], v12 offset:18432\n\t"
    "ds_read_b64_tr_b16 a[190:191], v12 offset:19456\n\t"
    "s_waitcnt vmcnt(8) lgkmcnt(0)\n\t"
    "s_barrier\n\t"
    "ds_read_b64_tr_b16 a[192:193], v11 offset:32768\n\t"
    "s_add_u32 m0, s0, 16384\n\t"
    "s_mov_b32 s1, 5013504\n\t"
    "buffer_load_dwordx4 v13, s[4:7], s1 offen lds\n\t"
    "ds_read_b64_tr_b16 a[194:195], v11 offset:33792\n\t"
    "buffer_load_dwordx4 v3, s[4:7], s1 offen offset:1024 lds\n\t"
    "ds_read_b64_tr_b16 a[196:197], v12 offset:32768\n\t"
    "buffer_load_dwordx4 v13, s[8:11], s1 offen offset:2048 lds\n\t"
    "ds_read_b64_tr_b16 a[198:199], v12 offset:33792\n\t"
    "buffer_load_dwordx4 v3, s[8:11], s1 offen offset:3072 lds\n\t"
    "ds_read_b64_tr_b16 a[200:201], v11 offset:34816\n\t"
    "ds_read_b64_tr_b16 a[202:203], v11 offset:35840\n\t"
    "ds_read_b64_tr_b16 a[204:205], v12 offset:34816\n\t"
    "ds_read_b64_tr_b16 a[206:207], v12 offset:35840\n\t"
    "s_waitcnt vmcnt(8) lgkmcnt(0)\n\t"
    "s_barrier\n\t"
    "ds_read_b64_tr_b16 a[224:225], v11 offset:49152\n\t"
    "ds_read_b64_tr_b16 a[226:227], v11 offset:50176\n\t"
    "ds_read_b64_tr_b16 a[228:229], v12 offset:49152\n\t"
    "ds_read_b64_tr_b16 a[230:231], v12 offset:50176\n\t"
    "ds_read_b64_tr_b16 a[232:233], v11 offset:51200\n\t"
    "ds_read_b64_tr_b16 a[234:235], v11 offset:52224\n\t"
    "ds_read_b64_tr_b16 a[236:237], v12 offset:51200\n\t"
    "ds_read_b64_tr_b16 a[238:239], v12 offset:52224\n\t"
    "global_load_dwordx2 v[124:125], v126, s[36:37] sc1\n\t"
    "s_waitcnt vmcnt(5) lgkmcnt(0)\n\t"
    "s_barrier\n\t"
    "ds_read_b64_tr_b16 a[240:241], v11 offset:0\n\t"
    "ds_read_b64_tr_b16 a[242:243], v11 offset:1024\n\t"
    "ds_read_b64_tr_b16 a[244:245], v12 offset:0\n\t"
    "ds_read_b64_tr_b16 a[246:247], v12 offset:1024\n\t"
    "ds_read_b64_tr_b16 a[248:249], v11 offset:2048\n\t"
    "ds_read_b64_tr_b16 a[250:251], v11 offset:3072\n\t"
    "ds_read_b64_tr_b16 a[252:253], v12 offset:2048\n\t"
    "ds_read_b64_tr_b16 a[254:255], v12 offset:3072\n\t"
    "s_waitcnt vmcnt(1) lgkmcnt(0)\n\t"
    "s_barrier\n\t"
    "ds_read_b64_tr_b16 v[240:241], v11 offset:16384\n\t"
    "ds_read_b64_tr_b16 v[242:243], v11 offset:17408\n\t"
    "ds_read_b64_tr_b16 v[244:245], v12 offset:16384\n\t"
    "ds_read_b64_tr_b16 v[246:247], v12 offset:17408\n\t"
    "ds_read_b64_tr_b16 v[248:249], v11 offset:18432\n\t"
    "ds_read_b64_tr_b16 v[250:251], v11 offset:19456\n\t"
    "ds_read_b64_tr_b16 v[252:253], v12 offset:18432\n\t"
    "ds_read_b64_tr_b16 v[254:255], v12 offset:19456\n\t"
    "s_branch L_wrj_%=\n\t"
    "L_wr2_%=:\n\t"
    "s_waitcnt vmcnt(12)\n\t"
    "s_barrier\n\t"
    "ds_read_b64_tr_b16 a[32:33], v11 offset:0\n\t"
    "ds_read_b64_tr_b16 a[34:35], v11 offset:1024\n\t"
    "ds_read_b64_tr_b16 a[36:37], v12 offset:0\n\t"
    "ds_read_b64_tr_b16 a[38:39], v12 offset:1024\n\t"
    "ds_read_b64_tr_b16 a[40:41], v11 offset:2048\n\t"
    "ds_read_b64_tr_b16 a[42:43], v11 offset:3072\n\t"
    "ds_read_b64_tr_b16 a[44:45], v12 offset:2048\n\t"
    "ds_read_b64_tr_b16 a[46:47], v12 offset:3072\n\t"
    "s_waitcnt vmcnt(8) lgkmcnt(0)\n\t"
    "s_barrier\n\t"
    "ds_read_b64_tr_b16 a[80:81], v11 offset:16384\n\t"
    "s_add_u32 m0, s0, 0\n\t"
    "s_mov_b32 s1, 4128768\n\t"
    "buffer_load_dwordx4 v13, s[4:7], s1 offen lds\n\t"
    "ds_read_b64_tr_b16 a[82:83], v11 offset:17408\n\t"
    "buffer_load_dwordx4 v3, s[4:7], s1 offen offset:1024 lds\n\t"
    "ds_read_b64_tr_b16 a[84:85], v12 offset:16384\n\t"
    "buffer_load_dwordx4 v13, s[8:11], s1 offen offset:2048 lds\n\t"
    "ds_read_b64_tr_b16 a[86:87], v12 offset:17408\n\t"
    "buffer_load_dwordx4 v3, s[8:11], s1 offen offset:3072 lds\n\t"
    "ds_read_b64_tr_b16 a[88:89], v11 offset:18432\n\t"
    "ds_read_b64_tr_b16 a[90:91], v11 offset:19456\n\t"
    "ds_read_b64_tr_b16 a[92:93], v12 offset:18432\n\t"
    "ds_read_b64_tr_b16 a[94:95], v12 offset:19456\n\t"
    "s_waitcnt vmcnt(8) lgkmcnt(0)\n\t"
    "s_barrier\n\t"
    "ds_read_b64_tr_b16 a[128:129], v11 offset:32768\n\t"
    "s_add_u32 m0, s0, 16384\n\t"
    "s_mov_b32 s1, 5013504\n\t"
    "buffer_load_dwordx4 v13, s[4:7], s1 offen lds\n\t"
    "ds_read_b64_tr_b16 a[130:131], v11 offset:33792\n\t"
    "buffer_load_dwordx4 v3, s[4:7], s1 offen offset:1024 lds\n\t"
    "ds_read_b64_tr_b16 a[132:133], v12 offset:32768\n\t"
    "buffer_load_dwordx4 v13, s[8:11], s1 offen offset:2048 lds\n\t"
    "ds_read_b64_tr_b16 a[134:135], v12 offset:33792\n\t"
    "buffer_load_dwordx4 v3, s[8:11], s1 offen offset:3072 lds\n\t"
    "ds_read_b64_tr_b16 a[136:137], v11 offset:34816\n\t"
    "ds_read_b64_tr_b16 a[138:139], v11 offset:35840\n\t"
    "ds_read_b64_tr_b16 a[140:141], v12 offset:34816\n\t"
    "ds_read_b64_tr_b16 a[142:143], v12 offset:35840\n\t"
    "s_waitcnt vmcnt(8) lgkmcnt(0)\n\t"
    "s_barrier\n\t"
    "ds_read_b64_tr_b16 a[176:177], v11 offset:49152\n\t"
    "s_add_u32 m0, s0, 32768\n\t"
    "s_mov_b32 s1, 0\n\t"
    "buffer_load_dwordx4 v13, s[4:7], s1 offen lds\n\t"
    "ds_read_b64_tr_b16 a[178:179], v11 offset:50176\n\t"
    "buffer_load_dwordx4 v3, s[4:7], s1 offen offset:1024 lds\n\t"
    "ds_read_b64_tr_b16 a[180:181], v12 offset:49152\n\t"
    "buffer_load_dwordx4 v13, s[8:11], s1 offen offset:2048 lds\n\t"
    "ds_read_b64_tr_b16 a[182:183], v12 offset:50176\n\t"
    "buffer_load_dwordx4 v3, s[8:11], s1 offen offset:3072 lds\n\t"
    "ds_read_b64_tr_b16 a[184:185], v11 offset:51200\n\t"
    "ds_read_b64_tr_b16 a[186:187], v11 offset:52224\n\t"
    "ds_read_b64_tr_b16 a[188:189], v12 offset:51200\n\t"
    "ds_read_b64_tr_b16 a[190:191], v12 offset:52224\n\t"
    "s_waitcnt vmcnt(8) lgkmcnt(0)\n\t"
    "s_barrier\n\t"
    "ds_read_b64_tr_b16 a[224:225], v11 offset:0\n\t"
    "s_add_u32 m0, s0, 49152\n\t"
    "s_mov_b32 s1, 294912\n\t"
    "buffer_load_dwordx4 v13, s[4:7], s1 offen lds\n\t"
    "ds_read_b64_tr_b16 a[226:227], v11 offset:1024\n\t"
    "buffer_load_dwordx4 v3, s[4:7], s1 offen offset:1024 lds\n\t"
    "ds_read_b64_tr_b16 a[228:229], v12 offset:0\n\t"
    "buffer_load_dwordx4 v13, s[8:11], s1 offen offset:2048 lds\n\t"
    "ds_read_b64_tr_b16 a[230:231], v12 offset:1024\n\t"
    "buffer_load_dwordx4 v3, s[8:11], s1 offen offset:3072 lds\n\t"
    "ds_read_b64_tr_b16 a[232:233], v11 offset:2048\n\t"
    "ds_read_b64_tr_b16 a[234:235], v11 offset:3072\n\t"
    "ds_read_b64_tr_b16 a[236:237], v12 offset:2048\n\t"
    "ds_read_b64_tr_b16 a[238:239], v12 offset:3072\n\t"
    "s_waitcnt vmcnt(8) lgkmcnt(0)\n\t"
    "s_barrier\n\t"
    "ds_read_b64_tr_b16 v[240:241], v11 offset:16384\n\t"
    "s_add_u32 m0, s0, 0\n\t"
    "s_mov_b32 s1, 884736\n\t"
    "buffer_load_dwordx4 v13, s[4:7], s1 offen lds\n\t"
    "ds_read_b64_tr_b16 v[242:243], v11 offset:17408\n\t"
    "buffer_load_dwordx4 v3, s[4:7], s1 offen offset:1024 lds\n\t"
    "ds_read_b64_tr_b16 v[244:245], v12 offset:16384\n\t"
    "buffer_load_dwordx4 v13, s[8:11], s1 offen offset:2048 lds\n\t"
    "ds_read_b64_tr_b16 v[246:247], v12 offset:17408\n\t"
    "buffer_load_dwordx4 v3, s[8:11], s1 offen offset:3072 lds\n\t"
    "ds_read_b64_tr_b16 v[248:249], v11 offset:18432\n\t"
    "ds_read_b64_tr_b16 v[250:251], v11 offset:19456\n\t"
    "ds_read_b64_tr_b16 v[252:253], v12 offset:18432\n\t"
    "ds_read_b64_tr_b16 v[254:255], v12 offset:19456\n\t"
    "s_waitcnt vmcnt(8) lgkmcnt(0)\n\t"
    "s_barrier\n\t"
    "ds_read_b64_tr_b16 a[0:1], v11 offset:32768\n\t"
    "s_add_u32 m0, s0, 16384\n\t"
    "s_mov_b32 s1, 1179648\n\t"
    "buffer_load_dwordx4 v13, s[4:7], s1 offen lds\n\t"
    "ds_read_b64_tr_b16 a[2:3], v11 offset:33792\n\t"
    "buffer_load_dwordx4 v3, s[4:7], s1 offen offset:1024 lds\n\t"
    "ds_read_b64_tr_b16 a[4:5], v12 offset:32768\n\t"
    "buffer_load_dwordx4 v13, s[8:11], s1 offen offset:2048 lds\n\t"
    "ds_read_b64_tr_b16 a[6:7], v12 offset:33792\n\t"
    "buffer_load_dwordx4 v3, s[8:11], s1 offen offset:3072 lds\n\t"
    "ds_read_b64_tr_b16 a[8:9], v11 offset:34816\n\t"
    "ds_read_b64_tr_b16 a[10:11], v11 offset:35840\n\t"
    "ds_read_b64_tr_b16 a[12:13], v12 offset:34816\n\t"
    "ds_read_b64_tr_b16 a[14:15], v12 offset:35840\n\t"
    "s_waitcnt vmcnt(8) lgkmcnt(0)\n\t"
    "s_barrier\n\t"
    "ds_read_b64_tr_b16 a[16:17], v11 offset:49152\n\t"
    "s_add_u32 m0, s0, 32768\n\t"
    "s_mov_b32 s1, 1769472\n\t"
    "buffer_load_dwordx4 v13, s[4:7], s1 offen lds\n\t"
    "ds_read_b64_tr_b16 a[18:19], v11 offset:50176\n\t"
    "buffer_load_dwordx4 v3, s[4:7], s1 offen offset:1024 lds\n\t"
    "ds_read_b64_tr_b16 a[20:21], v12 offset:49152\n\t"
    "buffer_load_dwordx4 v13, s[8:11], s1 offen offset:2048 lds\n\t"
    "ds_read_b64_tr_b16 a[22:23], v12 offset:50176\n\t"
    "buffer_load_dwordx4 v3, s[8:11], s1 offen offset:3072 lds\n\t"
    "ds_read_b64_tr_b16 a[24:25], v11 offset:51200\n\t"
    "ds_read_b64_tr_b16 a[26:27], v11 offset:52224\n\t"
    "ds_read_b64_tr_b16 a[28:29], v12 offset:51200\n\t"
    "ds_read_b64_tr_b16 a[30:31], v12 offset:52224\n\t"
    "s_waitcnt vmcnt(8) lgkmcnt(0)\n\t"
    "s_barrier\n\t"
    "ds_read_b64_tr_b16 a[48:49], v11 offset:0\n\t"
    "s_add_u32 m0, s0, 49152\n\t"
    "s_mov_b32 s1, 2064384\n\t"
    "buffer_load_dwordx4 v13, s[4:7], s1 offen lds\n\t"
    "ds_read_b64_tr_b16 a[50:51], v11 offset:1024\n\t"
    "buffer_load_dwordx4 v3, s[4:7], s1 offen offset:1024 lds\n\t"
    "ds_read_b64_tr_b16 a[52:53], v12 offset:0\n\t"
    "buffer_load_dwordx4 v13, s[8:11], s1 offen offset:2048 lds\n\t"
    "ds_read_b64_tr_b16 a[54:55], v12 offset:1024\n\t"
    "buffer_load_dwordx4 v3, s[8:11], s1 offen offset:3072 lds\n\t"
    "ds_read_b64_tr_b16 a[56:57], v11 offset:2048\n\t"
    "ds_read_b64_tr_b16 a[58:59], v11 offset:3072\n\t"
    "ds_read_b64_tr_b16 a[60:61], v12 offset:2048\n\t"
    "ds_read_b64_tr_b16 a[62:63], v12 offset:3072\n\t"
    "s_waitcnt vmcnt(8) lgkmcnt(0)\n\t"
    "s_barrier\n\t"
    "ds_read_b64_tr_b16 a[64:65], v11 offset:16384\n\t"
    "s_add_u32 m0, s0, 0\n\t"
    "s_mov_b32 s1, 2654208\n\t"
    "buffer_load_dwordx4 v13, s[4:7], s1 offen lds\n\t"
    "ds_read_b64_tr_b16 a[66:67], v11 offset:17408\n\t"
    "buffer_load_dwordx4 v3, s[4:7], s1 offen offset:1024 lds\n\t"
    "ds_read_b64_tr_b16 a[68:69], v12 offset:16384\n\t"
    "buffer_load_dwordx4 v13, s[8:11], s1 offen offset:2048 lds\n\t"
    "ds_read_b64_tr_b16 a[70:71], v12 offset:17408\n\t"
    "buffer_load_dwordx4 v3, s[8:11], s1 offen offset:3072 lds\n\t"
    "ds_read_b64_tr_b16 a[72:73], v11 offset:18432\n\t"
    "ds_read_b64_tr_b16 a[74:75], v11 offset:19456\n\t"
    "ds_read_b64_tr_b16 a[76:77], v12 offset:18432\n\t"
    "ds_read_b64_tr_b16 a[78:79], v12 offset:19456\n\t"
    "s_waitcnt vmcnt(8) lgkmcnt(0)\n\t"
    "s_barrier\n\t"
    "ds_read_b64_tr_b16 a[96:97], v11 offset:32768\n\t"
    "s_add_u32 m0, s0, 16384\n\t"
    "s_mov_b32 s1, 2949120\n\t"
    "buffer_load_dwordx4 v13, s[4:7], s1 offen lds\n\t"
    "ds_read_b64_tr_b16 a[98:99], v11 offset:33792\n\t"
    "buffer_load_dwordx4 v3, s[4:7], s1 offen offset:1024 lds\n\t"
    "ds_read_b64_tr_b16 a[100:101], v12 offset:32768\n\t"
    "buffer_load_dwordx4 v13, s[8:11], s1 offen offset:2048 lds\n\t"
    "ds_read_b64_tr_b16 a[102:103], v12 offset:33792\n\t"
    "buffer_load_dwordx4 v3, s[8:11], s1 offen offset:3072 lds\n\t"
    "ds_read_b64_tr_b16 a[104:105], v11 offset:34816\n\t"
    "ds_read_b64_tr_b16 a[106:107], v11 offset:35840\n\t"
    "ds_read_b64_tr_b16 a[108:109], v12 offset:34816\n\t"
    "ds_read_b64_tr_b16 a[110:111], v12 offset:35840\n\t"
    "s_waitcnt vmcnt(8) lgkmcnt(0)\n\t"
    "s_barrier\n\t"
    "ds_read_b64_tr_b16 a[112:113], v11 offset:49152\n\t"
    "s_add_u32 m0, s0, 32768\n\t"
    "s_mov_b32 s1, 3538944\n\t"
    "buffer_load_dwordx4 v13, s[4:7], s1 offen lds\n\t"
    "ds_read_b64_tr_b16 a[114:115], v11 offset:50176\n\t"
    "buffer_load_dwordx4 v3, s[4:7], s1 offen offset:1024 lds\n\t"
    "ds_read_b64_tr_b16 a[116:117], v12 offset:49152\n\t"
    "buffer_load_dwordx4 v13, s[8:11], s1 offen offset:2048 lds\n\t"
    "ds_read_b64_tr_b16 a[118:119], v12 offset:50176\n\t"
    "buffer_load_dwordx4 v3, s[8:11], s1 offen offset:3072 lds\n\t"
    "ds_read_b64_tr_b16 a[120:121], v11 offset:51200\n\t"
    "ds_read_b64_tr_b16 a[122:123], v11 offset:52224\n\t"
    "ds_read_b64_tr_b16 a[124:125], v12 offset:51200\n\t"
    "ds_read_b64_tr_b16 a[126:127], v12 offset:52224\n\t"
    "s_waitcnt vmcnt(8) lgkmcnt(0)\n\t"
    "s_barrier\n\t"
    "ds_read_b64_tr_b16 a[144:145], v11 offset:0\n\t"
    "s_add_u32 m0, s0, 49152\n\t"
    "s_mov_b32 s1, 3833856\n\t"
    "buffer_load_dwordx4 v13, s[4:7], s1 offen lds\n\t"
    "ds_read_b64_tr_b16 a[146:147], v11 offset:1024\n\t"
    "buffer_load_dwordx4 v3, s[4:7], s1 offen offset:1024 lds\n\t"
    "ds_read_b64_tr_b16 a[148:149], v12 offset:0\n\t"
    "buffer_load_dwordx4 v13, s[8:11], s1 offen offset:2048 lds\n\t"
    "ds_read_b64_tr_b16 a[150:151], v12 offset:1024\n\t"
    "buffer_load_dwordx4 v3, s[8:11], s1 offen offset:3072 lds\n\t"
    "ds_read_b64_tr_b16 a[152:153], v11 offset:2048\n\t"
    "ds_read_b64_tr_b16 a[154:155], v11 offset:3072\n\t"
    "ds_read_b64_tr_b16 a[156:157], v12 offset:2048\n\t"
    "ds_read_b64_tr_b16 a[158:159], v12 offset:3072\n\t"
    "s_waitcnt vmcnt(8) lgkmcnt(0)\n\t"
    "s_barrier\n\t"
    "ds_read_b64_tr_b16 a[160:161], v11 offset:16384\n\t"
    "s_add_u32 m0, s0, 0\n\t"
    "s_mov_b32 s1, 4423680\n\t"
    "buffer_load_dwordx4 v13, s[4:7], s1 offen lds\n\t"
    "ds_read_b64_tr_b16 a[162:163], v11 offset:17408\n\t"
    "buffer_load_dwordx4 v3, s[4:7], s1 offen offset:1024 lds\n\t"
    "ds_read_b64_tr_b16 a[164:165], v12 offset:16384\n\t"
    "buffer_load_dwordx4 v13, s[8:11], s1 offen offset:2048 lds\n\t"
    "ds_read_b64_tr_b16 a[166:167], v12 offset:17408\n\t"
    "buffer_load_dwordx4 v3, s[8:11], s1 offen offset:3072 lds\n\t"
    "ds_read_b64_tr_b16 a[168:169], v11 offset:18432\n\t"
    "ds_read_b64_tr_b16 a[170:171], v11 offset:19456\n\t"
    "ds_read_b64_tr_b16 a[172:173], v12 offset:18432\n\t"
    "ds_read_b64_tr_b16 a[174:175], v12 offset:19456\n\t"
    "s_waitcnt vmcnt(8) lgkmcnt(0)\n\t"
    "s_barrier\n\t"
    "ds_read_b64_tr_b16 a[192:193], v11 offset:32768\n\t"
    "s_add_u32 m0, s0, 16384\n\t"
    "s_mov_b32 s1, 4718592\n\t"
    "buffer_load_dwordx4 v13, s[4:7], s1 offen lds\n\t"
    "ds_read_b64_tr_b16 a[194:195], v11 offset:33792\n\t"
    "buffer_load_dwordx4 v3, s[4:7], s1 offen offset:1024 lds\n\t"
    "ds_read_b64_tr_b16 a[196:197], v12 offset:32768\n\t"
    "buffer_load_dwordx4 v13, s[8:11], s1 offen offset:2048 lds\n\t"
    "ds_read_b64_tr_b16 a[198:199], v12 offset:33792\n\t"
    "buffer_load_dwordx4 v3, s[8:11], s1 offen offset:3072 lds\n\t"
    "ds_read_b64_tr_b16 a[200:201], v11 offset:34816\n\t"
    "ds_read_b64_tr_b16 a[202:203], v11 offset:35840\n\t"
    "ds_read_b64_tr_b16 a[204:205], v12 offset:34816\n\t"
    "ds_read_b64_tr_b16 a[206:207], v12 offset:35840\n\t"
    "s_waitcnt vmcnt(8) lgkmcnt(0)\n\t"
    "s_barrier\n\t"
    "ds_read_b64_tr_b16 a[208:209], v11 offset:49152\n\t"
    "ds_read_b64_tr_b16 a[210:211], v11 offset:50176\n\t"
    "ds_read_b64_tr_b16 a[212:213], v12 offset:49152\n\t"
    "ds_read_b64_tr_b16 a[214:215], v12 offset:50176\n\t"
    "ds_read_b64_tr_b16 a[216:217], v11 offset:51200\n\t"
    "ds_read_b64_tr_b16 a[218:219], v11 offset:52224\n\t"
    "ds_read_b64_tr_b16 a[220:221], v12 offset:51200\n\t"
    "ds_read_b64_tr_b16 a[222:223], v12 offset:52224\n\t"
    "global_load_dwordx2 v[124:125], v126, s[36:37] sc1\n\t"
    "s_waitcnt vmcnt(5) lgkmcnt(0)\n\t"
    "s_barrier\n\t"
    "ds_read_b64_tr_b16 a[240:241], v11 offset:0\n\t"
    "ds_read_b64_tr_b16 a[242:243], v11 offset:1024\n\t"
    "ds_read_b64_tr_b16 a[244:245], v12 offset:0\n\t"
    "ds_read_b64_tr_b16 a[246:247], v12 offset:1024\n\t"
    "ds_read_b64_tr_b16 a[248:249], v11 offset:2048\n\t"
    "ds_read_b64_tr_b16 a[250:251], v11 offset:3072\n\t"
    "ds_read_b64_tr_b16 a[252:253], v12 offset:2048\n\t"
    "ds_read_b64_tr_b16 a[254:255], v12 offset:3072\n\t"
    "s_waitcnt vmcnt(1) lgkmcnt(0)\n\t"
    "s_barrier\n\t"
    "ds_read_b64_tr_b16 v[224:225], v11 offset:16384\n\t"
    "ds_read_b64_tr_b16 v[226:227], v11 offset:17408\n\t"
    "ds_read_b64_tr_b16 v[228:229], v12 offset:16384\n\t"
    "ds_read_b64_tr_b16 v[230:231], v12 offset:17408\n\t"
    "ds_read_b64_tr_b16 v[232:233], v11 offset:18432\n\t"
    "ds_read_b64_tr_b16 v[234:235], v11 offset:19456\n\t"
    "ds_read_b64_tr_b16 v[236:237], v12 offset:18432\n\t"
    "ds_read_b64_tr_b16 v[238:239], v12 offset:19456\n\t"
    "s_branch L_wrj_%=\n\t"
    "L_idle_%=:\n\t"
    "s_bfe_u32 s93, s2, 0x20001\n\t"
    "s_mul_i32 s28, s93, 216\n\t"
    "s_add_u32 s28, s28, 864\n\t"
    "v_min_u32 v6, 215, v0\n\t"
    "v_add_u32 v6, s28, v6\n\t"
    "v_lshlrev_b32 v80, 4, v6\n\t"
    "v_add_u32 v80, 81920, v80\n\t"
    "v_add_u32 v81, 27648, v80\n\t"
    "v_add_u32 v82, 55296, v80\n\t"
    "v_add_u32 v83, 82944, v80\n\t"
    "v_add_u32 v84, 110592, v80\n\t"
    "v_add_u32 v85, 138240, v80\n\t"
    "v_add_u32 v86, 165888, v80\n\t"
    "v_add_u32 v87, 193536, v80\n\t"
    "v_add_u32 v88, 221184, v80\n\t"
    "v_add_u32 v89, 248832, v80\n\t"
    "v_add_u32 v90, 276480, v80\n\t"
    "v_add_u32 v91, 304128, v80\n\t"
    "s_waitcnt lgkmcnt(0)\n\t"
    "s_mov_b64 s[36:37], s[66:67]\n\t"
    "v_mov_b32 v4, 41943040\n\t"
    "global_load_dwordx2 v[2:3], v4, s[36:37] sc1\n\t"
    "s_lshl_b32 s28, s93, 8\n\t"
    "v_add_u32 v100, s28, v0\n\t"
    "v_min_u32 v101, 579, v100\n\t"
    "v_lshlrev_b32 v101, 7, v101\n\t"
    "v_add_u32 v101, 41947136, v101\n\t"
    "global_load_dword v104, v101, s[36:37] sc1\n\t"
    "v_min_u32 v102, 831, v100\n\t"
    "v_mov_b32 v103, 42024960\n\t"
    "v_mov_b32 v101, 42131456\n\t"
    "v_cmp_gt_u32 vcc, 576, v102\n\t"
    "s_nop 1\n\t"
    "v_cndmask_b32 v103, v101, v103, vcc\n\t"
    "v_lshl_add_u32 v102, v102, 7, v103\n\t"
    "global_load_dword v105, v102, s[36:37] sc1\n\t"
    "v_min_u32 v106, 719, v100\n\t"
    "v_lshlrev_b32 v107, 7, v106\n\t"
    "v_add_u32 v107, 2883584, v107\n\t"
    "v_lshlrev_b32 v108, 8, v106\n\t"
    "v_add_u32 v108, 48197760, v108\n\t"
    "v_cmp_gt_u32 vcc, 144, v106\n\t"
    "s_nop 1\n\t"
    "v_cndmask_b32 v107, v108, v107, vcc\n\t"
    "global_load_dword v109, v107, s[36:37] sc1\n\t"
    "s_mov_b64 s[4:5], s[56:57]\n\t"
    "s_mul_i32 s28, s70, 144\n\t"
    "s_mul_i32 s29, s68, 36\n\t"
    "s_add_u32 s28, s28, s29\n\t"
    "s_mul_i32 s28, s28, 9216\n\t"
    "s_add_u32 s30, s28, 25165824\n\t"
    "s_add_u32 s60, s36, s30\n\t"
    "s_addc_u32 s61, s37, 0\n\t"
    "s_mov_b32 s62, 331776\n\t"
    "s_mov_b32 s63, 0x20000\n\t"
    "s_mul_i32 s71, s3, 9216\n\t"
    "s_xor_b32 s28, s68, 0\n\t"
    "s_lshl_b32 s28, s28, 11\n\t"
    "v_add_u32 v198, s28, v15\n\t"
    "s_xor_b32 s28, s68, 1\n\t"
    "s_lshl_b32 s28, s28, 11\n\t"
    "v_add_u32 v199, s28, v15\n\t"
    "s_xor_b32 s28, s68, 2\n\t"
    "s_lshl_b32 s28, s28, 11\n\t"
    "v_add_u32 v200, s28, v15\n\t"
    "s_xor_b32 s28, s68, 3\n\t"
    "s_lshl_b32 s28, s28, 11\n\t"
    "v_add_u32 v201, s28, v15\n\t"
    "v_add_u32 v202, 8192, v15\n\t"
    "s_mul_i32 s74, s3, 4096\n\t"
    "s_mul_i32 s75, s3, 5120\n\t"
    "s_mul_i32 s76, s3, 18432\n\t"
    "s_lshl_b32 s28, s69, 2\n\t"
    "s_add_u32 s28, s28, s70\n\t"
    "s_lshl_b32 s28, s28, 4\n\t"
    "s_add_u32 s28, s28, s3\n\t"
    "s_mul_i32 s28, s28, 10240\n\t"
    "s_add_u32 s28, s28, 31457280\n\t"
    "s_add_u32 s56, s36, s28\n\t"
    "s_addc_u32 s57, s37, 0\n\t"
    "s_mov_b32 s58, 143360\n\t"
    "s_mov_b32 s59, 0x20000\n\t"
    "s_and_b32 s28, s69, 7\n\t"
    "s_lshl_b32 s28, s28, 2\n\t"
    "s_add_u32 s28, s28, s3\n\t"
    "s_mul_i32 s28, s28, 10368\n\t"
    "s_add_u32 s84, s60, s28\n\t"
    "s_addc_u32 s85, s61, 0\n\t"
    "s_mov_b32 s86, 10368\n\t"
    "s_mov_b32 s87, 0x20000\n\t"
    "s_mov_b32 s82, 4294959104\n\t"
    "v_lshlrev_b32 v214, 7, v1\n\t"
    "s_add_u32 s88, s36, 5373952\n\t"
    "s_addc_u32 s89, s37, 0\n\t"
    "s_mov_b32 s90, 18874368\n\t"
    "s_mov_b32 s91, 0x20000\n\t"
    "s_mul_i32 s28, s69, 147456\n\t"
    "s_mul_i32 s29, s68, 36864\n\t"
    "s_add_u32 s28, s28, s29\n\t"
    "s_lshl_b32 s29, s70, 2\n\t"
    "s_add_u32 s29, s29, s3\n\t"
    "s_mul_i32 s29, s29, 2304\n\t"
    "s_add_u32 s28, s28, s29\n\t"
    "s_sub_u32 s83, s28, 7077888\n\t"
    "v_min_u32 v10, 53, v1\n\t"
    "v_mul_u32_u24 v9, 3641, v10\n\t"
    "v_lshrrev_b32 v9, 16, v9\n\t"
    "v_mul_u32_u24 v14, 18, v9\n\t"
    "v_sub_u32 v10, v10, v14\n\t"
    "v_lshlrev_b32 v10, 7, v10\n\t"
    "v_mul_u32_u24 v14, 2359296, v9\n\t"
    "v_add_u32 v215, v14, v10\n\t"
    "s_waitcnt vmcnt(0)\n\t"
    "v_readfirstlane_b32 s66, v2\n\t"
    "v_readfirstlane_b32 s67, v3\n\t"
    "s_nop 0\n\t"
    "s_add_u32 s66, s66, 1\n\t"
    "s_add_u32 s67, s67, 1\n\t"
    "v_mov_b32 v116, -1\n\t"
    "v_mov_b32 v117, -1\n\t"
    "v_mov_b32 v118, -1\n\t"
    "v_mov_b32 v119, -1\n\t"
    "v_mov_b32 v70, s66\n\t"
    "v_mov_b32 v71, s67\n\t"
    "s_sleep 40\n\t"
    "global_load_dwordx4 v[16:19], v80, s[36:37] sc1\n\t"
    "global_load_dwordx4 v[20:23], v81, s[36:37] sc1\n\t"
    "global_load_dwordx4 v[24:27], v82, s[36:37] sc1\n\t"
    "global_load_dwordx4 v[28:31], v83, s[36:37] sc1\n\t"
    "global_load_dwordx4 v[32:35], v84, s[36:37] sc1\n\t"
    "global_load_dwordx4 v[36:39], v85, s[36:37] sc1\n\t"
    "global_load_dwordx4 v[40:43], v86, s[36:37] sc1\n\t"
    "global_load_dwordx4 v[44:47], v87, s[36:37] sc1\n\t"
    "global_load_dwordx4 v[48:51], v88, s[36:37] sc1\n\t"
    "global_load_dwordx4 v[52:55], v89, s[36:37] sc1\n\t"
    "global_load_dwordx4 v[56:59], v90, s[36:37] sc1\n\t"
    "global_load_dwordx4 v[60:63], v91, s[36:37] sc1\n\t"
    "s_waitcnt vmcnt(0)\n\t"
    "v_cmpx_eq_u32 vcc, s66, v17\n\t"
    "v_cmpx_eq_u32 vcc, s67, v19\n\t"
    "v_cmpx_eq_u32 vcc, s66, v21\n\t"
    "v_cmpx_eq_u32 vcc, s67, v23\n\t"
    "v_cmpx_eq_u32 vcc, s66, v25\n\t"
    "v_cmpx_eq_u32 vcc, s67, v27\n\t"
    "v_cmpx_eq_u32 vcc, s66, v29\n\t"
    "v_cmpx_eq_u32 vcc, s67, v31\n\t"
    "v_cmpx_eq_u32 vcc, s66, v33\n\t"
    "v_cmpx_eq_u32 vcc, s67, v35\n\t"
    "v_cmpx_eq_u32 vcc, s66, v37\n\t"
    "v_cmpx_eq_u32 vcc, s67, v39\n\t"
    "v_cmpx_eq_u32 vcc, s66, v41\n\t"
    "v_cmpx_eq_u32 vcc, s67, v43\n\t"
    "v_cmpx_eq_u32 vcc, s66, v45\n\t"
    "v_cmpx_eq_u32 vcc, s67, v47\n\t"
    "v_cmpx_eq_u32 vcc, s66, v49\n\t"
    "v_cmpx_eq_u32 vcc, s67, v51\n\t"
    "v_cmpx_eq_u32 vcc, s66, v53\n\t"
    "v_cmpx_eq_u32 vcc, s67, v55\n\t"
    "v_cmpx_eq_u32 vcc, s66, v57\n\t"
    "v_cmpx_eq_u32 vcc, s67, v59\n\t"
    "v_cmpx_eq_u32 vcc, s66, v61\n\t"
    "v_cmpx_eq_u32 vcc, s67, v63\n\t"
    "s_cmp_eq_u64 exec, -1\n\t"
    "s_mov_b64 exec, -1\n\t"
    "s_cbranch_scc0 L_gsf_%=\n\t"
    "L_gbf_%=:\n\t"
    "v_add_f32 v16, v16, v20\n\t"
    "v_add_f32 v18, v18, v22\n\t"
    "v_add_f32 v24, v24, v28\n\t"
    "v_add_f32 v26, v26, v30\n\t"
    "v_add_f32 v32, v32, v36\n\t"
    "v_add_f32 v34, v34, v38\n\t"
    "v_add_f32 v40, v40, v44\n\t"
    "v_add_f32 v42, v42, v46\n\t"
    "v_add_f32 v48, v48, v52\n\t"
    "v_add_f32 v50, v50, v54\n\t"
    "v_add_f32 v56, v56, v60\n\t"
    "v_add_f32 v58, v58, v62\n\t"
    "v_add_f32 v16, v16, v24\n\t"
    "v_add_f32 v18, v18, v26\n\t"
    "v_add_f32 v32, v32, v40\n\t"
    "v_add_f32 v34, v34, v42\n\t"
    "v_add_f32 v48, v48, v56\n\t"
    "v_add_f32 v50, v50, v58\n\t"
    "v_add_f32 v16, v16, v32\n\t"
    "v_add_f32 v18, v18, v34\n\t"
    "v_add_f32 v16, v16, v48\n\t"
    "v_add_f32 v18, v18, v50\n\t"
    "v_cvt_pk_bf16_f32 v100, v16, v18\n\t"
    "v_lshlrev_b32 v102, 16, v100\n\t"
    "v_and_b32 v103, 0xffff0000, v100\n\t"
    "v_lshlrev_b32 v101, 3, v6\n\t"
    "v_add_u32 v101, 0, v101\n\t"
    "global_store_dwordx2 v101, v[102:103], s[36:37] sc1\n\t"
    "v_add_f32 v104, 1.0, v102\n\t"
    "v_add_f32 v105, 1.0, v103\n\t"
    "v_cvt_pk_bf16_f32 v104, v104, v105\n\t"
    "v_subrev_u32 v7, 864, v6\n\t"
    "v_cmp_le_u32 vcc, 288, v7\n\t"
    "v_subrev_u32 v8, 288, v7\n\t"
    "s_nop 0\n\t"
    "v_cndmask_b32 v105, 8, 0, vcc\n\t"
    "v_cndmask_b32 v104, v100, v104, vcc\n\t"
    "v_cndmask_b32 v7, v7, v8, vcc\n\t"
    "v_cmp_le_u32 vcc, 288, v7\n\t"
    "v_subrev_u32 v8, 288, v7\n\t"
    "s_nop 0\n\t"
    "v_cndmask_b32 v105, v105, 16, vcc\n\t"
    "v_cndmask_b32 v7, v7, v8, vcc\n\t"
    "v_lshrrev_b32 v8, 1, v7\n\t"
    "v_lshl_add_u32 v105, v8, 7, v105\n\t"
    "v_and_b32 v7, 1, v7\n\t"
    "v_lshl_add_u32 v105, v7, 2, v105\n\t"
    "v_add_u32 v105, 2883584, v105\n\t"
    "global_store_dword v105, v104, s[36:37] sc1\n\t"
    "s_waitcnt vmcnt(0)\n\t"
    "s_barrier\n\t"
    "s_cmp_lg_u32 s3, 0\n\t"
    "s_cbranch_scc1 L_pfm_%=\n\t"
    "s_lshl_b32 s28, s93, 7\n\t"
    "s_add_u32 s28, s28, 42020864\n\t"
    "v_mov_b32 v88, s66\n\t"
    "v_mov_b32 v89, s67\n\t"
    "v_mov_b32 v90, s28\n\t"
    "s_mov_b64 exec, 1\n\t"
    "global_store_dwordx2 v90, v[88:89], s[36:37] sc1\n\t"
    "s_mov_b64 exec, -1\n\t"
    "L_pfm_%=:\n\t"
    "s_xor_b32 s28, s68, 1\n\t"
    "s_mul_i32 s28, s28, 40960\n\t"
    "s_add_u32 s28, s28, 0\n\t"
    "buffer_store_dwordx4 v[116:119], v15, s[56:59], s28 offen offset:0 sc1\n\t"
    "s_xor_b32 s28, s68, 1\n\t"
    "s_mul_i32 s28, s28, 40960\n\t"
    "s_add_u32 s28, s28, 0\n\t"
    "buffer_store_dwordx4 v[116:119], v15, s[56:59], s28 offen offset:1024 sc1\n\t"
    "s_xor_b32 s28, s68, 2\n\t"
    "s_mul_i32 s28, s28, 40960\n\t"
    "s_add_u32 s28, s28, 2048\n\t"
    "buffer_store_dwordx4 v[116:119], v15, s[56:59], s28 offen offset:0 sc1\n\t"
    "s_xor_b32 s28, s68, 2\n\t"
    "s_mul_i32 s28, s28, 40960\n\t"
    "s_add_u32 s28, s28, 2048\n\t"
    "buffer_store_dwordx4 v[116:119], v15, s[56:59], s28 offen offset:1024 sc1\n\t"
    "s_xor_b32 s28, s68, 3\n\t"
    "s_mul_i32 s28, s28, 40960\n\t"
    "s_add_u32 s28, s28, 4096\n\t"
    "buffer_store_dwordx4 v[116:119], v15, s[56:59], s28 offen offset:0 sc1\n\t"
    "s_xor_b32 s28, s68, 3\n\t"
    "s_mul_i32 s28, s28, 40960\n\t"
    "s_add_u32 s28, s28, 4096\n\t"
    "buffer_store_dwordx4 v[116:119], v15, s[56:59], s28 offen offset:1024 sc1\n\t"
    "s_xor_b32 s28, s68, s3\n\t"
    "s_cselect_b32 s28, s28, 4\n\t"
    "s_add_u32 s28, s28, 5\n\t"
    "s_lshl_b32 s28, s28, 10\n\t"
    "s_mul_i32 s29, s3, 40960\n\t"
    "s_add_u32 s28, s28, s29\n\t"
    "buffer_store_dwordx4 v[116:119], v15, s[56:59], s28 offen sc1\n\t"
    "s_waitcnt vmcnt(0)\n\t"
    "s_barrier\n\t"
    "s_lshl_b32 s28, s2, 7\n\t"
    "s_add_u32 s28, s28, 41947144\n\t"
    "s_cmp_lg_u32 s3, 0\n\t"
    "s_cbranch_scc1 L_pfy_%=\n\t"
    "v_mov_b32 v2, s66\n\t"
    "v_mov_b32 v3, s67\n\t"
    "v_mov_b32 v4, s28\n\t"
    "s_mov_b64 exec, 1\n\t"
    "global_store_dwordx2 v4, v[2:3], s[36:37] sc1\n\t"
    "s_mov_b64 exec, -1\n\t"
    "L_pfy_%=:\n\t"
    "v_min_u32 v126, 35, v1\n\t"
    "s_mul_i32 s28, s3, 4608\n\t"
    "s_add_u32 s28, s28, 41979904\n\t"
    "v_lshl_add_u32 v126, v126, 7, s28\n\t"
    "s_sleep 10\n\t"
    "s_mov_b32 s28, 0\n\t"
    "s_add_u32 s28, s28, 41979904\n\t"
    "v_mov_b32 v14, s28\n\t"
    "s_mov_b32 s35, 0\n\t"
    "L_tqi_%=:\n\t"
    "global_load_dword v8, v14, s[36:37] sc1\n\t"
    "s_add_u32 s35, s35, 1\n\t"
    "s_waitcnt vmcnt(0)\n\t"
    "v_cmp_ne_u32 vcc, s66, v8\n\t"
    "s_cbranch_vccz L_tbi_%=\n\t"
    "s_sleep 4\n\t"
    "s_cmp_lt_u32 s35, 0x8000\n\t"
    "s_cbranch_scc1 L_tqi_%=\n\t"
    "L_tbi_%=:\n\t"
    "s_mov_b32 s35, 0\n\t"
    "L_fqi_%=:\n\t"
    "global_load_dwordx2 v[124:125], v126, s[36:37] offset:0 sc1\n\t"
    "s_add_u32 s35, s35, 1\n\t"
    "s_waitcnt vmcnt(0)\n\t"
    "v_cmp_ne_u64 vcc, s[66:67], v[124:125]\n\t"
    "s_cbranch_vccz L_fbi_%=\n\t"
    "s_sleep 12\n\t"
    "s_cmp_lt_u32 s35, 0x10000\n\t"
    "s_cbranch_scc1 L_fqi_%=\n\t"
    "L_fbi_%=:\n\t"
    "s_barrier\n\t"
    "v_and_b32 v7, 7, v1\n\t"
    "v_lshrrev_b32 v8, 5, v1\n\t"
    "v_lshl_add_u32 v7, v7, 1, v8\n\t"
    "v_lshlrev_b32 v7, 4, v7\n\t"
    "s_add_u32 s12, s36, 2949120\n\t"
    "s_addc_u32 s13, s37, 0\n\t"
    "s_mov_b32 s14, 2359296\n\t"
    "s_mov_b32 s15, 0x20000\n\t"
    "s_lshl_b32 s33, s3, 2\n\t"
    "s_add_u32 s33, s33, s93\n\t"
    "s_add_u32 s33, s33, 32\n\t"
    "s_add_u32 s34, s33, 16\n\t"
    "s_mov_b32 s35, 0\n\t"
    "L_i0_%=:\n\t"
    "s_mul_i32 s28, s33, 18432\n\t"
    "buffer_load_dwordx4 v[16:19], v15, s[12:15], s28 offen offset:0 sc1\n\t"
    "buffer_load_dwordx4 v[20:23], v15, s[12:15], s28 offen offset:1024 sc1\n\t"
    "buffer_load_dwordx4 v[24:27], v15, s[12:15], s28 offen offset:2048 sc1\n\t"
    "buffer_load_dwordx4 v[28:31], v15, s[12:15], s28 offen offset:3072 sc1\n\t"
    "s_add_u32 s28, s28, 4096\n\t"
    "buffer_load_dwordx4 v[32:35], v15, s[12:15], s28 offen offset:0 sc1\n\t"
    "buffer_load_dwordx4 v[36:39], v15, s[12:15], s28 offen offset:1024 sc1\n\t"
    "buffer_load_dwordx4 v[40:43], v15, s[12:15], s28 offen offset:2048 sc1\n\t"
    "buffer_load_dwordx4 v[44:47], v15, s[12:15], s28 offen offset:3072 sc1\n\t"
    "s_add_u32 s28, s28, 4096\n\t"
    "buffer_load_dwordx4 v[48:51], v15, s[12:15], s28 offen offset:0 sc1\n\t"
    "buffer_load_dwordx4 v[52:55], v15, s[12:15], s28 offen offset:1024 sc1\n\t"
    "buffer_load_dwordx4 v[56:59], v15, s[12:15], s28 offen offset:2048 sc1\n\t"
    "buffer_load_dwordx4 v[60:63], v15, s[12:15], s28 offen offset:3072 sc1\n\t"
    "s_add_u32 s28, s28, 4096\n\t"
    "buffer_load_dwordx4 v[64:67], v15, s[12:15], s28 offen offset:0 sc1\n\t"
    "buffer_load_dwordx4 v[68:71], v15, s[12:15], s28 offen offset:1024 sc1\n\t"
    "buffer_load_dwordx4 v[72:75], v15, s[12:15], s28 offen offset:2048 sc1\n\t"
    "buffer_load_dwordx4 v[76:79], v15, s[12:15], s28 offen offset:3072 sc1\n\t"
    "s_add_u32 s28, s28, 4096\n\t"
    "buffer_load_dwordx4 v[80:83], v15, s[12:15], s28 offen offset:0 sc1\n\t"
    "buffer_load_dwordx4 v[84:87], v15, s[12:15], s28 offen offset:1024 sc1\n\t"
    "s_add_u32 s35, s35, 1\n\t"
    "s_waitcnt vmcnt(0)\n\t"
    "v_max_u32 v8, v16, v20\n\t"
    "v_max3_u32 v8, v24, v28, v8\n\t"
    "v_max3_u32 v8, v32, v36, v8\n\t"
    "v_max3_u32 v8, v40, v44, v8\n\t"
    "v_max3_u32 v8, v48, v52, v8\n\t"
    "v_max3_u32 v8, v56, v60, v8\n\t"
    "v_max3_u32 v8, v64, v68, v8\n\t"
    "v_max3_u32 v8, v72, v76, v8\n\t"
    "v_max3_u32 v8, v80, v84, v8\n\t"
    "v_cmp_eq_u32 vcc, -1, v8\n\t"
    "s_cbranch_vccz L_i1_%=\n\t"
    "s_cmp_lt_u32 s35, 0x8000\n\t"
    "s_cbranch_scc1 L_i0_%=\n\t"
    "L_i1_%=:\n\t"
    "s_mul_i32 s28, s34, 18432\n\t"
    "buffer_load_dwordx4 v[88:91], v15, s[12:15], s28 offen offset:0 sc1\n\t"
    "buffer_load_dwordx4 v[92:95], v15, s[12:15], s28 offen offset:1024 sc1\n\t"
    "buffer_load_dwordx4 v[96:99], v15, s[12:15], s28 offen offset:2048 sc1\n\t"
    "buffer_load_dwordx4 v[100:103], v15, s[12:15], s28 offen offset:3072 sc1\n\t"
    "s_add_u32 s28, s28, 4096\n\t"
    "buffer_load_dwordx4 v[104:107], v15, s[12:15], s28 offen offset:0 sc1\n\t"
    "buffer_load_dwordx4 v[108:111], v15, s[12:15], s28 offen offset:1024 sc1\n\t"
    "buffer_load_dwordx4 v[112:115], v15, s[12:15], s28 offen offset:2048 sc1\n\t"
    "buffer_load_dwordx4 v[116:119], v15, s[12:15], s28 offen offset:3072 sc1\n\t"
    "s_add_u32 s28, s28, 4096\n\t"
    "buffer_load_dwordx4 v[120:123], v15, s[12:15], s28 offen offset:0 sc1\n\t"
    "buffer_load_dwordx4 v[124:127], v15, s[12:15], s28 offen offset:1024 sc1\n\t"
    "buffer_load_dwordx4 v[128:131], v15, s[12:15], s28 offen offset:2048 sc1\n\t"
    "buffer_load_dwordx4 v[132:135], v15, s[12:15], s28 offen offset:3072 sc1\n\t"
    "s_add_u32 s28, s28, 4096\n\t"
    "buffer_load_dwordx4 v[136:139], v15, s[12:15], s28 offen offset:0 sc1\n\t"
    "buffer_load_dwordx4 v[140:143], v15, s[12:15], s28 offen offset:1024 sc1\n\t"
    "buffer_load_dwordx4 v[144:147], v15, s[12:15], s28 offen offset:2048 sc1\n\t"
    "buffer_load_dwordx4 v[148:151], v15, s[12:15], s28 offen offset:3072 sc1\n\t"
    "s_add_u32 s28, s28, 4096\n\t"
    "buffer_load_dwordx4 v[152:155], v15, s[12:15], s28 offen offset:0 sc1\n\t"
    "buffer_load_dwordx4 v[156:159], v15, s[12:15], s28 offen offset:1024 sc1\n\t"
    "s_add_u32 s34, s34, 16\n\t"
    "L_il_%=:\n\t"
    "s_waitcnt vmcnt(19)\n\t"
    "v_max_u32 v8, v16, v20\n\t"
    "v_max3_u32 v8, v24, v28, v8\n\t"
    "v_max3_u32 v8, v32, v36, v8\n\t"
    "v_max3_u32 v8, v40, v44, v8\n\t"
    "v_max3_u32 v8, v48, v52, v8\n\t"
    "v_max3_u32 v8, v56, v60, v8\n\t"
    "v_max3_u32 v8, v64, v68, v8\n\t"
    "v_max3_u32 v8, v72, v76, v8\n\t"
    "v_max3_u32 v8, v80, v84, v8\n\t"
    "v_cmp_eq_u32 vcc, -1, v8\n\t"
    "s_cbranch_vccnz L_is0_%=\n\t"
    "L_ib0_%=:\n\t"
    "v_mov_b32 v180, 0\n\t"
    "v_mov_b32 v181, 0\n\t"
    "v_mov_b32 v182, 0\n\t"
    "v_mov_b32 v183, 0\n\t"
    "s_mov_b32 s29, 0x3f803f80\n\t"
    "v_dot2c_f32_bf16 v180, s29, v16\n\t"
    "v_dot2c_f32_bf16 v181, v16, v16\n\t"
    "v_dot2c_f32_bf16 v182, s29, v18\n\t"
    "v_dot2c_f32_bf16 v183, v18, v18\n\t"
    "v_dot2c_f32_bf16 v180, s29, v17\n\t"
    "v_dot2c_f32_bf16 v181, v17, v17\n\t"
    "v_dot2c_f32_bf16 v182, s29, v19\n\t"
    "v_dot2c_f32_bf16 v183, v19, v19\n\t"
    "v_dot2c_f32_bf16 v180, s29, v20\n\t"
    "v_dot2c_f32_bf16 v181, v20, v20\n\t"
    "v_dot2c_f32_bf16 v182, s29, v22\n\t"
    "v_dot2c_f32_bf16 v183, v22, v22\n\t"
    "v_dot2c_f32_bf16 v180, s29, v21\n\t"
    "v_dot2c_f32_bf16 v181, v21, v21\n\t"
    "v_dot2c_f32_bf16 v182, s29, v23\n\t"
    "v_dot2c_f32_bf16 v183, v23, v23\n\t"
    "v_dot2c_f32_bf16 v180, s29, v24\n\t"
    "v_dot2c_f32_bf16 v181, v24, v24\n\t"
    "v_dot2c_f32_bf16 v182, s29, v26\n\t"
    "v_dot2c_f32_bf16 v183, v26, v26\n\t"
    "v_dot2c_f32_bf16 v180, s29, v25\n\t"
    "v_dot2c_f32_bf16 v181, v25, v25\n\t"
    "v_dot2c_f32_bf16 v182, s29, v27\n\t"
    "v_dot2c_f32_bf16 v183, v27, v27\n\t"
    "v_dot2c_f32_bf16 v180, s29, v28\n\t"
    "v_dot2c_f32_bf16 v181, v28, v28\n\t"
    "v_dot2c_f32_bf16 v182, s29, v30\n\t"
    "v_dot2c_f32_bf16 v183, v30, v30\n\t"
    "v_dot2c_f32_bf16 v180, s29, v29\n\t"
    "v_dot2c_f32_bf16 v181, v29, v29\n\t"
    "v_dot2c_f32_bf16 v182, s29, v31\n\t"
    "v_dot2c_f32_bf16 v183, v31, v31\n\t"
    "v_dot2c_f32_bf16 v180, s29, v32\n\t"
    "v_dot2c_f32_bf16 v181, v32, v32\n\t"
    "v_dot2c_f32_bf16 v182, s29, v34\n\t"
    "v_dot2c_f32_bf16 v183, v34, v34\n\t"
    "v_dot2c_f32_bf16 v180, s29, v33\n\t"
    "v_dot2c_f32_bf16 v181, v33, v33\n\t"
    "v_dot2c_f32_bf16 v182, s29, v35\n\t"
    "v_dot2c_f32_bf16 v183, v35, v35\n\t"
    "v_dot2c_f32_bf16 v180, s29, v36\n\t"
    "v_dot2c_f32_bf16 v181, v36, v36\n\t"
    "v_dot2c_f32_bf16 v182, s29, v38\n\t"
    "v_dot2c_f32_bf16 v183, v38, v38\n\t"
    "v_dot2c_f32_bf16 v180, s29, v37\n\t"
    "v_dot2c_f32_bf16 v181, v37, v37\n\t"
    "v_dot2c_f32_bf16 v182, s29, v39\n\t"
    "v_dot2c_f32_bf16 v183, v39, v39\n\t"
    "v_dot2c_f32_bf16 v180, s29, v40\n\t"
    "v_dot2c_f32_bf16 v181, v40, v40\n\t"
    "v_dot2c_f32_bf16 v182, s29, v42\n\t"
    "v_dot2c_f32_bf16 v183, v42, v42\n\t"
    "v_dot2c_f32_bf16 v180, s29, v41\n\t"
    "v_dot2c_f32_bf16 v181, v41, v41\n\t"
    "v_dot2c_f32_bf16 v182, s29, v43\n\t"
    "v_dot2c_f32_bf16 v183, v43, v43\n\t"
    "v_dot2c_f32_bf16 v180, s29, v44\n\t"
    "v_dot2c_f32_bf16 v181, v44, v44\n\t"
    "v_dot2c_f32_bf16 v182, s29, v46\n\t"
    "v_dot2c_f32_bf16 v183, v46, v46\n\t"
    "v_dot2c_f32_bf16 v180, s29, v45\n\t"
    "v_dot2c_f32_bf16 v181, v45, v45\n\t"
    "v_dot2c_f32_bf16 v182, s29, v47\n\t"
    "v_dot2c_f32_bf16 v183, v47, v47\n\t"
    "v_dot2c_f32_bf16 v180, s29, v48\n\t"
    "v_dot2c_f32_bf16 v181, v48, v48\n\t"
    "v_dot2c_f32_bf16 v182, s29, v50\n\t"
    "v_dot2c_f32_bf16 v183, v50, v50\n\t"
    "v_dot2c_f32_bf16 v180, s29, v49\n\t"
    "v_dot2c_f32_bf16 v181, v49, v49\n\t"
    "v_dot2c_f32_bf16 v182, s29, v51\n\t"
    "v_dot2c_f32_bf16 v183, v51, v51\n\t"
    "v_dot2c_f32_bf16 v180, s29, v52\n\t"
    "v_dot2c_f32_bf16 v181, v52, v52\n\t"
    "v_dot2c_f32_bf16 v182, s29, v54\n\t"
    "v_dot2c_f32_bf16 v183, v54, v54\n\t"
    "v_dot2c_f32_bf16 v180, s29, v53\n\t"
    "v_dot2c_f32_bf16 v181, v53, v53\n\t"
    "v_dot2c_f32_bf16 v182, s29, v55\n\t"
    "v_dot2c_f32_bf16 v183, v55, v55\n\t"
    "v_dot2c_f32_bf16 v180, s29, v56\n\t"
    "v_dot2c_f32_bf16 v181, v56, v56\n\t"
    "v_dot2c_f32_bf16 v182, s29, v58\n\t"
    "v_dot2c_f32_bf16 v183, v58, v58\n\t"
    "v_dot2c_f32_bf16 v180, s29, v57\n\t"
    "v_dot2c_f32_bf16 v181, v57, v57\n\t"
    "v_dot2c_f32_bf16 v182, s29, v59\n\t"
    "v_dot2c_f32_bf16 v183, v59, v59\n\t"
    "v_dot2c_f32_bf16 v180, s29, v60\n\t"
    "v_dot2c_f32_bf16 v181, v60, v60\n\t"
    "v_dot2c_f32_bf16 v182, s29, v62\n\t"
    "v_dot2c_f32_bf16 v183, v62, v62\n\t"
    "v_dot2c_f32_bf16 v180, s29, v61\n\t"
    "v_dot2c_f32_bf16 v181, v61, v61\n\t"
    "v_dot2c_f32_bf16 v182, s29, v63\n\t"
    "v_dot2c_f32_bf16 v183, v63, v63\n\t"
    "v_dot2c_f32_bf16 v180, s29, v64\n\t"
    "v_dot2c_f32_bf16 v181, v64, v64\n\t"
    "v_dot2c_f32_bf16 v182, s29, v66\n\t"
    "v_dot2c_f32_bf16 v183, v66, v66\n\t"
    "v_dot2c_f32_bf16 v180, s29, v65\n\t"
    "v_dot2c_f32_bf16 v181, v65, v65\n\t"
    "v_dot2c_f32_bf16 v182, s29, v67\n\t"
    "v_dot2c_f32_bf16 v183, v67, v67\n\t"
    "v_dot2c_f32_bf16 v180, s29, v68\n\t"
    "v_dot2c_f32_bf16 v181, v68, v68\n\t"
    "v_dot2c_f32_bf16 v182, s29, v70\n\t"
    "v_dot2c_f32_bf16 v183, v70, v70\n\t"
    "v_dot2c_f32_bf16 v180, s29, v69\n\t"
    "v_dot2c_f32_bf16 v181, v69, v69\n\t"
    "v_dot2c_f32_bf16 v182, s29, v71\n\t"
    "v_dot2c_f32_bf16 v183, v71, v71\n\t"
    "v_dot2c_f32_bf16 v180, s29, v72\n\t"
    "v_dot2c_f32_bf16 v181, v72, v72\n\t"
    "v_dot2c_f32_bf16 v182, s29, v74\n\t"
    "v_dot2c_f32_bf16 v183, v74, v74\n\t"
    "v_dot2c_f32_bf16 v180, s29, v73\n\t"
    "v_dot2c_f32_bf16 v181, v73, v73\n\t"
    "v_dot2c_f32_bf16 v182, s29, v75\n\t"
    "v_dot2c_f32_bf16 v183, v75, v75\n\t"
    "v_dot2c_f32_bf16 v180, s29, v76\n\t"
    "v_dot2c_f32_bf16 v181, v76, v76\n\t"
    "v_dot2c_f32_bf16 v182, s29, v78\n\t"
    "v_dot2c_f32_bf16 v183, v78, v78\n\t"
    "v_dot2c_f32_bf16 v180, s29, v77\n\t"
    "v_dot2c_f32_bf16 v181, v77, v77\n\t"
    "v_dot2c_f32_bf16 v182, s29, v79\n\t"
    "v_dot2c_f32_bf16 v183, v79, v79\n\t"
    "v_dot2c_f32_bf16 v180, s29, v80\n\t"
    "v_dot2c_f32_bf16 v181, v80, v80\n\t"
    "v_dot2c_f32_bf16 v182, s29, v82\n\t"
    "v_dot2c_f32_bf16 v183, v82, v82\n\t"
    "v_dot2c_f32_bf16 v180, s29, v81\n\t"
    "v_dot2c_f32_bf16 v181, v81, v81\n\t"
    "v_dot2c_f32_bf16 v182, s29, v83\n\t"
    "v_dot2c_f32_bf16 v183, v83, v83\n\t"
    "v_dot2c_f32_bf16 v180, s29, v84\n\t"
    "v_dot2c_f32_bf16 v181, v84, v84\n\t"
    "v_dot2c_f32_bf16 v182, s29, v86\n\t"
    "v_dot2c_f32_bf16 v183, v86, v86\n\t"
    "v_dot2c_f32_bf16 v180, s29, v85\n\t"
    "v_dot2c_f32_bf16 v181, v85, v85\n\t"
    "v_dot2c_f32_bf16 v182, s29, v87\n\t"
    "v_dot2c_f32_bf16 v183, v87, v87\n\t"
    "s_nop 1\n\t"
    "v_permlane32_swap_b32 v180, v182\n\t"
    "v_permlane32_swap_b32 v181, v183\n\t"
    "v_add_f32 v180, v180, v182\n\t"
    "v_add_f32 v181, v181, v183\n\t"
    "s_nop 1\n\t"
    "v_permlane16_swap_b32 v180, v181\n\t"
    "v_add_f32 v180, v180, v181\n\t"
    "s_nop 1\n\t"
    "v_add_f32_dpp v180, v180, v180 row_ror:8 row_mask:0xf bank_mask:0xf\n\t"
    "v_mov_b32 v181, v180\n\t"
    "s_nop 1\n\t"
    "v_permlane16_swap_b32 v180, v181\n\t"
    "v_mul_f32 v180, 0x3ae38e39, v180\n\t"
    "v_mul_f32 v184, v180, v180\n\t"
    "s_mov_b32 s28, 0x3ae38e39\n\t"
    "v_fma_f32 v181, v181, s28, -v184\n\t"
    "v_max_f32 v181, 0, v181\n\t"
    "v_add_f32 v181, 0x3727c5ac, v181\n\t"
    "v_rsq_f32 v186, v181\n\t"
    "v_mov_b32 v185, s66\n\t"
    "v_mov_b32 v187, s67\n\t"
    "v_mul_f32 v184, v186, v180\n\t"
    "s_lshl_b32 s28, s33, 8\n\t"
    "s_add_u32 s28, s28, 16384\n\t"
    "v_add_u32 v182, s28, v7\n\t"
    "s_cmp_lt_u32 s33, 128\n\t"
    "s_cselect_b32 exec_lo, 0xff, 0\n\t"
    "s_cselect_b32 exec_hi, 0xff, 0\n\t"
    "global_store_dwordx4 v182, v[184:187], s[36:37] sc1\n\t"
    "s_mov_b64 exec, -1\n\t"
    "s_add_u32 s33, s33, 16\n\t"
    "s_mul_i32 s28, s34, 18432\n\t"
    "buffer_load_dwordx4 v[16:19], v15, s[12:15], s28 offen offset:0 sc1\n\t"
    "buffer_load_dwordx4 v[20:23], v15, s[12:15], s28 offen offset:1024 sc1\n\t"
    "buffer_load_dwordx4 v[24:27], v15, s[12:15], s28 offen offset:2048 sc1\n\t"
    "buffer_load_dwordx4 v[28:31], v15, s[12:15], s28 offen offset:3072 sc1\n\t"
    "s_add_u32 s28, s28, 4096\n\t"
    "buffer_load_dwordx4 v[32:35], v15, s[12:15], s28 offen offset:0 sc1\n\t"
    "buffer_load_dwordx4 v[36:39], v15, s[12:15], s28 offen offset:1024 sc1\n\t"
    "buffer_load_dwordx4 v[40:43], v15, s[12:15], s28 offen offset:2048 sc1\n\t"
    "buffer_load_dwordx4 v[44:47], v15, s[12:15], s28 offen offset:3072 sc1\n\t"
    "s_add_u32 s28, s28, 4096\n\t"
    "buffer_load_dwordx4 v[48:51], v15, s[12:15], s28 offen offset:0 sc1\n\t"
    "buffer_load_dwordx4 v[52:55], v15, s[12:15], s28 offen offset:1024 sc1\n\t"
    "buffer_load_dwordx4 v[56:59], v15, s[12:15], s28 offen offset:2048 sc1\n\t"
    "buffer_load_dwordx4 v[60:63], v15, s[12:15], s28 offen offset:3072 sc1\n\t"
    "s_add_u32 s28, s28, 4096\n\t"
    "buffer_load_dwordx4 v[64:67], v15, s[12:15], s28 offen offset:0 sc1\n\t"
    "buffer_load_dwordx4 v[68:71], v15, s[12:15], s28 offen offset:1024 sc1\n\t"
    "buffer_load_dwordx4 v[72:75], v15, s[12:15], s28 offen offset:2048 sc1\n\t"
    "buffer_load_dwordx4 v[76:79], v15, s[12:15], s28 offen offset:3072 sc1\n\t"
    "s_add_u32 s28, s28, 4096\n\t"
    "buffer_load_dwordx4 v[80:83], v15, s[12:15], s28 offen offset:0 sc1\n\t"
    "buffer_load_dwordx4 v[84:87], v15, s[12:15], s28 offen offset:1024 sc1\n\t"
    "s_add_u32 s34, s34, 16\n\t"
    "s_waitcnt vmcnt(19)\n\t"
    "v_max_u32 v8, v88, v92\n\t"
    "v_max3_u32 v8, v96, v100, v8\n\t"
    "v_max3_u32 v8, v104, v108, v8\n\t"
    "v_max3_u32 v8, v112, v116, v8\n\t"
    "v_max3_u32 v8, v120, v124, v8\n\t"
    "v_max3_u32 v8, v128, v132, v8\n\t"
    "v_max3_u32 v8, v136, v140, v8\n\t"
    "v_max3_u32 v8, v144, v148, v8\n\t"
    "v_max3_u32 v8, v152, v156, v8\n\t"
    "v_cmp_eq_u32 vcc, -1, v8\n\t"
    "s_cbranch_vccnz L_is1_%=\n\t"
    "L_ib1_%=:\n\t"
    "v_mov_b32 v180, 0\n\t"
    "v_mov_b32 v181, 0\n\t"
    "v_mov_b32 v182, 0\n\t"
    "v_mov_b32 v183, 0\n\t"
    "s_mov_b32 s29, 0x3f803f80\n\t"
    "v_dot2c_f32_bf16 v180, s29, v88\n\t"
    "v_dot2c_f32_bf16 v181, v88, v88\n\t"
    "v_dot2c_f32_bf16 v182, s29, v90\n\t"
    "v_dot2c_f32_bf16 v183, v90, v90\n\t"
    "v_dot2c_f32_bf16 v180, s29, v89\n\t"
    "v_dot2c_f32_bf16 v181, v89, v89\n\t"
    "v_dot2c_f32_bf16 v182, s29, v91\n\t"
    "v_dot2c_f32_bf16 v183, v91, v91\n\t"
    "v_dot2c_f32_bf16 v180, s29, v92\n\t"
    "v_dot2c_f32_bf16 v181, v92, v92\n\t"
    "v_dot2c_f32_bf16 v182, s29, v94\n\t"
    "v_dot2c_f32_bf16 v183, v94, v94\n\t"
    "v_dot2c_f32_bf16 v180, s29, v93\n\t"
    "v_dot2c_f32_bf16 v181, v93, v93\n\t"
    "v_dot2c_f32_bf16 v182, s29, v95\n\t"
    "v_dot2c_f32_bf16 v183, v95, v95\n\t"
    "v_dot2c_f32_bf16 v180, s29, v96\n\t"
    "v_dot2c_f32_bf16 v181, v96, v96\n\t"
    "v_dot2c_f32_bf16 v182, s29, v98\n\t"
    "v_dot2c_f32_bf16 v183, v98, v98\n\t"
    "v_dot2c_f32_bf16 v180, s29, v97\n\t"
    "v_dot2c_f32_bf16 v181, v97, v97\n\t"
    "v_dot2c_f32_bf16 v182, s29, v99\n\t"
    "v_dot2c_f32_bf16 v183, v99, v99\n\t"
    "v_dot2c_f32_bf16 v180, s29, v100\n\t"
    "v_dot2c_f32_bf16 v181, v100, v100\n\t"
    "v_dot2c_f32_bf16 v182, s29, v102\n\t"
    "v_dot2c_f32_bf16 v183, v102, v102\n\t"
    "v_dot2c_f32_bf16 v180, s29, v101\n\t"
    "v_dot2c_f32_bf16 v181, v101, v101\n\t"
    "v_dot2c_f32_bf16 v182, s29, v103\n\t"
    "v_dot2c_f32_bf16 v183, v103, v103\n\t"
    "v_dot2c_f32_bf16 v180, s29, v104\n\t"
    "v_dot2c_f32_bf16 v181, v104, v104\n\t"
    "v_dot2c_f32_bf16 v182, s29, v106\n\t"
    "v_dot2c_f32_bf16 v183, v106, v106\n\t"
    "v_dot2c_f32_bf16 v180, s29, v105\n\t"
    "v_dot2c_f32_bf16 v181, v105, v105\n\t"
    "v_dot2c_f32_bf16 v182, s29, v107\n\t"
    "v_dot2c_f32_bf16 v183, v107, v107\n\t"
    "v_dot2c_f32_bf16 v180, s29, v108\n\t"
    "v_dot2c_f32_bf16 v181, v108, v108\n\t"
    "v_dot2c_f32_bf16 v182, s29, v110\n\t"
    "v_dot2c_f32_bf16 v183, v110, v110\n\t"
    "v_dot2c_f32_bf16 v180, s29, v109\n\t"
    "v_dot2c_f32_bf16 v181, v109, v109\n\t"
    "v_dot2c_f32_bf16 v182, s29, v111\n\t"
    "v_dot2c_f32_bf16 v183, v111, v111\n\t"
    "v_dot2c_f32_bf16 v180, s29, v112\n\t"
    "v_dot2c_f32_bf16 v181, v112, v112\n\t"
    "v_dot2c_f32_bf16 v182, s29, v114\n\t"
    "v_dot2c_f32_bf16 v183, v114, v114\n\t"
    "v_dot2c_f32_bf16 v180, s29, v113\n\t"
    "v_dot2c_f32_bf16 v181, v113, v113\n\t"
    "v_dot2c_f32_bf16 v182, s29, v115\n\t"
    "v_dot2c_f32_bf16 v183, v115, v115\n\t"
    "v_dot2c_f32_bf16 v180, s29, v116\n\t"
    "v_dot2c_f32_bf16 v181, v116, v116\n\t"
    "v_dot2c_f32_bf16 v182, s29, v118\n\t"
    "v_dot2c_f32_bf16 v183, v118, v118\n\t"
    "v_dot2c_f32_bf16 v180, s29, v117\n\t"
    "v_dot2c_f32_bf16 v181, v117, v117\n\t"
    "v_dot2c_f32_bf16 v182, s29, v119\n\t"
    "v_dot2c_f32_bf16 v183, v119, v119\n\t"
    "v_dot2c_f32_bf16 v180, s29, v120\n\t"
    "v_dot2c_f32_bf16 v181, v120, v120\n\t"
    "v_dot2c_f32_bf16 v182, s29, v122\n\t"
    "v_dot2c_f32_bf16 v183, v122, v122\n\t"
    "v_dot2c_f32_bf16 v180, s29, v121\n\t"
    "v_dot2c_f32_bf16 v181, v121, v121\n\t"
    "v_dot2c_f32_bf16 v182, s29, v123\n\t"
    "v_dot2c_f32_bf16 v183, v123, v123\n\t"
    "v_dot2c_f32_bf16 v180, s29, v124\n\t"
    "v_dot2c_f32_bf16 v181, v124, v124\n\t"
    "v_dot2c_f32_bf16 v182, s29, v126\n\t"
    "v_dot2c_f32_bf16 v183, v126, v126\n\t"
    "v_dot2c_f32_bf16 v180, s29, v125\n\t"
    "v_dot2c_f32_bf16 v181, v125, v125\n\t"
    "v_dot2c_f32_bf16 v182, s29, v127\n\t"
    "v_dot2c_f32_bf16 v183, v127, v127\n\t"
    "v_dot2c_f32_bf16 v180, s29, v128\n\t"
    "v_dot2c_f32_bf16 v181, v128, v128\n\t"
    "v_dot2c_f32_bf16 v182, s29, v130\n\t"
    "v_dot2c_f32_bf16 v183, v130, v130\n\t"
    "v_dot2c_f32_bf16 v180, s29, v129\n\t"
    "v_dot2c_f32_bf16 v181, v129, v129\n\t"
    "v_dot2c_f32_bf16 v182, s29, v131\n\t"
    "v_dot2c_f32_bf16 v183, v131, v131\n\t"
    "v_dot2c_f32_bf16 v180, s29, v132\n\t"
    "v_dot2c_f32_bf16 v181, v132, v132\n\t"
    "v_dot2c_f32_bf16 v182, s29, v134\n\t"
    "v_dot2c_f32_bf16 v183, v134, v134\n\t"
    "v_dot2c_f32_bf16 v180, s29, v133\n\t"
    "v_dot2c_f32_bf16 v181, v133, v133\n\t"
    "v_dot2c_f32_bf16 v182, s29, v135\n\t"
    "v_dot2c_f32_bf16 v183, v135, v135\n\t"
    "v_dot2c_f32_bf16 v180, s29, v136\n\t"
    "v_dot2c_f32_bf16 v181, v136, v136\n\t"
    "v_dot2c_f32_bf16 v182, s29, v138\n\t"
    "v_dot2c_f32_bf16 v183, v138, v138\n\t"
    "v_dot2c_f32_bf16 v180, s29, v137\n\t"
    "v_dot2c_f32_bf16 v181, v137, v137\n\t"
    "v_dot2c_f32_bf16 v182, s29, v139\n\t"
    "v_dot2c_f32_bf16 v183, v139, v139\n\t"
    "v_dot2c_f32_bf16 v180, s29, v140\n\t"
    "v_dot2c_f32_bf16 v181, v140, v140\n\t"
    "v_dot2c_f32_bf16 v182, s29, v142\n\t"
    "v_dot2c_f32_bf16 v183, v142, v142\n\t"
    "v_dot2c_f32_bf16 v180, s29, v141\n\t"
    "v_dot2c_f32_bf16 v181, v141, v141\n\t"
    "v_dot2c_f32_bf16 v182, s29, v143\n\t"
    "v_dot2c_f32_bf16 v183, v143, v143\n\t"
    "v_dot2c_f32_bf16 v180, s29, v144\n\t"
    "v_dot2c_f32_bf16 v181, v144, v144\n\t"
    "v_dot2c_f32_bf16 v182, s29, v146\n\t"
    "v_dot2c_f32_bf16 v183, v146, v146\n\t"
    "v_dot2c_f32_bf16 v180, s29, v145\n\t"
    "v_dot2c_f32_bf16 v181, v145, v145\n\t"
    "v_dot2c_f32_bf16 v182, s29, v147\n\t"
    "v_dot2c_f32_bf16 v183, v147, v147\n\t"
    "v_dot2c_f32_bf16 v180, s29, v148\n\t"
    "v_dot2c_f32_bf16 v181, v148, v148\n\t"
    "v_dot2c_f32_bf16 v182, s29, v150\n\t"
    "v_dot2c_f32_bf16 v183, v150, v150\n\t"
    "v_dot2c_f32_bf16 v180, s29, v149\n\t"
    "v_dot2c_f32_bf16 v181, v149, v149\n\t"
    "v_dot2c_f32_bf16 v182, s29, v151\n\t"
    "v_dot2c_f32_bf16 v183, v151, v151\n\t"
    "v_dot2c_f32_bf16 v180, s29, v152\n\t"
    "v_dot2c_f32_bf16 v181, v152, v152\n\t"
    "v_dot2c_f32_bf16 v182, s29, v154\n\t"
    "v_dot2c_f32_bf16 v183, v154, v154\n\t"
    "v_dot2c_f32_bf16 v180, s29, v153\n\t"
    "v_dot2c_f32_bf16 v181, v153, v153\n\t"
    "v_dot2c_f32_bf16 v182, s29, v155\n\t"
    "v_dot2c_f32_bf16 v183, v155, v155\n\t"
    "v_dot2c_f32_bf16 v180, s29, v156\n\t"
    "v_dot2c_f32_bf16 v181, v156, v156\n\t"
    "v_dot2c_f32_bf16 v182, s29, v158\n\t"
    "v_dot2c_f32_bf16 v183, v158, v158\n\t"
    "v_dot2c_f32_bf16 v180, s29, v157\n\t"
    "v_dot2c_f32_bf16 v181, v157, v157\n\t"
    "v_dot2c_f32_bf16 v182, s29, v159\n\t"
    "v_dot2c_f32_bf16 v183, v159, v159\n\t"
    "s_nop 1\n\t"
    "v_permlane32_swap_b32 v180, v182\n\t"
    "v_permlane32_swap_b32 v181, v183\n\t"
    "v_add_f32 v180, v180, v182\n\t"
    "v_add_f32 v181, v181, v183\n\t"
    "s_nop 1\n\t"
    "v_permlane16_swap_b32 v180, v181\n\t"
    "v_add_f32 v180, v180, v181\n\t"
    "s_nop 1\n\t"
    "v_add_f32_dpp v180, v180, v180 row_ror:8 row_mask:0xf bank_mask:0xf\n\t"
    "v_mov_b32 v181, v180\n\t"
    "s_nop 1\n\t"
    "v_permlane16_swap_b32 v180, v181\n\t"
    "v_mul_f32 v180, 0x3ae38e39, v180\n\t"
    "v_mul_f32 v184, v180, v180\n\t"
    "s_mov_b32 s28, 0x3ae38e39\n\t"
    "v_fma_f32 v181, v181, s28, -v184\n\t"
    "v_max_f32 v181, 0, v181\n\t"
    "v_add_f32 v181, 0x3727c5ac, v181\n\t"
    "v_rsq_f32 v186, v181\n\t"
    "v_mov_b32 v185, s66\n\t"
    "v_mov_b32 v187, s67\n\t"
    "v_mul_f32 v184, v186, v180\n\t"
    "s_lshl_b32 s28, s33, 8\n\t"
    "s_add_u32 s28, s28, 16384\n\t"
    "v_add_u32 v182, s28, v7\n\t"
    "s_cmp_lt_u32 s33, 128\n\t"
    "s_cselect_b32 exec_lo, 0xff, 0\n\t"
    "s_cselect_b32 exec_hi, 0xff, 0\n\t"
    "global_store_dwordx4 v182, v[184:187], s[36:37] sc1\n\t"
    "s_mov_b64 exec, -1\n\t"
    "s_add_u32 s33, s33, 16\n\t"
    "s_mul_i32 s28, s34, 18432\n\t"
    "buffer_load_dwordx4 v[88:91], v15, s[12:15], s28 offen offset:0 sc1\n\t"
    "buffer_load_dwordx4 v[92:95], v15, s[12:15], s28 offen offset:1024 sc1\n\t"
    "buffer_load_dwordx4 v[96:99], v15, s[12:15], s28 offen offset:2048 sc1\n\t"
    "buffer_load_dwordx4 v[100:103], v15, s[12:15], s28 offen offset:3072 sc1\n\t"
    "s_add_u32 s28, s28, 4096\n\t"
    "buffer_load_dwordx4 v[104:107], v15, s[12:15], s28 offen offset:0 sc1\n\t"
    "buffer_load_dwordx4 v[108:111], v15, s[12:15], s28 offen offset:1024 sc1\n\t"
    "buffer_load_dwordx4 v[112:115], v15, s[12:15], s28 offen offset:2048 sc1\n\t"
    "buffer_load_dwordx4 v[116:119], v15, s[12:15], s28 offen offset:3072 sc1\n\t"
    "s_add_u32 s28, s28, 4096\n\t"
    "buffer_load_dwordx4 v[120:123], v15, s[12:15], s28 offen offset:0 sc1\n\t"
    "buffer_load_dwordx4 v[124:127], v15, s[12:15], s28 offen offset:1024 sc1\n\t"
    "buffer_load_dwordx4 v[128:131], v15, s[12:15], s28 offen offset:2048 sc1\n\t"
    "buffer_load_dwordx4 v[132:135], v15, s[12:15], s28 offen offset:3072 sc1\n\t"
    "s_add_u32 s28, s28, 4096\n\t"
    "buffer_load_dwordx4 v[136:139], v15, s[12:15], s28 offen offset:0 sc1\n\t"
    "buffer_load_dwordx4 v[140:143], v15, s[12:15], s28 offen offset:1024 sc1\n\t"
    "buffer_load_dwordx4 v[144:147], v15, s[12:15], s28 offen offset:2048 sc1\n\t"
    "buffer_load_dwordx4 v[148:151], v15, s[12:15], s28 offen offset:3072 sc1\n\t"
    "s_add_u32 s28, s28, 4096\n\t"
    "buffer_load_dwordx4 v[152:155], v15, s[12:15], s28 offen offset:0 sc1\n\t"
    "buffer_load_dwordx4 v[156:159], v15, s[12:15], s28 offen offset:1024 sc1\n\t"
    "s_add_u32 s34, s34, 16\n\t"
    "s_cmp_lt_u32 s33, 128\n\t"
    "s_cbranch_scc1 L_il_%=\n\t"
    "s_waitcnt vmcnt(0)\n\t"
    "v_add_u32 v204, 65536, v15\n\t"
    "v_add_u32 v205, 131072, v15\n\t"
    "s_lshl_b32 s29, s3, 4\n\t"
    "s_add_u32 s30, s69, s29\n\t"
    "s_mul_i32 s28, s30, 144\n\t"
    "s_mul_i32 s29, s68, 36\n\t"
    "s_add_u32 s28, s28, s29\n\t"
    "s_lshl_b32 s28, s28, 10\n\t"
    "s_add_u32 s28, s28, 5373952\n\t"
    "s_add_u32 s4, s36, s28\n\t"
    "s_addc_u32 s5, s37, 0\n\t"
    "s_mov_b32 s6, 9474048\n\t"
    "s_mov_b32 s7, 0x20000\n\t"
    "v_mov_b32 v196, v15\n\t"
    "v_add_u32 v197, 9437184, v15\n\t"
    "v_min_u32 v4, 17, v1\n\t"
    "v_and_b32 v2, 1, v4\n\t"
    "v_lshlrev_b32 v2, 6, v2\n\t"
    "v_add_u32 v2, s30, v2\n\t"
    "v_mul_u32_u24 v3, 37450, v2\n\t"
    "v_lshrrev_b32 v3, 18, v3\n\t"
    "v_mul_u32_u24 v3, 7, v3\n\t"
    "v_sub_u32 v2, v2, v3\n\t"
    "v_lshrrev_b32 v3, 1, v4\n\t"
    "v_mad_u32_u24 v2, v3, 7, v2\n\t"
    "v_lshrrev_b32 v3, 5, v2\n\t"
    "v_and_b32 v2, 31, v2\n\t"
    "v_lshl_add_u32 v2, v2, 3, v3\n\t"
    "s_lshl_b32 s28, s68, 1\n\t"
    "v_add_u32 v2, s28, v2\n\t"
    "v_lshlrev_b32 v2, 7, v2\n\t"
    "v_add_u32 v2, 41947136, v2\n\t"
    "s_mul_i32 s28, s70, 144\n\t"
    "s_mul_i32 s29, s68, 36\n\t"
    "s_add_u32 s28, s28, s29\n\t"
    "v_add_u32 v3, s28, v1\n\t"
    "v_mov_b32_dpp v216, v2 quad_perm:[0,0,2,2] row_mask:0xf bank_mask:0xf\n\t"
    "v_mov_b32_dpp v213, v2 quad_perm:[1,1,3,3] row_mask:0xf bank_mask:0xf\n\t"
    "v_and_b32 v4, 3, v1\n\t"
    "v_xor_b32 v4, s2, v4\n\t"
    "v_lshlrev_b32 v4, 7, v4\n\t"
    "v_add_u32 v4, 41947144, v4\n\t"
    "v_lshlrev_b32 v3, 7, v3\n\t"
    "v_cmp_gt_u32 vcc, 36, v1\n\t"
    "v_add_u32 v3, 42024960, v3\n\t"
    "v_add_u32 v216, 8, v216\n\t"
    "s_nop 1\n\t"
    "v_cndmask_b32 v218, v4, v3, vcc\n\t"
    "s_lshl_b32 s28, s2, 7\n\t"
    "s_add_u32 s28, s28, 41947144\n\t"
    "v_writelane_b32 v63, s28, 1\n\t"
    "s_cmp_eq_u32 s3, 0\n\t"
    "s_cselect_b32 s28, 2, 0\n\t"
    "s_or_b32 s80, s80, s28\n\t"
    "v_and_b32 v2, 15, v1\n\t"
    "v_lshrrev_b32 v3, 4, v1\n\t"
    "s_mul_i32 s28, s70, 144\n\t"
    "s_lshl_b32 s29, s68, 5\n\t"
    "s_add_u32 s29, s29, s28\n\t"
    "s_add_u32 s28, s28, 128\n\t"
    "s_and_b32 s33, s70, 1\n\t"
    "s_lshl_b32 s34, s33, 4\n\t"
    "s_add_u32 s29, s29, s34\n\t"
    "s_lshl_b32 s34, s33, 7\n\t"
    "s_sub_u32 s28, s28, s34\n\t"
    "s_mul_i32 s31, s30, 18432\n\t"
    "s_lshl_b32 s34, s29, 5\n\t"
    "s_add_u32 s33, s31, s34\n\t"
    "s_add_u32 s33, s33, 2949120\n\t"
    "v_lshlrev_b32 v4, 8, v3\n\t"
    "v_lshl_add_u32 v4, v2, 3, v4\n\t"
    "v_add_u32 v207, s33, v4\n\t"
    "s_lshl_b32 s34, s29, 1\n\t"
    "s_add_u32 s33, s31, s34\n\t"
    "v_mul_u32_u24 v4, 1152, v2\n\t"
    "v_lshl_add_u32 v5, v3, 4, v4\n\t"
    "v_add_u32 v208, s33, v5\n\t"
    "s_lshl_b32 s33, s29, 2\n\t"
    "s_add_u32 s33, s33, 11520\n\t"
    "v_lshlrev_b32 v5, 5, v3\n\t"
    "v_add_u32 v209, s33, v5\n\t"
    "s_lshl_b32 s34, s28, 5\n\t"
    "s_add_u32 s33, s31, s34\n\t"
    "s_add_u32 s33, s33, 2949120\n\t"
    "v_lshlrev_b32 v5, 7, v3\n\t"
    "v_lshl_add_u32 v5, v2, 3, v5\n\t"
    "v_add_u32 v210, s33, v5\n\t"
    "s_lshl_b32 s34, s28, 1\n\t"
    "s_add_u32 s33, s31, s34\n\t"
    "v_lshl_add_u32 v5, v3, 3, v4\n\t"
    "v_add_u32 v211, s33, v5\n\t"
    "s_lshl_b32 s33, s28, 2\n\t"
    "s_add_u32 s33, s33, 11520\n\t"
    "v_lshlrev_b32 v5, 4, v3\n\t"
    "v_add_u32 v212, s33, v5\n\t"
    "s_cmp_eq_u32 s68, s3\n\t"
    "s_cselect_b32 s77, -1, 0\n\t"
    "s_sleep 40\n\t"
    "global_load_dwordx2 v[220:221], v216, s[36:37] sc1\n\t"
    "s_waitcnt vmcnt(0)\n\t"
    "v_cmp_ne_u64 vcc, s[66:67], v[220:221]\n\t"
    "s_cbranch_vccnz L_psj_%=\n\t"
    "L_pbj_%=:\n\t"
    "global_load_dwordx2 v[220:221], v218, s[36:37] sc1\n\t"
    "s_waitcnt vmcnt(0)\n\t"
    "v_cmp_ne_u64 vcc, s[66:67], v[220:221]\n\t"
    "s_cbranch_vccnz L_psk_%=\n\t"
    "L_pbk_%=:\n\t"
    "s_add_u32 m0, s75, 122880\n\t"
    "s_add_u32 s72, s71, 0\n\t"
    "buffer_load_dwordx4 v199, s[60:63], s72 offen offset:0 sc1 lds\n\t"
    "buffer_load_dwordx4 v199, s[60:63], s72 offen offset:1024 sc1 lds\n\t"
    "s_add_u32 m0, s75, 124928\n\t"
    "s_nop 0\n\t"
    "buffer_load_dwordx4 v200, s[60:63], s72 offen offset:0 sc1 lds\n\t"
    "buffer_load_dwordx4 v200, s[60:63], s72 offen offset:1024 sc1 lds\n\t"
    "s_add_u32 m0, s75, 126976\n\t"
    "s_nop 0\n\t"
    "buffer_load_dwordx4 v202, s[60:63], s72 offen offset:0 sc1 lds\n\t"
    "s_add_u32 m0, s75, 143360\n\t"
    "s_add_u32 s72, s71, 36864\n\t"
    "buffer_load_dwordx4 v199, s[60:63], s72 offen offset:0 sc1 lds\n\t"
    "buffer_load_dwordx4 v199, s[60:63], s72 offen offset:1024 sc1 lds\n\t"
    "s_add_u32 m0, s75, 145408\n\t"
    "s_nop 0\n\t"
    "buffer_load_dwordx4 v200, s[60:63], s72 offen offset:0 sc1 lds\n\t"
    "buffer_load_dwordx4 v200, s[60:63], s72 offen offset:1024 sc1 lds\n\t"
    "s_add_u32 m0, s75, 147456\n\t"
    "s_nop 0\n\t"
    "buffer_load_dwordx4 v202, s[60:63], s72 offen offset:0 sc1 lds\n\t"
    "s_add_u32 m0, s76, 0\n\t"
    "s_add_u32 s72, s71, 0\n\t"
    "buffer_load_dwordx4 v201, s[60:63], s72 offen offset:0 sc1 lds\n\t"
    "buffer_load_dwordx4 v201, s[60:63], s72 offen offset:1024 sc1 lds\n\t"
    "s_add_u32 m0, s76, 2048\n\t"
    "s_add_u32 s72, s71, 36864\n\t"
    "buffer_load_dwordx4 v201, s[60:63], s72 offen offset:0 sc1 lds\n\t"
    "buffer_load_dwordx4 v201, s[60:63], s72 offen offset:1024 sc1 lds\n\t"
    "s_add_u32 m0, s76, 4096\n\t"
    "s_add_u32 s72, s71, 73728\n\t"
    "buffer_load_dwordx4 v201, s[60:63], s72 offen offset:0 sc1 lds\n\t"
    "buffer_load_dwordx4 v201, s[60:63], s72 offen offset:1024 sc1 lds\n\t"
    "s_add_u32 m0, s76, 6144\n\t"
    "s_add_u32 s72, s71, 110592\n\t"
    "buffer_load_dwordx4 v201, s[60:63], s72 offen offset:0 sc1 lds\n\t"
    "buffer_load_dwordx4 v201, s[60:63], s72 offen offset:1024 sc1 lds\n\t"
    "s_add_u32 m0, s76, 8192\n\t"
    "s_add_u32 s72, s71, 147456\n\t"
    "buffer_load_dwordx4 v201, s[60:63], s72 offen offset:0 sc1 lds\n\t"
    "buffer_load_dwordx4 v201, s[60:63], s72 offen offset:1024 sc1 lds\n\t"
    "s_add_u32 m0, s76, 10240\n\t"
    "s_add_u32 s72, s71, 184320\n\t"
    "buffer_load_dwordx4 v201, s[60:63], s72 offen offset:0 sc1 lds\n\t"
    "buffer_load_dwordx4 v201, s[60:63], s72 offen offset:1024 sc1 lds\n\t"
    "s_add_u32 m0, s76, 12288\n\t"
    "s_add_u32 s72, s71, 221184\n\t"
    "buffer_load_dwordx4 v201, s[60:63], s72 offen offset:0 sc1 lds\n\t"
    "buffer_load_dwordx4 v201, s[60:63], s72 offen offset:1024 sc1 lds\n\t"
    "s_add_u32 m0, s76, 14336\n\t"
    "s_add_u32 s72, s71, 258048\n\t"
    "buffer_load_dwordx4 v201, s[60:63], s72 offen offset:0 sc1 lds\n\t"
    "buffer_load_dwordx4 v201, s[60:63], s72 offen offset:1024 sc1 lds\n\t"
    "s_add_u32 m0, s76, 16384\n\t"
    "s_add_u32 s72, s71, 294912\n\t"
    "buffer_load_dwordx4 v201, s[60:63], s72 offen offset:0 sc1 lds\n\t"
    "buffer_load_dwordx4 v201, s[60:63], s72 offen offset:1024 sc1 lds\n\t"
    "s_mov_b32 s73, 0\n\t"
    "buffer_load_dwordx4 a[0:3], v196, s[4:7], s73 offen offset:0 sc1\n\t"
    "buffer_load_dwordx4 a[8:11], v196, s[4:7], s73 offen offset:1024 sc1\n\t"
    "buffer_load_dwordx4 a[16:19], v196, s[4:7], s73 offen offset:2048 sc1\n\t"
    "buffer_load_dwordx4 a[24:27], v196, s[4:7], s73 offen offset:3072 sc1\n\t"
    "s_mov_b32 s73, 4096\n\t"
    "buffer_load_dwordx4 a[32:35], v196, s[4:7], s73 offen offset:0 sc1\n\t"
    "buffer_load_dwordx4 a[40:43], v196, s[4:7], s73 offen offset:1024 sc1\n\t"
    "buffer_load_dwordx4 a[48:51], v196, s[4:7], s73 offen offset:2048 sc1\n\t"
    "buffer_load_dwordx4 a[56:59], v196, s[4:7], s73 offen offset:3072 sc1\n\t"
    "s_mov_b32 s73, 8192\n\t"
    "buffer_load_dwordx4 a[64:67], v196, s[4:7], s73 offen offset:0 sc1\n\t"
    "buffer_load_dwordx4 a[72:75], v196, s[4:7], s73 offen offset:1024 sc1\n\t"
    "buffer_load_dwordx4 a[80:83], v196, s[4:7], s73 offen offset:2048 sc1\n\t"
    "buffer_load_dwordx4 a[88:91], v196, s[4:7], s73 offen offset:3072 sc1\n\t"
    "s_mov_b32 s73, 12288\n\t"
    "buffer_load_dwordx4 a[96:99], v196, s[4:7], s73 offen offset:0 sc1\n\t"
    "buffer_load_dwordx4 a[104:107], v196, s[4:7], s73 offen offset:1024 sc1\n\t"
    "buffer_load_dwordx4 a[112:115], v196, s[4:7], s73 offen offset:2048 sc1\n\t"
    "buffer_load_dwordx4 a[120:123], v196, s[4:7], s73 offen offset:3072 sc1\n\t"
    "s_mov_b32 s73, 16384\n\t"
    "buffer_load_dwordx4 a[128:131], v196, s[4:7], s73 offen offset:0 sc1\n\t"
    "buffer_load_dwordx4 a[136:139], v196, s[4:7], s73 offen offset:1024 sc1\n\t"
    "s_waitcnt vmcnt(0)\n\t"
    "v_accvgpr_read_b32 v176, a112\n\t"
    "v_accvgpr_read_b32 v177, a113\n\t"
    "v_accvgpr_read_b32 v178, a114\n\t"
    "v_accvgpr_read_b32 v179, a115\n\t"
    "v_accvgpr_read_b32 v180, a120\n\t"
    "v_accvgpr_read_b32 v181, a121\n\t"
    "v_accvgpr_read_b32 v182, a122\n\t"
    "v_accvgpr_read_b32 v183, a123\n\t"
    "v_accvgpr_read_b32 v184, a128\n\t"
    "v_accvgpr_read_b32 v185, a129\n\t"
    "v_accvgpr_read_b32 v186, a130\n\t"
    "v_accvgpr_read_b32 v187, a131\n\t"
    "v_accvgpr_read_b32 v188, a136\n\t"
    "v_accvgpr_read_b32 v189, a137\n\t"
    "v_accvgpr_read_b32 v190, a138\n\t"
    "v_accvgpr_read_b32 v191, a139\n\t"
    "s_barrier\n\t"
    "ds_read_b128 v[144:147], v204 offset:57344\n\t"
    "ds_read_b128 v[148:151], v204 offset:62464\n\t"
    "ds_read_b128 v[152:155], v204 offset:58368\n\t"
    "ds_read_b128 v[156:159], v204 offset:63488\n\t"
    "ds_read_b128 v[160:163], v204 offset:59392\n\t"
    "ds_read_b128 v[164:167], v204 offset:64512\n\t"
    "ds_read_b128 v[168:171], v204 offset:60416\n\t"
    "ds_read_b128 v[172:175], v205 offset:0\n\t"
    "s_branch L_P_%=\n\t"
    "L_V1_%=:\n\t"
    "ds_read2st64_b64 v[128:131], v3 offset0:0 offset1:1\n\t"
    "ds_read2st64_b64 v[132:135], v3 offset0:2 offset1:3\n\t"
    "L_V1b_%=:\n\t"
    "s_waitcnt lgkmcnt(0)\n\t"
    "v_mfma_f32_16x16x32_bf16 v[16:19], a[0:3], v[128:131], 0\n\t"
    "ds_read2st64_b64 v[136:139], v3 offset0:4 offset1:5\n\t"
    "v_mfma_f32_16x16x32_bf16 v[16:19], a[16:19], v[132:135], v[16:19]\n\t"
    "global_load_dwordx4 v[48:51], v5, s[36:37] offset:0 sc1\n\t"
    "s_mov_b64 s[4:5], s[56:57]\n\t"
    "v_mfma_f32_16x16x32_bf16 v[20:23], a[4:7], v[128:131], 0\n\t"
    "ds_read2st64_b64 v[140:143], v3 offset0:6 offset1:7\n\t"
    "s_mov_b32 s6, 5308416\n\t"
    "v_mfma_f32_16x16x32_bf16 v[20:23], a[20:23], v[132:135], v[20:23]\n\t"
    "s_mov_b32 m0, s25\n\t"
    "s_add_u32 s25, s25, 18432\n\t"
    "buffer_load_dwordx4 v15, s[12:15], s24 offen offset:0 sc1 lds\n\t"
    "s_cmp_ge_u32 s25, 163840\n\t"
    "s_cselect_b32 s28, 92160, 0\n\t"
    "s_sub_u32 s25, s25, s28\n\t"
    "s_mov_b32 s7, 0x20000\n\t"
    "v_mfma_f32_16x16x32_bf16 v[24:27], a[8:11], v[128:131], 0\n\t"
    "v_add_u32 v5, 1792, v5\n\t"
    "s_mul_i32 s33, s70, 144\n\t"
    "v_mfma_f32_16x16x32_bf16 v[24:27], a[24:27], v[132:135], v[24:27]\n\t"
    "s_mul_i32 s34, s68, 36\n\t"
    "s_add_u32 s33, s33, s34\n\t"
    "buffer_load_dword v219, v190, s[4:7], 0 offen\n\t"
    "v_mfma_f32_16x16x32_bf16 v[28:31], a[12:15], v[128:131], 0\n\t"
    "s_mul_i32 s33, s33, 9216\n\t"
    "s_add_u32 s35, s33, 25165824\n\t"
    "v_fma_f32 v64, -v62, v72, v82\n\t"
    "v_fma_f32 v65, -v62, v93, v104\n\t"
    "v_fma_f32 v64, v60, v32, v64 clamp\n\t"
    "v_mfma_f32_16x16x32_bf16 v[28:31], a[28:31], v[132:135], v[28:31]\n\t"
    "s_waitcnt lgkmcnt(0)\n\t"
    "s_add_u32 s60, s36, s35\n\t"
    "s_addc_u32 s61, s37, 0\n\t"
    "s_mov_b32 s62, 331776\n\t"
    "v_fmac_f32 v65, v61, v40\n\t"
    "v_mfma_f32_16x16x32_bf16 v[16:19], a[32:35], v[136:139], v[16:19]\n\t"
    "ds_read2st64_b64 v[128:131], v3 offset0:8 offset1:9\n\t"
    "s_mov_b32 s63, 0x20000\n\t"
    "v_mfma_f32_16x16x32_bf16 v[16:19], a[48:51], v[140:143], v[16:19]\n\t"
    "s_mul_i32 s71, s3, 9216\n\t"
    "s_xor_b32 s33, s68, 0\n\t"
    "v_mul_f32 v68, v64, v65\n\t"
    "v_mfma_f32_16x16x32_bf16 v[20:23], a[36:39], v[136:139], v[20:23]\n\t"
    "ds_read2st64_b64 v[132:135], v3 offset0:10 offset1:11\n\t"
    "s_lshl_b32 s33, s33, 11\n\t"
    "v_mfma_f32_16x16x32_bf16 v[20:23], a[52:55], v[140:143], v[20:23]\n\t"
    "v_add_u32 v198, s33, v15\n\t"
    "s_xor_b32 s33, s68, 1\n\t"
    "v_fma_f32 v66, -v62, v73, v84\n\t"
    "v_mfma_f32_16x16x32_bf16 v[24:27], a[40:43], v[136:139], v[24:27]\n\t"
    "s_lshl_b32 s33, s33, 11\n\t"
    "v_add_u32 v199, s33, v15\n\t"
    "v_fma_f32 v67, -v62, v94, v105\n\t"
    "v_mfma_f32_16x16x32_bf16 v[24:27], a[56:59], v[140:143], v[24:27]\n\t"
    "buffer_load_dwordx4 v15, s[12:15], s24 offen offset:1024 sc1 lds\n\t"
    "s_xor_b32 s33, s68, 2\n\t"
    "v_mfma_f32_16x16x32_bf16 v[28:31], a[44:47], v[136:139], v[28:31]\n\t"
    "s_lshl_b32 s33, s33, 11\n\t"
    "v_add_u32 v200, s33, v15\n\t"
    "v_fma_f32 v66, v60, v33, v66 clamp\n\t"
    "v_mfma_f32_16x16x32_bf16 v[28:31], a[60:63], v[140:143], v[28:31]\n\t"
    "s_waitcnt lgkmcnt(0)\n\t"
    "s_xor_b32 s33, s68, 3\n\t"
    "s_lshl_b32 s33, s33, 11\n\t"
    "v_fmac_f32 v67, v61, v41\n\t"
    "v_mfma_f32_16x16x32_bf16 v[16:19], a[64:67], v[128:131], v[16:19]\n\t"
    "ds_read2st64_b64 v[136:139], v3 offset0:12 offset1:13\n\t"
    "v_add_u32 v201, s33, v15\n\t"
    "v_mfma_f32_16x16x32_bf16 v[16:19], a[80:83], v[132:135], v[16:19]\n\t"
    "v_add_u32 v202, 8192, v15\n\t"
    "s_mul_i32 s74, s3, 4096\n\t"
    "v_mul_f32 v69, v66, v67\n\t"
    "v_mfma_f32_16x16x32_bf16 v[20:23], a[68:71], v[128:131], v[20:23]\n\t"
    "ds_read2st64_b64 v[140:143], v3 offset0:14 offset1:15\n\t"
    "s_mul_i32 s75, s3, 5120\n\t"
    "v_mfma_f32_16x16x32_bf16 v[20:23], a[84:87], v[132:135], v[20:23]\n\t"
    "s_mul_i32 s76, s3, 18432\n\t"
    "s_lshl_b32 s33, s69, 2\n\t"
    "v_cvt_pk_bf16_f32 v120, v68, v69\n\t"
    "v_mfma_f32_16x16x32_bf16 v[24:27], a[72:75], v[128:131], v[24:27]\n\t"
    "s_add_u32 s33, s33, s70\n\t"
    "s_lshl_b32 s33, s33, 4\n\t"
    "v_fma_f32 v64, -v62, v74, v85\n\t"
    "v_mfma_f32_16x16x32_bf16 v[24:27], a[88:91], v[132:135], v[24:27]\n\t"
    "s_add_u32 s33, s33, s3\n\t"
    "s_mul_i32 s33, s33, 10240\n\t"
    "v_fma_f32 v65, -v62, v96, v106\n\t"
    "v_mfma_f32_16x16x32_bf16 v[28:31], a[76:79], v[128:131], v[28:31]\n\t"
    "s_add_u32 s33, s33, 31457280\n\t"
    "s_add_u32 s56, s36, s33\n\t"
    "s_addc_u32 s57, s37, 0\n\t"
    "v_fma_f32 v64, v60, v34, v64 clamp\n\t"
    "v_mfma_f32_16x16x32_bf16 v[28:31], a[92:95], v[132:135], v[28:31]\n\t"
    "s_waitcnt lgkmcnt(0)\n\t"
    "s_mov_b32 s58, 143360\n\t"
    "s_mov_b32 s59, 0x20000\n\t"
    "v_fmac_f32 v65, v61, v42\n\t"
    "v_mfma_f32_16x16x32_bf16 v[16:19], a[96:99], v[136:139], v[16:19]\n\t"
    "ds_read2st64_b64 v[128:131], v3 offset0:16 offset1:17\n\t"
    "s_and_b32 s33, s69, 7\n\t"
    "v_mfma_f32_16x16x32_bf16 v[16:19], a[112:115], v[140:143], v[16:19]\n\t"
    "s_lshl_b32 s33, s33, 2\n\t"
    "s_add_u32 s33, s33, s3\n\t"
    "v_mul_f32 v68, v64, v65\n\t"
    "v_mfma_f32_16x16x32_bf16 v[20:23], a[100:103], v[136:139], v[20:23]\n\t"
    "ds_read2st64_b64 v[132:135], v3 offset0:18 offset1:19\n\t"
    "s_mul_i32 s33, s33, 10368\n\t"
    "v_mfma_f32_16x16x32_bf16 v[20:23], a[116:119], v[140:143], v[20:23]\n\t"
    "s_add_u32 s84, s60, s33\n\t"
    "s_addc_u32 s85, s61, 0\n\t"
    "s_mov_b32 s86, 10368\n\t"
    "v_fma_f32 v66, -v62, v76, v86\n\t"
    "v_mfma_f32_16x16x32_bf16 v[24:27], a[104:107], v[136:139], v[24:27]\n\t"
    "buffer_load_dwordx4 v15, s[12:15], s24 offen offset:2048 sc1 lds\n\t"
    "s_mov_b32 s87, 0x20000\n\t"
    "v_mfma_f32_16x16x32_bf16 v[24:27], a[120:123], v[140:143], v[24:27]\n\t"
    "s_mov_b32 s82, 4294959104\n\t"
    "v_lshlrev_b32 v214, 7, v1\n\t"
    "v_fma_f32 v67, -v62, v97, v108\n\t"
    "v_mfma_f32_16x16x32_bf16 v[28:31], a[108:111], v[136:139], v[28:31]\n\t"
    "s_add_u32 s88, s36, 5373952\n\t"
    "s_addc_u32 s89, s37, 0\n\t"
    "s_mov_b32 s90, 18874368\n\t"
    "v_fma_f32 v66, v60, v35, v66 clamp\n\t"
    "v_mfma_f32_16x16x32_bf16 v[28:31], a[124:127], v[140:143], v[28:31]\n\t"
    "s_waitcnt lgkmcnt(0)\n\t"
    "s_mov_b32 s91, 0x20000\n\t"
    "s_mul_i32 s33, s69, 147456\n\t"
    "v_fmac_f32 v67, v61, v43\n\t"
    "v_mfma_f32_16x16x32_bf16 v[16:19], a[128:131], v[128:131], v[16:19]\n\t"
    "ds_read2st64_b64 v[136:139], v3 offset0:20 offset1:21\n\t"
    "s_mul_i32 s34, s68, 36864\n\t"
    "v_mfma_f32_16x16x32_bf16 v[16:19], a[144:147], v[132:135], v[16:19]\n\t"
    "s_add_u32 s33, s33, s34\n\t"
    "s_lshl_b32 s34, s70, 2\n\t"
    "v_mul_f32 v69, v66, v67\n\t"
    "v_mfma_f32_16x16x32_bf16 v[20:23], a[132:135], v[128:131], v[20:23]\n\t"
    "ds_read2st64_b64 v[140:143], v3 offset0:22 offset1:23\n\t"
    "s_add_u32 s34, s34, s3\n\t"
    "v_mfma_f32_16x16x32_bf16 v[20:23], a[148:151], v[132:135], v[20:23]\n\t"
    "s_mul_i32 s34, s34, 2304\n\t"
    "s_add_u32 s33, s33, s34\n\t"
    "v_cvt_pk_bf16_f32 v121, v68, v69\n\t"
    "v_mfma_f32_16x16x32_bf16 v[24:27], a[136:139], v[128:131], v[24:27]\n\t"
    "s_sub_u32 s83, s33, 7077888\n\t"
    "v_min_u32 v10, 53, v1\n\t"
    "v_fma_f32 v64, -v62, v77, v88\n\t"
    "v_mfma_f32_16x16x32_bf16 v[24:27], a[152:155], v[132:135], v[24:27]\n\t"
    "v_mul_u32_u24 v9, 3641, v10\n\t"
    "v_lshrrev_b32 v9, 16, v9\n\t"
    "v_fma_f32 v65, -v62, v98, v109\n\t"
    "v_mfma_f32_16x16x32_bf16 v[28:31], a[140:143], v[128:131], v[28:31]\n\t"
    "v_mul_u32_u24 v14, 18, v9\n\t"
    "v_sub_u32 v10, v10, v14\n\t"
    "v_fma_f32 v64, v60, v36, v64 clamp\n\t"
    "v_mfma_f32_16x16x32_bf16 v[28:31], a[156:159], v[132:135], v[28:31]\n\t"
    "s_waitcnt lgkmcnt(0)\n\t"
    "v_lshlrev_b32 v10, 7, v10\n\t"
    "v_mul_u32_u24 v14, 2359296, v9\n\t"
    "v_fmac_f32 v65, v61, v44\n\t"
    "v_mfma_f32_16x16x32_bf16 v[16:19], a[160:163], v[136:139], v[16:19]\n\t"
    "ds_read2st64_b64 v[128:131], v3 offset0:24 offset1:25\n\t"
    "v_add_u32 v215, v14, v10\n\t"
    "v_mfma_f32_16x16x32_bf16 v[16:19], a[176:179], v[140:143], v[16:19]\n\t"
    "v_mul_f32 v68, v64, v65\n\t"
    "v_mfma_f32_16x16x32_bf16 v[20:23], a[164:167], v[136:139], v[20:23]\n\t"
    "ds_read2st64_b64 v[132:135], v3 offset0:26 offset1:27\n\t"
    "v_mfma_f32_16x16x32_bf16 v[20:23], a[180:183], v[140:143], v[20:23]\n\t"
    "v_fma_f32 v66, -v62, v78, v89\n\t"
    "v_mfma_f32_16x16x32_bf16 v[24:27], a[168:171], v[136:139], v[24:27]\n\t"
    "buffer_load_dwordx4 v15, s[12:15], s24 offen offset:3072 sc1 lds\n\t"
    "v_mfma_f32_16x16x32_bf16 v[24:27], a[184:187], v[140:143], v[24:27]\n\t"
    "v_fma_f32 v67, -v62, v100, v110\n\t"
    "v_mfma_f32_16x16x32_bf16 v[28:31], a[172:175], v[136:139], v[28:31]\n\t"
    "v_fma_f32 v66, v60, v37, v66 clamp\n\t"
    "v_mfma_f32_16x16x32_bf16 v[28:31], a[188:191], v[140:143], v[28:31]\n\t"
    "s_waitcnt lgkmcnt(0)\n\t"
    "v_fmac_f32 v67, v61, v45\n\t"
    "v_mfma_f32_16x16x32_bf16 v[16:19], a[192:195], v[128:131], v[16:19]\n\t"
    "ds_read2st64_b64 v[136:139], v3 offset0:28 offset1:29\n\t"
    "v_mfma_f32_16x16x32_bf16 v[16:19], a[208:211], v[132:135], v[16:19]\n\t"
    "v_mul_f32 v69, v66, v67\n\t"
    "v_mfma_f32_16x16x32_bf16 v[20:23], a[196:199], v[128:131], v[20:23]\n\t"
    "ds_read2st64_b64 v[140:143], v3 offset0:30 offset1:31\n\t"
    "v_mfma_f32_16x16x32_bf16 v[20:23], a[212:215], v[132:135], v[20:23]\n\t"
    "v_cvt_pk_bf16_f32 v122, v68, v69\n\t"
    "v_mfma_f32_16x16x32_bf16 v[24:27], a[200:203], v[128:131], v[24:27]\n\t"
    "buffer_load_dwordx4 v15, s[12:15], s24 offen offset:3584 sc1 lds\n\t"
    "s_add_u32 s24, s24, 129024\n\t"
    "v_mfma_f32_16x16x32_bf16 v[24:27], a[216:219], v[132:135], v[24:27]\n\t"
    "v_fma_f32 v64, -v62, v80, v90\n\t"
    "v_mfma_f32_16x16x32_bf16 v[28:31], a[204:207], v[128:131], v[28:31]\n\t"
    "v_fma_f32 v65, -v62, v101, v112\n\t"
    "v_mfma_f32_16x16x32_bf16 v[28:31], a[220:223], v[132:135], v[28:31]\n\t"
    "s_waitcnt lgkmcnt(0)\n\t"
    "v_fma_f32 v64, v60, v38, v64 clamp\n\t"
    "v_mfma_f32_16x16x32_bf16 v[16:19], a[224:227], v[136:139], v[16:19]\n\t"
    "ds_read2st64_b64 v[128:131], v3 offset0:32 offset1:33\n\t"
    "s_add_u32 s26, s26, 18432\n\t"
    "v_mfma_f32_16x16x32_bf16 v[16:19], a[240:243], v[140:143], v[16:19]\n\t"
    "s_cmp_ge_u32 s26, 163840\n\t"
    "v_fmac_f32 v65, v61, v46\n\t"
    "v_mfma_f32_16x16x32_bf16 v[20:23], a[228:231], v[136:139], v[20:23]\n\t"
    "ds_read2st64_b64 v[132:135], v3 offset0:34 offset1:35\n\t"
    "s_cselect_b32 s26, 71680, s26\n\t"
    "v_mfma_f32_16x16x32_bf16 v[20:23], a[244:247], v[140:143], v[20:23]\n\t"
    "v_mul_f32 v68, v64, v65\n\t"
    "v_mfma_f32_16x16x32_bf16 v[24:27], a[232:235], v[136:139], v[24:27]\n\t"
    "v_fma_f32 v66, -v62, v81, v92\n\t"
    "v_mfma_f32_16x16x32_bf16 v[24:27], a[248:251], v[140:143], v[24:27]\n\t"
    "v_add_u32 v3, s26, v2\n\t"
    "v_mfma_f32_16x16x32_bf16 v[28:31], a[236:239], v[136:139], v[28:31]\n\t"
    "v_fma_f32 v67, -v62, v102, v113\n\t"
    "v_mfma_f32_16x16x32_bf16 v[28:31], a[252:255], v[140:143], v[28:31]\n\t"
    "s_waitcnt lgkmcnt(0)\n\t"
    "s_waitcnt vmcnt(12)\n\t"
    "s_barrier\n\t"
    "v_fma_f32 v66, v60, v39, v66 clamp\n\t"
    "v_mfma_f32_16x16x32_bf16 v[16:19], v[224:227], v[128:131], v[16:19]\n\t"
    "ds_read2st64_b64 v[136:139], v3 offset0:0 offset1:1\n\t"
    "s_nop 0\n\t"
    "v_mfma_f32_16x16x32_bf16 v[16:19], v[240:243], v[132:135], v[16:19]\n\t"
    "v_cmp_ne_u32 vcc, s66, v53\n\t"
    "v_cmp_ne_u32 s[30:31], s67, v55\n\t"
    "v_mfma_f32_16x16x32_bf16 v[20:23], v[228:231], v[128:131], v[20:23]\n\t"
    "ds_read2st64_b64 v[140:143], v3 offset0:2 offset1:3\n\t"
    "v_mfma_f32_16x16x32_bf16 v[20:23], v[244:247], v[132:135], v[20:23]\n\t"
    "s_or_b64 vcc, vcc, s[30:31]\n\t"
    "s_cbranch_vccnz L_ss01_%=\n\t"
    "L_sr01_%=:\n\t"
    "v_mov_b32 v58, v52\n\t"
    "v_fmac_f32 v67, v61, v47\n\t"
    "v_mfma_f32_16x16x32_bf16 v[24:27], v[232:235], v[128:131], v[24:27]\n\t"
    "v_mul_f32 v56, s78, v54\n\t"
    "v_mul_f32 v69, v66, v67\n\t"
    "v_mfma_f32_16x16x32_bf16 v[24:27], v[248:251], v[132:135], v[24:27]\n\t"
    "v_mul_f32 v57, s79, v54\n\t"
    "v_cvt_pk_bf16_f32 v123, v68, v69\n\t"
    "v_mfma_f32_16x16x32_bf16 v[28:31], v[236:239], v[128:131], v[28:31]\n\t"
    "buffer_store_dwordx4 v[120:123], v15, s[16:19], s27 offen sc1\n\t"
    "v_mfma_f32_16x16x32_bf16 v[28:31], v[252:255], v[132:135], v[28:31]\n\t"
    "s_mov_b32 s27, s38\n\t"
    "s_add_u32 s38, s38, 1032192\n\t"
    "s_waitcnt lgkmcnt(0)\n\t"
    "v_mfma_f32_16x16x32_bf16 v[32:35], a[0:3], v[136:139], 0\n\t"
    "ds_read2st64_b64 v[128:131], v3 offset0:4 offset1:5\n\t"
    "v_mfma_f32_16x16x32_bf16 v[32:35], a[16:19], v[140:143], v[32:35]\n\t"
    "global_load_dwordx4 v[52:55], v5, s[36:37] offset:0 sc1\n\t"
    "v_mfma_f32_16x16x32_bf16 v[36:39], a[4:7], v[136:139], 0\n\t"
    "ds_read2st64_b64 v[132:135], v3 offset0:6 offset1:7\n\t"
    "v_mfma_f32_16x16x32_bf16 v[36:39], a[20:23], v[140:143], v[36:39]\n\t"
    "v_add_u32 v5, 1792, v5\n\t"
    "v_mfma_f32_16x16x32_bf16 v[40:43], a[8:11], v[136:139], 0\n\t"
    "v_fma_f32 v64, -v58, v72, v82\n\t"
    "v_mfma_f32_16x16x32_bf16 v[40:43], a[24:27], v[140:143], v[40:43]\n\t"
    "v_fma_f32 v65, -v58, v93, v104\n\t"
    "v_mfma_f32_16x16x32_bf16 v[44:47], a[12:15], v[136:139], 0\n\t"
    "v_min_u32 v126, 35, v1\n\t"
    "s_mul_i32 s28, s3, 4608\n\t"
    "s_add_u32 s28, s28, 42000384\n\t"
    "v_lshl_add_u32 v126, v126, 7, s28\n\t"
    "v_mfma_f32_16x16x32_bf16 v[44:47], a[28:31], v[140:143], v[44:47]\n\t"
    "s_waitcnt lgkmcnt(0)\n\t"
    "v_fma_f32 v64, v56, v16, v64 clamp\n\t"
    "v_mfma_f32_16x16x32_bf16 v[32:35], a[32:35], v[128:131], v[32:35]\n\t"
    "ds_read2st64_b64 v[136:139], v3 offset0:8 offset1:9\n\t"
    "v_mfma_f32_16x16x32_bf16 v[32:35], a[48:51], v[132:135], v[32:35]\n\t"
    "v_fmac_f32 v65, v57, v24\n\t"
    "v_mfma_f32_16x16x32_bf16 v[36:39], a[36:39], v[128:131], v[36:39]\n\t"
    "ds_read2st64_b64 v[140:143], v3 offset0:10 offset1:11\n\t"
    "v_mfma_f32_16x16x32_bf16 v[36:39], a[52:55], v[132:135], v[36:39]\n\t"
    "v_mul_f32 v68, v64, v65\n\t"
    "v_mfma_f32_16x16x32_bf16 v[40:43], a[40:43], v[128:131], v[40:43]\n\t"
    "v_fma_f32 v66, -v58, v73, v84\n\t"
    "v_mfma_f32_16x16x32_bf16 v[40:43], a[56:59], v[132:135], v[40:43]\n\t"
    "v_fma_f32 v67, -v58, v94, v105\n\t"
    "v_mfma_f32_16x16x32_bf16 v[44:47], a[44:47], v[128:131], v[44:47]\n\t"
    "v_fma_f32 v66, v56, v17, v66 clamp\n\t"
    "v_mfma_f32_16x16x32_bf16 v[44:47], a[60:63], v[132:135], v[44:47]\n\t"
    "s_waitcnt lgkmcnt(0)\n\t"
    "v_fmac_f32 v67, v57, v25\n\t"
    "v_mfma_f32_16x16x32_bf16 v[32:35], a[64:67], v[136:139], v[32:35]\n\t"
    "ds_read2st64_b64 v[128:131], v3 offset0:12 offset1:13\n\t"
    "v_mfma_f32_16x16x32_bf16 v[32:35], a[80:83], v[140:143], v[32:35]\n\t"
    "v_mul_f32 v69, v66, v67\n\t"
    "v_mfma_f32_16x16x32_bf16 v[36:39], a[68:71], v[136:139], v[36:39]\n\t"
    "ds_read2st64_b64 v[132:135], v3 offset0:14 offset1:15\n\t"
    "v_mfma_f32_16x16x32_bf16 v[36:39], a[84:87], v[140:143], v[36:39]\n\t"
    "v_cvt_pk_bf16_f32 v120, v68, v69\n\t"
    "v_mfma_f32_16x16x32_bf16 v[40:43], a[72:75], v[136:139], v[40:43]\n\t"
    "v_fma_f32 v64, -v58, v74, v85\n\t"
    "v_mfma_f32_16x16x32_bf16 v[40:43], a[88:91], v[140:143], v[40:43]\n\t"
    "v_fma_f32 v65, -v58, v96, v106\n\t"
    "v_mfma_f32_16x16x32_bf16 v[44:47], a[76:79], v[136:139], v[44:47]\n\t"
    "v_fma_f32 v64, v56, v18, v64 clamp\n\t"
    "v_mfma_f32_16x16x32_bf16 v[44:47], a[92:95], v[140:143], v[44:47]\n\t"
    "s_waitcnt lgkmcnt(0)\n\t"
    "v_fmac_f32 v65, v57, v26\n\t"
    "v_mfma_f32_16x16x32_bf16 v[32:35], a[96:99], v[128:131], v[32:35]\n\t"
    "ds_read2st64_b64 v[136:139], v3 offset0:16 offset1:17\n\t"
    "v_mfma_f32_16x16x32_bf16 v[32:35], a[112:115], v[132:135], v[32:35]\n\t"
    "v_mul_f32 v68, v64, v65\n\t"
    "v_mfma_f32_16x16x32_bf16 v[36:39], a[100:103], v[128:131], v[36:39]\n\t"
    "ds_read2st64_b64 v[140:143], v3 offset0:18 offset1:19\n\t"
    "v_mfma_f32_16x16x32_bf16 v[36:39], a[116:119], v[132:135], v[36:39]\n\t"
    "v_fma_f32 v66, -v58, v76, v86\n\t"
    "v_mfma_f32_16x16x32_bf16 v[40:43], a[104:107], v[128:131], v[40:43]\n\t"
    "v_fma_f32 v67, -v58, v97, v108\n\t"
    "v_mfma_f32_16x16x32_bf16 v[40:43], a[120:123], v[132:135], v[40:43]\n\t"
    "v_fma_f32 v66, v56, v19, v66 clamp\n\t"
    "v_mfma_f32_16x16x32_bf16 v[44:47], a[108:111], v[128:131], v[44:47]\n\t"
    "v_fmac_f32 v67, v57, v27\n\t"
    "v_mfma_f32_16x16x32_bf16 v[44:47], a[124:127], v[132:135], v[44:47]\n\t"
    "s_waitcnt lgkmcnt(0)\n\t"
    "v_mul_f32 v69, v66, v67\n\t"
    "v_mfma_f32_16x16x32_bf16 v[32:35], a[128:131], v[136:139], v[32:35]\n\t"
    "ds_read2st64_b64 v[128:131], v3 offset0:20 offset1:21\n\t"
    "v_mfma_f32_16x16x32_bf16 v[32:35], a[144:147], v[140:143], v[32:35]\n\t"
    "v_cvt_pk_bf16_f32 v121, v68, v69\n\t"
    "v_mfma_f32_16x16x32_bf16 v[36:39], a[132:135], v[136:139], v[36:39]\n\t"
    "ds_read2st64_b64 v[132:135], v3 offset0:22 offset1:23\n\t"
    "v_mfma_f32_16x16x32_bf16 v[36:39], a[148:151], v[140:143], v[36:39]\n\t"
    "v_fma_f32 v64, -v58, v77, v88\n\t"
    "v_mfma_f32_16x16x32_bf16 v[40:43], a[136:139], v[136:139], v[40:43]\n\t"
    "v_fma_f32 v65, -v58, v98, v109\n\t"
    "v_mfma_f32_16x16x32_bf16 v[40:43], a[152:155], v[140:143], v[40:43]\n\t"
    "v_fma_f32 v64, v56, v20, v64 clamp\n\t"
    "v_mfma_f32_16x16x32_bf16 v[44:47], a[140:143], v[136:139], v[44:47]\n\t"
    "v_fmac_f32 v65, v57, v28\n\t"
    "v_mfma_f32_16x16x32_bf16 v[44:47], a[156:159], v[140:143], v[44:47]\n\t"
    "s_waitcnt lgkmcnt(0)\n\t"
    "v_mul_f32 v68, v64, v65\n\t"
    "v_mfma_f32_16x16x32_bf16 v[32:35], a[160:163], v[128:131], v[32:35]\n\t"
    "ds_read2st64_b64 v[136:139], v3 offset0:24 offset1:25\n\t"
    "v_mfma_f32_16x16x32_bf16 v[32:35], a[176:179], v[132:135], v[32:35]\n\t"
    "v_fma_f32 v66, -v58, v78, v89\n\t"
    "v_mfma_f32_16x16x32_bf16 v[36:39], a[164:167], v[128:131], v[36:39]\n\t"
    "ds_read2st64_b64 v[140:143], v3 offset0:26 offset1:27\n\t"
    "v_mfma_f32_16x16x32_bf16 v[36:39], a[180:183], v[132:135], v[36:39]\n\t"
    "v_fma_f32 v67, -v58, v100, v110\n\t"
    "v_mfma_f32_16x16x32_bf16 v[40:43], a[168:171], v[128:131], v[40:43]\n\t"
    "v_fma_f32 v66, v56, v21, v66 clamp\n\t"
    "v_mfma_f32_16x16x32_bf16 v[40:43], a[184:187], v[132:135], v[40:43]\n\t"
    "v_fmac_f32 v67, v57, v29\n\t"
    "v_mfma_f32_16x16x32_bf16 v[44:47], a[172:175], v[128:131], v[44:47]\n\t"
    "v_mul_f32 v69, v66, v67\n\t"
    "v_mfma_f32_16x16x32_bf16 v[44:47], a[188:191], v[132:135], v[44:47]\n\t"
    "s_waitcnt lgkmcnt(0)\n\t"
    "v_cvt_pk_bf16_f32 v122, v68, v69\n\t"
    "v_mfma_f32_16x16x32_bf16 v[32:35], a[192:195], v[136:139], v[32:35]\n\t"
    "ds_read2st64_b64 v[128:131], v3 offset0:28 offset1:29\n\t"
    "v_mfma_f32_16x16x32_bf16 v[32:35], a[208:211], v[140:143], v[32:35]\n\t"
    "v_fma_f32 v64, -v58, v80, v90\n\t"
    "v_mfma_f32_16x16x32_bf16 v[36:39], a[196:199], v[136:139], v[36:39]\n\t"
    "ds_read2st64_b64 v[132:135], v3 offset0:30 offset1:31\n\t"
    "v_mfma_f32_16x16x32_bf16 v[36:39], a[212:215], v[140:143], v[36:39]\n\t"
    "v_fma_f32 v65, -v58, v101, v112\n\t"
    "v_mfma_f32_16x16x32_bf16 v[40:43], a[200:203], v[136:139], v[40:43]\n\t"
    "v_fma_f32 v64, v56, v22, v64 clamp\n\t"
    "v_mfma_f32_16x16x32_bf16 v[40:43], a[216:219], v[140:143], v[40:43]\n\t"
    "v_fmac_f32 v65, v57, v30\n\t"
    "v_mfma_f32_16x16x32_bf16 v[44:47], a[204:207], v[136:139], v[44:47]\n\t"
    "v_mul_f32 v68, v64, v65\n\t"
    "v_mfma_f32_16x16x32_bf16 v[44:47], a[220:223], v[140:143], v[44:47]\n\t"
    "s_waitcnt lgkmcnt(0)\n\t"
    "v_fma_f32 v66, -v58, v81, v92\n\t"
    "v_mfma_f32_16x16x32_bf16 v[32:35], a[224:227], v[128:131], v[32:35]\n\t"
    "ds_read2st64_b64 v[136:139], v3 offset0:32 offset1:33\n\t"
    "s_add_u32 s26, s26, 18432\n\t"
    "v_mfma_f32_16x16x32_bf16 v[32:35], a[240:243], v[132:135], v[32:35]\n\t"
    "s_cmp_ge_u32 s26, 163840\n\t"
    "v_fma_f32 v67, -v58, v102, v113\n\t"
    "v_mfma_f32_16x16x32_bf16 v[36:39], a[228:231], v[128:131], v[36:39]\n\t"
    "ds_read2st64_b64 v[140:143], v3 offset0:34 offset1:35\n\t"
    "s_cselect_b32 s26, 71680, s26\n\t"
    "v_mfma_f32_16x16x32_bf16 v[36:39], a[244:247], v[132:135], v[36:39]\n\t"
    "v_fma_f32 v66, v56, v23, v66 clamp\n\t"
    "v_mfma_f32_16x16x32_bf16 v[40:43], a[232:235], v[128:131], v[40:43]\n\t"
    "v_fmac_f32 v67, v57, v31\n\t"
    "v_mfma_f32_16x16x32_bf16 v[40:43], a[248:251], v[132:135], v[40:43]\n\t"
    "v_add_u32 v3, s26, v2\n\t"
    "v_mfma_f32_16x16x32_bf16 v[44:47], a[236:239], v[128:131], v[44:47]\n\t"
    "v_mul_f32 v69, v66, v67\n\t"
    "v_mfma_f32_16x16x32_bf16 v[44:47], a[252:255], v[132:135], v[44:47]\n\t"
    "s_waitcnt lgkmcnt(0)\n\t"
    "s_waitcnt vmcnt(8)\n\t"
    "s_barrier\n\t"
    "v_cvt_pk_bf16_f32 v123, v68, v69\n\t"
    "v_mfma_f32_16x16x32_bf16 v[32:35], v[224:227], v[136:139], v[32:35]\n\t"
    "ds_read2st64_b64 v[128:131], v3 offset0:0 offset1:1\n\t"
    "s_nop 0\n\t"
    "v_mfma_f32_16x16x32_bf16 v[32:35], v[240:243], v[140:143], v[32:35]\n\t"
    "v_cmp_ne_u32 vcc, s66, v49\n\t"
    "v_cmp_ne_u32 s[30:31], s67, v51\n\t"
    "v_mfma_f32_16x16x32_bf16 v[36:39], v[228:231], v[136:139], v[36:39]\n\t"
    "ds_read2st64_b64 v[132:135], v3 offset0:2 offset1:3\n\t"
    "v_mfma_f32_16x16x32_bf16 v[36:39], v[244:247], v[140:143], v[36:39]\n\t"
    "s_or_b64 vcc, vcc, s[30:31]\n\t"
    "s_cbranch_vccnz L_ss12_%=\n\t"
    "L_sr12_%=:\n\t"
    "v_mov_b32 v62, v48\n\t"
    "buffer_store_dwordx4 v[120:123], v15, s[16:19], s27 offen sc1\n\t"
    "v_mfma_f32_16x16x32_bf16 v[40:43], v[232:235], v[136:139], v[40:43]\n\t"
    "v_mul_f32 v60, s78, v50\n\t"
    "s_mov_b32 s27, s38\n\t"
    "s_add_u32 s38, s38, 1032192\n\t"
    "v_mfma_f32_16x16x32_bf16 v[40:43], v[248:251], v[140:143], v[40:43]\n\t"
    "v_mul_f32 v61, s79, v50\n\t"
    "v_mfma_f32_16x16x32_bf16 v[44:47], v[236:239], v[136:139], v[44:47]\n\t"
    "v_mfma_f32_16x16x32_bf16 v[44:47], v[252:255], v[140:143], v[44:47]\n\t"
    "s_waitcnt lgkmcnt(0)\n\t"
    "v_mfma_f32_16x16x32_bf16 v[16:19], a[0:3], v[128:131], 0\n\t"
    "ds_read2st64_b64 v[136:139], v3 offset0:4 offset1:5\n\t"
    "v_mfma_f32_16x16x32_bf16 v[16:19], a[16:19], v[132:135], v[16:19]\n\t"
    "global_load_dwordx4 v[48:51], v5, s[36:37] offset:0 sc1\n\t"
    "v_mfma_f32_16x16x32_bf16 v[20:23], a[4:7], v[128:131], 0\n\t"
    "ds_read2st64_b64 v[140:143], v3 offset0:6 offset1:7\n\t"
    "v_mfma_f32_16x16x32_bf16 v[20:23], a[20:23], v[132:135], v[20:23]\n\t"
    "v_add_u32 v5, 1792, v5\n\t"
    "v_mfma_f32_16x16x32_bf16 v[24:27], a[8:11], v[128:131], 0\n\t"
    "v_fma_f32 v64, -v62, v72, v82\n\t"
    "v_mfma_f32_16x16x32_bf16 v[24:27], a[24:27], v[132:135], v[24:27]\n\t"
    "v_fma_f32 v65, -v62, v93, v104\n\t"
    "v_mfma_f32_16x16x32_bf16 v[28:31], a[12:15], v[128:131], 0\n\t"
    "v_fma_f32 v64, v60, v32, v64 clamp\n\t"
    "v_mfma_f32_16x16x32_bf16 v[28:31], a[28:31], v[132:135], v[28:31]\n\t"
    "s_waitcnt lgkmcnt(0)\n\t"
    "v_fmac_f32 v65, v61, v40\n\t"
    "v_mfma_f32_16x16x32_bf16 v[16:19], a[32:35], v[136:139], v[16:19]\n\t"
    "ds_read2st64_b64 v[128:131], v3 offset0:8 offset1:9\n\t"
    "v_mfma_f32_16x16x32_bf16 v[16:19], a[48:51], v[140:143], v[16:19]\n\t"
    "v_mul_f32 v68, v64, v65\n\t"
    "v_mfma_f32_16x16x32_bf16 v[20:23], a[36:39], v[136:139], v[20:23]\n\t"
    "ds_read2st64_b64 v[132:135], v3 offset0:10 offset1:11\n\t"
    "v_mfma_f32_16x16x32_bf16 v[20:23], a[52:55], v[140:143], v[20:23]\n\t"
    "v_fma_f32 v66, -v62, v73, v84\n\t"
    "v_mfma_f32_16x16x32_bf16 v[24:27], a[40:43], v[136:139], v[24:27]\n\t"
    "v_fma_f32 v67, -v62, v94, v105\n\t"
    "v_mfma_f32_16x16x32_bf16 v[24:27], a[56:59], v[140:143], v[24:27]\n\t"
    "v_fma_f32 v66, v60, v33, v66 clamp\n\t"
    "v_mfma_f32_16x16x32_bf16 v[28:31], a[44:47], v[136:139], v[28:31]\n\t"
    "v_fmac_f32 v67, v61, v41\n\t"
    "v_mfma_f32_16x16x32_bf16 v[28:31], a[60:63], v[140:143], v[28:31]\n\t"
    "s_waitcnt lgkmcnt(0)\n\t"
    "v_mul_f32 v69, v66, v67\n\t"
    "v_mfma_f32_16x16x32_bf16 v[16:19], a[64:67], v[128:131], v[16:19]\n\t"
    "ds_read2st64_b64 v[136:139], v3 offset0:12 offset1:13\n\t"
    "v_mfma_f32_16x16x32_bf16 v[16:19], a[80:83], v[132:135], v[16:19]\n\t"
    "v_cvt_pk_bf16_f32 v120, v68, v69\n\t"
    "v_mfma_f32_16x16x32_bf16 v[20:23], a[68:71], v[128:131], v[20:23]\n\t"
    "ds_read2st64_b64 v[140:143], v3 offset0:14 offset1:15\n\t"
    "v_mfma_f32_16x16x32_bf16 v[20:23], a[84:87], v[132:135], v[20:23]\n\t"
    "v_fma_f32 v64, -v62, v74, v85\n\t"
    "v_mfma_f32_16x16x32_bf16 v[24:27], a[72:75], v[128:131], v[24:27]\n\t"
    "v_fma_f32 v65, -v62, v96, v106\n\t"
    "v_mfma_f32_16x16x32_bf16 v[24:27], a[88:91], v[132:135], v[24:27]\n\t"
    "v_fma_f32 v64, v60, v34, v64 clamp\n\t"
    "v_mfma_f32_16x16x32_bf16 v[28:31], a[76:79], v[128:131], v[28:31]\n\t"
    "v_fmac_f32 v65, v61, v42\n\t"
    "v_mfma_f32_16x16x32_bf16 v[28:31], a[92:95], v[132:135], v[28:31]\n\t"
    "s_waitcnt lgkmcnt(0)\n\t"
    "v_mul_f32 v68, v64, v65\n\t"
    "v_mfma_f32_16x16x32_bf16 v[16:19], a[96:99], v[136:139], v[16:19]\n\t"
    "ds_read2st64_b64 v[128:131], v3 offset0:16 offset1:17\n\t"
    "v_mfma_f32_16x16x32_bf16 v[16:19], a[112:115], v[140:143], v[16:19]\n\t"
    "v_fma_f32 v66, -v62, v76, v86\n\t"
    "v_mfma_f32_16x16x32_bf16 v[20:23], a[100:103], v[136:139], v[20:23]\n\t"
    "ds_read2st64_b64 v[132:135], v3 offset0:18 offset1:19\n\t"
    "v_mfma_f32_16x16x32_bf16 v[20:23], a[116:119], v[140:143], v[20:23]\n\t"
    "v_fma_f32 v67, -v62, v97, v108\n\t"
    "v_mfma_f32_16x16x32_bf16 v[24:27], a[104:107], v[136:139], v[24:27]\n\t"
    "v_fma_f32 v66, v60, v35, v66 clamp\n\t"
    "v_mfma_f32_16x16x32_bf16 v[24:27], a[120:123], v[140:143], v[24:27]\n\t"
    "v_fmac_f32 v67, v61, v43\n\t"
    "v_mfma_f32_16x16x32_bf16 v[28:31], a[108:111], v[136:139], v[28:31]\n\t"
    "v_mul_f32 v69, v66, v67\n\t"
    "v_mfma_f32_16x16x32_bf16 v[28:31], a[124:127], v[140:143], v[28:31]\n\t"
    "s_waitcnt lgkmcnt(0)\n\t"
    "v_cvt_pk_bf16_f32 v121, v68, v69\n\t"
    "v_mfma_f32_16x16x32_bf16 v[16:19], a[128:131], v[128:131], v[16:19]\n\t"
    "ds_read2st64_b64 v[136:139], v3 offset0:20 offset1:21\n\t"
    "v_mfma_f32_16x16x32_bf16 v[16:19], a[144:147], v[132:135], v[16:19]\n\t"
    "v_fma_f32 v64, -v62, v77, v88\n\t"
    "v_mfma_f32_16x16x32_bf16 v[20:23], a[132:135], v[128:131], v[20:23]\n\t"
    "ds_read2st64_b64 v[140:143], v3 offset0:22 offset1:23\n\t"
    "v_mfma_f32_16x16x32_bf16 v[20:23], a[148:151], v[132:135], v[20:23]\n\t"
    "v_fma_f32 v65, -v62, v98, v109\n\t"
    "v_mfma_f32_16x16x32_bf16 v[24:27], a[136:139], v[128:131], v[24:27]\n\t"
    "v_fma_f32 v64, v60, v36, v64 clamp\n\t"
    "v_mfma_f32_16x16x32_bf16 v[24:27], a[152:155], v[132:135], v[24:27]\n\t"
    "v_fmac_f32 v65, v61, v44\n\t"
    "v_mfma_f32_16x16x32_bf16 v[28:31], a[140:143], v[128:131], v[28:31]\n\t"
    "v_mul_f32 v68, v64, v65\n\t"
    "v_mfma_f32_16x16x32_bf16 v[28:31], a[156:159], v[132:135], v[28:31]\n\t"
    "s_waitcnt lgkmcnt(0)\n\t"
    "v_fma_f32 v66, -v62, v78, v89\n\t"
    "v_mfma_f32_16x16x32_bf16 v[16:19], a[160:163], v[136:139], v[16:19]\n\t"
    "ds_read2st64_b64 v[128:131], v3 offset0:24 offset1:25\n\t"
    "v_mfma_f32_16x16x32_bf16 v[16:19], a[176:179], v[140:143], v[16:19]\n\t"
    "v_fma_f32 v67, -v62, v100, v110\n\t"
    "v_mfma_f32_16x16x32_bf16 v[20:23], a[164:167], v[136:139], v[20:23]\n\t"
    "ds_read2st64_b64 v[132:135], v3 offset0:26 offset1:27\n\t"
    "v_mfma_f32_16x16x32_bf16 v[20:23], a[180:183], v[140:143], v[20:23]\n\t"
    "v_fma_f32 v66, v60, v37, v66 clamp\n\t"
    "v_mfma_f32_16x16x32_bf16 v[24:27], a[168:171], v[136:139], v[24:27]\n\t"
    "global_load_dwordx2 v[124:125], v126, s[36:37] offset:0 sc1\n\t"
    "v_mfma_f32_16x16x32_bf16 v[24:27], a[184:187], v[140:143], v[24:27]\n\t"
    "v_fmac_f32 v67, v61, v45\n\t"
    "v_mfma_f32_16x16x32_bf16 v[28:31], a[172:175], v[136:139], v[28:31]\n\t"
    "v_mul_f32 v69, v66, v67\n\t"
    "v_mfma_f32_16x16x32_bf16 v[28:31], a[188:191], v[140:143], v[28:31]\n\t"
    "s_waitcnt lgkmcnt(0)\n\t"
    "v_cvt_pk_bf16_f32 v122, v68, v69\n\t"
    "v_mfma_f32_16x16x32_bf16 v[16:19], a[192:195], v[128:131], v[16:19]\n\t"
    "ds_read2st64_b64 v[136:139], v3 offset0:28 offset1:29\n\t"
    "v_mfma_f32_16x16x32_bf16 v[16:19], a[208:211], v[132:135], v[16:19]\n\t"
    "v_fma_f32 v64, -v62, v80, v90\n\t"
    "v_mfma_f32_16x16x32_bf16 v[20:23], a[196:199], v[128:131], v[20:23]\n\t"
    "ds_read2st64_b64 v[140:143], v3 offset0:30 offset1:31\n\t"
    "v_mfma_f32_16x16x32_bf16 v[20:23], a[212:215], v[132:135], v[20:23]\n\t"
    "v_fma_f32 v65, -v62, v101, v112\n\t"
    "v_mfma_f32_16x16x32_bf16 v[24:27], a[200:203], v[128:131], v[24:27]\n\t"
    "v_fma_f32 v64, v60, v38, v64 clamp\n\t"
    "v_mfma_f32_16x16x32_bf16 v[24:27], a[216:219], v[132:135], v[24:27]\n\t"
    "v_fmac_f32 v65, v61, v46\n\t"
    "v_mfma_f32_16x16x32_bf16 v[28:31], a[204:207], v[128:131], v[28:31]\n\t"
    "v_mul_f32 v68, v64, v65\n\t"
    "v_mfma_f32_16x16x32_bf16 v[28:31], a[220:223], v[132:135], v[28:31]\n\t"
    "s_waitcnt lgkmcnt(0)\n\t"
    "v_fma_f32 v66, -v62, v81, v92\n\t"
    "v_mfma_f32_16x16x32_bf16 v[16:19], a[224:227], v[136:139], v[16:19]\n\t"
    "ds_read2st64_b64 v[128:131], v3 offset0:32 offset1:33\n\t"
    "s_add_u32 s26, s26, 18432\n\t"
    "v_mfma_f32_16x16x32_bf16 v[16:19], a[240:243], v[140:143], v[16:19]\n\t"
    "s_cmp_ge_u32 s26, 163840\n\t"
    "v_fma_f32 v67, -v62, v102, v113\n\t"
    "v_mfma_f32_16x16x32_bf16 v[20:23], a[228:231], v[136:139], v[20:23]\n\t"
    "ds_read2st64_b64 v[132:135], v3 offset0:34 offset1:35\n\t"
    "s_cselect_b32 s26, 71680, s26\n\t"
    "v_mfma_f32_16x16x32_bf16 v[20:23], a[244:247], v[140:143], v[20:23]\n\t"
    "v_fma_f32 v66, v60, v39, v66 clamp\n\t"
    "v_mfma_f32_16x16x32_bf16 v[24:27], a[232:235], v[136:139], v[24:27]\n\t"
    "v_fmac_f32 v67, v61, v47\n\t"
    "v_mfma_f32_16x16x32_bf16 v[24:27], a[248:251], v[140:143], v[24:27]\n\t"
    "v_add_u32 v3, s26, v2\n\t"
    "v_mfma_f32_16x16x32_bf16 v[28:31], a[236:239], v[136:139], v[28:31]\n\t"
    "s_waitcnt vmcnt(0)\n\t"
    "v_cmp_ne_u64 vcc, s[66:67], v[124:125]\n\t"
    "s_cbranch_vccnz L_psf_%=\n\t"
    "L_pbf_%=:\n\t"
    "v_mul_f32 v69, v66, v67\n\t"
    "v_mfma_f32_16x16x32_bf16 v[28:31], a[252:255], v[140:143], v[28:31]\n\t"
    "s_waitcnt lgkmcnt(0)\n\t"
    "s_waitcnt vmcnt(0)\n\t"
    "s_barrier\n\t"
    "v_cvt_pk_bf16_f32 v123, v68, v69\n\t"
    "v_mfma_f32_16x16x32_bf16 v[16:19], v[224:227], v[128:131], v[16:19]\n\t"
    "ds_read2st64_b64 v[136:139], v3 offset0:0 offset1:1\n\t"
    "v_mfma_f32_16x16x32_bf16 v[16:19], v[240:243], v[132:135], v[16:19]\n\t"
    "v_cmp_ne_u32 vcc, s66, v53\n\t"
    "v_cmp_ne_u32 s[30:31], s67, v55\n\t"
    "v_mfma_f32_16x16x32_bf16 v[20:23], v[228:231], v[128:131], v[20:23]\n\t"
    "ds_read2st64_b64 v[140:143], v3 offset0:2 offset1:3\n\t"
    "v_mfma_f32_16x16x32_bf16 v[20:23], v[244:247], v[132:135], v[20:23]\n\t"
    "s_or_b64 vcc, vcc, s[30:31]\n\t"
    "s_cbranch_vccnz L_ss03_%=\n\t"
    "L_sr03_%=:\n\t"
    "v_mov_b32 v58, v52\n\t"
    "buffer_store_dwordx4 v[120:123], v15, s[16:19], s27 offen sc1\n\t"
    "v_mfma_f32_16x16x32_bf16 v[24:27], v[232:235], v[128:131], v[24:27]\n\t"
    "v_mul_f32 v56, s78, v54\n\t"
    "s_mov_b32 s27, s38\n\t"
    "s_add_u32 s38, s38, 1032192\n\t"
    "v_mfma_f32_16x16x32_bf16 v[24:27], v[248:251], v[132:135], v[24:27]\n\t"
    "v_mul_f32 v57, s79, v54\n\t"
    "v_mfma_f32_16x16x32_bf16 v[28:31], v[236:239], v[128:131], v[28:31]\n\t"
    "v_mfma_f32_16x16x32_bf16 v[28:31], v[252:255], v[132:135], v[28:31]\n\t"
    "s_waitcnt lgkmcnt(0)\n\t"
    "v_mfma_f32_16x16x32_bf16 v[32:35], a[0:3], v[136:139], 0\n\t"
    "ds_read2st64_b64 v[128:131], v3 offset0:4 offset1:5\n\t"
    "v_mfma_f32_16x16x32_bf16 v[32:35], a[16:19], v[140:143], v[32:35]\n\t"
    "s_mov_b32 m0, s25\n\t"
    "s_add_u32 s25, s25, 18432\n\t"
    "buffer_load_dwordx4 v15, s[12:15], s24 offen offset:0 sc1 lds\n\t"
    "s_cmp_ge_u32 s25, 163840\n\t"
    "s_cselect_b32 s28, 92160, 0\n\t"
    "s_sub_u32 s25, s25, s28\n\t"
    "v_mfma_f32_16x16x32_bf16 v[36:39], a[4:7], v[136:139], 0\n\t"
    "ds_read2st64_b64 v[132:135], v3 offset0:6 offset1:7\n\t"
    "v_mfma_f32_16x16x32_bf16 v[36:39], a[20:23], v[140:143], v[36:39]\n\t"
    "buffer_load_dwordx4 v15, s[12:15], s24 offen offset:1024 sc1 lds\n\t"
    "v_mfma_f32_16x16x32_bf16 v[40:43], a[8:11], v[136:139], 0\n\t"
    "s_lshl_b32 s33, s20, 2\n\t"
    "v_fma_f32 v64, -v58, v72, v82\n\t"
    "v_fma_f32 v65, -v58, v93, v104\n\t"
    "v_fma_f32 v64, v56, v16, v64 clamp\n\t"
    "v_fmac_f32 v65, v57, v24\n\t"
    "v_mul_f32 v68, v64, v65\n\t"
    "v_fma_f32 v66, -v58, v73, v84\n\t"
    "v_mfma_f32_16x16x32_bf16 v[40:43], a[24:27], v[140:143], v[40:43]\n\t"
    "buffer_load_dwordx4 v15, s[12:15], s24 offen offset:2048 sc1 lds\n\t"
    "v_mfma_f32_16x16x32_bf16 v[44:47], a[12:15], v[136:139], 0\n\t"
    "s_add_u32 s33, s33, s21\n\t"
    "v_fma_f32 v67, -v58, v94, v105\n\t"
    "v_mfma_f32_16x16x32_bf16 v[44:47], a[28:31], v[140:143], v[44:47]\n\t"
    "s_waitcnt lgkmcnt(0)\n\t"
    "buffer_load_dwordx4 v15, s[12:15], s24 offen offset:3072 sc1 lds\n\t"
    "v_mfma_f32_16x16x32_bf16 v[32:35], a[32:35], v[128:131], v[32:35]\n\t"
    "ds_read2st64_b64 v[136:139], v3 offset0:8 offset1:9\n\t"
    "v_mfma_f32_16x16x32_bf16 v[32:35], a[48:51], v[132:135], v[32:35]\n\t"
    "buffer_load_dwordx4 v15, s[12:15], s24 offen offset:3584 sc1 lds\n\t"
    "s_add_u32 s24, s24, 129024\n\t"
    "v_mfma_f32_16x16x32_bf16 v[36:39], a[36:39], v[128:131], v[36:39]\n\t"
    "ds_read2st64_b64 v[140:143], v3 offset0:10 offset1:11\n\t"
    "v_mfma_f32_16x16x32_bf16 v[36:39], a[52:55], v[132:135], v[36:39]\n\t"
    "s_sub_u32 s33, s33, 3\n\t"
    "v_fma_f32 v66, v56, v17, v66 clamp\n\t"
    "v_mfma_f32_16x16x32_bf16 v[40:43], a[40:43], v[128:131], v[40:43]\n\t"
    "s_cmp_ge_u32 s21, 3\n\t"
    "s_cselect_b32 s6, 9216, 0\n\t"
    "s_cselect_b32 s10, 9216, 0\n\t"
    "s_cselect_b64 s[80:81], 1, 0\n\t"
    "s_cselect_b32 s33, s33, 0\n\t"
    "v_fmac_f32 v67, v57, v25\n\t"
    "v_mfma_f32_16x16x32_bf16 v[40:43], a[56:59], v[132:135], v[40:43]\n\t"
    "s_mov_b32 s7, 0x20000\n\t"
    "v_mul_f32 v69, v66, v67\n\t"
    "v_mfma_f32_16x16x32_bf16 v[44:47], a[44:47], v[128:131], v[44:47]\n\t"
    "s_mov_b32 s11, 0x20000\n\t"
    "v_cvt_pk_bf16_f32 v120, v68, v69\n\t"
    "v_mfma_f32_16x16x32_bf16 v[44:47], a[60:63], v[132:135], v[44:47]\n\t"
    "s_waitcnt lgkmcnt(0)\n\t"
    "s_mul_i32 s35, s3, 9216\n\t"
    "v_fma_f32 v64, -v58, v74, v85\n\t"
    "v_mfma_f32_16x16x32_bf16 v[32:35], a[64:67], v[136:139], v[32:35]\n\t"
    "ds_read2st64_b64 v[128:131], v3 offset0:12 offset1:13\n\t"
    "v_mfma_f32_16x16x32_bf16 v[32:35], a[80:83], v[140:143], v[32:35]\n\t"
    "s_mul_i32 s34, s33, 36864\n\t"
    "v_fma_f32 v65, -v58, v96, v106\n\t"
    "v_mfma_f32_16x16x32_bf16 v[36:39], a[68:71], v[136:139], v[36:39]\n\t"
    "ds_read2st64_b64 v[132:135], v3 offset0:14 offset1:15\n\t"
    "v_mfma_f32_16x16x32_bf16 v[36:39], a[84:87], v[140:143], v[36:39]\n\t"
    "s_add_u32 s34, s34, s35\n\t"
    "v_fma_f32 v64, v56, v18, v64 clamp\n\t"
    "v_mfma_f32_16x16x32_bf16 v[40:43], a[72:75], v[136:139], v[40:43]\n\t"
    "s_mov_b32 m0, s25\n\t"
    "s_add_u32 s25, s25, 18432\n\t"
    "buffer_load_dwordx4 v15, s[12:15], s24 offen offset:0 sc1 lds\n\t"
    "s_cmp_ge_u32 s25, 163840\n\t"
    "s_cselect_b32 s28, 92160, 0\n\t"
    "s_sub_u32 s25, s25, s28\n\t"
    "v_mfma_f32_16x16x32_bf16 v[40:43], a[88:91], v[140:143], v[40:43]\n\t"
    "s_add_u32 s4, s4, s34\n\t"
    "s_addc_u32 s5, s5, 0\n\t"
    "v_fmac_f32 v65, v57, v26\n\t"
    "v_mfma_f32_16x16x32_bf16 v[44:47], a[76:79], v[136:139], v[44:47]\n\t"
    "s_mul_i32 s34, s3, 144\n\t"
    "v_mul_f32 v68, v64, v65\n\t"
    "v_mfma_f32_16x16x32_bf16 v[44:47], a[92:95], v[140:143], v[44:47]\n\t"
    "s_waitcnt lgkmcnt(0)\n\t"
    "s_add_u32 s33, s33, s34\n\t"
    "v_fma_f32 v66, -v58, v76, v86\n\t"
    "v_mfma_f32_16x16x32_bf16 v[32:35], a[96:99], v[128:131], v[32:35]\n\t"
    "ds_read2st64_b64 v[136:139], v3 offset0:16 offset1:17\n\t"
    "v_mfma_f32_16x16x32_bf16 v[32:35], a[112:115], v[132:135], v[32:35]\n\t"
    "s_lshl_b32 s34, s33, 7\n\t"
    "v_fma_f32 v67, -v58, v97, v108\n\t"
    "v_mfma_f32_16x16x32_bf16 v[36:39], a[100:103], v[128:131], v[36:39]\n\t"
    "ds_read2st64_b64 v[140:143], v3 offset0:18 offset1:19\n\t"
    "v_mfma_f32_16x16x32_bf16 v[36:39], a[116:119], v[132:135], v[36:39]\n\t"
    "s_add_u32 s34, s34, 42024960\n\t"
    "v_fma_f32 v66, v56, v19, v66 clamp\n\t"
    "v_mfma_f32_16x16x32_bf16 v[40:43], a[104:107], v[128:131], v[40:43]\n\t"
    "buffer_load_dwordx4 v15, s[12:15], s24 offen offset:1024 sc1 lds\n\t"
    "v_mfma_f32_16x16x32_bf16 v[40:43], a[120:123], v[132:135], v[40:43]\n\t"
    "v_mov_b32 v63, s34\n\t"
    "v_fmac_f32 v67, v57, v27\n\t"
    "v_mfma_f32_16x16x32_bf16 v[44:47], a[108:111], v[128:131], v[44:47]\n\t"
    "s_mul_i32 s34, s33, 9216\n\t"
    "v_mul_f32 v69, v66, v67\n\t"
    "v_mfma_f32_16x16x32_bf16 v[44:47], a[124:127], v[132:135], v[44:47]\n\t"
    "s_waitcnt lgkmcnt(0)\n\t"
    "s_add_u32 s34, s34, 25165824\n\t"
    "v_cvt_pk_bf16_f32 v121, v68, v69\n\t"
    "v_mfma_f32_16x16x32_bf16 v[32:35], a[128:131], v[136:139], v[32:35]\n\t"
    "ds_read2st64_b64 v[128:131], v3 offset0:20 offset1:21\n\t"
    "v_mfma_f32_16x16x32_bf16 v[32:35], a[144:147], v[140:143], v[32:35]\n\t"
    "s_add_u32 s8, s36, s34\n\t"
    "s_addc_u32 s9, s37, 0\n\t"
    "v_fma_f32 v64, -v58, v77, v88\n\t"
    "v_mfma_f32_16x16x32_bf16 v[36:39], a[132:135], v[136:139], v[36:39]\n\t"
    "ds_read2st64_b64 v[132:135], v3 offset0:22 offset1:23\n\t"
    "v_mfma_f32_16x16x32_bf16 v[36:39], a[148:151], v[140:143], v[36:39]\n\t"
    "s_mul_i32 s34, s3, 288\n\t"
    "v_fma_f32 v65, -v58, v98, v109\n\t"
    "v_mfma_f32_16x16x32_bf16 v[40:43], a[136:139], v[136:139], v[40:43]\n\t"
    "buffer_load_dwordx4 v15, s[12:15], s24 offen offset:2048 sc1 lds\n\t"
    "v_mfma_f32_16x16x32_bf16 v[40:43], a[152:155], v[140:143], v[40:43]\n\t"
    "v_lshrrev_b32 v144, 2, v1\n\t"
    "v_fma_f32 v64, v56, v20, v64 clamp\n\t"
    "v_mfma_f32_16x16x32_bf16 v[44:47], a[140:143], v[136:139], v[44:47]\n\t"
    "v_mul_u32_u24 v144, 1152, v144\n\t"
    "v_fmac_f32 v65, v57, v28\n\t"
    "v_mfma_f32_16x16x32_bf16 v[44:47], a[156:159], v[140:143], v[44:47]\n\t"
    "s_waitcnt lgkmcnt(0)\n\t"
    "v_add_u32 v144, s34, v144\n\t"
    "v_mul_f32 v68, v64, v65\n\t"
    "v_mfma_f32_16x16x32_bf16 v[32:35], a[160:163], v[128:131], v[32:35]\n\t"
    "ds_read2st64_b64 v[136:139], v3 offset0:24 offset1:25\n\t"
    "v_mfma_f32_16x16x32_bf16 v[32:35], a[176:179], v[132:135], v[32:35]\n\t"
    "v_and_b32 v145, 3, v1\n\t"
    "v_fma_f32 v66, -v58, v78, v89\n\t"
    "v_mfma_f32_16x16x32_bf16 v[36:39], a[164:167], v[128:131], v[36:39]\n\t"
    "ds_read2st64_b64 v[140:143], v3 offset0:26 offset1:27\n\t"
    "v_mfma_f32_16x16x32_bf16 v[36:39], a[180:183], v[132:135], v[36:39]\n\t"
    "v_lshl_add_u32 v8, v145, 4, v144\n\t"
    "v_fma_f32 v67, -v58, v100, v110\n\t"
    "v_mfma_f32_16x16x32_bf16 v[40:43], a[168:171], v[128:131], v[40:43]\n\t"
    "buffer_load_dwordx4 v15, s[12:15], s24 offen offset:3072 sc1 lds\n\t"
    "v_mfma_f32_16x16x32_bf16 v[40:43], a[184:187], v[132:135], v[40:43]\n\t"
    "v_lshl_add_u32 v9, v145, 3, v144\n\t"
    "v_fma_f32 v66, v56, v21, v66 clamp\n\t"
    "v_mfma_f32_16x16x32_bf16 v[44:47], a[172:175], v[128:131], v[44:47]\n\t"
    "s_and_b32 s33, s3, 1\n\t"
    "v_fmac_f32 v67, v57, v29\n\t"
    "v_mfma_f32_16x16x32_bf16 v[44:47], a[188:191], v[132:135], v[44:47]\n\t"
    "s_waitcnt lgkmcnt(0)\n\t"
    "s_lshl_b32 s34, s33, 5\n\t"
    "v_mul_f32 v69, v66, v67\n\t"
    "v_mfma_f32_16x16x32_bf16 v[32:35], a[192:195], v[136:139], v[32:35]\n\t"
    "ds_read2st64_b64 v[128:131], v3 offset0:28 offset1:29\n\t"
    "v_mfma_f32_16x16x32_bf16 v[32:35], a[208:211], v[140:143], v[32:35]\n\t"
    "v_add_u32 v8, s34, v8\n\t"
    "v_cvt_pk_bf16_f32 v122, v68, v69\n\t"
    "v_mfma_f32_16x16x32_bf16 v[36:39], a[196:199], v[136:139], v[36:39]\n\t"
    "ds_read2st64_b64 v[132:135], v3 offset0:30 offset1:31\n\t"
    "v_mfma_f32_16x16x32_bf16 v[36:39], a[212:215], v[140:143], v[36:39]\n\t"
    "s_lshl_b32 s34, s33, 8\n\t"
    "v_fma_f32 v64, -v58, v80, v90\n\t"
    "v_mfma_f32_16x16x32_bf16 v[40:43], a[200:203], v[136:139], v[40:43]\n\t"
    "buffer_load_dwordx4 v15, s[12:15], s24 offen offset:3584 sc1 lds\n\t"
    "s_add_u32 s24, s24, 129024\n\t"
    "v_mfma_f32_16x16x32_bf16 v[40:43], a[216:219], v[140:143], v[40:43]\n\t"
    "v_subrev_u32 v9, s34, v9\n\t"
    "v_fma_f32 v65, -v58, v101, v112\n\t"
    "v_mfma_f32_16x16x32_bf16 v[44:47], a[204:207], v[136:139], v[44:47]\n\t"
    "s_mov_b32 s33, 4096\n\t"
    "v_fma_f32 v64, v56, v22, v64 clamp\n\t"
    "v_mfma_f32_16x16x32_bf16 v[44:47], a[220:223], v[140:143], v[44:47]\n\t"
    "s_waitcnt lgkmcnt(0)\n\t"
    "s_mov_b32 s34, 8192\n\t"
    "v_fmac_f32 v65, v57, v30\n\t"
    "v_mfma_f32_16x16x32_bf16 v[32:35], a[224:227], v[128:131], v[32:35]\n\t"
    "ds_read2st64_b64 v[136:139], v3 offset0:32 offset1:33\n\t"
    "s_add_u32 s26, s26, 18432\n\t"
    "v_mfma_f32_16x16x32_bf16 v[32:35], a[240:243], v[132:135], v[32:35]\n\t"
    "s_cmp_ge_u32 s26, 163840\n\t"
    "v_mul_f32 v68, v64, v65\n\t"
    "v_mfma_f32_16x16x32_bf16 v[36:39], a[228:231], v[128:131], v[36:39]\n\t"
    "ds_read2st64_b64 v[140:143], v3 offset0:34 offset1:35\n\t"
    "s_cselect_b32 s26, 71680, s26\n\t"
    "v_mfma_f32_16x16x32_bf16 v[36:39], a[244:247], v[132:135], v[36:39]\n\t"
    "v_fma_f32 v66, -v58, v81, v92\n\t"
    "v_mfma_f32_16x16x32_bf16 v[40:43], a[232:235], v[128:131], v[40:43]\n\t"
    "s_xor_b32 s28, s68, 1\n\t"
    "s_mul_i32 s28, s28, 40960\n\t"
    "s_add_u32 s28, s28, 0\n\t"
    "buffer_store_dwordx4 v[116:119], v15, s[56:59], s28 offen offset:0 sc1\n\t"
    "v_mfma_f32_16x16x32_bf16 v[40:43], a[248:251], v[132:135], v[40:43]\n\t"
    "v_add_u32 v3, s26, v2\n\t"
    "v_mfma_f32_16x16x32_bf16 v[44:47], a[236:239], v[128:131], v[44:47]\n\t"
    "v_fma_f32 v67, -v58, v102, v113\n\t"
    "v_mfma_f32_16x16x32_bf16 v[44:47], a[252:255], v[132:135], v[44:47]\n\t"
    "s_waitcnt lgkmcnt(0)\n\t"
    "s_waitcnt vmcnt(6)\n\t"
    "s_barrier\n\t"
    "v_fma_f32 v66, v56, v23, v66 clamp\n\t"
    "v_mfma_f32_16x16x32_bf16 v[32:35], v[224:227], v[136:139], v[32:35]\n\t"
    "ds_read2st64_b64 v[128:131], v3 offset0:0 offset1:1\n\t"
    "v_mfma_f32_16x16x32_bf16 v[32:35], v[240:243], v[140:143], v[32:35]\n\t"
    "v_cmp_ne_u32 vcc, s66, v49\n\t"
    "v_cmp_ne_u32 s[30:31], s67, v51\n\t"
    "v_mfma_f32_16x16x32_bf16 v[36:39], v[228:231], v[136:139], v[36:39]\n\t"
    "ds_read2st64_b64 v[132:135], v3 offset0:2 offset1:3\n\t"
    "v_mfma_f32_16x16x32_bf16 v[36:39], v[244:247], v[140:143], v[36:39]\n\t"
    "s_or_b64 vcc, vcc, s[30:31]\n\t"
    "s_cbranch_vccnz L_ss14_%=\n\t"
    "L_sr14_%=:\n\t"
    "v_mov_b32 v62, v48\n\t"
    "v_fmac_f32 v67, v57, v31\n\t"
    "v_mfma_f32_16x16x32_bf16 v[40:43], v[232:235], v[136:139], v[40:43]\n\t"
    "v_mul_f32 v60, s78, v50\n\t"
    "v_mul_f32 v69, v66, v67\n\t"
    "v_mfma_f32_16x16x32_bf16 v[40:43], v[248:251], v[140:143], v[40:43]\n\t"
    "v_mul_f32 v61, s79, v50\n\t"
    "v_cvt_pk_bf16_f32 v123, v68, v69\n\t"
    "v_mfma_f32_16x16x32_bf16 v[44:47], v[236:239], v[136:139], v[44:47]\n\t"
    "buffer_store_dwordx4 v[120:123], v15, s[16:19], s27 offen sc1\n\t"
    "v_mfma_f32_16x16x32_bf16 v[44:47], v[252:255], v[140:143], v[44:47]\n\t"
    "s_mov_b32 s27, s38\n\t"
    "s_add_u32 s38, s38, 1032192\n\t"
    "s_waitcnt lgkmcnt(0)\n\t"
    "v_mfma_f32_16x16x32_bf16 v[16:19], a[0:3], v[128:131], 0\n\t"
    "ds_read2st64_b64 v[136:139], v3 offset0:4 offset1:5\n\t"
    "v_mfma_f32_16x16x32_bf16 v[16:19], a[16:19], v[132:135], v[16:19]\n\t"
    "v_mfma_f32_16x16x32_bf16 v[20:23], a[4:7], v[128:131], 0\n\t"
    "ds_read2st64_b64 v[140:143], v3 offset0:6 offset1:7\n\t"
    "v_mfma_f32_16x16x32_bf16 v[20:23], a[20:23], v[132:135], v[20:23]\n\t"
    "global_load_dwordx4 v[52:55], v5, s[36:37] offset:0 sc1\n\t"
    "v_mfma_f32_16x16x32_bf16 v[24:27], a[8:11], v[128:131], 0\n\t"
    "v_add_u32 v5, 1792, v5\n\t"
    "v_mfma_f32_16x16x32_bf16 v[24:27], a[24:27], v[132:135], v[24:27]\n\t"
    "v_fma_f32 v64, -v62, v72, v82\n\t"
    "v_fma_f32 v65, -v62, v93, v104\n\t"
    "v_fma_f32 v64, v60, v32, v64 clamp\n\t"
    "v_fmac_f32 v65, v61, v40\n\t"
    "v_mul_f32 v68, v64, v65\n\t"
    "v_fma_f32 v66, -v62, v73, v84\n\t"
    "v_fma_f32 v67, -v62, v94, v105\n\t"
    "v_fma_f32 v66, v60, v33, v66 clamp\n\t"
    "v_fmac_f32 v67, v61, v41\n\t"
    "v_mul_f32 v69, v66, v67\n\t"
    "v_cvt_pk_bf16_f32 v120, v68, v69\n\t"
    "v_mfma_f32_16x16x32_bf16 v[28:31], a[12:15], v[128:131], 0\n\t"
    "s_mov_b32 m0, s25\n\t"
    "s_add_u32 s25, s25, 18432\n\t"
    "buffer_load_dwordx4 v15, s[12:15], s24 offen offset:0 sc1 lds\n\t"
    "s_cmp_ge_u32 s25, 163840\n\t"
    "s_cselect_b32 s28, 92160, 0\n\t"
    "s_sub_u32 s25, s25, s28\n\t"
    "v_mfma_f32_16x16x32_bf16 v[28:31], a[28:31], v[132:135], v[28:31]\n\t"
    "s_waitcnt lgkmcnt(0)\n\t"
    "v_fma_f32 v64, -v62, v74, v85\n\t"
    "v_mfma_f32_16x16x32_bf16 v[16:19], a[32:35], v[136:139], v[16:19]\n\t"
    "ds_read2st64_b64 v[128:131], v3 offset0:8 offset1:9\n\t"
    "v_mfma_f32_16x16x32_bf16 v[16:19], a[48:51], v[140:143], v[16:19]\n\t"
    "v_fma_f32 v65, -v62, v96, v106\n\t"
    "v_mfma_f32_16x16x32_bf16 v[20:23], a[36:39], v[136:139], v[20:23]\n\t"
    "ds_read2st64_b64 v[132:135], v3 offset0:10 offset1:11\n\t"
    "v_mfma_f32_16x16x32_bf16 v[20:23], a[52:55], v[140:143], v[20:23]\n\t"
    "buffer_load_dwordx4 v15, s[12:15], s24 offen offset:1024 sc1 lds\n\t"
    "v_mfma_f32_16x16x32_bf16 v[24:27], a[40:43], v[136:139], v[24:27]\n\t"
    "v_fma_f32 v64, v60, v34, v64 clamp\n\t"
    "v_mfma_f32_16x16x32_bf16 v[24:27], a[56:59], v[140:143], v[24:27]\n\t"
    "v_fmac_f32 v65, v61, v42\n\t"
    "v_mfma_f32_16x16x32_bf16 v[28:31], a[44:47], v[136:139], v[28:31]\n\t"
    "v_mul_f32 v68, v64, v65\n\t"
    "v_mfma_f32_16x16x32_bf16 v[28:31], a[60:63], v[140:143], v[28:31]\n\t"
    "s_waitcnt lgkmcnt(0)\n\t"
    "buffer_load_dwordx4 v15, s[12:15], s24 offen offset:2048 sc1 lds\n\t"
    "v_mfma_f32_16x16x32_bf16 v[16:19], a[64:67], v[128:131], v[16:19]\n\t"
    "ds_read2st64_b64 v[136:139], v3 offset0:12 offset1:13\n\t"
    "v_mfma_f32_16x16x32_bf16 v[16:19], a[80:83], v[132:135], v[16:19]\n\t"
    "v_fma_f32 v66, -v62, v76, v86\n\t"
    "v_mfma_f32_16x16x32_bf16 v[20:23], a[68:71], v[128:131], v[20:23]\n\t"
    "ds_read2st64_b64 v[140:143], v3 offset0:14 offset1:15\n\t"
    "v_mfma_f32_16x16x32_bf16 v[20:23], a[84:87], v[132:135], v[20:23]\n\t"
    "buffer_load_dwordx4 v15, s[12:15], s24 offen offset:3072 sc1 lds\n\t"
    "v_mfma_f32_16x16x32_bf16 v[24:27], a[72:75], v[128:131], v[24:27]\n\t"
    "v_fma_f32 v67, -v62, v97, v108\n\t"
    "v_mfma_f32_16x16x32_bf16 v[24:27], a[88:91], v[132:135], v[24:27]\n\t"
    "v_fma_f32 v66, v60, v35, v66 clamp\n\t"
    "v_mfma_f32_16x16x32_bf16 v[28:31], a[76:79], v[128:131], v[28:31]\n\t"
    "v_fmac_f32 v67, v61, v43\n\t"
    "v_mfma_f32_16x16x32_bf16 v[28:31], a[92:95], v[132:135], v[28:31]\n\t"
    "s_waitcnt lgkmcnt(0)\n\t"
    "buffer_load_dwordx4 v15, s[12:15], s24 offen offset:3584 sc1 lds\n\t"
    "s_add_u32 s24, s24, 129024\n\t"
    "v_mfma_f32_16x16x32_bf16 v[16:19], a[96:99], v[136:139], v[16:19]\n\t"
    "ds_read2st64_b64 v[128:131], v3 offset0:16 offset1:17\n\t"
    "v_mfma_f32_16x16x32_bf16 v[16:19], a[112:115], v[140:143], v[16:19]\n\t"
    "v_mul_f32 v69, v66, v67\n\t"
    "v_mfma_f32_16x16x32_bf16 v[20:23], a[100:103], v[136:139], v[20:23]\n\t"
    "ds_read2st64_b64 v[132:135], v3 offset0:18 offset1:19\n\t"
    "v_mfma_f32_16x16x32_bf16 v[20:23], a[116:119], v[140:143], v[20:23]\n\t"
    "v_cvt_pk_bf16_f32 v121, v68, v69\n\t"
    "v_mfma_f32_16x16x32_bf16 v[24:27], a[104:107], v[136:139], v[24:27]\n\t"
    "v_fma_f32 v64, -v62, v77, v88\n\t"
    "v_mfma_f32_16x16x32_bf16 v[24:27], a[120:123], v[140:143], v[24:27]\n\t"
    "global_load_dwordx4 v[48:51], v5, s[36:37] offset:0 sc1\n\t"
    "v_mfma_f32_16x16x32_bf16 v[28:31], a[108:111], v[136:139], v[28:31]\n\t"
    "v_fma_f32 v65, -v62, v98, v109\n\t"
    "v_mfma_f32_16x16x32_bf16 v[28:31], a[124:127], v[140:143], v[28:31]\n\t"
    "s_waitcnt lgkmcnt(0)\n\t"
    "v_add_u32 v5, 1792, v5\n\t"
    "v_mfma_f32_16x16x32_bf16 v[16:19], a[128:131], v[128:131], v[16:19]\n\t"
    "ds_read2st64_b64 v[136:139], v3 offset0:20 offset1:21\n\t"
    "v_mfma_f32_16x16x32_bf16 v[16:19], a[144:147], v[132:135], v[16:19]\n\t"
    "s_mov_b32 m0, s25\n\t"
    "s_add_u32 s25, s25, 18432\n\t"
    "buffer_load_dwordx4 v15, s[12:15], s24 offen offset:0 sc1 lds\n\t"
    "s_cmp_ge_u32 s25, 163840\n\t"
    "s_cselect_b32 s28, 92160, 0\n\t"
    "s_sub_u32 s25, s25, s28\n\t"
    "v_mfma_f32_16x16x32_bf16 v[20:23], a[132:135], v[128:131], v[20:23]\n\t"
    "ds_read2st64_b64 v[140:143], v3 offset0:22 offset1:23\n\t"
    "v_mfma_f32_16x16x32_bf16 v[20:23], a[148:151], v[132:135], v[20:23]\n\t"
    "v_fma_f32 v64, v60, v36, v64 clamp\n\t"
    "v_mfma_f32_16x16x32_bf16 v[24:27], a[136:139], v[128:131], v[24:27]\n\t"
    "v_fmac_f32 v65, v61, v44\n\t"
    "v_mfma_f32_16x16x32_bf16 v[24:27], a[152:155], v[132:135], v[24:27]\n\t"
    "v_mul_f32 v68, v64, v65\n\t"
    "v_mfma_f32_16x16x32_bf16 v[28:31], a[140:143], v[128:131], v[28:31]\n\t"
    "v_fma_f32 v66, -v62, v78, v89\n\t"
    "v_mfma_f32_16x16x32_bf16 v[28:31], a[156:159], v[132:135], v[28:31]\n\t"
    "s_waitcnt lgkmcnt(0)\n\t"
    "buffer_load_dwordx4 v15, s[12:15], s24 offen offset:1024 sc1 lds\n\t"
    "v_mfma_f32_16x16x32_bf16 v[16:19], a[160:163], v[136:139], v[16:19]\n\t"
    "ds_read2st64_b64 v[128:131], v3 offset0:24 offset1:25\n\t"
    "v_mfma_f32_16x16x32_bf16 v[16:19], a[176:179], v[140:143], v[16:19]\n\t"
    "v_fma_f32 v67, -v62, v100, v110\n\t"
    "v_mfma_f32_16x16x32_bf16 v[20:23], a[164:167], v[136:139], v[20:23]\n\t"
    "ds_read2st64_b64 v[132:135], v3 offset0:26 offset1:27\n\t"
    "v_mfma_f32_16x16x32_bf16 v[20:23], a[180:183], v[140:143], v[20:23]\n\t"
    "v_fma_f32 v66, v60, v37, v66 clamp\n\t"
    "v_mfma_f32_16x16x32_bf16 v[24:27], a[168:171], v[136:139], v[24:27]\n\t"
    "v_fmac_f32 v67, v61, v45\n\t"
    "v_mfma_f32_16x16x32_bf16 v[24:27], a[184:187], v[140:143], v[24:27]\n\t"
    "buffer_load_dwordx4 v15, s[12:15], s24 offen offset:2048 sc1 lds\n\t"
    "v_mfma_f32_16x16x32_bf16 v[28:31], a[172:175], v[136:139], v[28:31]\n\t"
    "v_mul_f32 v69, v66, v67\n\t"
    "v_mfma_f32_16x16x32_bf16 v[28:31], a[188:191], v[140:143], v[28:31]\n\t"
    "s_waitcnt lgkmcnt(0)\n\t"
    "v_cvt_pk_bf16_f32 v122, v68, v69\n\t"
    "v_mfma_f32_16x16x32_bf16 v[16:19], a[192:195], v[128:131], v[16:19]\n\t"
    "ds_read2st64_b64 v[136:139], v3 offset0:28 offset1:29\n\t"
    "v_mfma_f32_16x16x32_bf16 v[16:19], a[208:211], v[132:135], v[16:19]\n\t"
    "v_fma_f32 v64, -v62, v80, v90\n\t"
    "v_mfma_f32_16x16x32_bf16 v[20:23], a[196:199], v[128:131], v[20:23]\n\t"
    "ds_read2st64_b64 v[140:143], v3 offset0:30 offset1:31\n\t"
    "v_mfma_f32_16x16x32_bf16 v[20:23], a[212:215], v[132:135], v[20:23]\n\t"
    "buffer_load_dwordx4 v15, s[12:15], s24 offen offset:3072 sc1 lds\n\t"
    "v_mfma_f32_16x16x32_bf16 v[24:27], a[200:203], v[128:131], v[24:27]\n\t"
    "v_fma_f32 v65, -v62, v101, v112\n\t"
    "v_mfma_f32_16x16x32_bf16 v[24:27], a[216:219], v[132:135], v[24:27]\n\t"
    "v_fma_f32 v64, v60, v38, v64 clamp\n\t"
    "v_mfma_f32_16x16x32_bf16 v[28:31], a[204:207], v[128:131], v[28:31]\n\t"
    "v_fmac_f32 v65, v61, v46\n\t"
    "v_mfma_f32_16x16x32_bf16 v[28:31], a[220:223], v[132:135], v[28:31]\n\t"
    "s_waitcnt lgkmcnt(0)\n\t"
    "buffer_load_dwordx4 v15, s[12:15], s24 offen offset:3584 sc1 lds\n\t"
    "s_add_u32 s24, s24, 129024\n\t"
    "v_mfma_f32_16x16x32_bf16 v[16:19], a[224:227], v[136:139], v[16:19]\n\t"
    "ds_read2st64_b64 v[128:131], v3 offset0:32 offset1:33\n\t"
    "s_add_u32 s26, s26, 18432\n\t"
    "v_mfma_f32_16x16x32_bf16 v[16:19], a[240:243], v[140:143], v[16:19]\n\t"
    "s_cmp_ge_u32 s26, 163840\n\t"
    "v_mul_f32 v68, v64, v65\n\t"
    "v_mfma_f32_16x16x32_bf16 v[20:23], a[228:231], v[136:139], v[20:23]\n\t"
    "ds_read2st64_b64 v[132:135], v3 offset0:34 offset1:35\n\t"
    "s_cselect_b32 s26, 71680, s26\n\t"
    "v_mfma_f32_16x16x32_bf16 v[20:23], a[244:247], v[140:143], v[20:23]\n\t"
    "v_fma_f32 v66, -v62, v81, v92\n\t"
    "v_mfma_f32_16x16x32_bf16 v[24:27], a[232:235], v[136:139], v[24:27]\n\t"
    "s_xor_b32 s28, s68, 1\n\t"
    "s_mul_i32 s28, s28, 40960\n\t"
    "s_add_u32 s28, s28, 0\n\t"
    "buffer_store_dwordx4 v[116:119], v15, s[56:59], s28 offen offset:1024 sc1\n\t"
    "v_mfma_f32_16x16x32_bf16 v[24:27], a[248:251], v[140:143], v[24:27]\n\t"
    "v_add_u32 v3, s26, v2\n\t"
    "v_mfma_f32_16x16x32_bf16 v[28:31], a[236:239], v[136:139], v[28:31]\n\t"
    "v_fma_f32 v67, -v62, v102, v113\n\t"
    "v_mfma_f32_16x16x32_bf16 v[28:31], a[252:255], v[140:143], v[28:31]\n\t"
    "s_waitcnt lgkmcnt(0)\n\t"
    "s_waitcnt vmcnt(12)\n\t"
    "s_barrier\n\t"
    "v_fma_f32 v66, v60, v39, v66 clamp\n\t"
    "v_mfma_f32_16x16x32_bf16 v[16:19], v[224:227], v[128:131], v[16:19]\n\t"
    "ds_read2st64_b64 v[136:139], v3 offset0:0 offset1:1\n\t"
    "v_mfma_f32_16x16x32_bf16 v[16:19], v[240:243], v[132:135], v[16:19]\n\t"
    "v_cmp_ne_u32 vcc, s66, v53\n\t"
    "v_cmp_ne_u32 s[30:31], s67, v55\n\t"
    "v_mfma_f32_16x16x32_bf16 v[20:23], v[228:231], v[128:131], v[20:23]\n\t"
    "ds_read2st64_b64 v[140:143], v3 offset0:2 offset1:3\n\t"
    "v_mfma_f32_16x16x32_bf16 v[20:23], v[244:247], v[132:135], v[20:23]\n\t"
    "s_or_b64 vcc, vcc, s[30:31]\n\t"
    "s_cbranch_vccnz L_ss05_%=\n\t"
    "L_sr05_%=:\n\t"
    "v_mov_b32 v58, v52\n\t"
    "v_fmac_f32 v67, v61, v47\n\t"
    "v_mfma_f32_16x16x32_bf16 v[24:27], v[232:235], v[128:131], v[24:27]\n\t"
    "v_mul_f32 v56, s78, v54\n\t"
    "v_mul_f32 v69, v66, v67\n\t"
    "v_mfma_f32_16x16x32_bf16 v[24:27], v[248:251], v[132:135], v[24:27]\n\t"
    "v_mul_f32 v57, s79, v54\n\t"
    "v_cvt_pk_bf16_f32 v123, v68, v69\n\t"
    "v_mfma_f32_16x16x32_bf16 v[28:31], v[236:239], v[128:131], v[28:31]\n\t"
    "buffer_store_dwordx4 v[120:123], v15, s[16:19], s27 offen sc1\n\t"
    "v_mfma_f32_16x16x32_bf16 v[28:31], v[252:255], v[132:135], v[28:31]\n\t"
    "s_mov_b32 s27, s38\n\t"
    "s_add_u32 s38, s38, 1032192\n\t"
    "s_waitcnt lgkmcnt(0)\n\t"
    "v_mfma_f32_16x16x32_bf16 v[32:35], a[0:3], v[136:139], 0\n\t"
    "ds_read2st64_b64 v[128:131], v3 offset0:4 offset1:5\n\t"
    "v_mfma_f32_16x16x32_bf16 v[32:35], a[16:19], v[140:143], v[32:35]\n\t"
    "global_load_dwordx4 v[52:55], v5, s[36:37] offset:0 sc1\n\t"
    "v_mfma_f32_16x16x32_bf16 v[36:39], a[4:7], v[136:139], 0\n\t"
    "ds_read2st64_b64 v[132:135], v3 offset0:6 offset1:7\n\t"
    "v_mfma_f32_16x16x32_bf16 v[36:39], a[20:23], v[140:143], v[36:39]\n\t"
    "s_mov_b32 m0, s25\n\t"
    "s_add_u32 s25, s25, 18432\n\t"
    "buffer_load_dwordx4 v15, s[12:15], s24 offen offset:0 sc1 lds\n\t"
    "s_cmp_ge_u32 s25, 163840\n\t"
    "s_cselect_b32 s28, 92160, 0\n\t"
    "s_sub_u32 s25, s25, s28\n\t"
    "v_mfma_f32_16x16x32_bf16 v[40:43], a[8:11], v[136:139], 0\n\t"
    "v_add_u32 v5, 1792, v5\n\t"
    "v_mfma_f32_16x16x32_bf16 v[40:43], a[24:27], v[140:143], v[40:43]\n\t"
    "v_fma_f32 v64, -v58, v72, v82\n\t"
    "v_fma_f32 v65, -v58, v93, v104\n\t"
    "v_fma_f32 v64, v56, v16, v64 clamp\n\t"
    "v_fmac_f32 v65, v57, v24\n\t"
    "v_mul_f32 v68, v64, v65\n\t"
    "v_fma_f32 v66, -v58, v73, v84\n\t"
    "v_fma_f32 v67, -v58, v94, v105\n\t"
    "v_fma_f32 v66, v56, v17, v66 clamp\n\t"
    "v_mfma_f32_16x16x32_bf16 v[44:47], a[12:15], v[136:139], 0\n\t"
    "v_fmac_f32 v67, v57, v25\n\t"
    "v_mfma_f32_16x16x32_bf16 v[44:47], a[28:31], v[140:143], v[44:47]\n\t"
    "s_waitcnt lgkmcnt(0)\n\t"
    "buffer_load_dwordx4 v15, s[12:15], s24 offen offset:1024 sc1 lds\n\t"
    "v_mfma_f32_16x16x32_bf16 v[32:35], a[32:35], v[128:131], v[32:35]\n\t"
    "ds_read2st64_b64 v[136:139], v3 offset0:8 offset1:9\n\t"
    "v_mfma_f32_16x16x32_bf16 v[32:35], a[48:51], v[132:135], v[32:35]\n\t"
    "v_mul_f32 v69, v66, v67\n\t"
    "v_mfma_f32_16x16x32_bf16 v[36:39], a[36:39], v[128:131], v[36:39]\n\t"
    "ds_read2st64_b64 v[140:143], v3 offset0:10 offset1:11\n\t"
    "v_mfma_f32_16x16x32_bf16 v[36:39], a[52:55], v[132:135], v[36:39]\n\t"
    "buffer_load_dwordx4 v15, s[12:15], s24 offen offset:2048 sc1 lds\n\t"
    "v_mfma_f32_16x16x32_bf16 v[40:43], a[40:43], v[128:131], v[40:43]\n\t"
    "v_cvt_pk_bf16_f32 v120, v68, v69\n\t"
    "v_mfma_f32_16x16x32_bf16 v[40:43], a[56:59], v[132:135], v[40:43]\n\t"
    "v_fma_f32 v64, -v58, v74, v85\n\t"
    "v_mfma_f32_16x16x32_bf16 v[44:47], a[44:47], v[128:131], v[44:47]\n\t"
    "v_fma_f32 v65, -v58, v96, v106\n\t"
    "v_mfma_f32_16x16x32_bf16 v[44:47], a[60:63], v[132:135], v[44:47]\n\t"
    "s_waitcnt lgkmcnt(0)\n\t"
    "buffer_load_dwordx4 v15, s[12:15], s24 offen offset:3072 sc1 lds\n\t"
    "v_mfma_f32_16x16x32_bf16 v[32:35], a[64:67], v[136:139], v[32:35]\n\t"
    "ds_read2st64_b64 v[128:131], v3 offset0:12 offset1:13\n\t"
    "v_mfma_f32_16x16x32_bf16 v[32:35], a[80:83], v[140:143], v[32:35]\n\t"
    "v_fma_f32 v64, v56, v18, v64 clamp\n\t"
    "v_mfma_f32_16x16x32_bf16 v[36:39], a[68:71], v[136:139], v[36:39]\n\t"
    "ds_read2st64_b64 v[132:135], v3 offset0:14 offset1:15\n\t"
    "v_mfma_f32_16x16x32_bf16 v[36:39], a[84:87], v[140:143], v[36:39]\n\t"
    "buffer_load_dwordx4 v15, s[12:15], s24 offen offset:3584 sc1 lds\n\t"
    "s_add_u32 s24, s24, 129024\n\t"
    "v_mfma_f32_16x16x32_bf16 v[40:43], a[72:75], v[136:139], v[40:43]\n\t"
    "v_fmac_f32 v65, v57, v26\n\t"
    "v_mfma_f32_16x16x32_bf16 v[40:43], a[88:91], v[140:143], v[40:43]\n\t"
    "v_mul_f32 v68, v64, v65\n\t"
    "v_mfma_f32_16x16x32_bf16 v[44:47], a[76:79], v[136:139], v[44:47]\n\t"
    "v_fma_f32 v66, -v58, v76, v86\n\t"
    "v_mfma_f32_16x16x32_bf16 v[44:47], a[92:95], v[140:143], v[44:47]\n\t"
    "s_waitcnt lgkmcnt(0)\n\t"
    "v_fma_f32 v67, -v58, v97, v108\n\t"
    "v_mfma_f32_16x16x32_bf16 v[32:35], a[96:99], v[128:131], v[32:35]\n\t"
    "ds_read2st64_b64 v[136:139], v3 offset0:16 offset1:17\n\t"
    "s_add_u32 m0, s35, 0\n\t"
    "v_mfma_f32_16x16x32_bf16 v[32:35], a[112:115], v[132:135], v[32:35]\n\t"
    "buffer_load_dwordx4 v15, s[4:7], 0 offen offset:0 lds\n\t"
    "v_mfma_f32_16x16x32_bf16 v[36:39], a[100:103], v[128:131], v[36:39]\n\t"
    "ds_read2st64_b64 v[140:143], v3 offset0:18 offset1:19\n\t"
    "v_mfma_f32_16x16x32_bf16 v[36:39], a[116:119], v[132:135], v[36:39]\n\t"
    "v_fma_f32 v66, v56, v19, v66 clamp\n\t"
    "v_mfma_f32_16x16x32_bf16 v[40:43], a[104:107], v[128:131], v[40:43]\n\t"
    "v_fmac_f32 v67, v57, v27\n\t"
    "v_mfma_f32_16x16x32_bf16 v[40:43], a[120:123], v[132:135], v[40:43]\n\t"
    "v_mul_f32 v69, v66, v67\n\t"
    "v_mfma_f32_16x16x32_bf16 v[44:47], a[108:111], v[128:131], v[44:47]\n\t"
    "v_cvt_pk_bf16_f32 v121, v68, v69\n\t"
    "v_mfma_f32_16x16x32_bf16 v[44:47], a[124:127], v[132:135], v[44:47]\n\t"
    "s_waitcnt lgkmcnt(0)\n\t"
    "v_fma_f32 v64, -v58, v77, v88\n\t"
    "v_mfma_f32_16x16x32_bf16 v[32:35], a[128:131], v[136:139], v[32:35]\n\t"
    "ds_read2st64_b64 v[128:131], v3 offset0:20 offset1:21\n\t"
    "v_mfma_f32_16x16x32_bf16 v[32:35], a[144:147], v[140:143], v[32:35]\n\t"
    "v_fma_f32 v65, -v58, v98, v109\n\t"
    "v_mfma_f32_16x16x32_bf16 v[36:39], a[132:135], v[136:139], v[36:39]\n\t"
    "ds_read2st64_b64 v[132:135], v3 offset0:22 offset1:23\n\t"
    "s_add_u32 m0, s35, 0\n\t"
    "v_mfma_f32_16x16x32_bf16 v[36:39], a[148:151], v[140:143], v[36:39]\n\t"
    "buffer_load_dwordx4 v15, s[4:7], 0 offen offset:1024 lds\n\t"
    "v_mfma_f32_16x16x32_bf16 v[40:43], a[136:139], v[136:139], v[40:43]\n\t"
    "v_fma_f32 v64, v56, v20, v64 clamp\n\t"
    "v_mfma_f32_16x16x32_bf16 v[40:43], a[152:155], v[140:143], v[40:43]\n\t"
    "v_fmac_f32 v65, v57, v28\n\t"
    "v_mfma_f32_16x16x32_bf16 v[44:47], a[140:143], v[136:139], v[44:47]\n\t"
    "v_mul_f32 v68, v64, v65\n\t"
    "v_mfma_f32_16x16x32_bf16 v[44:47], a[156:159], v[140:143], v[44:47]\n\t"
    "s_waitcnt lgkmcnt(0)\n\t"
    "v_fma_f32 v66, -v58, v78, v89\n\t"
    "v_mfma_f32_16x16x32_bf16 v[32:35], a[160:163], v[128:131], v[32:35]\n\t"
    "ds_read2st64_b64 v[136:139], v3 offset0:24 offset1:25\n\t"
    "v_mfma_f32_16x16x32_bf16 v[32:35], a[176:179], v[132:135], v[32:35]\n\t"
    "v_fma_f32 v67, -v58, v100, v110\n\t"
    "v_mfma_f32_16x16x32_bf16 v[36:39], a[164:167], v[128:131], v[36:39]\n\t"
    "ds_read2st64_b64 v[140:143], v3 offset0:26 offset1:27\n\t"
    "v_mfma_f32_16x16x32_bf16 v[36:39], a[180:183], v[132:135], v[36:39]\n\t"
    "v_fma_f32 v66, v56, v21, v66 clamp\n\t"
    "v_mfma_f32_16x16x32_bf16 v[40:43], a[168:171], v[128:131], v[40:43]\n\t"
    "s_add_u32 m0, s35, 0\n\t"
    "v_fmac_f32 v67, v57, v29\n\t"
    "v_mfma_f32_16x16x32_bf16 v[40:43], a[184:187], v[132:135], v[40:43]\n\t"
    "buffer_load_dwordx4 v15, s[4:7], 0 offen offset:2048 lds\n\t"
    "v_mfma_f32_16x16x32_bf16 v[44:47], a[172:175], v[128:131], v[44:47]\n\t"
    "v_mul_f32 v69, v66, v67\n\t"
    "v_mfma_f32_16x16x32_bf16 v[44:47], a[188:191], v[132:135], v[44:47]\n\t"
    "s_waitcnt lgkmcnt(0)\n\t"
    "v_cvt_pk_bf16_f32 v122, v68, v69\n\t"
    "v_mfma_f32_16x16x32_bf16 v[32:35], a[192:195], v[136:139], v[32:35]\n\t"
    "ds_read2st64_b64 v[128:131], v3 offset0:28 offset1:29\n\t"
    "v_mfma_f32_16x16x32_bf16 v[32:35], a[208:211], v[140:143], v[32:35]\n\t"
    "v_fma_f32 v64, -v58, v80, v90\n\t"
    "v_mfma_f32_16x16x32_bf16 v[36:39], a[196:199], v[136:139], v[36:39]\n\t"
    "ds_read2st64_b64 v[132:135], v3 offset0:30 offset1:31\n\t"
    "v_mfma_f32_16x16x32_bf16 v[36:39], a[212:215], v[140:143], v[36:39]\n\t"
    "v_fma_f32 v65, -v58, v101, v112\n\t"
    "v_mfma_f32_16x16x32_bf16 v[40:43], a[200:203], v[136:139], v[40:43]\n\t"
    "v_fma_f32 v64, v56, v22, v64 clamp\n\t"
    "v_mfma_f32_16x16x32_bf16 v[40:43], a[216:219], v[140:143], v[40:43]\n\t"
    "v_fmac_f32 v65, v57, v30\n\t"
    "v_mfma_f32_16x16x32_bf16 v[44:47], a[204:207], v[136:139], v[44:47]\n\t"
    "s_add_u32 m0, s35, 0\n\t"
    "v_mul_f32 v68, v64, v65\n\t"
    "v_mfma_f32_16x16x32_bf16 v[44:47], a[220:223], v[140:143], v[44:47]\n\t"
    "s_waitcnt lgkmcnt(0)\n\t"
    "buffer_load_dwordx4 v15, s[4:7], 0 offen offset:3072 lds\n\t"
    "v_mfma_f32_16x16x32_bf16 v[32:35], a[224:227], v[128:131], v[32:35]\n\t"
    "ds_read2st64_b64 v[136:139], v3 offset0:32 offset1:33\n\t"
    "s_add_u32 s26, s26, 18432\n\t"
    "v_mfma_f32_16x16x32_bf16 v[32:35], a[240:243], v[132:135], v[32:35]\n\t"
    "s_cmp_ge_u32 s26, 163840\n\t"
    "v_fma_f32 v66, -v58, v81, v92\n\t"
    "v_mfma_f32_16x16x32_bf16 v[36:39], a[228:231], v[128:131], v[36:39]\n\t"
    "ds_read2st64_b64 v[140:143], v3 offset0:34 offset1:35\n\t"
    "s_cselect_b32 s26, 71680, s26\n\t"
    "v_mfma_f32_16x16x32_bf16 v[36:39], a[244:247], v[132:135], v[36:39]\n\t"
    "v_fma_f32 v67, -v58, v102, v113\n\t"
    "v_mfma_f32_16x16x32_bf16 v[40:43], a[232:235], v[128:131], v[40:43]\n\t"
    "v_fma_f32 v66, v56, v23, v66 clamp\n\t"
    "v_mfma_f32_16x16x32_bf16 v[40:43], a[248:251], v[132:135], v[40:43]\n\t"
    "v_add_u32 v3, s26, v2\n\t"
    "v_mfma_f32_16x16x32_bf16 v[44:47], a[236:239], v[128:131], v[44:47]\n\t"
    "s_xor_b32 s28, s68, 2\n\t"
    "s_mul_i32 s28, s28, 40960\n\t"
    "s_add_u32 s28, s28, 2048\n\t"
    "buffer_store_dwordx4 v[116:119], v15, s[56:59], s28 offen offset:0 sc1\n\t"
    "v_mfma_f32_16x16x32_bf16 v[44:47], a[252:255], v[132:135], v[44:47]\n\t"
    "s_waitcnt lgkmcnt(0)\n\t"
    "s_waitcnt vmcnt(18)\n\t"
    "s_barrier\n\t"
    "v_fmac_f32 v67, v57, v31\n\t"
    "v_mfma_f32_16x16x32_bf16 v[32:35], v[224:227], v[136:139], v[32:35]\n\t"
    "ds_read2st64_b64 v[128:131], v3 offset0:0 offset1:1\n\t"
    "v_mfma_f32_16x16x32_bf16 v[32:35], v[240:243], v[140:143], v[32:35]\n\t"
    "v_cmp_ne_u32 vcc, s66, v49\n\t"
    "v_cmp_ne_u32 s[30:31], s67, v51\n\t"
    "v_mfma_f32_16x16x32_bf16 v[36:39], v[228:231], v[136:139], v[36:39]\n\t"
    "ds_read2st64_b64 v[132:135], v3 offset0:2 offset1:3\n\t"
    "s_add_u32 m0, s35, 4096\n\t"
    "v_mfma_f32_16x16x32_bf16 v[36:39], v[244:247], v[140:143], v[36:39]\n\t"
    "s_or_b64 vcc, vcc, s[30:31]\n\t"
    "s_cbranch_vccnz L_ss16_%=\n\t"
    "L_sr16_%=:\n\t"
    "v_mov_b32 v62, v48\n\t"
    "buffer_load_dwordx4 v15, s[4:7], s33 offen offset:0 lds\n\t"
    "v_mfma_f32_16x16x32_bf16 v[40:43], v[232:235], v[136:139], v[40:43]\n\t"
    "v_mul_f32 v60, s78, v50\n\t"
    "v_mul_f32 v69, v66, v67\n\t"
    "v_mfma_f32_16x16x32_bf16 v[40:43], v[248:251], v[140:143], v[40:43]\n\t"
    "v_mul_f32 v61, s79, v50\n\t"
    "v_cvt_pk_bf16_f32 v123, v68, v69\n\t"
    "v_mfma_f32_16x16x32_bf16 v[44:47], v[236:239], v[136:139], v[44:47]\n\t"
    "buffer_store_dwordx4 v[120:123], v15, s[16:19], s27 offen sc1\n\t"
    "v_mfma_f32_16x16x32_bf16 v[44:47], v[252:255], v[140:143], v[44:47]\n\t"
    "s_mov_b32 s27, s38\n\t"
    "s_add_u32 s38, s38, 1032192\n\t"
    "s_waitcnt lgkmcnt(0)\n\t"
    "v_mfma_f32_16x16x32_bf16 v[16:19], a[0:3], v[128:131], 0\n\t"
    "ds_read2st64_b64 v[136:139], v3 offset0:4 offset1:5\n\t"
    "v_mfma_f32_16x16x32_bf16 v[16:19], a[16:19], v[132:135], v[16:19]\n\t"
    "global_load_dwordx4 v[48:51], v5, s[36:37] offset:0 sc1\n\t"
    "v_mfma_f32_16x16x32_bf16 v[20:23], a[4:7], v[128:131], 0\n\t"
    "ds_read2st64_b64 v[140:143], v3 offset0:6 offset1:7\n\t"
    "v_mfma_f32_16x16x32_bf16 v[20:23], a[20:23], v[132:135], v[20:23]\n\t"
    "v_add_u32 v5, 1792, v5\n\t"
    "s_add_u32 m0, s35, 4096\n\t"
    "v_mfma_f32_16x16x32_bf16 v[24:27], a[8:11], v[128:131], 0\n\t"
    "buffer_load_dwordx4 v15, s[4:7], s33 offen offset:1024 lds\n\t"
    "v_mfma_f32_16x16x32_bf16 v[24:27], a[24:27], v[132:135], v[24:27]\n\t"
    "v_fma_f32 v64, -v62, v72, v82\n\t"
    "v_fma_f32 v65, -v62, v93, v104\n\t"
    "v_fma_f32 v64, v60, v32, v64 clamp\n\t"
    "v_fmac_f32 v65, v61, v40\n\t"
    "v_mul_f32 v68, v64, v65\n\t"
    "v_fma_f32 v66, -v62, v73, v84\n\t"
    "v_fma_f32 v67, -v62, v94, v105\n\t"
    "v_mfma_f32_16x16x32_bf16 v[28:31], a[12:15], v[128:131], 0\n\t"
    "v_fma_f32 v66, v60, v33, v66 clamp\n\t"
    "v_mfma_f32_16x16x32_bf16 v[28:31], a[28:31], v[132:135], v[28:31]\n\t"
    "s_waitcnt lgkmcnt(0)\n\t"
    "v_fmac_f32 v67, v61, v41\n\t"
    "v_mfma_f32_16x16x32_bf16 v[16:19], a[32:35], v[136:139], v[16:19]\n\t"
    "ds_read2st64_b64 v[128:131], v3 offset0:8 offset1:9\n\t"
    "v_mfma_f32_16x16x32_bf16 v[16:19], a[48:51], v[140:143], v[16:19]\n\t"
    "v_mul_f32 v69, v66, v67\n\t"
    "v_mfma_f32_16x16x32_bf16 v[20:23], a[36:39], v[136:139], v[20:23]\n\t"
    "ds_read2st64_b64 v[132:135], v3 offset0:10 offset1:11\n\t"
    "v_mfma_f32_16x16x32_bf16 v[20:23], a[52:55], v[140:143], v[20:23]\n\t"
    "s_add_u32 m0, s35, 4096\n\t"
    "v_cvt_pk_bf16_f32 v120, v68, v69\n\t"
    "v_mfma_f32_16x16x32_bf16 v[24:27], a[40:43], v[136:139], v[24:27]\n\t"
    "buffer_load_dwordx4 v15, s[4:7], s33 offen offset:2048 lds\n\t"
    "v_mfma_f32_16x16x32_bf16 v[24:27], a[56:59], v[140:143], v[24:27]\n\t"
    "v_fma_f32 v64, -v62, v74, v85\n\t"
    "v_mfma_f32_16x16x32_bf16 v[28:31], a[44:47], v[136:139], v[28:31]\n\t"
    "v_fma_f32 v65, -v62, v96, v106\n\t"
    "v_mfma_f32_16x16x32_bf16 v[28:31], a[60:63], v[140:143], v[28:31]\n\t"
    "s_waitcnt lgkmcnt(0)\n\t"
    "v_fma_f32 v64, v60, v34, v64 clamp\n\t"
    "v_mfma_f32_16x16x32_bf16 v[16:19], a[64:67], v[128:131], v[16:19]\n\t"
    "ds_read2st64_b64 v[136:139], v3 offset0:12 offset1:13\n\t"
    "v_mfma_f32_16x16x32_bf16 v[16:19], a[80:83], v[132:135], v[16:19]\n\t"
    "v_fmac_f32 v65, v61, v42\n\t"
    "v_mfma_f32_16x16x32_bf16 v[20:23], a[68:71], v[128:131], v[20:23]\n\t"
    "ds_read2st64_b64 v[140:143], v3 offset0:14 offset1:15\n\t"
    "v_mfma_f32_16x16x32_bf16 v[20:23], a[84:87], v[132:135], v[20:23]\n\t"
    "s_add_u32 m0, s35, 4096\n\t"
    "v_mul_f32 v68, v64, v65\n\t"
    "v_mfma_f32_16x16x32_bf16 v[24:27], a[72:75], v[128:131], v[24:27]\n\t"
    "buffer_load_dwordx4 v15, s[4:7], s33 offen offset:3072 lds\n\t"
    "v_mfma_f32_16x16x32_bf16 v[24:27], a[88:91], v[132:135], v[24:27]\n\t"
    "v_fma_f32 v66, -v62, v76, v86\n\t"
    "v_mfma_f32_16x16x32_bf16 v[28:31], a[76:79], v[128:131], v[28:31]\n\t"
    "v_fma_f32 v67, -v62, v97, v108\n\t"
    "v_mfma_f32_16x16x32_bf16 v[28:31], a[92:95], v[132:135], v[28:31]\n\t"
    "s_waitcnt lgkmcnt(0)\n\t"
    "v_fma_f32 v66, v60, v35, v66 clamp\n\t"
    "v_mfma_f32_16x16x32_bf16 v[16:19], a[96:99], v[136:139], v[16:19]\n\t"
    "ds_read2st64_b64 v[128:131], v3 offset0:16 offset1:17\n\t"
    "v_mfma_f32_16x16x32_bf16 v[16:19], a[112:115], v[140:143], v[16:19]\n\t"
    "v_fmac_f32 v67, v61, v43\n\t"
    "v_mfma_f32_16x16x32_bf16 v[20:23], a[100:103], v[136:139], v[20:23]\n\t"
    "ds_read2st64_b64 v[132:135], v3 offset0:18 offset1:19\n\t"
    "v_mfma_f32_16x16x32_bf16 v[20:23], a[116:119], v[140:143], v[20:23]\n\t"
    "s_add_u32 m0, s35, 8192\n\t"
    "v_mul_f32 v69, v66, v67\n\t"
    "v_mfma_f32_16x16x32_bf16 v[24:27], a[104:107], v[136:139], v[24:27]\n\t"
    "buffer_load_dwordx4 v15, s[4:7], s34 offen offset:0 lds\n\t"
    "v_mfma_f32_16x16x32_bf16 v[24:27], a[120:123], v[140:143], v[24:27]\n\t"
    "v_cvt_pk_bf16_f32 v121, v68, v69\n\t"
    "v_mfma_f32_16x16x32_bf16 v[28:31], a[108:111], v[136:139], v[28:31]\n\t"
    "v_fma_f32 v64, -v62, v77, v88\n\t"
    "v_mfma_f32_16x16x32_bf16 v[28:31], a[124:127], v[140:143], v[28:31]\n\t"
    "s_waitcnt lgkmcnt(0)\n\t"
    "v_fma_f32 v65, -v62, v98, v109\n\t"
    "v_mfma_f32_16x16x32_bf16 v[16:19], a[128:131], v[128:131], v[16:19]\n\t"
    "ds_read2st64_b64 v[136:139], v3 offset0:20 offset1:21\n\t"
    "v_mfma_f32_16x16x32_bf16 v[16:19], a[144:147], v[132:135], v[16:19]\n\t"
    "v_fma_f32 v64, v60, v36, v64 clamp\n\t"
    "v_mfma_f32_16x16x32_bf16 v[20:23], a[132:135], v[128:131], v[20:23]\n\t"
    "ds_read2st64_b64 v[140:143], v3 offset0:22 offset1:23\n\t"
    "v_mfma_f32_16x16x32_bf16 v[20:23], a[148:151], v[132:135], v[20:23]\n\t"
    "s_mov_b32 m0, s25\n\t"
    "s_add_u32 s25, s25, 18432\n\t"
    "buffer_load_dwordx4 v15, s[12:15], s24 offen offset:0 sc1 lds\n\t"
    "s_cmp_ge_u32 s25, 163840\n\t"
    "s_cselect_b32 s28, 92160, 0\n\t"
    "s_sub_u32 s25, s25, s28\n\t"
    "v_mfma_f32_16x16x32_bf16 v[24:27], a[136:139], v[128:131], v[24:27]\n\t"
    "v_fmac_f32 v65, v61, v44\n\t"
    "v_mfma_f32_16x16x32_bf16 v[24:27], a[152:155], v[132:135], v[24:27]\n\t"
    "v_mul_f32 v68, v64, v65\n\t"
    "v_mfma_f32_16x16x32_bf16 v[28:31], a[140:143], v[128:131], v[28:31]\n\t"
    "v_fma_f32 v66, -v62, v78, v89\n\t"
    "v_mfma_f32_16x16x32_bf16 v[28:31], a[156:159], v[132:135], v[28:31]\n\t"
    "s_waitcnt lgkmcnt(0)\n\t"
    "v_fma_f32 v67, -v62, v100, v110\n\t"
    "v_mfma_f32_16x16x32_bf16 v[16:19], a[160:163], v[136:139], v[16:19]\n\t"
    "ds_read2st64_b64 v[128:131], v3 offset0:24 offset1:25\n\t"
    "v_mfma_f32_16x16x32_bf16 v[16:19], a[176:179], v[140:143], v[16:19]\n\t"
    "buffer_load_dwordx4 v15, s[12:15], s24 offen offset:1024 sc1 lds\n\t"
    "v_mfma_f32_16x16x32_bf16 v[20:23], a[164:167], v[136:139], v[20:23]\n\t"
    "ds_read2st64_b64 v[132:135], v3 offset0:26 offset1:27\n\t"
    "v_mfma_f32_16x16x32_bf16 v[20:23], a[180:183], v[140:143], v[20:23]\n\t"
    "v_fma_f32 v66, v60, v37, v66 clamp\n\t"
    "v_mfma_f32_16x16x32_bf16 v[24:27], a[168:171], v[136:139], v[24:27]\n\t"
    "v_fmac_f32 v67, v61, v45\n\t"
    "v_mfma_f32_16x16x32_bf16 v[24:27], a[184:187], v[140:143], v[24:27]\n\t"
    "v_mul_f32 v69, v66, v67\n\t"
    "v_mfma_f32_16x16x32_bf16 v[28:31], a[172:175], v[136:139], v[28:31]\n\t"
    "v_cvt_pk_bf16_f32 v122, v68, v69\n\t"
    "v_mfma_f32_16x16x32_bf16 v[28:31], a[188:191], v[140:143], v[28:31]\n\t"
    "s_waitcnt lgkmcnt(0)\n\t"
    "buffer_load_dwordx4 v15, s[12:15], s24 offen offset:2048 sc1 lds\n\t"
    "v_mfma_f32_16x16x32_bf16 v[16:19], a[192:195], v[128:131], v[16:19]\n\t"
    "ds_read2st64_b64 v[136:139], v3 offset0:28 offset1:29\n\t"
    "v_mfma_f32_16x16x32_bf16 v[16:19], a[208:211], v[132:135], v[16:19]\n\t"
    "v_fma_f32 v64, -v62, v80, v90\n\t"
    "v_mfma_f32_16x16x32_bf16 v[20:23], a[196:199], v[128:131], v[20:23]\n\t"
    "ds_read2st64_b64 v[140:143], v3 offset0:30 offset1:31\n\t"
    "v_mfma_f32_16x16x32_bf16 v[20:23], a[212:215], v[132:135], v[20:23]\n\t"
    "v_fma_f32 v65, -v62, v101, v112\n\t"
    "v_mfma_f32_16x16x32_bf16 v[24:27], a[200:203], v[128:131], v[24:27]\n\t"
    "v_fma_f32 v64, v60, v38, v64 clamp\n\t"
    "v_mfma_f32_16x16x32_bf16 v[24:27], a[216:219], v[132:135], v[24:27]\n\t"
    "buffer_load_dwordx4 v15, s[12:15], s24 offen offset:3072 sc1 lds\n\t"
    "v_mfma_f32_16x16x32_bf16 v[28:31], a[204:207], v[128:131], v[28:31]\n\t"
    "v_fmac_f32 v65, v61, v46\n\t"
    "v_mfma_f32_16x16x32_bf16 v[28:31], a[220:223], v[132:135], v[28:31]\n\t"
    "s_waitcnt lgkmcnt(0)\n\t"
    "v_mul_f32 v68, v64, v65\n\t"
    "v_mfma_f32_16x16x32_bf16 v[16:19], a[224:227], v[136:139], v[16:19]\n\t"
    "ds_read2st64_b64 v[128:131], v3 offset0:32 offset1:33\n\t"
    "s_add_u32 s26, s26, 18432\n\t"
    "v_mfma_f32_16x16x32_bf16 v[16:19], a[240:243], v[140:143], v[16:19]\n\t"
    "s_cmp_ge_u32 s26, 163840\n\t"
    "v_fma_f32 v66, -v62, v81, v92\n\t"
    "v_mfma_f32_16x16x32_bf16 v[20:23], a[228:231], v[136:139], v[20:23]\n\t"
    "ds_read2st64_b64 v[132:135], v3 offset0:34 offset1:35\n\t"
    "s_cselect_b32 s26, 71680, s26\n\t"
    "v_mfma_f32_16x16x32_bf16 v[20:23], a[244:247], v[140:143], v[20:23]\n\t"
    "buffer_load_dwordx4 v15, s[12:15], s24 offen offset:3584 sc1 lds\n\t"
    "s_add_u32 s24, s24, 129024\n\t"
    "v_mfma_f32_16x16x32_bf16 v[24:27], a[232:235], v[136:139], v[24:27]\n\t"
    "v_fma_f32 v67, -v62, v102, v113\n\t"
    "v_mfma_f32_16x16x32_bf16 v[24:27], a[248:251], v[140:143], v[24:27]\n\t"
    "v_add_u32 v3, s26, v2\n\t"
    "v_mfma_f32_16x16x32_bf16 v[28:31], a[236:239], v[136:139], v[28:31]\n\t"
    "v_fma_f32 v66, v60, v39, v66 clamp\n\t"
    "v_mfma_f32_16x16x32_bf16 v[28:31], a[252:255], v[140:143], v[28:31]\n\t"
    "s_waitcnt lgkmcnt(0)\n\t"
    "s_waitcnt vmcnt(22)\n\t"
    "s_barrier\n\t"
    "v_fmac_f32 v67, v61, v47\n\t"
    "v_mfma_f32_16x16x32_bf16 v[16:19], v[224:227], v[128:131], v[16:19]\n\t"
    "ds_read2st64_b64 v[136:139], v3 offset0:0 offset1:1\n\t"
    "v_mfma_f32_16x16x32_bf16 v[16:19], v[240:243], v[132:135], v[16:19]\n\t"
    "v_cmp_ne_u32 vcc, s66, v53\n\t"
    "v_cmp_ne_u32 s[30:31], s67, v55\n\t"
    "v_mfma_f32_16x16x32_bf16 v[20:23], v[228:231], v[128:131], v[20:23]\n\t"
    "ds_read2st64_b64 v[140:143], v3 offset0:2 offset1:3\n\t"
    "v_mfma_f32_16x16x32_bf16 v[20:23], v[244:247], v[132:135], v[20:23]\n\t"
    "s_or_b64 vcc, vcc, s[30:31]\n\t"
    "s_cbranch_vccnz L_ss07_%=\n\t"
    "L_sr07_%=:\n\t"
    "v_mov_b32 v58, v52\n\t"
    "s_xor_b32 s28, s68, 2\n\t"
    "s_mul_i32 s28, s28, 40960\n\t"
    "s_add_u32 s28, s28, 2048\n\t"
    "buffer_store_dwordx4 v[116:119], v15, s[56:59], s28 offen offset:1024 sc1\n\t"
    "v_mfma_f32_16x16x32_bf16 v[24:27], v[232:235], v[128:131], v[24:27]\n\t"
    "v_mul_f32 v56, s78, v54\n\t"
    "v_mul_f32 v69, v66, v67\n\t"
    "v_mfma_f32_16x16x32_bf16 v[24:27], v[248:251], v[132:135], v[24:27]\n\t"
    "v_mul_f32 v57, s79, v54\n\t"
    "v_cvt_pk_bf16_f32 v123, v68, v69\n\t"
    "v_mfma_f32_16x16x32_bf16 v[28:31], v[236:239], v[128:131], v[28:31]\n\t"
    "buffer_store_dwordx4 v[120:123], v15, s[16:19], s27 offen sc1\n\t"
    "v_mfma_f32_16x16x32_bf16 v[28:31], v[252:255], v[132:135], v[28:31]\n\t"
    "s_mov_b32 s27, s38\n\t"
    "s_add_u32 s38, s38, 1032192\n\t"
    "s_waitcnt lgkmcnt(0)\n\t"
    "v_mfma_f32_16x16x32_bf16 v[32:35], a[0:3], v[136:139], 0\n\t"
    "ds_read2st64_b64 v[128:131], v3 offset0:4 offset1:5\n\t"
    "v_mfma_f32_16x16x32_bf16 v[32:35], a[16:19], v[140:143], v[32:35]\n\t"
    "global_load_dwordx4 v[52:55], v5, s[36:37] offset:0 sc1\n\t"
    "v_add_u32 v204, 65536, v15\n\t"
    "v_mfma_f32_16x16x32_bf16 v[36:39], a[4:7], v[136:139], 0\n\t"
    "ds_read2st64_b64 v[132:135], v3 offset0:6 offset1:7\n\t"
    "v_add_u32 v205, 131072, v15\n\t"
    "v_mfma_f32_16x16x32_bf16 v[36:39], a[20:23], v[140:143], v[36:39]\n\t"
    "s_mov_b32 m0, s25\n\t"
    "s_add_u32 s25, s25, 18432\n\t"
    "buffer_load_dwordx4 v15, s[12:15], s24 offen offset:0 sc1 lds\n\t"
    "s_cmp_ge_u32 s25, 163840\n\t"
    "s_cselect_b32 s28, 92160, 0\n\t"
    "s_sub_u32 s25, s25, s28\n\t"
    "s_lshl_b32 s90, s3, 4\n\t"
    "v_mfma_f32_16x16x32_bf16 v[40:43], a[8:11], v[136:139], 0\n\t"
    "v_add_u32 v5, 1792, v5\n\t"
    "s_add_u32 s91, s69, s90\n\t"
    "v_mfma_f32_16x16x32_bf16 v[40:43], a[24:27], v[140:143], v[40:43]\n\t"
    "s_mul_i32 s35, s91, 144\n\t"
    "s_mul_i32 s90, s68, 36\n\t"
    "v_fma_f32 v64, -v58, v72, v82\n\t"
    "v_fma_f32 v65, -v58, v93, v104\n\t"
    "v_fma_f32 v64, v56, v16, v64 clamp\n\t"
    "v_mfma_f32_16x16x32_bf16 v[44:47], a[12:15], v[136:139], 0\n\t"
    "s_add_u32 s35, s35, s90\n\t"
    "s_lshl_b32 s35, s35, 10\n\t"
    "v_fmac_f32 v65, v57, v24\n\t"
    "v_mfma_f32_16x16x32_bf16 v[44:47], a[28:31], v[140:143], v[44:47]\n\t"
    "s_waitcnt lgkmcnt(0)\n\t"
    "s_add_u32 s35, s35, 5373952\n\t"
    "s_add_u32 s4, s36, s35\n\t"
    "s_addc_u32 s5, s37, 0\n\t"
    "v_mul_f32 v68, v64, v65\n\t"
    "v_mfma_f32_16x16x32_bf16 v[32:35], a[32:35], v[128:131], v[32:35]\n\t"
    "ds_read2st64_b64 v[136:139], v3 offset0:8 offset1:9\n\t"
    "s_mov_b32 s6, 9474048\n\t"
    "v_mfma_f32_16x16x32_bf16 v[32:35], a[48:51], v[132:135], v[32:35]\n\t"
    "s_mov_b32 s7, 0x20000\n\t"
    "v_mov_b32 v196, v15\n\t"
    "v_fma_f32 v66, -v58, v73, v84\n\t"
    "v_mfma_f32_16x16x32_bf16 v[36:39], a[36:39], v[128:131], v[36:39]\n\t"
    "ds_read2st64_b64 v[140:143], v3 offset0:10 offset1:11\n\t"
    "v_add_u32 v197, 9437184, v15\n\t"
    "v_mfma_f32_16x16x32_bf16 v[36:39], a[52:55], v[132:135], v[36:39]\n\t"
    "v_min_u32 v146, 17, v1\n\t"
    "v_and_b32 v144, 1, v146\n\t"
    "v_fma_f32 v67, -v58, v94, v105\n\t"
    "v_mfma_f32_16x16x32_bf16 v[40:43], a[40:43], v[128:131], v[40:43]\n\t"
    "v_lshlrev_b32 v144, 6, v144\n\t"
    "v_add_u32 v144, s91, v144\n\t"
    "v_fma_f32 v66, v56, v17, v66 clamp\n\t"
    "v_mfma_f32_16x16x32_bf16 v[40:43], a[56:59], v[132:135], v[40:43]\n\t"
    "buffer_load_dwordx4 v15, s[12:15], s24 offen offset:1024 sc1 lds\n\t"
    "v_mul_u32_u24 v145, 37450, v144\n\t"
    "v_mfma_f32_16x16x32_bf16 v[44:47], a[44:47], v[128:131], v[44:47]\n\t"
    "v_lshrrev_b32 v145, 18, v145\n\t"
    "v_mul_u32_u24 v145, 7, v145\n\t"
    "v_fmac_f32 v67, v57, v25\n\t"
    "v_mfma_f32_16x16x32_bf16 v[44:47], a[60:63], v[132:135], v[44:47]\n\t"
    "s_waitcnt lgkmcnt(0)\n\t"
    "v_sub_u32 v144, v144, v145\n\t"
    "v_lshrrev_b32 v145, 1, v146\n\t"
    "v_mul_f32 v69, v66, v67\n\t"
    "v_mfma_f32_16x16x32_bf16 v[32:35], a[64:67], v[136:139], v[32:35]\n\t"
    "ds_read2st64_b64 v[128:131], v3 offset0:12 offset1:13\n\t"
    "v_mad_u32_u24 v144, v145, 7, v144\n\t"
    "v_mfma_f32_16x16x32_bf16 v[32:35], a[80:83], v[140:143], v[32:35]\n\t"
    "v_lshrrev_b32 v145, 5, v144\n\t"
    "v_and_b32 v144, 31, v144\n\t"
    "v_cvt_pk_bf16_f32 v120, v68, v69\n\t"
    "v_mfma_f32_16x16x32_bf16 v[36:39], a[68:71], v[136:139], v[36:39]\n\t"
    "ds_read2st64_b64 v[132:135], v3 offset0:14 offset1:15\n\t"
    "v_lshl_add_u32 v144, v144, 3, v145\n\t"
    "v_mfma_f32_16x16x32_bf16 v[36:39], a[84:87], v[140:143], v[36:39]\n\t"
    "s_lshl_b32 s35, s68, 1\n\t"
    "v_add_u32 v144, s35, v144\n\t"
    "v_fma_f32 v64, -v58, v74, v85\n\t"
    "v_mfma_f32_16x16x32_bf16 v[40:43], a[72:75], v[136:139], v[40:43]\n\t"
    "s_xor_b32 s28, s68, 3\n\t"
    "s_mul_i32 s28, s28, 40960\n\t"
    "s_add_u32 s28, s28, 4096\n\t"
    "buffer_store_dwordx4 v[116:119], v15, s[56:59], s28 offen offset:0 sc1\n\t"
    "v_lshlrev_b32 v144, 7, v144\n\t"
    "v_mfma_f32_16x16x32_bf16 v[40:43], a[88:91], v[140:143], v[40:43]\n\t"
    "v_add_u32 v144, 41947136, v144\n\t"
    "s_mul_i32 s35, s70, 144\n\t"
    "v_fma_f32 v65, -v58, v96, v106\n\t"
    "v_mfma_f32_16x16x32_bf16 v[44:47], a[76:79], v[136:139], v[44:47]\n\t"
    "s_mul_i32 s90, s68, 36\n\t"
    "s_add_u32 s35, s35, s90\n\t"
    "v_fma_f32 v64, v56, v18, v64 clamp\n\t"
    "v_mfma_f32_16x16x32_bf16 v[44:47], a[92:95], v[140:143], v[44:47]\n\t"
    "s_waitcnt lgkmcnt(0)\n\t"
    "v_add_u32 v145, s35, v1\n\t"
    "v_mov_b32_dpp v216, v144 quad_perm:[0,0,2,2] row_mask:0xf bank_mask:0xf\n\t"
    "v_fmac_f32 v65, v57, v26\n\t"
    "v_mfma_f32_16x16x32_bf16 v[32:35], a[96:99], v[128:131], v[32:35]\n\t"
    "ds_read2st64_b64 v[136:139], v3 offset0:16 offset1:17\n\t"
    "v_mov_b32_dpp v213, v144 quad_perm:[1,1,3,3] row_mask:0xf bank_mask:0xf\n\t"
    "v_mfma_f32_16x16x32_bf16 v[32:35], a[112:115], v[132:135], v[32:35]\n\t"
    "v_and_b32 v146, 3, v1\n\t"
    "v_xor_b32 v146, s2, v146\n\t"
    "v_mul_f32 v68, v64, v65\n\t"
    "v_mfma_f32_16x16x32_bf16 v[36:39], a[100:103], v[128:131], v[36:39]\n\t"
    "ds_read2st64_b64 v[140:143], v3 offset0:18 offset1:19\n\t"
    "v_lshlrev_b32 v146, 7, v146\n\t"
    "v_mfma_f32_16x16x32_bf16 v[36:39], a[116:119], v[132:135], v[36:39]\n\t"
    "v_add_u32 v146, 41947144, v146\n\t"
    "v_lshlrev_b32 v145, 7, v145\n\t"
    "v_fma_f32 v66, -v58, v76, v86\n\t"
    "v_mfma_f32_16x16x32_bf16 v[40:43], a[104:107], v[128:131], v[40:43]\n\t"
    "buffer_load_dwordx4 v15, s[12:15], s24 offen offset:2048 sc1 lds\n\t"
    "v_cmp_gt_u32 vcc, 36, v1\n\t"
    "v_mfma_f32_16x16x32_bf16 v[40:43], a[120:123], v[132:135], v[40:43]\n\t"
    "v_add_u32 v145, 42024960, v145\n\t"
    "v_add_u32 v216, 8, v216\n\t"
    "v_fma_f32 v67, -v58, v97, v108\n\t"
    "v_mfma_f32_16x16x32_bf16 v[44:47], a[108:111], v[128:131], v[44:47]\n\t"
    "v_cndmask_b32 v218, v146, v145, vcc\n\t"
    "v_fma_f32 v66, v56, v19, v66 clamp\n\t"
    "v_mfma_f32_16x16x32_bf16 v[44:47], a[124:127], v[132:135], v[44:47]\n\t"
    "s_waitcnt lgkmcnt(0)\n\t"
    "s_lshl_b32 s35, s2, 7\n\t"
    "s_add_u32 s35, s35, 41947144\n\t"
    "v_fmac_f32 v67, v57, v27\n\t"
    "v_mfma_f32_16x16x32_bf16 v[32:35], a[128:131], v[136:139], v[32:35]\n\t"
    "ds_read2st64_b64 v[128:131], v3 offset0:20 offset1:21\n\t"
    "v_writelane_b32 v63, s35, 1\n\t"
    "v_mfma_f32_16x16x32_bf16 v[32:35], a[144:147], v[140:143], v[32:35]\n\t"
    "s_cmp_eq_u32 s3, 0\n\t"
    "s_cselect_b32 s35, 2, 0\n\t"
    "s_or_b32 s80, s80, s35\n\t"
    "v_mul_f32 v69, v66, v67\n\t"
    "v_mfma_f32_16x16x32_bf16 v[36:39], a[132:135], v[136:139], v[36:39]\n\t"
    "ds_read2st64_b64 v[132:135], v3 offset0:22 offset1:23\n\t"
    "v_mfma_f32_16x16x32_bf16 v[36:39], a[148:151], v[140:143], v[36:39]\n\t"
    "v_cvt_pk_bf16_f32 v121, v68, v69\n\t"
    "v_mfma_f32_16x16x32_bf16 v[40:43], a[136:139], v[136:139], v[40:43]\n\t"
    "v_fma_f32 v64, -v58, v77, v88\n\t"
    "v_mfma_f32_16x16x32_bf16 v[40:43], a[152:155], v[140:143], v[40:43]\n\t"
    "v_fma_f32 v65, -v58, v98, v109\n\t"
    "v_mfma_f32_16x16x32_bf16 v[44:47], a[140:143], v[136:139], v[44:47]\n\t"
    "v_fma_f32 v64, v56, v20, v64 clamp\n\t"
    "v_mfma_f32_16x16x32_bf16 v[44:47], a[156:159], v[140:143], v[44:47]\n\t"
    "s_waitcnt lgkmcnt(0)\n\t"
    "v_fmac_f32 v65, v57, v28\n\t"
    "v_mfma_f32_16x16x32_bf16 v[32:35], a[160:163], v[128:131], v[32:35]\n\t"
    "ds_read2st64_b64 v[136:139], v3 offset0:24 offset1:25\n\t"
    "v_mfma_f32_16x16x32_bf16 v[32:35], a[176:179], v[132:135], v[32:35]\n\t"
    "v_mul_f32 v68, v64, v65\n\t"
    "v_mfma_f32_16x16x32_bf16 v[36:39], a[164:167], v[128:131], v[36:39]\n\t"
    "ds_read2st64_b64 v[140:143], v3 offset0:26 offset1:27\n\t"
    "v_mfma_f32_16x16x32_bf16 v[36:39], a[180:183], v[132:135], v[36:39]\n\t"
    "v_fma_f32 v66, -v58, v78, v89\n\t"
    "v_mfma_f32_16x16x32_bf16 v[40:43], a[168:171], v[128:131], v[40:43]\n\t"
    "buffer_load_dwordx4 v15, s[12:15], s24 offen offset:3072 sc1 lds\n\t"
    "v_mfma_f32_16x16x32_bf16 v[40:43], a[184:187], v[132:135], v[40:43]\n\t"
    "v_fma_f32 v67, -v58, v100, v110\n\t"
    "v_mfma_f32_16x16x32_bf16 v[44:47], a[172:175], v[128:131], v[44:47]\n\t"
    "v_fma_f32 v66, v56, v21, v66 clamp\n\t"
    "v_mfma_f32_16x16x32_bf16 v[44:47], a[188:191], v[132:135], v[44:47]\n\t"
    "s_waitcnt lgkmcnt(0)\n\t"
    "v_fmac_f32 v67, v57, v29\n\t"
    "v_mfma_f32_16x16x32_bf16 v[32:35], a[192:195], v[136:139], v[32:35]\n\t"
    "ds_read2st64_b64 v[128:131], v3 offset0:28 offset1:29\n\t"
    "v_mfma_f32_16x16x32_bf16 v[32:35], a[208:211], v[140:143], v[32:35]\n\t"
    "v_mul_f32 v69, v66, v67\n\t"
    "v_mfma_f32_16x16x32_bf16 v[36:39], a[196:199], v[136:139], v[36:39]\n\t"
    "ds_read2st64_b64 v[132:135], v3 offset0:30 offset1:31\n\t"
    "v_mfma_f32_16x16x32_bf16 v[36:39], a[212:215], v[140:143], v[36:39]\n\t"
    "v_cvt_pk_bf16_f32 v122, v68, v69\n\t"
    "v_mfma_f32_16x16x32_bf16 v[40:43], a[200:203], v[136:139], v[40:43]\n\t"
    "buffer_load_dwordx4 v15, s[12:15], s24 offen offset:3584 sc1 lds\n\t"
    "s_add_u32 s24, s24, 129024\n\t"
    "v_mfma_f32_16x16x32_bf16 v[40:43], a[216:219], v[140:143], v[40:43]\n\t"
    "v_fma_f32 v64, -v58, v80, v90\n\t"
    "v_mfma_f32_16x16x32_bf16 v[44:47], a[204:207], v[136:139], v[44:47]\n\t"
    "v_fma_f32 v65, -v58, v101, v112\n\t"
    "v_mfma_f32_16x16x32_bf16 v[44:47], a[220:223], v[140:143], v[44:47]\n\t"
    "s_waitcnt lgkmcnt(0)\n\t"
    "v_fma_f32 v64, v56, v22, v64 clamp\n\t"
    "v_mfma_f32_16x16x32_bf16 v[32:35], a[224:227], v[128:131], v[32:35]\n\t"
    "ds_read2st64_b64 v[136:139], v3 offset0:32 offset1:33\n\t"
    "s_add_u32 s26, s26, 18432\n\t"
    "v_mfma_f32_16x16x32_bf16 v[32:35], a[240:243], v[132:135], v[32:35]\n\t"
    "s_cmp_ge_u32 s26, 163840\n\t"
    "v_fmac_f32 v65, v57, v30\n\t"
    "v_mfma_f32_16x16x32_bf16 v[36:39], a[228:231], v[128:131], v[36:39]\n\t"
    "ds_read2st64_b64 v[140:143], v3 offset0:34 offset1:35\n\t"
    "s_cselect_b32 s26, 71680, s26\n\t"
    "v_mfma_f32_16x16x32_bf16 v[36:39], a[244:247], v[132:135], v[36:39]\n\t"
    "v_mul_f32 v68, v64, v65\n\t"
    "v_mfma_f32_16x16x32_bf16 v[40:43], a[232:235], v[128:131], v[40:43]\n\t"
    "v_fma_f32 v66, -v58, v81, v92\n\t"
    "v_mfma_f32_16x16x32_bf16 v[40:43], a[248:251], v[132:135], v[40:43]\n\t"
    "v_add_u32 v3, s26, v2\n\t"
    "v_mfma_f32_16x16x32_bf16 v[44:47], a[236:239], v[128:131], v[44:47]\n\t"
    "s_waitcnt vmcnt(14)\n\t"
    "v_fma_f32 v67, -v58, v102, v113\n\t"
    "v_mfma_f32_16x16x32_bf16 v[44:47], a[252:255], v[132:135], v[44:47]\n\t"
    "s_waitcnt lgkmcnt(0)\n\t"
    "s_waitcnt vmcnt(18)\n\t"
    "s_barrier\n\t"
    "s_nop 0\n\t"
    "v_fma_f32 v66, v56, v23, v66 clamp\n\t"
    "v_mfma_f32_16x16x32_bf16 v[32:35], v[224:227], v[136:139], v[32:35]\n\t"
    "ds_read2st64_b64 v[128:131], v3 offset0:0 offset1:1\n\t"
    "v_mfma_f32_16x16x32_bf16 v[32:35], v[240:243], v[140:143], v[32:35]\n\t"
    "v_cmp_ne_u32 vcc, s66, v49\n\t"
    "v_cmp_ne_u32 s[30:31], s67, v51\n\t"
    "v_mfma_f32_16x16x32_bf16 v[36:39], v[228:231], v[136:139], v[36:39]\n\t"
    "ds_read2st64_b64 v[132:135], v3 offset0:2 offset1:3\n\t"
    "v_mfma_f32_16x16x32_bf16 v[36:39], v[244:247], v[140:143], v[36:39]\n\t"
    "s_or_b64 vcc, vcc, s[30:31]\n\t"
    "s_cbranch_vccnz L_ss18_%=\n\t"
    "L_sr18_%=:\n\t"
    "v_mov_b32 v62, v48\n\t"
    "v_fmac_f32 v67, v57, v31\n\t"
    "v_mfma_f32_16x16x32_bf16 v[40:43], v[232:235], v[136:139], v[40:43]\n\t"
    "v_mul_f32 v60, s78, v50\n\t"
    "v_mul_f32 v69, v66, v67\n\t"
    "v_mfma_f32_16x16x32_bf16 v[40:43], v[248:251], v[140:143], v[40:43]\n\t"
    "v_mul_f32 v61, s79, v50\n\t"
    "v_cvt_pk_bf16_f32 v123, v68, v69\n\t"
    "v_mfma_f32_16x16x32_bf16 v[44:47], v[236:239], v[136:139], v[44:47]\n\t"
    "buffer_store_dwordx4 v[120:123], v15, s[16:19], s27 offen sc1\n\t"
    "v_mfma_f32_16x16x32_bf16 v[44:47], v[252:255], v[140:143], v[44:47]\n\t"
    "s_mov_b32 s27, s38\n\t"
    "s_add_u32 s38, s38, 1032192\n\t"
    "s_waitcnt lgkmcnt(0)\n\t"
    "v_mfma_f32_16x16x32_bf16 v[16:19], a[0:3], v[128:131], 0\n\t"
    "ds_read2st64_b64 v[136:139], v3 offset0:4 offset1:5\n\t"
    "v_mfma_f32_16x16x32_bf16 v[16:19], a[16:19], v[132:135], v[16:19]\n\t"
    "global_load_dwordx4 v[48:51], v5, s[36:37] offset:0 sc1\n\t"
    "v_mfma_f32_16x16x32_bf16 v[20:23], a[4:7], v[128:131], 0\n\t"
    "ds_read2st64_b64 v[140:143], v3 offset0:6 offset1:7\n\t"
    "v_mfma_f32_16x16x32_bf16 v[20:23], a[20:23], v[132:135], v[20:23]\n\t"
    "v_add_u32 v5, 1792, v5\n\t"
    "v_mfma_f32_16x16x32_bf16 v[24:27], a[8:11], v[128:131], 0\n\t"
    "ds_read_b64_tr_b16 v[144:145], v8 offset:0\n\t"
    "v_mfma_f32_16x16x32_bf16 v[24:27], a[24:27], v[132:135], v[24:27]\n\t"
    "s_mov_b32 m0, s25\n\t"
    "s_add_u32 s25, s25, 18432\n\t"
    "buffer_load_dwordx4 v15, s[12:15], s24 offen offset:0 sc1 lds\n\t"
    "s_cmp_ge_u32 s25, 163840\n\t"
    "s_cselect_b32 s28, 92160, 0\n\t"
    "s_sub_u32 s25, s25, s28\n\t"
    "v_mfma_f32_16x16x32_bf16 v[28:31], a[12:15], v[128:131], 0\n\t"
    "v_fma_f32 v64, -v62, v72, v82\n\t"
    "v_fma_f32 v65, -v62, v93, v104\n\t"
    "v_fma_f32 v64, v60, v32, v64 clamp\n\t"
    "v_fmac_f32 v65, v61, v40\n\t"
    "v_mul_f32 v68, v64, v65\n\t"
    "v_fma_f32 v66, -v62, v73, v84\n\t"
    "v_fma_f32 v67, -v62, v94, v105\n\t"
    "v_fma_f32 v66, v60, v33, v66 clamp\n\t"
    "v_fmac_f32 v67, v61, v41\n\t"
    "v_mul_f32 v69, v66, v67\n\t"
    "v_cvt_pk_bf16_f32 v120, v68, v69\n\t"
    "v_fma_f32 v64, -v62, v74, v85\n\t"
    "v_fma_f32 v65, -v62, v96, v106\n\t"
    "v_fma_f32 v64, v60, v34, v64 clamp\n\t"
    "v_fmac_f32 v65, v61, v42\n\t"
    "v_mul_f32 v68, v64, v65\n\t"
    "v_fma_f32 v66, -v62, v76, v86\n\t"
    "v_fma_f32 v67, -v62, v97, v108\n\t"
    "v_fma_f32 v66, v60, v35, v66 clamp\n\t"
    "v_fmac_f32 v67, v61, v43\n\t"
    "v_mul_f32 v69, v66, v67\n\t"
    "v_cvt_pk_bf16_f32 v121, v68, v69\n\t"
    "v_fma_f32 v64, -v62, v77, v88\n\t"
    "v_fma_f32 v65, -v62, v98, v109\n\t"
    "v_fma_f32 v64, v60, v36, v64 clamp\n\t"
    "v_mfma_f32_16x16x32_bf16 v[28:31], a[28:31], v[132:135], v[28:31]\n\t"
    "s_waitcnt lgkmcnt(1)\n\t"
    "ds_read_b64_tr_b16 v[146:147], v8 offset:18432\n\t"
    "v_mfma_f32_16x16x32_bf16 v[16:19], a[32:35], v[136:139], v[16:19]\n\t"
    "ds_read2st64_b64 v[128:131], v3 offset0:8 offset1:9\n\t"
    "v_mfma_f32_16x16x32_bf16 v[16:19], a[48:51], v[140:143], v[16:19]\n\t"
    "v_fmac_f32 v65, v61, v44\n\t"
    "v_mfma_f32_16x16x32_bf16 v[20:23], a[36:39], v[136:139], v[20:23]\n\t"
    "ds_read2st64_b64 v[132:135], v3 offset0:10 offset1:11\n\t"
    "v_mfma_f32_16x16x32_bf16 v[20:23], a[52:55], v[140:143], v[20:23]\n\t"
    "ds_read_b64_tr_b16 v[148:149], v8 offset:8\n\t"
    "v_mfma_f32_16x16x32_bf16 v[24:27], a[40:43], v[136:139], v[24:27]\n\t"
    "v_mul_f32 v68, v64, v65\n\t"
    "v_mfma_f32_16x16x32_bf16 v[24:27], a[56:59], v[140:143], v[24:27]\n\t"
    "v_fma_f32 v66, -v62, v78, v89\n\t"
    "v_mfma_f32_16x16x32_bf16 v[28:31], a[44:47], v[136:139], v[28:31]\n\t"
    "ds_read_b64_tr_b16 v[150:151], v8 offset:18440\n\t"
    "v_mfma_f32_16x16x32_bf16 v[28:31], a[60:63], v[140:143], v[28:31]\n\t"
    "s_waitcnt lgkmcnt(2)\n\t"
    "v_fma_f32 v67, -v62, v100, v110\n\t"
    "v_mfma_f32_16x16x32_bf16 v[16:19], a[64:67], v[128:131], v[16:19]\n\t"
    "ds_read2st64_b64 v[136:139], v3 offset0:12 offset1:13\n\t"
    "v_mfma_f32_16x16x32_bf16 v[16:19], a[80:83], v[132:135], v[16:19]\n\t"
    "ds_read_b64_tr_b16 v[152:153], v8 offset:64\n\t"
    "v_mfma_f32_16x16x32_bf16 v[20:23], a[68:71], v[128:131], v[20:23]\n\t"
    "ds_read2st64_b64 v[140:143], v3 offset0:14 offset1:15\n\t"
    "v_mfma_f32_16x16x32_bf16 v[20:23], a[84:87], v[132:135], v[20:23]\n\t"
    "buffer_load_dwordx4 v15, s[12:15], s24 offen offset:1024 sc1 lds\n\t"
    "v_mfma_f32_16x16x32_bf16 v[24:27], a[72:75], v[128:131], v[24:27]\n\t"
    "ds_read_b64_tr_b16 v[154:155], v8 offset:18496\n\t"
    "v_mfma_f32_16x16x32_bf16 v[24:27], a[88:91], v[132:135], v[24:27]\n\t"
    "buffer_store_dwordx4 v[144:147], v15, s[8:11], 0 offen offset:0 sc1\n\t"
    "v_mfma_f32_16x16x32_bf16 v[28:31], a[76:79], v[128:131], v[28:31]\n\t"
    "v_fma_f32 v66, v60, v37, v66 clamp\n\t"
    "v_mfma_f32_16x16x32_bf16 v[28:31], a[92:95], v[132:135], v[28:31]\n\t"
    "s_waitcnt lgkmcnt(1)\n\t"
    "ds_read_b64_tr_b16 v[156:157], v8 offset:72\n\t"
    "v_mfma_f32_16x16x32_bf16 v[16:19], a[96:99], v[136:139], v[16:19]\n\t"
    "ds_read2st64_b64 v[128:131], v3 offset0:16 offset1:17\n\t"
    "v_mfma_f32_16x16x32_bf16 v[16:19], a[112:115], v[140:143], v[16:19]\n\t"
    "v_fmac_f32 v67, v61, v45\n\t"
    "v_mfma_f32_16x16x32_bf16 v[20:23], a[100:103], v[136:139], v[20:23]\n\t"
    "ds_read2st64_b64 v[132:135], v3 offset0:18 offset1:19\n\t"
    "v_mfma_f32_16x16x32_bf16 v[20:23], a[116:119], v[140:143], v[20:23]\n\t"
    "ds_read_b64_tr_b16 v[158:159], v8 offset:18504\n\t"
    "v_mfma_f32_16x16x32_bf16 v[24:27], a[104:107], v[136:139], v[24:27]\n\t"
    "v_mul_f32 v69, v66, v67\n\t"
    "v_mfma_f32_16x16x32_bf16 v[24:27], a[120:123], v[140:143], v[24:27]\n\t"
    "v_cvt_pk_bf16_f32 v122, v68, v69\n\t"
    "v_mfma_f32_16x16x32_bf16 v[28:31], a[108:111], v[136:139], v[28:31]\n\t"
    "ds_read_b64_tr_b16 v[160:161], v8 offset:128\n\t"
    "v_mfma_f32_16x16x32_bf16 v[28:31], a[124:127], v[140:143], v[28:31]\n\t"
    "s_waitcnt lgkmcnt(2)\n\t"
    "v_fma_f32 v64, -v62, v80, v90\n\t"
    "v_mfma_f32_16x16x32_bf16 v[16:19], a[128:131], v[128:131], v[16:19]\n\t"
    "ds_read2st64_b64 v[136:139], v3 offset0:20 offset1:21\n\t"
    "v_mfma_f32_16x16x32_bf16 v[16:19], a[144:147], v[132:135], v[16:19]\n\t"
    "buffer_load_dwordx4 v15, s[12:15], s24 offen offset:2048 sc1 lds\n\t"
    "v_mfma_f32_16x16x32_bf16 v[20:23], a[132:135], v[128:131], v[20:23]\n\t"
    "ds_read2st64_b64 v[140:143], v3 offset0:22 offset1:23\n\t"
    "v_mfma_f32_16x16x32_bf16 v[20:23], a[148:151], v[132:135], v[20:23]\n\t"
    "ds_read_b64_tr_b16 v[162:163], v8 offset:18560\n\t"
    "v_mfma_f32_16x16x32_bf16 v[24:27], a[136:139], v[128:131], v[24:27]\n\t"
    "buffer_store_dwordx4 v[148:151], v15, s[8:11], 0 offen offset:1024 sc1\n\t"
    "v_mfma_f32_16x16x32_bf16 v[24:27], a[152:155], v[132:135], v[24:27]\n\t"
    "v_fma_f32 v65, -v62, v101, v112\n\t"
    "v_mfma_f32_16x16x32_bf16 v[28:31], a[140:143], v[128:131], v[28:31]\n\t"
    "ds_read_b64_tr_b16 v[164:165], v8 offset:136\n\t"
    "v_mfma_f32_16x16x32_bf16 v[28:31], a[156:159], v[132:135], v[28:31]\n\t"
    "s_waitcnt lgkmcnt(2)\n\t"
    "v_fma_f32 v64, v60, v38, v64 clamp\n\t"
    "v_mfma_f32_16x16x32_bf16 v[16:19], a[160:163], v[136:139], v[16:19]\n\t"
    "ds_read2st64_b64 v[128:131], v3 offset0:24 offset1:25\n\t"
    "v_mfma_f32_16x16x32_bf16 v[16:19], a[176:179], v[140:143], v[16:19]\n\t"
    "ds_read_b64_tr_b16 v[166:167], v8 offset:18568\n\t"
    "v_mfma_f32_16x16x32_bf16 v[20:23], a[164:167], v[136:139], v[20:23]\n\t"
    "ds_read2st64_b64 v[132:135], v3 offset0:26 offset1:27\n\t"
    "v_mfma_f32_16x16x32_bf16 v[20:23], a[180:183], v[140:143], v[20:23]\n\t"
    "v_fmac_f32 v65, v61, v46\n\t"
    "v_mfma_f32_16x16x32_bf16 v[24:27], a[168:171], v[136:139], v[24:27]\n\t"
    "ds_read_b64_tr_b16 v[168:169], v8 offset:192\n\t"
    "v_mfma_f32_16x16x32_bf16 v[24:27], a[184:187], v[140:143], v[24:27]\n\t"
    "v_mul_f32 v68, v64, v65\n\t"
    "v_mfma_f32_16x16x32_bf16 v[28:31], a[172:175], v[136:139], v[28:31]\n\t"
    "v_fma_f32 v66, -v62, v81, v92\n\t"
    "v_mfma_f32_16x16x32_bf16 v[28:31], a[188:191], v[140:143], v[28:31]\n\t"
    "s_waitcnt lgkmcnt(1)\n\t"
    "buffer_load_dwordx4 v15, s[12:15], s24 offen offset:3072 sc1 lds\n\t"
    "v_mfma_f32_16x16x32_bf16 v[16:19], a[192:195], v[128:131], v[16:19]\n\t"
    "ds_read2st64_b64 v[136:139], v3 offset0:28 offset1:29\n\t"
    "v_mfma_f32_16x16x32_bf16 v[16:19], a[208:211], v[132:135], v[16:19]\n\t"
    "ds_read_b64_tr_b16 v[170:171], v8 offset:18624\n\t"
    "v_mfma_f32_16x16x32_bf16 v[20:23], a[196:199], v[128:131], v[20:23]\n\t"
    "ds_read2st64_b64 v[140:143], v3 offset0:30 offset1:31\n\t"
    "v_mfma_f32_16x16x32_bf16 v[20:23], a[212:215], v[132:135], v[20:23]\n\t"
    "buffer_store_dwordx4 v[152:155], v15, s[8:11], 0 offen offset:2048 sc1\n\t"
    "v_mfma_f32_16x16x32_bf16 v[24:27], a[200:203], v[128:131], v[24:27]\n\t"
    "ds_read_b64_tr_b16 v[172:173], v8 offset:200\n\t"
    "v_mfma_f32_16x16x32_bf16 v[24:27], a[216:219], v[132:135], v[24:27]\n\t"
    "v_fma_f32 v67, -v62, v102, v113\n\t"
    "v_mfma_f32_16x16x32_bf16 v[28:31], a[204:207], v[128:131], v[28:31]\n\t"
    "v_fma_f32 v66, v60, v39, v66 clamp\n\t"
    "v_mfma_f32_16x16x32_bf16 v[28:31], a[220:223], v[132:135], v[28:31]\n\t"
    "s_waitcnt lgkmcnt(1)\n\t"
    "ds_read_b64_tr_b16 v[174:175], v8 offset:18632\n\t"
    "v_mfma_f32_16x16x32_bf16 v[16:19], a[224:227], v[136:139], v[16:19]\n\t"
    "ds_read2st64_b64 v[128:131], v3 offset0:32 offset1:33\n\t"
    "s_add_u32 s26, s26, 18432\n\t"
    "v_mfma_f32_16x16x32_bf16 v[16:19], a[240:243], v[140:143], v[16:19]\n\t"
    "s_cmp_ge_u32 s26, 163840\n\t"
    "v_fmac_f32 v67, v61, v47\n\t"
    "v_mfma_f32_16x16x32_bf16 v[20:23], a[228:231], v[136:139], v[20:23]\n\t"
    "ds_read2st64_b64 v[132:135], v3 offset0:34 offset1:35\n\t"
    "s_cselect_b32 s26, 71680, s26\n\t"
    "v_mfma_f32_16x16x32_bf16 v[20:23], a[244:247], v[140:143], v[20:23]\n\t"
    "buffer_load_dwordx4 v15, s[12:15], s24 offen offset:3584 sc1 lds\n\t"
    "s_add_u32 s24, s24, 129024\n\t"
    "v_mfma_f32_16x16x32_bf16 v[24:27], a[232:235], v[136:139], v[24:27]\n\t"
    "ds_read_b64_tr_b16 v[176:177], v9 offset:256\n\t"
    "v_mfma_f32_16x16x32_bf16 v[24:27], a[248:251], v[140:143], v[24:27]\n\t"
    "v_add_u32 v3, s26, v2\n\t"
    "v_mfma_f32_16x16x32_bf16 v[28:31], a[236:239], v[136:139], v[28:31]\n\t"
    "v_mul_f32 v69, v66, v67\n\t"
    "v_mfma_f32_16x16x32_bf16 v[28:31], a[252:255], v[140:143], v[28:31]\n\t"
    "s_waitcnt lgkmcnt(1)\n\t"
    "s_waitcnt vmcnt(16)\n\t"
    "s_barrier\n\t"
    "ds_read_b64_tr_b16 v[178:179], v9 offset:18688\n\t"
    "v_mfma_f32_16x16x32_bf16 v[16:19], v[224:227], v[128:131], v[16:19]\n\t"
    "ds_read2st64_b64 v[136:139], v3 offset0:0 offset1:1\n\t"
    "v_mfma_f32_16x16x32_bf16 v[16:19], v[240:243], v[132:135], v[16:19]\n\t"
    "v_cmp_ne_u32 vcc, s66, v53\n\t"
    "v_cmp_ne_u32 s[30:31], s67, v55\n\t"
    "v_mfma_f32_16x16x32_bf16 v[20:23], v[228:231], v[128:131], v[20:23]\n\t"
    "ds_read2st64_b64 v[140:143], v3 offset0:2 offset1:3\n\t"
    "v_mfma_f32_16x16x32_bf16 v[20:23], v[244:247], v[132:135], v[20:23]\n\t"
    "s_or_b64 vcc, vcc, s[30:31]\n\t"
    "s_cbranch_vccnz L_ss09_%=\n\t"
    "L_sr09_%=:\n\t"
    "v_mov_b32 v58, v52\n\t"
    "s_xor_b32 s28, s68, 3\n\t"
    "s_mul_i32 s28, s28, 40960\n\t"
    "s_add_u32 s28, s28, 4096\n\t"
    "buffer_store_dwordx4 v[116:119], v15, s[56:59], s28 offen offset:1024 sc1\n\t"
    "v_mfma_f32_16x16x32_bf16 v[24:27], v[232:235], v[128:131], v[24:27]\n\t"
    "v_mul_f32 v56, s78, v54\n\t"
    "buffer_store_dwordx4 v[156:159], v15, s[8:11], 0 offen offset:3072 sc1\n\t"
    "v_mfma_f32_16x16x32_bf16 v[24:27], v[248:251], v[132:135], v[24:27]\n\t"
    "v_mul_f32 v57, s79, v54\n\t"
    "v_cvt_pk_bf16_f32 v123, v68, v69\n\t"
    "v_mfma_f32_16x16x32_bf16 v[28:31], v[236:239], v[128:131], v[28:31]\n\t"
    "buffer_store_dwordx4 v[120:123], v15, s[16:19], s27 offen sc1\n\t"
    "v_mfma_f32_16x16x32_bf16 v[28:31], v[252:255], v[132:135], v[28:31]\n\t"
    "s_mov_b32 s27, s38\n\t"
    "s_add_u32 s38, s38, 1032192\n\t"
    "s_waitcnt lgkmcnt(0)\n\t"
    "v_mfma_f32_16x16x32_bf16 v[32:35], a[0:3], v[136:139], 0\n\t"
    "ds_read2st64_b64 v[128:131], v3 offset0:4 offset1:5\n\t"
    "v_mfma_f32_16x16x32_bf16 v[32:35], a[16:19], v[140:143], v[32:35]\n\t"
    "global_load_dwordx4 v[52:55], v5, s[36:37] offset:0 sc1\n\t"
    "v_mfma_f32_16x16x32_bf16 v[36:39], a[4:7], v[136:139], 0\n\t"
    "ds_read2st64_b64 v[132:135], v3 offset0:6 offset1:7\n\t"
    "v_mfma_f32_16x16x32_bf16 v[36:39], a[20:23], v[140:143], v[36:39]\n\t"
    "s_mov_b32 m0, s25\n\t"
    "s_add_u32 s25, s25, 18432\n\t"
    "buffer_load_dwordx4 v15, s[12:15], s24 offen offset:0 sc1 lds\n\t"
    "s_cmp_ge_u32 s25, 163840\n\t"
    "s_cselect_b32 s28, 92160, 0\n\t"
    "s_sub_u32 s25, s25, s28\n\t"
    "v_mfma_f32_16x16x32_bf16 v[40:43], a[8:11], v[136:139], 0\n\t"
    "v_add_u32 v5, 1792, v5\n\t"
    "v_mfma_f32_16x16x32_bf16 v[40:43], a[24:27], v[140:143], v[40:43]\n\t"
    "buffer_store_dwordx4 v[160:163], v15, s[8:11], s33 offen offset:0 sc1\n\t"
    "v_mfma_f32_16x16x32_bf16 v[44:47], a[12:15], v[136:139], 0\n\t"
    "v_fma_f32 v64, -v58, v72, v82\n\t"
    "v_fma_f32 v65, -v58, v93, v104\n\t"
    "v_fma_f32 v64, v56, v16, v64 clamp\n\t"
    "v_fmac_f32 v65, v57, v24\n\t"
    "v_mul_f32 v68, v64, v65\n\t"
    "v_fma_f32 v66, -v58, v73, v84\n\t"
    "v_fma_f32 v67, -v58, v94, v105\n\t"
    "v_fma_f32 v66, v56, v17, v66 clamp\n\t"
    "v_fmac_f32 v67, v57, v25\n\t"
    "v_mfma_f32_16x16x32_bf16 v[44:47], a[28:31], v[140:143], v[44:47]\n\t"
    "s_waitcnt lgkmcnt(0)\n\t"
    "v_mul_f32 v69, v66, v67\n\t"
    "v_mfma_f32_16x16x32_bf16 v[32:35], a[32:35], v[128:131], v[32:35]\n\t"
    "ds_read2st64_b64 v[136:139], v3 offset0:8 offset1:9\n\t"
    "v_mfma_f32_16x16x32_bf16 v[32:35], a[48:51], v[132:135], v[32:35]\n\t"
    "buffer_load_dword v219, v215, s[88:91], s83 offen sc1\n\t"
    "s_add_u32 s83, s83, 7077888\n\t"
    "v_mfma_f32_16x16x32_bf16 v[36:39], a[36:39], v[128:131], v[36:39]\n\t"
    "ds_read2st64_b64 v[140:143], v3 offset0:10 offset1:11\n\t"
    "v_mfma_f32_16x16x32_bf16 v[36:39], a[52:55], v[132:135], v[36:39]\n\t"
    "v_cvt_pk_bf16_f32 v120, v68, v69\n\t"
    "v_mfma_f32_16x16x32_bf16 v[40:43], a[40:43], v[128:131], v[40:43]\n\t"
    "v_fma_f32 v64, -v58, v74, v85\n\t"
    "v_mfma_f32_16x16x32_bf16 v[40:43], a[56:59], v[132:135], v[40:43]\n\t"
    "buffer_load_dwordx4 v15, s[12:15], s24 offen offset:1024 sc1 lds\n\t"
    "v_mfma_f32_16x16x32_bf16 v[44:47], a[44:47], v[128:131], v[44:47]\n\t"
    "v_fma_f32 v65, -v58, v96, v106\n\t"
    "v_mfma_f32_16x16x32_bf16 v[44:47], a[60:63], v[132:135], v[44:47]\n\t"
    "s_waitcnt lgkmcnt(0)\n\t"
    "buffer_store_dwordx4 v[164:167], v15, s[8:11], s33 offen offset:1024 sc1\n\t"
    "v_mfma_f32_16x16x32_bf16 v[32:35], a[64:67], v[136:139], v[32:35]\n\t"
    "ds_read2st64_b64 v[128:131], v3 offset0:12 offset1:13\n\t"
    "v_mfma_f32_16x16x32_bf16 v[32:35], a[80:83], v[140:143], v[32:35]\n\t"
    "v_fma_f32 v64, v56, v18, v64 clamp\n\t"
    "v_mfma_f32_16x16x32_bf16 v[36:39], a[68:71], v[136:139], v[36:39]\n\t"
    "ds_read2st64_b64 v[132:135], v3 offset0:14 offset1:15\n\t"
    "v_mfma_f32_16x16x32_bf16 v[36:39], a[84:87], v[140:143], v[36:39]\n\t"
    "v_fmac_f32 v65, v57, v26\n\t"
    "v_mfma_f32_16x16x32_bf16 v[40:43], a[72:75], v[136:139], v[40:43]\n\t"
    "v_mul_f32 v68, v64, v65\n\t"
    "v_mfma_f32_16x16x32_bf16 v[40:43], a[88:91], v[140:143], v[40:43]\n\t"
    "v_fma_f32 v66, -v58, v76, v86\n\t"
    "v_mfma_f32_16x16x32_bf16 v[44:47], a[76:79], v[136:139], v[44:47]\n\t"
    "v_fma_f32 v67, -v58, v97, v108\n\t"
    "v_mfma_f32_16x16x32_bf16 v[44:47], a[92:95], v[140:143], v[44:47]\n\t"
    "s_waitcnt lgkmcnt(0)\n\t"
    "v_fma_f32 v66, v56, v19, v66 clamp\n\t"
    "v_mfma_f32_16x16x32_bf16 v[32:35], a[96:99], v[128:131], v[32:35]\n\t"
    "ds_read2st64_b64 v[136:139], v3 offset0:16 offset1:17\n\t"
    "v_mfma_f32_16x16x32_bf16 v[32:35], a[112:115], v[132:135], v[32:35]\n\t"
    "buffer_store_dwordx4 v[168:171], v15, s[8:11], s33 offen offset:2048 sc1\n\t"
    "v_mfma_f32_16x16x32_bf16 v[36:39], a[100:103], v[128:131], v[36:39]\n\t"
    "ds_read2st64_b64 v[140:143], v3 offset0:18 offset1:19\n\t"
    "v_mfma_f32_16x16x32_bf16 v[36:39], a[116:119], v[132:135], v[36:39]\n\t"
    "v_fmac_f32 v67, v57, v27\n\t"
    "v_mfma_f32_16x16x32_bf16 v[40:43], a[104:107], v[128:131], v[40:43]\n\t"
    "buffer_load_dwordx4 v15, s[12:15], s24 offen offset:2048 sc1 lds\n\t"
    "v_mfma_f32_16x16x32_bf16 v[40:43], a[120:123], v[132:135], v[40:43]\n\t"
    "v_mul_f32 v69, v66, v67\n\t"
    "v_mfma_f32_16x16x32_bf16 v[44:47], a[108:111], v[128:131], v[44:47]\n\t"
    "v_cvt_pk_bf16_f32 v121, v68, v69\n\t"
    "v_mfma_f32_16x16x32_bf16 v[44:47], a[124:127], v[132:135], v[44:47]\n\t"
    "s_waitcnt lgkmcnt(0)\n\t"
    "v_fma_f32 v64, -v58, v77, v88\n\t"
    "v_mfma_f32_16x16x32_bf16 v[32:35], a[128:131], v[136:139], v[32:35]\n\t"
    "ds_read2st64_b64 v[128:131], v3 offset0:20 offset1:21\n\t"
    "v_mfma_f32_16x16x32_bf16 v[32:35], a[144:147], v[140:143], v[32:35]\n\t"
    "v_fma_f32 v65, -v58, v98, v109\n\t"
    "v_mfma_f32_16x16x32_bf16 v[36:39], a[132:135], v[136:139], v[36:39]\n\t"
    "ds_read2st64_b64 v[132:135], v3 offset0:22 offset1:23\n\t"
    "v_mfma_f32_16x16x32_bf16 v[36:39], a[148:151], v[140:143], v[36:39]\n\t"
    "buffer_store_dwordx4 v[172:175], v15, s[8:11], s33 offen offset:3072 sc1\n\t"
    "v_mfma_f32_16x16x32_bf16 v[40:43], a[136:139], v[136:139], v[40:43]\n\t"
    "v_fma_f32 v64, v56, v20, v64 clamp\n\t"
    "v_mfma_f32_16x16x32_bf16 v[40:43], a[152:155], v[140:143], v[40:43]\n\t"
    "v_fmac_f32 v65, v57, v28\n\t"
    "v_mfma_f32_16x16x32_bf16 v[44:47], a[140:143], v[136:139], v[44:47]\n\t"
    "v_mul_f32 v68, v64, v65\n\t"
    "v_mfma_f32_16x16x32_bf16 v[44:47], a[156:159], v[140:143], v[44:47]\n\t"
    "s_waitcnt lgkmcnt(0)\n\t"
    "v_fma_f32 v66, -v58, v78, v89\n\t"
    "v_mfma_f32_16x16x32_bf16 v[32:35], a[160:163], v[128:131], v[32:35]\n\t"
    "ds_read2st64_b64 v[136:139], v3 offset0:24 offset1:25\n\t"
    "v_mfma_f32_16x16x32_bf16 v[32:35], a[176:179], v[132:135], v[32:35]\n\t"
    "v_fma_f32 v67, -v58, v100, v110\n\t"
    "v_mfma_f32_16x16x32_bf16 v[36:39], a[164:167], v[128:131], v[36:39]\n\t"
    "ds_read2st64_b64 v[140:143], v3 offset0:26 offset1:27\n\t"
    "v_mfma_f32_16x16x32_bf16 v[36:39], a[180:183], v[132:135], v[36:39]\n\t"
    "v_fma_f32 v66, v56, v21, v66 clamp\n\t"
    "v_mfma_f32_16x16x32_bf16 v[40:43], a[168:171], v[128:131], v[40:43]\n\t"
    "buffer_load_dwordx4 v15, s[12:15], s24 offen offset:3072 sc1 lds\n\t"
    "v_mfma_f32_16x16x32_bf16 v[40:43], a[184:187], v[132:135], v[40:43]\n\t"
    "buffer_store_dwordx4 v[176:179], v15, s[8:11], s34 offen offset:0 sc1\n\t"
    "v_mfma_f32_16x16x32_bf16 v[44:47], a[172:175], v[128:131], v[44:47]\n\t"
    "v_fmac_f32 v67, v57, v29\n\t"
    "v_mfma_f32_16x16x32_bf16 v[44:47], a[188:191], v[132:135], v[44:47]\n\t"
    "s_waitcnt lgkmcnt(0)\n\t"
    "v_mul_f32 v69, v66, v67\n\t"
    "v_mfma_f32_16x16x32_bf16 v[32:35], a[192:195], v[136:139], v[32:35]\n\t"
    "ds_read2st64_b64 v[128:131], v3 offset0:28 offset1:29\n\t"
    "v_mfma_f32_16x16x32_bf16 v[32:35], a[208:211], v[140:143], v[32:35]\n\t"
    "v_cvt_pk_bf16_f32 v122, v68, v69\n\t"
    "v_mfma_f32_16x16x32_bf16 v[36:39], a[196:199], v[136:139], v[36:39]\n\t"
    "ds_read2st64_b64 v[132:135], v3 offset0:30 offset1:31\n\t"
    "v_mfma_f32_16x16x32_bf16 v[36:39], a[212:215], v[140:143], v[36:39]\n\t"
    "v_fma_f32 v64, -v58, v80, v90\n\t"
    "v_mfma_f32_16x16x32_bf16 v[40:43], a[200:203], v[136:139], v[40:43]\n\t"
    "buffer_load_dwordx4 v15, s[12:15], s24 offen offset:3584 sc1 lds\n\t"
    "s_add_u32 s24, s24, 129024\n\t"
    "v_mfma_f32_16x16x32_bf16 v[40:43], a[216:219], v[140:143], v[40:43]\n\t"
    "v_fma_f32 v65, -v58, v101, v112\n\t"
    "v_mfma_f32_16x16x32_bf16 v[44:47], a[204:207], v[136:139], v[44:47]\n\t"
    "v_fma_f32 v64, v56, v22, v64 clamp\n\t"
    "v_mfma_f32_16x16x32_bf16 v[44:47], a[220:223], v[140:143], v[44:47]\n\t"
    "s_waitcnt lgkmcnt(0)\n\t"
    "v_fmac_f32 v65, v57, v30\n\t"
    "v_mfma_f32_16x16x32_bf16 v[32:35], a[224:227], v[128:131], v[32:35]\n\t"
    "ds_read2st64_b64 v[136:139], v3 offset0:32 offset1:33\n\t"
    "s_add_u32 s26, s26, 18432\n\t"
    "v_mfma_f32_16x16x32_bf16 v[32:35], a[240:243], v[132:135], v[32:35]\n\t"
    "s_cmp_ge_u32 s26, 163840\n\t"
    "v_mul_f32 v68, v64, v65\n\t"
    "v_mfma_f32_16x16x32_bf16 v[36:39], a[228:231], v[128:131], v[36:39]\n\t"
    "ds_read2st64_b64 v[140:143], v3 offset0:34 offset1:35\n\t"
    "s_cselect_b32 s26, 71680, s26\n\t"
    "v_mfma_f32_16x16x32_bf16 v[36:39], a[244:247], v[132:135], v[36:39]\n\t"
    "v_fma_f32 v66, -v58, v81, v92\n\t"
    "v_mfma_f32_16x16x32_bf16 v[40:43], a[232:235], v[128:131], v[40:43]\n\t"
    "s_xor_b32 s28, s68, s3\n\t"
    "s_cselect_b32 s28, s28, 4\n\t"
    "s_add_u32 s28, s28, 5\n\t"
    "s_lshl_b32 s28, s28, 10\n\t"
    "s_mul_i32 s29, s3, 40960\n\t"
    "s_add_u32 s28, s28, s29\n\t"
    "buffer_store_dwordx4 v[116:119], v15, s[56:59], s28 offen sc1\n\t"
    "v_mfma_f32_16x16x32_bf16 v[40:43], a[248:251], v[132:135], v[40:43]\n\t"
    "v_add_u32 v3, s26, v2\n\t"
    "v_mfma_f32_16x16x32_bf16 v[44:47], a[236:239], v[128:131], v[44:47]\n\t"
    "v_fma_f32 v67, -v58, v102, v113\n\t"
    "v_mfma_f32_16x16x32_bf16 v[44:47], a[252:255], v[132:135], v[44:47]\n\t"
    "s_waitcnt lgkmcnt(0)\n\t"
    "s_waitcnt vmcnt(24)\n\t"
    "s_barrier\n\t"
    "v_fma_f32 v66, v56, v23, v66 clamp\n\t"
    "v_mfma_f32_16x16x32_bf16 v[32:35], v[224:227], v[136:139], v[32:35]\n\t"
    "ds_read2st64_b64 v[128:131], v3 offset0:0 offset1:1\n\t"
    "v_mfma_f32_16x16x32_bf16 v[32:35], v[240:243], v[140:143], v[32:35]\n\t"
    "v_cmp_ne_u32 vcc, s66, v49\n\t"
    "v_cmp_ne_u32 s[30:31], s67, v51\n\t"
    "v_mfma_f32_16x16x32_bf16 v[36:39], v[228:231], v[136:139], v[36:39]\n\t"
    "ds_read2st64_b64 v[132:135], v3 offset0:2 offset1:3\n\t"
    "v_mfma_f32_16x16x32_bf16 v[36:39], v[244:247], v[140:143], v[36:39]\n\t"
    "s_or_b64 vcc, vcc, s[30:31]\n\t"
    "s_cbranch_vccnz L_ss110_%=\n\t"
    "L_sr110_%=:\n\t"
    "v_mov_b32 v62, v48\n\t"
    "v_fmac_f32 v67, v57, v31\n\t"
    "v_mfma_f32_16x16x32_bf16 v[40:43], v[232:235], v[136:139], v[40:43]\n\t"
    "v_mul_f32 v60, s78, v50\n\t"
    "v_mul_f32 v69, v66, v67\n\t"
    "v_mfma_f32_16x16x32_bf16 v[40:43], v[248:251], v[140:143], v[40:43]\n\t"
    "v_mul_f32 v61, s79, v50\n\t"
    "v_cvt_pk_bf16_f32 v123, v68, v69\n\t"
    "v_mfma_f32_16x16x32_bf16 v[44:47], v[236:239], v[136:139], v[44:47]\n\t"
    "buffer_store_dwordx4 v[120:123], v15, s[16:19], s27 offen sc1\n\t"
    "v_mfma_f32_16x16x32_bf16 v[44:47], v[252:255], v[140:143], v[44:47]\n\t"
    "s_mov_b32 s27, s38\n\t"
    "s_add_u32 s38, s38, 1032192\n\t"
    "s_add_u32 s23, s23, 10\n\t"
    "L_E_%=:\n\t"
    "s_waitcnt lgkmcnt(0)\n\t"
    "v_mfma_f32_16x16x32_bf16 v[16:19], a[0:3], v[128:131], 0\n\t"
    "ds_read2st64_b64 v[136:139], v3 offset0:4 offset1:5\n\t"
    "s_add_u32 s23, s23, 1\n\t"
    "s_add_u32 s28, s23, 3\n\t"
    "v_mfma_f32_16x16x32_bf16 v[16:19], a[16:19], v[132:135], v[16:19]\n\t"
    "s_mov_b32 m0, s25\n\t"
    "global_load_dwordx4 v[48:51], v5, s[36:37] offset:0 sc1\n\t"
    "s_cmp_le_u32 s28, s22\n\t"
    "s_cselect_b32 s14, 2377728, 0\n\t"
    "v_mfma_f32_16x16x32_bf16 v[20:23], a[4:7], v[128:131], 0\n\t"
    "ds_read2st64_b64 v[140:143], v3 offset0:6 offset1:7\n\t"
    "v_mfma_f32_16x16x32_bf16 v[20:23], a[20:23], v[132:135], v[20:23]\n\t"
    "buffer_load_dwordx4 v15, s[12:15], s24 offen offset:0 sc1 lds\n\t"
    "v_mfma_f32_16x16x32_bf16 v[24:27], a[8:11], v[128:131], 0\n\t"
    "v_fma_f32 v64, -v62, v72, v82\n\t"
    "v_mfma_f32_16x16x32_bf16 v[24:27], a[24:27], v[132:135], v[24:27]\n\t"
    "v_add_u32 v5, 1792, v5\n\t"
    "v_mfma_f32_16x16x32_bf16 v[28:31], a[12:15], v[128:131], 0\n\t"
    "s_add_u32 s25, s25, 18432\n\t"
    "v_fma_f32 v65, -v62, v93, v104\n\t"
    "v_mfma_f32_16x16x32_bf16 v[28:31], a[28:31], v[132:135], v[28:31]\n\t"
    "s_waitcnt lgkmcnt(0)\n\t"
    "s_cmp_ge_u32 s25, 163840\n\t"
    "v_fma_f32 v64, v60, v32, v64 clamp\n\t"
    "v_mfma_f32_16x16x32_bf16 v[16:19], a[32:35], v[136:139], v[16:19]\n\t"
    "ds_read2st64_b64 v[128:131], v3 offset0:8 offset1:9\n\t"
    "s_cselect_b32 s28, 92160, 0\n\t"
    "v_mfma_f32_16x16x32_bf16 v[16:19], a[48:51], v[140:143], v[16:19]\n\t"
    "s_sub_u32 s25, s25, s28\n\t"
    "v_fmac_f32 v65, v61, v40\n\t"
    "v_mfma_f32_16x16x32_bf16 v[20:23], a[36:39], v[136:139], v[20:23]\n\t"
    "ds_read2st64_b64 v[132:135], v3 offset0:10 offset1:11\n\t"
    "v_mfma_f32_16x16x32_bf16 v[20:23], a[52:55], v[140:143], v[20:23]\n\t"
    "v_mul_f32 v68, v64, v65\n\t"
    "v_mfma_f32_16x16x32_bf16 v[24:27], a[40:43], v[136:139], v[24:27]\n\t"
    "v_fma_f32 v66, -v62, v73, v84\n\t"
    "v_mfma_f32_16x16x32_bf16 v[24:27], a[56:59], v[140:143], v[24:27]\n\t"
    "buffer_load_dwordx4 v15, s[12:15], s24 offen offset:1024 sc1 lds\n\t"
    "v_mfma_f32_16x16x32_bf16 v[28:31], a[44:47], v[136:139], v[28:31]\n\t"
    "v_fma_f32 v67, -v62, v94, v105\n\t"
    "v_mfma_f32_16x16x32_bf16 v[28:31], a[60:63], v[140:143], v[28:31]\n\t"
    "s_waitcnt lgkmcnt(0)\n\t"
    "v_fma_f32 v66, v60, v33, v66 clamp\n\t"
    "v_mfma_f32_16x16x32_bf16 v[16:19], a[64:67], v[128:131], v[16:19]\n\t"
    "ds_read2st64_b64 v[136:139], v3 offset0:12 offset1:13\n\t"
    "v_mfma_f32_16x16x32_bf16 v[16:19], a[80:83], v[132:135], v[16:19]\n\t"
    "v_fmac_f32 v67, v61, v41\n\t"
    "v_mfma_f32_16x16x32_bf16 v[20:23], a[68:71], v[128:131], v[20:23]\n\t"
    "ds_read2st64_b64 v[140:143], v3 offset0:14 offset1:15\n\t"
    "v_mfma_f32_16x16x32_bf16 v[20:23], a[84:87], v[132:135], v[20:23]\n\t"
    "v_mul_f32 v69, v66, v67\n\t"
    "v_mfma_f32_16x16x32_bf16 v[24:27], a[72:75], v[128:131], v[24:27]\n\t"
    "v_cvt_pk_bf16_f32 v120, v68, v69\n\t"
    "v_mfma_f32_16x16x32_bf16 v[24:27], a[88:91], v[132:135], v[24:27]\n\t"
    "v_fma_f32 v64, -v62, v74, v85\n\t"
    "v_fma_f32 v65, -v62, v96, v106\n\t"
    "v_mfma_f32_16x16x32_bf16 v[28:31], a[76:79], v[128:131], v[28:31]\n\t"
    "v_fma_f32 v64, v60, v34, v64 clamp\n\t"
    "v_mfma_f32_16x16x32_bf16 v[28:31], a[92:95], v[132:135], v[28:31]\n\t"
    "s_waitcnt lgkmcnt(0)\n\t"
    "v_fmac_f32 v65, v61, v42\n\t"
    "v_mfma_f32_16x16x32_bf16 v[16:19], a[96:99], v[136:139], v[16:19]\n\t"
    "ds_read2st64_b64 v[128:131], v3 offset0:16 offset1:17\n\t"
    "v_mfma_f32_16x16x32_bf16 v[16:19], a[112:115], v[140:143], v[16:19]\n\t"
    "v_mul_f32 v68, v64, v65\n\t"
    "v_mfma_f32_16x16x32_bf16 v[20:23], a[100:103], v[136:139], v[20:23]\n\t"
    "ds_read2st64_b64 v[132:135], v3 offset0:18 offset1:19\n\t"
    "v_mfma_f32_16x16x32_bf16 v[20:23], a[116:119], v[140:143], v[20:23]\n\t"
    "v_fma_f32 v66, -v62, v76, v86\n\t"
    "v_mfma_f32_16x16x32_bf16 v[24:27], a[104:107], v[136:139], v[24:27]\n\t"
    "buffer_load_dwordx4 v15, s[12:15], s24 offen offset:2048 sc1 lds\n\t"
    "v_mfma_f32_16x16x32_bf16 v[24:27], a[120:123], v[140:143], v[24:27]\n\t"
    "v_fma_f32 v67, -v62, v97, v108\n\t"
    "v_mfma_f32_16x16x32_bf16 v[28:31], a[108:111], v[136:139], v[28:31]\n\t"
    "v_fma_f32 v66, v60, v35, v66 clamp\n\t"
    "v_mfma_f32_16x16x32_bf16 v[28:31], a[124:127], v[140:143], v[28:31]\n\t"
    "s_waitcnt lgkmcnt(0)\n\t"
    "v_fmac_f32 v67, v61, v43\n\t"
    "v_mfma_f32_16x16x32_bf16 v[16:19], a[128:131], v[128:131], v[16:19]\n\t"
    "ds_read2st64_b64 v[136:139], v3 offset0:20 offset1:21\n\t"
    "v_mfma_f32_16x16x32_bf16 v[16:19], a[144:147], v[132:135], v[16:19]\n\t"
    "v_mul_f32 v69, v66, v67\n\t"
    "v_mfma_f32_16x16x32_bf16 v[20:23], a[132:135], v[128:131], v[20:23]\n\t"
    "ds_read2st64_b64 v[140:143], v3 offset0:22 offset1:23\n\t"
    "v_mfma_f32_16x16x32_bf16 v[20:23], a[148:151], v[132:135], v[20:23]\n\t"
    "v_cvt_pk_bf16_f32 v121, v68, v69\n\t"
    "v_mfma_f32_16x16x32_bf16 v[24:27], a[136:139], v[128:131], v[24:27]\n\t"
    "v_fma_f32 v64, -v62, v77, v88\n\t"
    "v_mfma_f32_16x16x32_bf16 v[24:27], a[152:155], v[132:135], v[24:27]\n\t"
    "v_fma_f32 v65, -v62, v98, v109\n\t"
    "v_fma_f32 v64, v60, v36, v64 clamp\n\t"
    "v_mfma_f32_16x16x32_bf16 v[28:31], a[140:143], v[128:131], v[28:31]\n\t"
    "v_fmac_f32 v65, v61, v44\n\t"
    "v_mfma_f32_16x16x32_bf16 v[28:31], a[156:159], v[132:135], v[28:31]\n\t"
    "s_waitcnt lgkmcnt(0)\n\t"
    "v_mul_f32 v68, v64, v65\n\t"
    "v_mfma_f32_16x16x32_bf16 v[16:19], a[160:163], v[136:139], v[16:19]\n\t"
    "ds_read2st64_b64 v[128:131], v3 offset0:24 offset1:25\n\t"
    "v_mfma_f32_16x16x32_bf16 v[16:19], a[176:179], v[140:143], v[16:19]\n\t"
    "v_fma_f32 v66, -v62, v78, v89\n\t"
    "v_mfma_f32_16x16x32_bf16 v[20:23], a[164:167], v[136:139], v[20:23]\n\t"
    "ds_read2st64_b64 v[132:135], v3 offset0:26 offset1:27\n\t"
    "v_mfma_f32_16x16x32_bf16 v[20:23], a[180:183], v[140:143], v[20:23]\n\t"
    "v_fma_f32 v67, -v62, v100, v110\n\t"
    "v_mfma_f32_16x16x32_bf16 v[24:27], a[168:171], v[136:139], v[24:27]\n\t"
    "buffer_load_dwordx4 v15, s[12:15], s24 offen offset:3072 sc1 lds\n\t"
    "v_mfma_f32_16x16x32_bf16 v[24:27], a[184:187], v[140:143], v[24:27]\n\t"
    "v_fma_f32 v66, v60, v37, v66 clamp\n\t"
    "v_mfma_f32_16x16x32_bf16 v[28:31], a[172:175], v[136:139], v[28:31]\n\t"
    "v_fmac_f32 v67, v61, v45\n\t"
    "v_mfma_f32_16x16x32_bf16 v[28:31], a[188:191], v[140:143], v[28:31]\n\t"
    "s_waitcnt lgkmcnt(0)\n\t"
    "v_mul_f32 v69, v66, v67\n\t"
    "v_cvt_pk_bf16_f32 v122, v68, v69\n\t"
    "v_mfma_f32_16x16x32_bf16 v[16:19], a[192:195], v[128:131], v[16:19]\n\t"
    "ds_read2st64_b64 v[136:139], v3 offset0:28 offset1:29\n\t"
    "v_mfma_f32_16x16x32_bf16 v[16:19], a[208:211], v[132:135], v[16:19]\n\t"
    "v_fma_f32 v64, -v62, v80, v90\n\t"
    "v_mfma_f32_16x16x32_bf16 v[20:23], a[196:199], v[128:131], v[20:23]\n\t"
    "ds_read2st64_b64 v[140:143], v3 offset0:30 offset1:31\n\t"
    "v_mfma_f32_16x16x32_bf16 v[20:23], a[212:215], v[132:135], v[20:23]\n\t"
    "v_fma_f32 v65, -v62, v101, v112\n\t"
    "v_mfma_f32_16x16x32_bf16 v[24:27], a[200:203], v[128:131], v[24:27]\n\t"
    "buffer_load_dwordx4 v15, s[12:15], s24 offen offset:3584 sc1 lds\n\t"
    "v_mfma_f32_16x16x32_bf16 v[24:27], a[216:219], v[132:135], v[24:27]\n\t"
    "s_add_u32 s24, s24, 129024\n\t"
    "v_fma_f32 v64, v60, v38, v64 clamp\n\t"
    "v_mfma_f32_16x16x32_bf16 v[28:31], a[204:207], v[128:131], v[28:31]\n\t"
    "v_fmac_f32 v65, v61, v46\n\t"
    "v_mfma_f32_16x16x32_bf16 v[28:31], a[220:223], v[132:135], v[28:31]\n\t"
    "s_waitcnt lgkmcnt(0)\n\t"
    "v_mul_f32 v68, v64, v65\n\t"
    "v_mfma_f32_16x16x32_bf16 v[16:19], a[224:227], v[136:139], v[16:19]\n\t"
    "ds_read2st64_b64 v[128:131], v3 offset0:32 offset1:33\n\t"
    "s_add_u32 s26, s26, 18432\n\t"
    "v_mfma_f32_16x16x32_bf16 v[16:19], a[240:243], v[140:143], v[16:19]\n\t"
    "s_cmp_ge_u32 s26, 163840\n\t"
    "v_fma_f32 v66, -v62, v81, v92\n\t"
    "v_mfma_f32_16x16x32_bf16 v[20:23], a[228:231], v[136:139], v[20:23]\n\t"
    "ds_read2st64_b64 v[132:135], v3 offset0:34 offset1:35\n\t"
    "s_cselect_b32 s26, 71680, s26\n\t"
    "v_mfma_f32_16x16x32_bf16 v[20:23], a[244:247], v[140:143], v[20:23]\n\t"
    "v_fma_f32 v67, -v62, v102, v113\n\t"
    "v_mfma_f32_16x16x32_bf16 v[24:27], a[232:235], v[136:139], v[24:27]\n\t"
    "v_fma_f32 v66, v60, v39, v66 clamp\n\t"
    "v_mfma_f32_16x16x32_bf16 v[24:27], a[248:251], v[140:143], v[24:27]\n\t"
    "v_add_u32 v3, s26, v2\n\t"
    "v_mfma_f32_16x16x32_bf16 v[28:31], a[236:239], v[136:139], v[28:31]\n\t"
    "v_fmac_f32 v67, v61, v47\n\t"
    "v_mfma_f32_16x16x32_bf16 v[28:31], a[252:255], v[140:143], v[28:31]\n\t"
    "s_waitcnt lgkmcnt(0)\n\t"
    "s_waitcnt vmcnt(14)\n\t"
    "s_barrier\n\t"
    "v_mul_f32 v69, v66, v67\n\t"
    "v_mfma_f32_16x16x32_bf16 v[16:19], v[224:227], v[128:131], v[16:19]\n\t"
    "ds_read2st64_b64 v[136:139], v3 offset0:0 offset1:1\n\t"
    "v_mfma_f32_16x16x32_bf16 v[16:19], v[240:243], v[132:135], v[16:19]\n\t"
    "v_cmp_ne_u32 vcc, s66, v53\n\t"
    "v_cmp_ne_u32 s[30:31], s67, v55\n\t"
    "v_mfma_f32_16x16x32_bf16 v[20:23], v[228:231], v[128:131], v[20:23]\n\t"
    "ds_read2st64_b64 v[140:143], v3 offset0:2 offset1:3\n\t"
    "v_mfma_f32_16x16x32_bf16 v[20:23], v[244:247], v[132:135], v[20:23]\n\t"
    "s_or_b64 vcc, vcc, s[30:31]\n\t"
    "s_cbranch_vccnz L_ss0_%=\n\t"
    "L_sr0_%=:\n\t"
    "v_mov_b32 v58, v52\n\t"
    "v_cvt_pk_bf16_f32 v123, v68, v69\n\t"
    "v_mfma_f32_16x16x32_bf16 v[24:27], v[232:235], v[128:131], v[24:27]\n\t"
    "v_mul_f32 v56, s78, v54\n\t"
    "buffer_store_dwordx4 v[120:123], v15, s[16:19], s27 offen sc1\n\t"
    "s_mov_b32 s27, s38\n\t"
    "s_add_u32 s38, s38, 1032192\n\t"
    "v_mfma_f32_16x16x32_bf16 v[24:27], v[248:251], v[132:135], v[24:27]\n\t"
    "v_mul_f32 v57, s79, v54\n\t"
    "v_mfma_f32_16x16x32_bf16 v[28:31], v[236:239], v[128:131], v[28:31]\n\t"
    "v_mfma_f32_16x16x32_bf16 v[28:31], v[252:255], v[132:135], v[28:31]\n\t"
    "L_O_%=:\n\t"
    "s_waitcnt lgkmcnt(0)\n\t"
    "v_mfma_f32_16x16x32_bf16 v[32:35], a[0:3], v[136:139], 0\n\t"
    "ds_read2st64_b64 v[128:131], v3 offset0:4 offset1:5\n\t"
    "s_add_u32 s23, s23, 1\n\t"
    "s_add_u32 s28, s23, 3\n\t"
    "v_mfma_f32_16x16x32_bf16 v[32:35], a[16:19], v[140:143], v[32:35]\n\t"
    "s_mov_b32 m0, s25\n\t"
    "global_load_dwordx4 v[52:55], v5, s[36:37] offset:0 sc1\n\t"
    "s_cmp_le_u32 s28, s22\n\t"
    "s_cselect_b32 s14, 2377728, 0\n\t"
    "v_mfma_f32_16x16x32_bf16 v[36:39], a[4:7], v[136:139], 0\n\t"
    "ds_read2st64_b64 v[132:135], v3 offset0:6 offset1:7\n\t"
    "v_mfma_f32_16x16x32_bf16 v[36:39], a[20:23], v[140:143], v[36:39]\n\t"
    "buffer_load_dwordx4 v15, s[12:15], s24 offen offset:0 sc1 lds\n\t"
    "v_mfma_f32_16x16x32_bf16 v[40:43], a[8:11], v[136:139], 0\n\t"
    "v_fma_f32 v64, -v58, v72, v82\n\t"
    "v_mfma_f32_16x16x32_bf16 v[40:43], a[24:27], v[140:143], v[40:43]\n\t"
    "v_add_u32 v5, 1792, v5\n\t"
    "v_mfma_f32_16x16x32_bf16 v[44:47], a[12:15], v[136:139], 0\n\t"
    "s_add_u32 s25, s25, 18432\n\t"
    "v_fma_f32 v65, -v58, v93, v104\n\t"
    "v_mfma_f32_16x16x32_bf16 v[44:47], a[28:31], v[140:143], v[44:47]\n\t"
    "s_waitcnt lgkmcnt(0)\n\t"
    "s_cmp_ge_u32 s25, 163840\n\t"
    "v_fma_f32 v64, v56, v16, v64 clamp\n\t"
    "v_mfma_f32_16x16x32_bf16 v[32:35], a[32:35], v[128:131], v[32:35]\n\t"
    "ds_read2st64_b64 v[136:139], v3 offset0:8 offset1:9\n\t"
    "s_cselect_b32 s28, 92160, 0\n\t"
    "v_mfma_f32_16x16x32_bf16 v[32:35], a[48:51], v[132:135], v[32:35]\n\t"
    "s_sub_u32 s25, s25, s28\n\t"
    "buffer_load_dword v219, v215, s[88:91], s83 offen sc1\n\t"
    "s_add_u32 s83, s83, 7077888\n\t"
    "v_mfma_f32_16x16x32_bf16 v[36:39], a[36:39], v[128:131], v[36:39]\n\t"
    "ds_read2st64_b64 v[140:143], v3 offset0:10 offset1:11\n\t"
    "v_mfma_f32_16x16x32_bf16 v[36:39], a[52:55], v[132:135], v[36:39]\n\t"
    "v_fmac_f32 v65, v57, v24\n\t"
    "v_mfma_f32_16x16x32_bf16 v[40:43], a[40:43], v[128:131], v[40:43]\n\t"
    "v_mul_f32 v68, v64, v65\n\t"
    "v_mfma_f32_16x16x32_bf16 v[40:43], a[56:59], v[132:135], v[40:43]\n\t"
    "buffer_load_dwordx4 v15, s[12:15], s24 offen offset:1024 sc1 lds\n\t"
    "v_mfma_f32_16x16x32_bf16 v[44:47], a[44:47], v[128:131], v[44:47]\n\t"
    "v_fma_f32 v66, -v58, v73, v84\n\t"
    "v_mfma_f32_16x16x32_bf16 v[44:47], a[60:63], v[132:135], v[44:47]\n\t"
    "s_waitcnt lgkmcnt(0)\n\t"
    "v_fma_f32 v67, -v58, v94, v105\n\t"
    "v_mfma_f32_16x16x32_bf16 v[32:35], a[64:67], v[136:139], v[32:35]\n\t"
    "ds_read2st64_b64 v[128:131], v3 offset0:12 offset1:13\n\t"
    "v_mfma_f32_16x16x32_bf16 v[32:35], a[80:83], v[140:143], v[32:35]\n\t"
    "v_fma_f32 v66, v56, v17, v66 clamp\n\t"
    "v_mfma_f32_16x16x32_bf16 v[36:39], a[68:71], v[136:139], v[36:39]\n\t"
    "ds_read2st64_b64 v[132:135], v3 offset0:14 offset1:15\n\t"
    "v_mfma_f32_16x16x32_bf16 v[36:39], a[84:87], v[140:143], v[36:39]\n\t"
    "v_fmac_f32 v67, v57, v25\n\t"
    "v_mfma_f32_16x16x32_bf16 v[40:43], a[72:75], v[136:139], v[40:43]\n\t"
    "v_mul_f32 v69, v66, v67\n\t"
    "v_mfma_f32_16x16x32_bf16 v[40:43], a[88:91], v[140:143], v[40:43]\n\t"
    "v_cvt_pk_bf16_f32 v120, v68, v69\n\t"
    "v_fma_f32 v64, -v58, v74, v85\n\t"
    "v_mfma_f32_16x16x32_bf16 v[44:47], a[76:79], v[136:139], v[44:47]\n\t"
    "v_fma_f32 v65, -v58, v96, v106\n\t"
    "v_mfma_f32_16x16x32_bf16 v[44:47], a[92:95], v[140:143], v[44:47]\n\t"
    "s_waitcnt lgkmcnt(0)\n\t"
    "v_fma_f32 v64, v56, v18, v64 clamp\n\t"
    "v_mfma_f32_16x16x32_bf16 v[32:35], a[96:99], v[128:131], v[32:35]\n\t"
    "ds_read2st64_b64 v[136:139], v3 offset0:16 offset1:17\n\t"
    "v_mfma_f32_16x16x32_bf16 v[32:35], a[112:115], v[132:135], v[32:35]\n\t"
    "v_fmac_f32 v65, v57, v26\n\t"
    "v_mfma_f32_16x16x32_bf16 v[36:39], a[100:103], v[128:131], v[36:39]\n\t"
    "ds_read2st64_b64 v[140:143], v3 offset0:18 offset1:19\n\t"
    "v_mfma_f32_16x16x32_bf16 v[36:39], a[116:119], v[132:135], v[36:39]\n\t"
    "v_mul_f32 v68, v64, v65\n\t"
    "v_mfma_f32_16x16x32_bf16 v[40:43], a[104:107], v[128:131], v[40:43]\n\t"
    "buffer_load_dwordx4 v15, s[12:15], s24 offen offset:2048 sc1 lds\n\t"
    "v_mfma_f32_16x16x32_bf16 v[40:43], a[120:123], v[132:135], v[40:43]\n\t"
    "v_fma_f32 v66, -v58, v76, v86\n\t"
    "v_mfma_f32_16x16x32_bf16 v[44:47], a[108:111], v[128:131], v[44:47]\n\t"
    "v_fma_f32 v67, -v58, v97, v108\n\t"
    "v_mfma_f32_16x16x32_bf16 v[44:47], a[124:127], v[132:135], v[44:47]\n\t"
    "s_waitcnt lgkmcnt(0)\n\t"
    "v_fma_f32 v66, v56, v19, v66 clamp\n\t"
    "v_mfma_f32_16x16x32_bf16 v[32:35], a[128:131], v[136:139], v[32:35]\n\t"
    "ds_read2st64_b64 v[128:131], v3 offset0:20 offset1:21\n\t"
    "v_mfma_f32_16x16x32_bf16 v[32:35], a[144:147], v[140:143], v[32:35]\n\t"
    "v_fmac_f32 v67, v57, v27\n\t"
    "v_mfma_f32_16x16x32_bf16 v[36:39], a[132:135], v[136:139], v[36:39]\n\t"
    "ds_read2st64_b64 v[132:135], v3 offset0:22 offset1:23\n\t"
    "v_mfma_f32_16x16x32_bf16 v[36:39], a[148:151], v[140:143], v[36:39]\n\t"
    "v_mul_f32 v69, v66, v67\n\t"
    "v_mfma_f32_16x16x32_bf16 v[40:43], a[136:139], v[136:139], v[40:43]\n\t"
    "v_cvt_pk_bf16_f32 v121, v68, v69\n\t"
    "v_mfma_f32_16x16x32_bf16 v[40:43], a[152:155], v[140:143], v[40:43]\n\t"
    "v_fma_f32 v64, -v58, v77, v88\n\t"
    "v_fma_f32 v65, -v58, v98, v109\n\t"
    "v_mfma_f32_16x16x32_bf16 v[44:47], a[140:143], v[136:139], v[44:47]\n\t"
    "v_fma_f32 v64, v56, v20, v64 clamp\n\t"
    "v_mfma_f32_16x16x32_bf16 v[44:47], a[156:159], v[140:143], v[44:47]\n\t"
    "s_waitcnt lgkmcnt(0)\n\t"
    "v_fmac_f32 v65, v57, v28\n\t"
    "v_mfma_f32_16x16x32_bf16 v[32:35], a[160:163], v[128:131], v[32:35]\n\t"
    "ds_read2st64_b64 v[136:139], v3 offset0:24 offset1:25\n\t"
    "v_mfma_f32_16x16x32_bf16 v[32:35], a[176:179], v[132:135], v[32:35]\n\t"
    "v_mul_f32 v68, v64, v65\n\t"
    "v_mfma_f32_16x16x32_bf16 v[36:39], a[164:167], v[128:131], v[36:39]\n\t"
    "ds_read2st64_b64 v[140:143], v3 offset0:26 offset1:27\n\t"
    "v_mfma_f32_16x16x32_bf16 v[36:39], a[180:183], v[132:135], v[36:39]\n\t"
    "v_fma_f32 v66, -v58, v78, v89\n\t"
    "v_mfma_f32_16x16x32_bf16 v[40:43], a[168:171], v[128:131], v[40:43]\n\t"
    "buffer_load_dwordx4 v15, s[12:15], s24 offen offset:3072 sc1 lds\n\t"
    "v_mfma_f32_16x16x32_bf16 v[40:43], a[184:187], v[132:135], v[40:43]\n\t"
    "v_fma_f32 v67, -v58, v100, v110\n\t"
    "v_mfma_f32_16x16x32_bf16 v[44:47], a[172:175], v[128:131], v[44:47]\n\t"
    "v_fma_f32 v66, v56, v21, v66 clamp\n\t"
    "v_mfma_f32_16x16x32_bf16 v[44:47], a[188:191], v[132:135], v[44:47]\n\t"
    "s_waitcnt lgkmcnt(0)\n\t"
    "v_fmac_f32 v67, v57, v29\n\t"
    "v_mul_f32 v69, v66, v67\n\t"
    "v_mfma_f32_16x16x32_bf16 v[32:35], a[192:195], v[136:139], v[32:35]\n\t"
    "ds_read2st64_b64 v[128:131], v3 offset0:28 offset1:29\n\t"
    "v_mfma_f32_16x16x32_bf16 v[32:35], a[208:211], v[140:143], v[32:35]\n\t"
    "v_cvt_pk_bf16_f32 v122, v68, v69\n\t"
    "v_mfma_f32_16x16x32_bf16 v[36:39], a[196:199], v[136:139], v[36:39]\n\t"
    "ds_read2st64_b64 v[132:135], v3 offset0:30 offset1:31\n\t"
    "v_mfma_f32_16x16x32_bf16 v[36:39], a[212:215], v[140:143], v[36:39]\n\t"
    "v_fma_f32 v64, -v58, v80, v90\n\t"
    "v_mfma_f32_16x16x32_bf16 v[40:43], a[200:203], v[136:139], v[40:43]\n\t"
    "buffer_load_dwordx4 v15, s[12:15], s24 offen offset:3584 sc1 lds\n\t"
    "v_mfma_f32_16x16x32_bf16 v[40:43], a[216:219], v[140:143], v[40:43]\n\t"
    "s_add_u32 s24, s24, 129024\n\t"
    "v_fma_f32 v65, -v58, v101, v112\n\t"
    "v_mfma_f32_16x16x32_bf16 v[44:47], a[204:207], v[136:139], v[44:47]\n\t"
    "v_fma_f32 v64, v56, v22, v64 clamp\n\t"
    "v_mfma_f32_16x16x32_bf16 v[44:47], a[220:223], v[140:143], v[44:47]\n\t"
    "s_waitcnt lgkmcnt(0)\n\t"
    "v_fmac_f32 v65, v57, v30\n\t"
    "v_mfma_f32_16x16x32_bf16 v[32:35], a[224:227], v[128:131], v[32:35]\n\t"
    "ds_read2st64_b64 v[136:139], v3 offset0:32 offset1:33\n\t"
    "s_add_u32 s26, s26, 18432\n\t"
    "v_mfma_f32_16x16x32_bf16 v[32:35], a[240:243], v[132:135], v[32:35]\n\t"
    "s_cmp_ge_u32 s26, 163840\n\t"
    "v_mul_f32 v68, v64, v65\n\t"
    "v_mfma_f32_16x16x32_bf16 v[36:39], a[228:231], v[128:131], v[36:39]\n\t"
    "ds_read2st64_b64 v[140:143], v3 offset0:34 offset1:35\n\t"
    "s_cselect_b32 s26, 71680, s26\n\t"
    "v_mfma_f32_16x16x32_bf16 v[36:39], a[244:247], v[132:135], v[36:39]\n\t"
    "v_fma_f32 v66, -v58, v81, v92\n\t"
    "v_mfma_f32_16x16x32_bf16 v[40:43], a[232:235], v[128:131], v[40:43]\n\t"
    "v_fma_f32 v67, -v58, v102, v113\n\t"
    "v_mfma_f32_16x16x32_bf16 v[40:43], a[248:251], v[132:135], v[40:43]\n\t"
    "v_add_u32 v3, s26, v2\n\t"
    "v_mfma_f32_16x16x32_bf16 v[44:47], a[236:239], v[128:131], v[44:47]\n\t"
    "v_fma_f32 v66, v56, v23, v66 clamp\n\t"
    "v_mfma_f32_16x16x32_bf16 v[44:47], a[252:255], v[132:135], v[44:47]\n\t"
    "s_waitcnt lgkmcnt(0)\n\t"
    "s_waitcnt vmcnt(13)\n\t"
    "s_barrier\n\t"
    "v_fmac_f32 v67, v57, v31\n\t"
    "v_mfma_f32_16x16x32_bf16 v[32:35], v[224:227], v[136:139], v[32:35]\n\t"
    "ds_read2st64_b64 v[128:131], v3 offset0:0 offset1:1\n\t"
    "s_mov_b64 exec, s[80:81]\n\t"
    "global_store_dwordx2 v63, v[70:71], s[36:37] sc1\n\t"
    "s_mov_b64 exec, -1\n\t"
    "s_mov_b64 s[80:81], 0\n\t"
    "v_mfma_f32_16x16x32_bf16 v[32:35], v[240:243], v[140:143], v[32:35]\n\t"
    "v_cmp_ne_u32 vcc, s66, v49\n\t"
    "v_cmp_ne_u32 s[30:31], s67, v51\n\t"
    "v_mfma_f32_16x16x32_bf16 v[36:39], v[228:231], v[136:139], v[36:39]\n\t"
    "ds_read2st64_b64 v[132:135], v3 offset0:2 offset1:3\n\t"
    "v_mfma_f32_16x16x32_bf16 v[36:39], v[244:247], v[140:143], v[36:39]\n\t"
    "s_or_b64 vcc, vcc, s[30:31]\n\t"
    "s_cbranch_vccnz L_ss1_%=\n\t"
    "L_sr1_%=:\n\t"
    "v_mov_b32 v62, v48\n\t"
    "v_mul_f32 v69, v66, v67\n\t"
    "v_mfma_f32_16x16x32_bf16 v[40:43], v[232:235], v[136:139], v[40:43]\n\t"
    "v_mul_f32 v60, s78, v50\n\t"
    "v_cvt_pk_bf16_f32 v123, v68, v69\n\t"
    "v_mfma_f32_16x16x32_bf16 v[40:43], v[248:251], v[140:143], v[40:43]\n\t"
    "v_mul_f32 v61, s79, v50\n\t"
    "buffer_store_dwordx4 v[120:123], v15, s[16:19], s27 offen sc1\n\t"
    "s_mov_b32 s27, s38\n\t"
    "s_add_u32 s38, s38, 1032192\n\t"
    "v_mfma_f32_16x16x32_bf16 v[44:47], v[236:239], v[136:139], v[44:47]\n\t"
    "v_mfma_f32_16x16x32_bf16 v[44:47], v[252:255], v[140:143], v[44:47]\n\t"
    "s_add_u32 s28, s23, 4\n\t"
    "s_cmp_lt_u32 s28, s22\n\t"
    "s_cbranch_scc1 L_E_%=\n\t"
    "s_mov_b32 s14, 2377728\n\t"
    "s_waitcnt lgkmcnt(0)\n\t"
    "v_mfma_f32_16x16x32_bf16 v[16:19], a[0:3], v[128:131], 0\n\t"
    "ds_read2st64_b64 v[136:139], v3 offset0:4 offset1:5\n\t"
    "global_load_dwordx2 v[220:221], v218, s[36:37] sc1\n\t"
    "v_mfma_f32_16x16x32_bf16 v[16:19], a[16:19], v[132:135], v[16:19]\n\t"
    "global_load_dwordx4 v[48:51], v5, s[36:37] offset:0 sc1\n\t"
    "v_mfma_f32_16x16x32_bf16 v[20:23], a[4:7], v[128:131], 0\n\t"
    "ds_read2st64_b64 v[140:143], v3 offset0:6 offset1:7\n\t"
    "v_mfma_f32_16x16x32_bf16 v[20:23], a[20:23], v[132:135], v[20:23]\n\t"
    "s_mov_b32 m0, s25\n\t"
    "s_add_u32 s25, s25, 18432\n\t"
    "buffer_load_dwordx4 v15, s[12:15], s24 offen offset:0 sc1 lds\n\t"
    "s_cmp_ge_u32 s25, 163840\n\t"
    "s_cselect_b32 s28, 92160, 0\n\t"
    "s_sub_u32 s25, s25, s28\n\t"
    "v_mfma_f32_16x16x32_bf16 v[24:27], a[8:11], v[128:131], 0\n\t"
    "v_add_u32 v5, 1792, v5\n\t"
    "v_mfma_f32_16x16x32_bf16 v[24:27], a[24:27], v[132:135], v[24:27]\n\t"
    "global_load_dwordx2 v[222:223], v216, s[36:37] sc1\n\t"
    "v_mfma_f32_16x16x32_bf16 v[28:31], a[12:15], v[128:131], 0\n\t"
    "v_fma_f32 v64, -v62, v72, v82\n\t"
    "v_fma_f32 v65, -v62, v93, v104\n\t"
    "v_fma_f32 v64, v60, v32, v64 clamp\n\t"
    "v_fmac_f32 v65, v61, v40\n\t"
    "v_mul_f32 v68, v64, v65\n\t"
    "v_fma_f32 v66, -v62, v73, v84\n\t"
    "v_fma_f32 v67, -v62, v94, v105\n\t"
    "v_fma_f32 v66, v60, v33, v66 clamp\n\t"
    "v_fmac_f32 v67, v61, v41\n\t"
    "v_mfma_f32_16x16x32_bf16 v[28:31], a[28:31], v[132:135], v[28:31]\n\t"
    "s_waitcnt lgkmcnt(0)\n\t"
    "v_mul_f32 v69, v66, v67\n\t"
    "v_mfma_f32_16x16x32_bf16 v[16:19], a[32:35], v[136:139], v[16:19]\n\t"
    "ds_read2st64_b64 v[128:131], v3 offset0:8 offset1:9\n\t"
    "v_mfma_f32_16x16x32_bf16 v[16:19], a[48:51], v[140:143], v[16:19]\n\t"
    "buffer_load_dwordx4 v15, s[12:15], s24 offen offset:1024 sc1 lds\n\t"
    "v_mfma_f32_16x16x32_bf16 v[20:23], a[36:39], v[136:139], v[20:23]\n\t"
    "ds_read2st64_b64 v[132:135], v3 offset0:10 offset1:11\n\t"
    "v_mfma_f32_16x16x32_bf16 v[20:23], a[52:55], v[140:143], v[20:23]\n\t"
    "v_cvt_pk_bf16_f32 v120, v68, v69\n\t"
    "v_mfma_f32_16x16x32_bf16 v[24:27], a[40:43], v[136:139], v[24:27]\n\t"
    "v_fma_f32 v64, -v62, v74, v85\n\t"
    "v_mfma_f32_16x16x32_bf16 v[24:27], a[56:59], v[140:143], v[24:27]\n\t"
    "v_fma_f32 v65, -v62, v96, v106\n\t"
    "v_mfma_f32_16x16x32_bf16 v[28:31], a[44:47], v[136:139], v[28:31]\n\t"
    "v_fma_f32 v64, v60, v34, v64 clamp\n\t"
    "v_mfma_f32_16x16x32_bf16 v[28:31], a[60:63], v[140:143], v[28:31]\n\t"
    "s_waitcnt lgkmcnt(0)\n\t"
    "buffer_load_dwordx4 v15, s[12:15], s24 offen offset:2048 sc1 lds\n\t"
    "v_mfma_f32_16x16x32_bf16 v[16:19], a[64:67], v[128:131], v[16:19]\n\t"
    "ds_read2st64_b64 v[136:139], v3 offset0:12 offset1:13\n\t"
    "v_mfma_f32_16x16x32_bf16 v[16:19], a[80:83], v[132:135], v[16:19]\n\t"
    "v_fmac_f32 v65, v61, v42\n\t"
    "v_mfma_f32_16x16x32_bf16 v[20:23], a[68:71], v[128:131], v[20:23]\n\t"
    "ds_read2st64_b64 v[140:143], v3 offset0:14 offset1:15\n\t"
    "v_mfma_f32_16x16x32_bf16 v[20:23], a[84:87], v[132:135], v[20:23]\n\t"
    "v_mul_f32 v68, v64, v65\n\t"
    "v_mfma_f32_16x16x32_bf16 v[24:27], a[72:75], v[128:131], v[24:27]\n\t"
    "v_fma_f32 v66, -v62, v76, v86\n\t"
    "v_mfma_f32_16x16x32_bf16 v[24:27], a[88:91], v[132:135], v[24:27]\n\t"
    "buffer_load_dwordx4 v15, s[12:15], s24 offen offset:3072 sc1 lds\n\t"
    "v_mfma_f32_16x16x32_bf16 v[28:31], a[76:79], v[128:131], v[28:31]\n\t"
    "v_fma_f32 v67, -v62, v97, v108\n\t"
    "v_mfma_f32_16x16x32_bf16 v[28:31], a[92:95], v[132:135], v[28:31]\n\t"
    "s_waitcnt lgkmcnt(0)\n\t"
    "v_fma_f32 v66, v60, v35, v66 clamp\n\t"
    "v_mfma_f32_16x16x32_bf16 v[16:19], a[96:99], v[136:139], v[16:19]\n\t"
    "ds_read2st64_b64 v[128:131], v3 offset0:16 offset1:17\n\t"
    "v_mfma_f32_16x16x32_bf16 v[16:19], a[112:115], v[140:143], v[16:19]\n\t"
    "v_fmac_f32 v67, v61, v43\n\t"
    "v_mfma_f32_16x16x32_bf16 v[20:23], a[100:103], v[136:139], v[20:23]\n\t"
    "ds_read2st64_b64 v[132:135], v3 offset0:18 offset1:19\n\t"
    "v_mfma_f32_16x16x32_bf16 v[20:23], a[116:119], v[140:143], v[20:23]\n\t"
    "buffer_load_dwordx4 v15, s[12:15], s24 offen offset:3584 sc1 lds\n\t"
    "s_add_u32 s24, s24, 129024\n\t"
    "v_mfma_f32_16x16x32_bf16 v[24:27], a[104:107], v[136:139], v[24:27]\n\t"
    "v_mul_f32 v69, v66, v67\n\t"
    "v_mfma_f32_16x16x32_bf16 v[24:27], a[120:123], v[140:143], v[24:27]\n\t"
    "v_cvt_pk_bf16_f32 v121, v68, v69\n\t"
    "v_mfma_f32_16x16x32_bf16 v[28:31], a[108:111], v[136:139], v[28:31]\n\t"
    "v_fma_f32 v64, -v62, v77, v88\n\t"
    "v_mfma_f32_16x16x32_bf16 v[28:31], a[124:127], v[140:143], v[28:31]\n\t"
    "s_waitcnt lgkmcnt(0)\n\t"
    "v_fma_f32 v65, -v62, v98, v109\n\t"
    "v_mfma_f32_16x16x32_bf16 v[16:19], a[128:131], v[128:131], v[16:19]\n\t"
    "ds_read2st64_b64 v[136:139], v3 offset0:20 offset1:21\n\t"
    "v_mfma_f32_16x16x32_bf16 v[16:19], a[144:147], v[132:135], v[16:19]\n\t"
    "v_fma_f32 v64, v60, v36, v64 clamp\n\t"
    "v_mfma_f32_16x16x32_bf16 v[20:23], a[132:135], v[128:131], v[20:23]\n\t"
    "ds_read2st64_b64 v[140:143], v3 offset0:22 offset1:23\n\t"
    "v_mfma_f32_16x16x32_bf16 v[20:23], a[148:151], v[132:135], v[20:23]\n\t"
    "v_fmac_f32 v65, v61, v44\n\t"
    "v_mfma_f32_16x16x32_bf16 v[24:27], a[136:139], v[128:131], v[24:27]\n\t"
    "v_mul_f32 v68, v64, v65\n\t"
    "v_mfma_f32_16x16x32_bf16 v[24:27], a[152:155], v[132:135], v[24:27]\n\t"
    "v_fma_f32 v66, -v62, v78, v89\n\t"
    "v_mfma_f32_16x16x32_bf16 v[28:31], a[140:143], v[128:131], v[28:31]\n\t"
    "v_fma_f32 v67, -v62, v100, v110\n\t"
    "v_mfma_f32_16x16x32_bf16 v[28:31], a[156:159], v[132:135], v[28:31]\n\t"
    "s_waitcnt lgkmcnt(0)\n\t"
    "v_fma_f32 v66, v60, v37, v66 clamp\n\t"
    "v_mfma_f32_16x16x32_bf16 v[16:19], a[160:163], v[136:139], v[16:19]\n\t"
    "ds_read2st64_b64 v[128:131], v3 offset0:24 offset1:25\n\t"
    "v_mfma_f32_16x16x32_bf16 v[16:19], a[176:179], v[140:143], v[16:19]\n\t"
    "v_fmac_f32 v67, v61, v45\n\t"
    "v_mfma_f32_16x16x32_bf16 v[20:23], a[164:167], v[136:139], v[20:23]\n\t"
    "ds_read2st64_b64 v[132:135], v3 offset0:26 offset1:27\n\t"
    "v_mfma_f32_16x16x32_bf16 v[20:23], a[180:183], v[140:143], v[20:23]\n\t"
    "v_mul_f32 v69, v66, v67\n\t"
    "v_mfma_f32_16x16x32_bf16 v[24:27], a[168:171], v[136:139], v[24:27]\n\t"
    "s_waitcnt vmcnt(7)\n\t"
    "v_cmp_ne_u64 vcc, s[66:67], v[220:221]\n\t"
    "s_cbranch_vccnz L_psw_%=\n\t"
    "L_pbw_%=:\n\t"
    "s_waitcnt vmcnt(4)\n\t"
    "v_cmp_ne_u64 vcc, s[66:67], v[222:223]\n\t"
    "s_cbranch_vccnz L_psh_%=\n\t"
    "L_pbh_%=:\n\t"
    "v_cvt_pk_bf16_f32 v122, v68, v69\n\t"
    "v_mfma_f32_16x16x32_bf16 v[24:27], a[184:187], v[140:143], v[24:27]\n\t"
    "s_add_u32 m0, s76, 0\n\t"
    "s_add_u32 s72, s71, 0\n\t"
    "v_fma_f32 v64, -v62, v80, v90\n\t"
    "v_mfma_f32_16x16x32_bf16 v[28:31], a[172:175], v[136:139], v[28:31]\n\t"
    "buffer_load_dwordx4 v201, s[60:63], s72 offen offset:0 sc1 lds\n\t"
    "v_mfma_f32_16x16x32_bf16 v[28:31], a[188:191], v[140:143], v[28:31]\n\t"
    "s_waitcnt lgkmcnt(0)\n\t"
    "v_fma_f32 v65, -v62, v101, v112\n\t"
    "v_mfma_f32_16x16x32_bf16 v[16:19], a[192:195], v[128:131], v[16:19]\n\t"
    "ds_read2st64_b64 v[136:139], v3 offset0:28 offset1:29\n\t"
    "v_mfma_f32_16x16x32_bf16 v[16:19], a[208:211], v[132:135], v[16:19]\n\t"
    "v_fma_f32 v64, v60, v38, v64 clamp\n\t"
    "v_mfma_f32_16x16x32_bf16 v[20:23], a[196:199], v[128:131], v[20:23]\n\t"
    "ds_read2st64_b64 v[140:143], v3 offset0:30 offset1:31\n\t"
    "v_mfma_f32_16x16x32_bf16 v[20:23], a[212:215], v[132:135], v[20:23]\n\t"
    "buffer_load_dwordx4 v201, s[60:63], s72 offen offset:1024 sc1 lds\n\t"
    "v_mfma_f32_16x16x32_bf16 v[24:27], a[200:203], v[128:131], v[24:27]\n\t"
    "v_fmac_f32 v65, v61, v46\n\t"
    "v_mfma_f32_16x16x32_bf16 v[24:27], a[216:219], v[132:135], v[24:27]\n\t"
    "v_mul_f32 v68, v64, v65\n\t"
    "v_mfma_f32_16x16x32_bf16 v[28:31], a[204:207], v[128:131], v[28:31]\n\t"
    "v_fma_f32 v66, -v62, v81, v92\n\t"
    "v_mfma_f32_16x16x32_bf16 v[28:31], a[220:223], v[132:135], v[28:31]\n\t"
    "s_waitcnt lgkmcnt(0)\n\t"
    "v_fma_f32 v67, -v62, v102, v113\n\t"
    "v_mfma_f32_16x16x32_bf16 v[16:19], a[224:227], v[136:139], v[16:19]\n\t"
    "ds_read2st64_b64 v[128:131], v3 offset0:32 offset1:33\n\t"
    "s_add_u32 s26, s26, 18432\n\t"
    "s_add_u32 m0, s76, 2048\n\t"
    "s_add_u32 s72, s71, 36864\n\t"
    "v_mfma_f32_16x16x32_bf16 v[16:19], a[240:243], v[140:143], v[16:19]\n\t"
    "s_cmp_ge_u32 s26, 163840\n\t"
    "buffer_load_dwordx4 v201, s[60:63], s72 offen offset:0 sc1 lds\n\t"
    "v_mfma_f32_16x16x32_bf16 v[20:23], a[228:231], v[136:139], v[20:23]\n\t"
    "ds_read2st64_b64 v[132:135], v3 offset0:34 offset1:35\n\t"
    "s_cselect_b32 s26, 71680, s26\n\t"
    "v_mfma_f32_16x16x32_bf16 v[20:23], a[244:247], v[140:143], v[20:23]\n\t"
    "v_fma_f32 v66, v60, v39, v66 clamp\n\t"
    "v_mfma_f32_16x16x32_bf16 v[24:27], a[232:235], v[136:139], v[24:27]\n\t"
    "v_fmac_f32 v67, v61, v47\n\t"
    "v_mfma_f32_16x16x32_bf16 v[24:27], a[248:251], v[140:143], v[24:27]\n\t"
    "v_add_u32 v3, s26, v2\n\t"
    "v_mfma_f32_16x16x32_bf16 v[28:31], a[236:239], v[136:139], v[28:31]\n\t"
    "buffer_load_dwordx4 v201, s[60:63], s72 offen offset:1024 sc1 lds\n\t"
    "v_mfma_f32_16x16x32_bf16 v[28:31], a[252:255], v[140:143], v[28:31]\n\t"
    "s_waitcnt lgkmcnt(0)\n\t"
    "s_waitcnt vmcnt(20)\n\t"
    "s_barrier\n\t"
    "v_mul_f32 v69, v66, v67\n\t"
    "v_mfma_f32_16x16x32_bf16 v[16:19], v[224:227], v[128:131], v[16:19]\n\t"
    "ds_read2st64_b64 v[136:139], v3 offset0:0 offset1:1\n\t"
    "v_mfma_f32_16x16x32_bf16 v[16:19], v[240:243], v[132:135], v[16:19]\n\t"
    "v_cmp_ne_u32 vcc, s66, v53\n\t"
    "v_cmp_ne_u32 s[30:31], s67, v55\n\t"
    "v_mfma_f32_16x16x32_bf16 v[20:23], v[228:231], v[128:131], v[20:23]\n\t"
    "ds_read2st64_b64 v[140:143], v3 offset0:2 offset1:3\n\t"
    "v_mfma_f32_16x16x32_bf16 v[20:23], v[244:247], v[132:135], v[20:23]\n\t"
    "s_or_b64 vcc, vcc, s[30:31]\n\t"
    "s_cbranch_vccnz L_ss015_%=\n\t"
    "L_sr015_%=:\n\t"
    "v_mov_b32 v58, v52\n\t"
    "s_movk_i32 s73, 12288\n\t"
    "buffer_load_dwordx4 v[176:179], v196, s[4:7], s73 offen offset:2048 sc1\n\t"
    "v_mfma_f32_16x16x32_bf16 v[24:27], v[232:235], v[128:131], v[24:27]\n\t"
    "v_mul_f32 v56, s78, v54\n\t"
    "v_cvt_pk_bf16_f32 v123, v68, v69\n\t"
    "v_mfma_f32_16x16x32_bf16 v[24:27], v[248:251], v[132:135], v[24:27]\n\t"
    "v_mul_f32 v57, s79, v54\n\t"
    "buffer_store_dwordx4 v[120:123], v15, s[16:19], s27 offen sc1\n\t"
    "v_mfma_f32_16x16x32_bf16 v[28:31], v[236:239], v[128:131], v[28:31]\n\t"
    "s_movk_i32 s73, 12288\n\t"
    "buffer_load_dwordx4 v[180:183], v196, s[4:7], s73 offen offset:3072 sc1\n\t"
    "s_mov_b32 s27, s38\n\t"
    "s_add_u32 s38, s38, 1032192\n\t"
    "v_mfma_f32_16x16x32_bf16 v[28:31], v[252:255], v[132:135], v[28:31]\n\t"
    "s_waitcnt lgkmcnt(0)\n\t"
    "v_mfma_f32_16x16x32_bf16 v[32:35], a[0:3], v[136:139], 0\n\t"
    "ds_read2st64_b64 v[128:131], v3 offset0:4 offset1:5\n\t"
    "v_mfma_f32_16x16x32_bf16 v[32:35], a[16:19], v[140:143], v[32:35]\n\t"
    "global_load_dwordx4 v[52:55], v5, s[36:37] offset:0 sc1\n\t"
    "v_mfma_f32_16x16x32_bf16 v[36:39], a[4:7], v[136:139], 0\n\t"
    "ds_read2st64_b64 v[132:135], v3 offset0:6 offset1:7\n\t"
    "v_mfma_f32_16x16x32_bf16 v[36:39], a[20:23], v[140:143], v[36:39]\n\t"
    "v_add_u32 v5, 1792, v5\n\t"
    "s_add_u32 m0, s76, 4096\n\t"
    "s_add_u32 s72, s71, 73728\n\t"
    "v_mfma_f32_16x16x32_bf16 v[40:43], a[8:11], v[136:139], 0\n\t"
    "buffer_load_dwordx4 v201, s[60:63], s72 offen offset:0 sc1 lds\n\t"
    "v_mfma_f32_16x16x32_bf16 v[40:43], a[24:27], v[140:143], v[40:43]\n\t"
    "v_and_b32 v144, 15, v1\n\t"
    "v_fma_f32 v64, -v58, v72, v82\n\t"
    "v_fma_f32 v65, -v58, v93, v104\n\t"
    "v_fma_f32 v64, v56, v16, v64 clamp\n\t"
    "v_fmac_f32 v65, v57, v24\n\t"
    "v_mul_f32 v68, v64, v65\n\t"
    "v_fma_f32 v66, -v58, v73, v84\n\t"
    "v_fma_f32 v67, -v58, v94, v105\n\t"
    "v_fma_f32 v66, v56, v17, v66 clamp\n\t"
    "v_fmac_f32 v67, v57, v25\n\t"
    "v_mul_f32 v69, v66, v67\n\t"
    "v_cvt_pk_bf16_f32 v120, v68, v69\n\t"
    "v_mfma_f32_16x16x32_bf16 v[44:47], a[12:15], v[136:139], 0\n\t"
    "v_lshrrev_b32 v145, 4, v1\n\t"
    "v_fma_f32 v64, -v58, v74, v85\n\t"
    "v_mfma_f32_16x16x32_bf16 v[44:47], a[28:31], v[140:143], v[44:47]\n\t"
    "s_waitcnt lgkmcnt(0)\n\t"
    "s_mul_i32 s35, s70, 144\n\t"
    "v_fma_f32 v65, -v58, v96, v106\n\t"
    "v_mfma_f32_16x16x32_bf16 v[32:35], a[32:35], v[128:131], v[32:35]\n\t"
    "ds_read2st64_b64 v[136:139], v3 offset0:8 offset1:9\n\t"
    "v_mfma_f32_16x16x32_bf16 v[32:35], a[48:51], v[132:135], v[32:35]\n\t"
    "buffer_load_dwordx4 v201, s[60:63], s72 offen offset:1024 sc1 lds\n\t"
    "v_mfma_f32_16x16x32_bf16 v[36:39], a[36:39], v[128:131], v[36:39]\n\t"
    "ds_read2st64_b64 v[140:143], v3 offset0:10 offset1:11\n\t"
    "v_mfma_f32_16x16x32_bf16 v[36:39], a[52:55], v[132:135], v[36:39]\n\t"
    "s_lshl_b32 s90, s68, 5\n\t"
    "v_fma_f32 v64, v56, v18, v64 clamp\n\t"
    "v_mfma_f32_16x16x32_bf16 v[40:43], a[40:43], v[128:131], v[40:43]\n\t"
    "s_add_u32 s90, s90, s35\n\t"
    "v_fmac_f32 v65, v57, v26\n\t"
    "v_mfma_f32_16x16x32_bf16 v[40:43], a[56:59], v[132:135], v[40:43]\n\t"
    "s_add_u32 m0, s76, 6144\n\t"
    "s_add_u32 s72, s71, 110592\n\t"
    "s_add_u32 s35, s35, 128\n\t"
    "v_mul_f32 v68, v64, v65\n\t"
    "v_mfma_f32_16x16x32_bf16 v[44:47], a[44:47], v[128:131], v[44:47]\n\t"
    "buffer_load_dwordx4 v201, s[60:63], s72 offen offset:0 sc1 lds\n\t"
    "v_mfma_f32_16x16x32_bf16 v[44:47], a[60:63], v[132:135], v[44:47]\n\t"
    "s_waitcnt lgkmcnt(0)\n\t"
    "s_and_b32 s0, s70, 1\n\t"
    "v_fma_f32 v66, -v58, v76, v86\n\t"
    "v_mfma_f32_16x16x32_bf16 v[32:35], a[64:67], v[136:139], v[32:35]\n\t"
    "ds_read2st64_b64 v[128:131], v3 offset0:12 offset1:13\n\t"
    "v_mfma_f32_16x16x32_bf16 v[32:35], a[80:83], v[140:143], v[32:35]\n\t"
    "s_lshl_b32 s1, s0, 4\n\t"
    "v_fma_f32 v67, -v58, v97, v108\n\t"
    "v_mfma_f32_16x16x32_bf16 v[36:39], a[68:71], v[136:139], v[36:39]\n\t"
    "ds_read2st64_b64 v[132:135], v3 offset0:14 offset1:15\n\t"
    "v_mfma_f32_16x16x32_bf16 v[36:39], a[84:87], v[140:143], v[36:39]\n\t"
    "buffer_load_dwordx4 v201, s[60:63], s72 offen offset:1024 sc1 lds\n\t"
    "v_mfma_f32_16x16x32_bf16 v[40:43], a[72:75], v[136:139], v[40:43]\n\t"
    "s_add_u32 s90, s90, s1\n\t"
    "v_fma_f32 v66, v56, v19, v66 clamp\n\t"
    "v_mfma_f32_16x16x32_bf16 v[40:43], a[88:91], v[140:143], v[40:43]\n\t"
    "s_lshl_b32 s1, s0, 7\n\t"
    "v_fmac_f32 v67, v57, v27\n\t"
    "v_mfma_f32_16x16x32_bf16 v[44:47], a[76:79], v[136:139], v[44:47]\n\t"
    "s_sub_u32 s35, s35, s1\n\t"
    "v_mul_f32 v69, v66, v67\n\t"
    "v_mfma_f32_16x16x32_bf16 v[44:47], a[92:95], v[140:143], v[44:47]\n\t"
    "s_waitcnt lgkmcnt(0)\n\t"
    "s_mul_i32 s94, s91, 18432\n\t"
    "v_cvt_pk_bf16_f32 v121, v68, v69\n\t"
    "v_mfma_f32_16x16x32_bf16 v[32:35], a[96:99], v[128:131], v[32:35]\n\t"
    "ds_read2st64_b64 v[136:139], v3 offset0:16 offset1:17\n\t"
    "s_add_u32 m0, s76, 8192\n\t"
    "s_add_u32 s72, s71, 147456\n\t"
    "v_mfma_f32_16x16x32_bf16 v[32:35], a[112:115], v[132:135], v[32:35]\n\t"
    "buffer_load_dwordx4 v201, s[60:63], s72 offen offset:0 sc1 lds\n\t"
    "v_mfma_f32_16x16x32_bf16 v[36:39], a[100:103], v[128:131], v[36:39]\n\t"
    "ds_read2st64_b64 v[140:143], v3 offset0:18 offset1:19\n\t"
    "v_mfma_f32_16x16x32_bf16 v[36:39], a[116:119], v[132:135], v[36:39]\n\t"
    "s_lshl_b32 s1, s90, 5\n\t"
    "v_fma_f32 v64, -v58, v77, v88\n\t"
    "v_mfma_f32_16x16x32_bf16 v[40:43], a[104:107], v[128:131], v[40:43]\n\t"
    "s_add_u32 s0, s94, s1\n\t"
    "v_fma_f32 v65, -v58, v98, v109\n\t"
    "v_mfma_f32_16x16x32_bf16 v[40:43], a[120:123], v[132:135], v[40:43]\n\t"
    "s_add_u32 s0, s0, 2949120\n\t"
    "v_fma_f32 v64, v56, v20, v64 clamp\n\t"
    "v_mfma_f32_16x16x32_bf16 v[44:47], a[108:111], v[128:131], v[44:47]\n\t"
    "buffer_load_dwordx4 v201, s[60:63], s72 offen offset:1024 sc1 lds\n\t"
    "v_mfma_f32_16x16x32_bf16 v[44:47], a[124:127], v[132:135], v[44:47]\n\t"
    "s_waitcnt lgkmcnt(0)\n\t"
    "v_lshlrev_b32 v146, 8, v145\n\t"
    "v_fmac_f32 v65, v57, v28\n\t"
    "v_mfma_f32_16x16x32_bf16 v[32:35], a[128:131], v[136:139], v[32:35]\n\t"
    "ds_read2st64_b64 v[128:131], v3 offset0:20 offset1:21\n\t"
    "v_mfma_f32_16x16x32_bf16 v[32:35], a[144:147], v[140:143], v[32:35]\n\t"
    "v_mul_f32 v68, v64, v65\n\t"
    "v_mfma_f32_16x16x32_bf16 v[36:39], a[132:135], v[136:139], v[36:39]\n\t"
    "ds_read2st64_b64 v[132:135], v3 offset0:22 offset1:23\n\t"
    "s_add_u32 m0, s76, 10240\n\t"
    "s_add_u32 s72, s71, 184320\n\t"
    "v_mfma_f32_16x16x32_bf16 v[36:39], a[148:151], v[140:143], v[36:39]\n\t"
    "buffer_load_dwordx4 v201, s[60:63], s72 offen offset:0 sc1 lds\n\t"
    "v_mfma_f32_16x16x32_bf16 v[40:43], a[136:139], v[136:139], v[40:43]\n\t"
    "v_fma_f32 v66, -v58, v78, v89\n\t"
    "v_mfma_f32_16x16x32_bf16 v[40:43], a[152:155], v[140:143], v[40:43]\n\t"
    "v_fma_f32 v67, -v58, v100, v110\n\t"
    "v_mfma_f32_16x16x32_bf16 v[44:47], a[140:143], v[136:139], v[44:47]\n\t"
    "v_fma_f32 v66, v56, v21, v66 clamp\n\t"
    "v_mfma_f32_16x16x32_bf16 v[44:47], a[156:159], v[140:143], v[44:47]\n\t"
    "s_waitcnt lgkmcnt(0)\n\t"
    "v_fmac_f32 v67, v57, v29\n\t"
    "v_mfma_f32_16x16x32_bf16 v[32:35], a[160:163], v[128:131], v[32:35]\n\t"
    "ds_read2st64_b64 v[136:139], v3 offset0:24 offset1:25\n\t"
    "v_mfma_f32_16x16x32_bf16 v[32:35], a[176:179], v[132:135], v[32:35]\n\t"
    "buffer_load_dwordx4 v201, s[60:63], s72 offen offset:1024 sc1 lds\n\t"
    "v_mfma_f32_16x16x32_bf16 v[36:39], a[164:167], v[128:131], v[36:39]\n\t"
    "ds_read2st64_b64 v[140:143], v3 offset0:26 offset1:27\n\t"
    "v_mfma_f32_16x16x32_bf16 v[36:39], a[180:183], v[132:135], v[36:39]\n\t"
    "v_mul_f32 v69, v66, v67\n\t"
    "v_mfma_f32_16x16x32_bf16 v[40:43], a[168:171], v[128:131], v[40:43]\n\t"
    "v_cvt_pk_bf16_f32 v122, v68, v69\n\t"
    "v_mfma_f32_16x16x32_bf16 v[40:43], a[184:187], v[132:135], v[40:43]\n\t"
    "s_add_u32 m0, s76, 12288\n\t"
    "s_add_u32 s72, s71, 221184\n\t"
    "v_fma_f32 v64, -v58, v80, v90\n\t"
    "v_mfma_f32_16x16x32_bf16 v[44:47], a[172:175], v[128:131], v[44:47]\n\t"
    "buffer_load_dwordx4 v201, s[60:63], s72 offen offset:0 sc1 lds\n\t"
    "v_mfma_f32_16x16x32_bf16 v[44:47], a[188:191], v[132:135], v[44:47]\n\t"
    "s_waitcnt lgkmcnt(0)\n\t"
    "v_fma_f32 v65, -v58, v101, v112\n\t"
    "v_mfma_f32_16x16x32_bf16 v[32:35], a[192:195], v[136:139], v[32:35]\n\t"
    "ds_read2st64_b64 v[128:131], v3 offset0:28 offset1:29\n\t"
    "v_mfma_f32_16x16x32_bf16 v[32:35], a[208:211], v[140:143], v[32:35]\n\t"
    "v_fma_f32 v64, v56, v22, v64 clamp\n\t"
    "v_mfma_f32_16x16x32_bf16 v[36:39], a[196:199], v[136:139], v[36:39]\n\t"
    "ds_read2st64_b64 v[132:135], v3 offset0:30 offset1:31\n\t"
    "v_mfma_f32_16x16x32_bf16 v[36:39], a[212:215], v[140:143], v[36:39]\n\t"
    "buffer_load_dwordx4 v201, s[60:63], s72 offen offset:1024 sc1 lds\n\t"
    "v_mfma_f32_16x16x32_bf16 v[40:43], a[200:203], v[136:139], v[40:43]\n\t"
    "v_fmac_f32 v65, v57, v30\n\t"
    "v_mfma_f32_16x16x32_bf16 v[40:43], a[216:219], v[140:143], v[40:43]\n\t"
    "v_mul_f32 v68, v64, v65\n\t"
    "v_mfma_f32_16x16x32_bf16 v[44:47], a[204:207], v[136:139], v[44:47]\n\t"
    "v_fma_f32 v66, -v58, v81, v92\n\t"
    "v_mfma_f32_16x16x32_bf16 v[44:47], a[220:223], v[140:143], v[44:47]\n\t"
    "s_waitcnt lgkmcnt(0)\n\t"
    "v_fma_f32 v67, -v58, v102, v113\n\t"
    "v_mfma_f32_16x16x32_bf16 v[32:35], a[224:227], v[128:131], v[32:35]\n\t"
    "ds_read2st64_b64 v[136:139], v3 offset0:32 offset1:33\n\t"
    "s_add_u32 s26, s26, 18432\n\t"
    "s_add_u32 m0, s76, 14336\n\t"
    "s_add_u32 s72, s71, 258048\n\t"
    "v_mfma_f32_16x16x32_bf16 v[32:35], a[240:243], v[132:135], v[32:35]\n\t"
    "s_cmp_ge_u32 s26, 163840\n\t"
    "buffer_load_dwordx4 v201, s[60:63], s72 offen offset:0 sc1 lds\n\t"
    "v_mfma_f32_16x16x32_bf16 v[36:39], a[228:231], v[128:131], v[36:39]\n\t"
    "ds_read2st64_b64 v[140:143], v3 offset0:34 offset1:35\n\t"
    "s_cselect_b32 s26, 71680, s26\n\t"
    "v_mfma_f32_16x16x32_bf16 v[36:39], a[244:247], v[132:135], v[36:39]\n\t"
    "v_fma_f32 v66, v56, v23, v66 clamp\n\t"
    "v_mfma_f32_16x16x32_bf16 v[40:43], a[232:235], v[128:131], v[40:43]\n\t"
    "v_fmac_f32 v67, v57, v31\n\t"
    "v_mfma_f32_16x16x32_bf16 v[40:43], a[248:251], v[132:135], v[40:43]\n\t"
    "v_add_u32 v3, s26, v2\n\t"
    "v_mfma_f32_16x16x32_bf16 v[44:47], a[236:239], v[128:131], v[44:47]\n\t"
    "buffer_load_dwordx4 v201, s[60:63], s72 offen offset:1024 sc1 lds\n\t"
    "v_mfma_f32_16x16x32_bf16 v[44:47], a[252:255], v[132:135], v[44:47]\n\t"
    "s_waitcnt lgkmcnt(0)\n\t"
    "s_waitcnt vmcnt(26)\n\t"
    "s_barrier\n\t"
    "v_mul_f32 v69, v66, v67\n\t"
    "v_mfma_f32_16x16x32_bf16 v[32:35], v[224:227], v[136:139], v[32:35]\n\t"
    "ds_read2st64_b64 v[128:131], v3 offset0:0 offset1:1\n\t"
    "v_mfma_f32_16x16x32_bf16 v[32:35], v[240:243], v[140:143], v[32:35]\n\t"
    "v_cmp_ne_u32 vcc, s66, v49\n\t"
    "v_cmp_ne_u32 s[30:31], s67, v51\n\t"
    "v_mfma_f32_16x16x32_bf16 v[36:39], v[228:231], v[136:139], v[36:39]\n\t"
    "ds_read2st64_b64 v[132:135], v3 offset0:2 offset1:3\n\t"
    "v_mfma_f32_16x16x32_bf16 v[36:39], v[244:247], v[140:143], v[36:39]\n\t"
    "s_or_b64 vcc, vcc, s[30:31]\n\t"
    "s_cbranch_vccnz L_ss116_%=\n\t"
    "L_sr116_%=:\n\t"
    "v_mov_b32 v62, v48\n\t"
    "s_movk_i32 s73, 16384\n\t"
    "buffer_load_dwordx4 v[184:187], v196, s[4:7], s73 offen offset:0 sc1\n\t"
    "v_mfma_f32_16x16x32_bf16 v[40:43], v[232:235], v[136:139], v[40:43]\n\t"
    "v_mul_f32 v60, s78, v50\n\t"
    "s_movk_i32 s73, 16384\n\t"
    "buffer_load_dwordx4 v[188:191], v196, s[4:7], s73 offen offset:1024 sc1\n\t"
    "v_mfma_f32_16x16x32_bf16 v[40:43], v[248:251], v[140:143], v[40:43]\n\t"
    "v_mul_f32 v61, s79, v50\n\t"
    "v_cvt_pk_bf16_f32 v123, v68, v69\n\t"
    "v_mfma_f32_16x16x32_bf16 v[44:47], v[236:239], v[136:139], v[44:47]\n\t"
    "buffer_store_dwordx4 v[120:123], v15, s[16:19], s27 offen sc1\n\t"
    "v_mfma_f32_16x16x32_bf16 v[44:47], v[252:255], v[140:143], v[44:47]\n\t"
    "s_mov_b32 s27, s38\n\t"
    "s_add_u32 s38, s38, 1032192\n\t"
    "s_waitcnt lgkmcnt(0)\n\t"
    "v_mfma_f32_16x16x32_bf16 v[16:19], a[0:3], v[128:131], 0\n\t"
    "ds_read2st64_b64 v[136:139], v3 offset0:4 offset1:5\n\t"
    "v_mfma_f32_16x16x32_bf16 v[16:19], a[16:19], v[132:135], v[16:19]\n\t"
    "global_load_dwordx4 v[48:51], v5, s[36:37] offset:0 sc1\n\t"
    "v_mfma_f32_16x16x32_bf16 v[20:23], a[4:7], v[128:131], 0\n\t"
    "ds_read2st64_b64 v[140:143], v3 offset0:6 offset1:7\n\t"
    "v_mfma_f32_16x16x32_bf16 v[20:23], a[20:23], v[132:135], v[20:23]\n\t"
    "v_add_u32 v5, 1792, v5\n\t"
    "v_mfma_f32_16x16x32_bf16 v[24:27], a[8:11], v[128:131], 0\n\t"
    "v_fma_f32 v64, -v62, v72, v82\n\t"
    "v_fma_f32 v65, -v62, v93, v104\n\t"
    "v_fma_f32 v64, v60, v32, v64 clamp\n\t"
    "v_fmac_f32 v65, v61, v40\n\t"
    "v_mul_f32 v68, v64, v65\n\t"
    "v_fma_f32 v66, -v62, v73, v84\n\t"
    "v_fma_f32 v67, -v62, v94, v105\n\t"
    "v_mfma_f32_16x16x32_bf16 v[24:27], a[24:27], v[132:135], v[24:27]\n\t"
    "s_add_u32 m0, s75, 122880\n\t"
    "s_add_u32 s72, s71, 0\n\t"
    "v_lshl_add_u32 v146, v144, 3, v146\n\t"
    "v_fma_f32 v66, v60, v33, v66 clamp\n\t"
    "v_mfma_f32_16x16x32_bf16 v[28:31], a[12:15], v[128:131], 0\n\t"
    "buffer_load_dwordx4 v199, s[60:63], s72 offen offset:0 sc1 lds\n\t"
    "v_mfma_f32_16x16x32_bf16 v[28:31], a[28:31], v[132:135], v[28:31]\n\t"
    "s_waitcnt lgkmcnt(0)\n\t"
    "v_add_u32 v207, s0, v146\n\t"
    "v_fmac_f32 v67, v61, v41\n\t"
    "v_mfma_f32_16x16x32_bf16 v[16:19], a[32:35], v[136:139], v[16:19]\n\t"
    "ds_read2st64_b64 v[128:131], v3 offset0:8 offset1:9\n\t"
    "v_mfma_f32_16x16x32_bf16 v[16:19], a[48:51], v[140:143], v[16:19]\n\t"
    "s_lshl_b32 s1, s90, 1\n\t"
    "v_mul_f32 v69, v66, v67\n\t"
    "v_mfma_f32_16x16x32_bf16 v[20:23], a[36:39], v[136:139], v[20:23]\n\t"
    "ds_read2st64_b64 v[132:135], v3 offset0:10 offset1:11\n\t"
    "v_mfma_f32_16x16x32_bf16 v[20:23], a[52:55], v[140:143], v[20:23]\n\t"
    "buffer_load_dwordx4 v199, s[60:63], s72 offen offset:1024 sc1 lds\n\t"
    "v_mfma_f32_16x16x32_bf16 v[24:27], a[40:43], v[136:139], v[24:27]\n\t"
    "s_add_u32 s0, s94, s1\n\t"
    "v_cvt_pk_bf16_f32 v120, v68, v69\n\t"
    "v_mfma_f32_16x16x32_bf16 v[24:27], a[56:59], v[140:143], v[24:27]\n\t"
    "v_mul_u32_u24 v146, 1152, v144\n\t"
    "v_fma_f32 v64, -v62, v74, v85\n\t"
    "v_mfma_f32_16x16x32_bf16 v[28:31], a[44:47], v[136:139], v[28:31]\n\t"
    "v_lshl_add_u32 v147, v145, 4, v146\n\t"
    "v_fma_f32 v65, -v62, v96, v106\n\t"
    "v_mfma_f32_16x16x32_bf16 v[28:31], a[60:63], v[140:143], v[28:31]\n\t"
    "s_waitcnt lgkmcnt(0)\n\t"
    "v_add_u32 v208, s0, v147\n\t"
    "v_fma_f32 v64, v60, v34, v64 clamp\n\t"
    "v_mfma_f32_16x16x32_bf16 v[16:19], a[64:67], v[128:131], v[16:19]\n\t"
    "ds_read2st64_b64 v[136:139], v3 offset0:12 offset1:13\n\t"
    "s_add_u32 m0, s75, 124928\n\t"
    "v_mfma_f32_16x16x32_bf16 v[16:19], a[80:83], v[132:135], v[16:19]\n\t"
    "buffer_load_dwordx4 v200, s[60:63], s72 offen offset:0 sc1 lds\n\t"
    "v_mfma_f32_16x16x32_bf16 v[20:23], a[68:71], v[128:131], v[20:23]\n\t"
    "ds_read2st64_b64 v[140:143], v3 offset0:14 offset1:15\n\t"
    "v_mfma_f32_16x16x32_bf16 v[20:23], a[84:87], v[132:135], v[20:23]\n\t"
    "s_lshl_b32 s0, s90, 2\n\t"
    "v_fmac_f32 v65, v61, v42\n\t"
    "v_mfma_f32_16x16x32_bf16 v[24:27], a[72:75], v[128:131], v[24:27]\n\t"
    "s_add_u32 s0, s0, 11520\n\t"
    "v_mul_f32 v68, v64, v65\n\t"
    "v_mfma_f32_16x16x32_bf16 v[24:27], a[88:91], v[132:135], v[24:27]\n\t"
    "v_lshlrev_b32 v147, 5, v145\n\t"
    "v_fma_f32 v66, -v62, v76, v86\n\t"
    "v_mfma_f32_16x16x32_bf16 v[28:31], a[76:79], v[128:131], v[28:31]\n\t"
    "buffer_load_dwordx4 v200, s[60:63], s72 offen offset:1024 sc1 lds\n\t"
    "v_mfma_f32_16x16x32_bf16 v[28:31], a[92:95], v[132:135], v[28:31]\n\t"
    "s_waitcnt lgkmcnt(0)\n\t"
    "v_add_u32 v209, s0, v147\n\t"
    "v_fma_f32 v67, -v62, v97, v108\n\t"
    "v_mfma_f32_16x16x32_bf16 v[16:19], a[96:99], v[136:139], v[16:19]\n\t"
    "ds_read2st64_b64 v[128:131], v3 offset0:16 offset1:17\n\t"
    "v_mfma_f32_16x16x32_bf16 v[16:19], a[112:115], v[140:143], v[16:19]\n\t"
    "s_lshl_b32 s1, s35, 5\n\t"
    "v_fma_f32 v66, v60, v35, v66 clamp\n\t"
    "v_mfma_f32_16x16x32_bf16 v[20:23], a[100:103], v[136:139], v[20:23]\n\t"
    "ds_read2st64_b64 v[132:135], v3 offset0:18 offset1:19\n\t"
    "s_add_u32 m0, s75, 126976\n\t"
    "v_mfma_f32_16x16x32_bf16 v[20:23], a[116:119], v[140:143], v[20:23]\n\t"
    "buffer_load_dwordx4 v202, s[60:63], s72 offen offset:0 sc1 lds\n\t"
    "v_mfma_f32_16x16x32_bf16 v[24:27], a[104:107], v[136:139], v[24:27]\n\t"
    "s_add_u32 s0, s94, s1\n\t"
    "v_fmac_f32 v67, v61, v43\n\t"
    "v_mfma_f32_16x16x32_bf16 v[24:27], a[120:123], v[140:143], v[24:27]\n\t"
    "s_add_u32 s0, s0, 2949120\n\t"
    "v_mul_f32 v69, v66, v67\n\t"
    "v_mfma_f32_16x16x32_bf16 v[28:31], a[108:111], v[136:139], v[28:31]\n\t"
    "v_lshlrev_b32 v147, 7, v145\n\t"
    "v_cvt_pk_bf16_f32 v121, v68, v69\n\t"
    "v_mfma_f32_16x16x32_bf16 v[28:31], a[124:127], v[140:143], v[28:31]\n\t"
    "s_waitcnt lgkmcnt(0)\n\t"
    "v_lshl_add_u32 v147, v144, 3, v147\n\t"
    "v_fma_f32 v64, -v62, v77, v88\n\t"
    "v_mfma_f32_16x16x32_bf16 v[16:19], a[128:131], v[128:131], v[16:19]\n\t"
    "ds_read2st64_b64 v[136:139], v3 offset0:20 offset1:21\n\t"
    "s_add_u32 m0, s75, 143360\n\t"
    "s_add_u32 s72, s71, 36864\n\t"
    "v_mfma_f32_16x16x32_bf16 v[16:19], a[144:147], v[132:135], v[16:19]\n\t"
    "buffer_load_dwordx4 v199, s[60:63], s72 offen offset:0 sc1 lds\n\t"
    "v_mfma_f32_16x16x32_bf16 v[20:23], a[132:135], v[128:131], v[20:23]\n\t"
    "ds_read2st64_b64 v[140:143], v3 offset0:22 offset1:23\n\t"
    "v_mfma_f32_16x16x32_bf16 v[20:23], a[148:151], v[132:135], v[20:23]\n\t"
    "v_add_u32 v210, s0, v147\n\t"
    "v_fma_f32 v65, -v62, v98, v109\n\t"
    "v_mfma_f32_16x16x32_bf16 v[24:27], a[136:139], v[128:131], v[24:27]\n\t"
    "s_lshl_b32 s1, s35, 1\n\t"
    "v_fma_f32 v64, v60, v36, v64 clamp\n\t"
    "v_mfma_f32_16x16x32_bf16 v[24:27], a[152:155], v[132:135], v[24:27]\n\t"
    "s_add_u32 s0, s94, s1\n\t"
    "v_fmac_f32 v65, v61, v44\n\t"
    "v_mfma_f32_16x16x32_bf16 v[28:31], a[140:143], v[128:131], v[28:31]\n\t"
    "buffer_load_dwordx4 v199, s[60:63], s72 offen offset:1024 sc1 lds\n\t"
    "v_mfma_f32_16x16x32_bf16 v[28:31], a[156:159], v[132:135], v[28:31]\n\t"
    "s_waitcnt lgkmcnt(0)\n\t"
    "v_lshl_add_u32 v147, v145, 3, v146\n\t"
    "v_mul_f32 v68, v64, v65\n\t"
    "v_mfma_f32_16x16x32_bf16 v[16:19], a[160:163], v[136:139], v[16:19]\n\t"
    "ds_read2st64_b64 v[128:131], v3 offset0:24 offset1:25\n\t"
    "v_mfma_f32_16x16x32_bf16 v[16:19], a[176:179], v[140:143], v[16:19]\n\t"
    "v_add_u32 v211, s0, v147\n\t"
    "v_fma_f32 v66, -v62, v78, v89\n\t"
    "v_mfma_f32_16x16x32_bf16 v[20:23], a[164:167], v[136:139], v[20:23]\n\t"
    "ds_read2st64_b64 v[132:135], v3 offset0:26 offset1:27\n\t"
    "s_add_u32 m0, s75, 145408\n\t"
    "v_mfma_f32_16x16x32_bf16 v[20:23], a[180:183], v[140:143], v[20:23]\n\t"
    "buffer_load_dwordx4 v200, s[60:63], s72 offen offset:0 sc1 lds\n\t"
    "v_mfma_f32_16x16x32_bf16 v[24:27], a[168:171], v[136:139], v[24:27]\n\t"
    "s_lshl_b32 s0, s35, 2\n\t"
    "v_fma_f32 v67, -v62, v100, v110\n\t"
    "v_mfma_f32_16x16x32_bf16 v[24:27], a[184:187], v[140:143], v[24:27]\n\t"
    "s_add_u32 s0, s0, 11520\n\t"
    "v_fma_f32 v66, v60, v37, v66 clamp\n\t"
    "v_mfma_f32_16x16x32_bf16 v[28:31], a[172:175], v[136:139], v[28:31]\n\t"
    "v_lshlrev_b32 v147, 4, v145\n\t"
    "v_fmac_f32 v67, v61, v45\n\t"
    "v_mfma_f32_16x16x32_bf16 v[28:31], a[188:191], v[140:143], v[28:31]\n\t"
    "s_waitcnt lgkmcnt(0)\n\t"
    "v_add_u32 v212, s0, v147\n\t"
    "v_mul_f32 v69, v66, v67\n\t"
    "v_mfma_f32_16x16x32_bf16 v[16:19], a[192:195], v[128:131], v[16:19]\n\t"
    "ds_read2st64_b64 v[136:139], v3 offset0:28 offset1:29\n\t"
    "v_mfma_f32_16x16x32_bf16 v[16:19], a[208:211], v[132:135], v[16:19]\n\t"
    "buffer_load_dwordx4 v200, s[60:63], s72 offen offset:1024 sc1 lds\n\t"
    "v_mfma_f32_16x16x32_bf16 v[20:23], a[196:199], v[128:131], v[20:23]\n\t"
    "ds_read2st64_b64 v[140:143], v3 offset0:30 offset1:31\n\t"
    "v_mfma_f32_16x16x32_bf16 v[20:23], a[212:215], v[132:135], v[20:23]\n\t"
    "s_cmp_eq_u32 s68, s3\n\t"
    "s_cselect_b32 s77, -1, 0\n\t"
    "v_cvt_pk_bf16_f32 v122, v68, v69\n\t"
    "v_mfma_f32_16x16x32_bf16 v[24:27], a[200:203], v[128:131], v[24:27]\n\t"
    "v_fma_f32 v64, -v62, v80, v90\n\t"
    "v_mfma_f32_16x16x32_bf16 v[24:27], a[216:219], v[132:135], v[24:27]\n\t"
    "s_add_u32 m0, s75, 147456\n\t"
    "v_fma_f32 v65, -v62, v101, v112\n\t"
    "v_mfma_f32_16x16x32_bf16 v[28:31], a[204:207], v[128:131], v[28:31]\n\t"
    "buffer_load_dwordx4 v202, s[60:63], s72 offen offset:0 sc1 lds\n\t"
    "v_mfma_f32_16x16x32_bf16 v[28:31], a[220:223], v[132:135], v[28:31]\n\t"
    "s_waitcnt lgkmcnt(0)\n\t"
    "v_fma_f32 v64, v60, v38, v64 clamp\n\t"
    "v_mfma_f32_16x16x32_bf16 v[16:19], a[224:227], v[136:139], v[16:19]\n\t"
    "ds_read2st64_b64 v[128:131], v3 offset0:32 offset1:33\n\t"
    "s_add_u32 s26, s26, 18432\n\t"
    "v_mfma_f32_16x16x32_bf16 v[16:19], a[240:243], v[140:143], v[16:19]\n\t"
    "s_cmp_ge_u32 s26, 163840\n\t"
    "v_fmac_f32 v65, v61, v46\n\t"
    "v_mfma_f32_16x16x32_bf16 v[20:23], a[228:231], v[136:139], v[20:23]\n\t"
    "ds_read2st64_b64 v[132:135], v3 offset0:34 offset1:35\n\t"
    "s_cselect_b32 s26, 71680, s26\n\t"
    "v_mfma_f32_16x16x32_bf16 v[20:23], a[244:247], v[140:143], v[20:23]\n\t"
    "v_mul_f32 v68, v64, v65\n\t"
    "v_mfma_f32_16x16x32_bf16 v[24:27], a[232:235], v[136:139], v[24:27]\n\t"
    "v_fma_f32 v66, -v62, v81, v92\n\t"
    "v_mfma_f32_16x16x32_bf16 v[24:27], a[248:251], v[140:143], v[24:27]\n\t"
    "v_add_u32 v3, s26, v2\n\t"
    "v_mfma_f32_16x16x32_bf16 v[28:31], a[236:239], v[136:139], v[28:31]\n\t"
    "v_fma_f32 v67, -v62, v102, v113\n\t"
    "v_mfma_f32_16x16x32_bf16 v[28:31], a[252:255], v[140:143], v[28:31]\n\t"
    "s_waitcnt lgkmcnt(0)\n\t"
    "s_waitcnt vmcnt(26)\n\t"
    "s_barrier\n\t"
    "v_fma_f32 v66, v60, v39, v66 clamp\n\t"
    "v_mfma_f32_16x16x32_bf16 v[16:19], v[224:227], v[128:131], v[16:19]\n\t"
    "ds_read2st64_b64 v[136:139], v3 offset0:0 offset1:1\n\t"
    "v_mfma_f32_16x16x32_bf16 v[16:19], v[240:243], v[132:135], v[16:19]\n\t"
    "v_cmp_ne_u32 vcc, s66, v53\n\t"
    "v_cmp_ne_u32 s[30:31], s67, v55\n\t"
    "v_mfma_f32_16x16x32_bf16 v[20:23], v[228:231], v[128:131], v[20:23]\n\t"
    "ds_read2st64_b64 v[140:143], v3 offset0:2 offset1:3\n\t"
    "v_mfma_f32_16x16x32_bf16 v[20:23], v[244:247], v[132:135], v[20:23]\n\t"
    "s_or_b64 vcc, vcc, s[30:31]\n\t"
    "s_cbranch_vccnz L_ss017_%=\n\t"
    "L_sr017_%=:\n\t"
    "v_mov_b32 v58, v52\n\t"
    "v_fmac_f32 v67, v61, v47\n\t"
    "v_mfma_f32_16x16x32_bf16 v[24:27], v[232:235], v[128:131], v[24:27]\n\t"
    "v_mul_f32 v56, s78, v54\n\t"
    "v_mul_f32 v69, v66, v67\n\t"
    "v_mfma_f32_16x16x32_bf16 v[24:27], v[248:251], v[132:135], v[24:27]\n\t"
    "v_mul_f32 v57, s79, v54\n\t"
    "v_cvt_pk_bf16_f32 v123, v68, v69\n\t"
    "v_mfma_f32_16x16x32_bf16 v[28:31], v[236:239], v[128:131], v[28:31]\n\t"
    "buffer_store_dwordx4 v[120:123], v15, s[16:19], s27 offen sc1\n\t"
    "v_mfma_f32_16x16x32_bf16 v[28:31], v[252:255], v[132:135], v[28:31]\n\t"
    "s_mov_b32 s27, s38\n\t"
    "s_add_u32 s38, s38, 1032192\n\t"
    "s_waitcnt lgkmcnt(0)\n\t"
    "v_mfma_f32_16x16x32_bf16 v[32:35], a[0:3], v[136:139], 0\n\t"
    "ds_read2st64_b64 v[128:131], v3 offset0:4 offset1:5\n\t"
    "s_add_u32 m0, s76, 16384\n\t"
    "s_add_u32 s72, s71, 294912\n\t"
    "v_mfma_f32_16x16x32_bf16 v[32:35], a[16:19], v[140:143], v[32:35]\n\t"
    "buffer_load_dwordx4 v201, s[60:63], s72 offen offset:0 sc1 lds\n\t"
    "v_mfma_f32_16x16x32_bf16 v[36:39], a[4:7], v[136:139], 0\n\t"
    "ds_read2st64_b64 v[132:135], v3 offset0:6 offset1:7\n\t"
    "v_mfma_f32_16x16x32_bf16 v[36:39], a[20:23], v[140:143], v[36:39]\n\t"
    "v_fma_f32 v64, -v58, v72, v82\n\t"
    "v_fma_f32 v65, -v58, v93, v104\n\t"
    "v_fma_f32 v64, v56, v16, v64 clamp\n\t"
    "v_fmac_f32 v65, v57, v24\n\t"
    "v_mul_f32 v68, v64, v65\n\t"
    "v_fma_f32 v66, -v58, v73, v84\n\t"
    "v_fma_f32 v67, -v58, v94, v105\n\t"
    "v_fma_f32 v66, v56, v17, v66 clamp\n\t"
    "v_fmac_f32 v67, v57, v25\n\t"
    "v_mfma_f32_16x16x32_bf16 v[40:43], a[8:11], v[136:139], 0\n\t"
    "v_mul_f32 v69, v66, v67\n\t"
    "v_mfma_f32_16x16x32_bf16 v[40:43], a[24:27], v[140:143], v[40:43]\n\t"
    "v_cvt_pk_bf16_f32 v120, v68, v69\n\t"
    "v_mfma_f32_16x16x32_bf16 v[44:47], a[12:15], v[136:139], 0\n\t"
    "v_fma_f32 v64, -v58, v74, v85\n\t"
    "v_mfma_f32_16x16x32_bf16 v[44:47], a[28:31], v[140:143], v[44:47]\n\t"
    "s_waitcnt lgkmcnt(0)\n\t"
    "buffer_load_dwordx4 v201, s[60:63], s72 offen offset:1024 sc1 lds\n\t"
    "v_mfma_f32_16x16x32_bf16 v[32:35], a[32:35], v[128:131], v[32:35]\n\t"
    "ds_read2st64_b64 v[136:139], v3 offset0:8 offset1:9\n\t"
    "v_mfma_f32_16x16x32_bf16 v[32:35], a[48:51], v[132:135], v[32:35]\n\t"
    "s_mov_b32 s73, 0\n\t"
    "buffer_load_dwordx4 a[0:3], v196, s[4:7], s73 offen offset:0 sc1\n\t"
    "v_mfma_f32_16x16x32_bf16 v[36:39], a[36:39], v[128:131], v[36:39]\n\t"
    "ds_read2st64_b64 v[140:143], v3 offset0:10 offset1:11\n\t"
    "v_mfma_f32_16x16x32_bf16 v[36:39], a[52:55], v[132:135], v[36:39]\n\t"
    "v_fma_f32 v65, -v58, v96, v106\n\t"
    "v_mfma_f32_16x16x32_bf16 v[40:43], a[40:43], v[128:131], v[40:43]\n\t"
    "v_fma_f32 v64, v56, v18, v64 clamp\n\t"
    "v_mfma_f32_16x16x32_bf16 v[40:43], a[56:59], v[132:135], v[40:43]\n\t"
    "buffer_load_dwordx4 a[8:11], v196, s[4:7], s73 offen offset:1024 sc1\n\t"
    "v_mfma_f32_16x16x32_bf16 v[44:47], a[44:47], v[128:131], v[44:47]\n\t"
    "v_fmac_f32 v65, v57, v26\n\t"
    "v_mfma_f32_16x16x32_bf16 v[44:47], a[60:63], v[132:135], v[44:47]\n\t"
    "s_waitcnt lgkmcnt(0)\n\t"
    "v_mul_f32 v68, v64, v65\n\t"
    "v_mfma_f32_16x16x32_bf16 v[32:35], a[64:67], v[136:139], v[32:35]\n\t"
    "ds_read2st64_b64 v[128:131], v3 offset0:12 offset1:13\n\t"
    "v_mfma_f32_16x16x32_bf16 v[32:35], a[80:83], v[140:143], v[32:35]\n\t"
    "buffer_load_dwordx4 a[16:19], v196, s[4:7], s73 offen offset:2048 sc1\n\t"
    "v_mfma_f32_16x16x32_bf16 v[36:39], a[68:71], v[136:139], v[36:39]\n\t"
    "ds_read2st64_b64 v[132:135], v3 offset0:14 offset1:15\n\t"
    "v_mfma_f32_16x16x32_bf16 v[36:39], a[84:87], v[140:143], v[36:39]\n\t"
    "v_fma_f32 v66, -v58, v76, v86\n\t"
    "v_mfma_f32_16x16x32_bf16 v[40:43], a[72:75], v[136:139], v[40:43]\n\t"
    "v_fma_f32 v67, -v58, v97, v108\n\t"
    "v_mfma_f32_16x16x32_bf16 v[40:43], a[88:91], v[140:143], v[40:43]\n\t"
    "buffer_load_dwordx4 a[24:27], v196, s[4:7], s73 offen offset:3072 sc1\n\t"
    "v_mfma_f32_16x16x32_bf16 v[44:47], a[76:79], v[136:139], v[44:47]\n\t"
    "v_fma_f32 v66, v56, v19, v66 clamp\n\t"
    "v_mfma_f32_16x16x32_bf16 v[44:47], a[92:95], v[140:143], v[44:47]\n\t"
    "s_waitcnt lgkmcnt(0)\n\t"
    "v_fmac_f32 v67, v57, v27\n\t"
    "v_mfma_f32_16x16x32_bf16 v[32:35], a[96:99], v[128:131], v[32:35]\n\t"
    "ds_read2st64_b64 v[136:139], v3 offset0:16 offset1:17\n\t"
    "s_waitcnt vmcnt(19)\n\t"
    "v_mfma_f32_16x16x32_bf16 v[32:35], a[112:115], v[132:135], v[32:35]\n\t"
    "s_mov_b32 s73, 4096\n\t"
    "buffer_load_dwordx4 a[32:35], v196, s[4:7], s73 offen offset:0 sc1\n\t"
    "v_mfma_f32_16x16x32_bf16 v[36:39], a[100:103], v[128:131], v[36:39]\n\t"
    "ds_read2st64_b64 v[140:143], v3 offset0:18 offset1:19\n\t"
    "v_mfma_f32_16x16x32_bf16 v[36:39], a[116:119], v[132:135], v[36:39]\n\t"
    "v_mul_f32 v69, v66, v67\n\t"
    "v_mfma_f32_16x16x32_bf16 v[40:43], a[104:107], v[128:131], v[40:43]\n\t"
    "v_cvt_pk_bf16_f32 v121, v68, v69\n\t"
    "v_mfma_f32_16x16x32_bf16 v[40:43], a[120:123], v[132:135], v[40:43]\n\t"
    "buffer_load_dwordx4 a[40:43], v196, s[4:7], s73 offen offset:1024 sc1\n\t"
    "v_mfma_f32_16x16x32_bf16 v[44:47], a[108:111], v[128:131], v[44:47]\n\t"
    "v_fma_f32 v64, -v58, v77, v88\n\t"
    "v_mfma_f32_16x16x32_bf16 v[44:47], a[124:127], v[132:135], v[44:47]\n\t"
    "s_waitcnt lgkmcnt(0)\n\t"
    "v_fma_f32 v65, -v58, v98, v109\n\t"
    "v_mfma_f32_16x16x32_bf16 v[32:35], a[128:131], v[136:139], v[32:35]\n\t"
    "ds_read2st64_b64 v[128:131], v3 offset0:20 offset1:21\n\t"
    "v_mfma_f32_16x16x32_bf16 v[32:35], a[144:147], v[140:143], v[32:35]\n\t"
    "buffer_load_dwordx4 a[48:51], v196, s[4:7], s73 offen offset:2048 sc1\n\t"
    "v_mfma_f32_16x16x32_bf16 v[36:39], a[132:135], v[136:139], v[36:39]\n\t"
    "ds_read2st64_b64 v[132:135], v3 offset0:22 offset1:23\n\t"
    "v_mfma_f32_16x16x32_bf16 v[36:39], a[148:151], v[140:143], v[36:39]\n\t"
    "v_fma_f32 v64, v56, v20, v64 clamp\n\t"
    "v_mfma_f32_16x16x32_bf16 v[40:43], a[136:139], v[136:139], v[40:43]\n\t"
    "v_fmac_f32 v65, v57, v28\n\t"
    "v_mfma_f32_16x16x32_bf16 v[40:43], a[152:155], v[140:143], v[40:43]\n\t"
    "buffer_load_dwordx4 a[56:59], v196, s[4:7], s73 offen offset:3072 sc1\n\t"
    "v_mfma_f32_16x16x32_bf16 v[44:47], a[140:143], v[136:139], v[44:47]\n\t"
    "v_mul_f32 v68, v64, v65\n\t"
    "v_mfma_f32_16x16x32_bf16 v[44:47], a[156:159], v[140:143], v[44:47]\n\t"
    "s_waitcnt lgkmcnt(0)\n\t"
    "v_fma_f32 v66, -v58, v78, v89\n\t"
    "v_mfma_f32_16x16x32_bf16 v[32:35], a[160:163], v[128:131], v[32:35]\n\t"
    "ds_read2st64_b64 v[136:139], v3 offset0:24 offset1:25\n\t"
    "v_mfma_f32_16x16x32_bf16 v[32:35], a[176:179], v[132:135], v[32:35]\n\t"
    "s_mov_b32 s73, 8192\n\t"
    "buffer_load_dwordx4 a[64:67], v196, s[4:7], s73 offen offset:0 sc1\n\t"
    "v_mfma_f32_16x16x32_bf16 v[36:39], a[164:167], v[128:131], v[36:39]\n\t"
    "ds_read2st64_b64 v[140:143], v3 offset0:26 offset1:27\n\t"
    "v_mfma_f32_16x16x32_bf16 v[36:39], a[180:183], v[132:135], v[36:39]\n\t"
    "v_fma_f32 v67, -v58, v100, v110\n\t"
    "v_mfma_f32_16x16x32_bf16 v[40:43], a[168:171], v[128:131], v[40:43]\n\t"
    "v_fma_f32 v66, v56, v21, v66 clamp\n\t"
    "v_mfma_f32_16x16x32_bf16 v[40:43], a[184:187], v[132:135], v[40:43]\n\t"
    "buffer_load_dwordx4 a[72:75], v196, s[4:7], s73 offen offset:1024 sc1\n\t"
    "v_mfma_f32_16x16x32_bf16 v[44:47], a[172:175], v[128:131], v[44:47]\n\t"
    "v_fmac_f32 v67, v57, v29\n\t"
    "v_mfma_f32_16x16x32_bf16 v[44:47], a[188:191], v[132:135], v[44:47]\n\t"
    "s_waitcnt lgkmcnt(0)\n\t"
    "v_mul_f32 v69, v66, v67\n\t"
    "v_mfma_f32_16x16x32_bf16 v[32:35], a[192:195], v[136:139], v[32:35]\n\t"
    "ds_read2st64_b64 v[128:131], v3 offset0:28 offset1:29\n\t"
    "v_mfma_f32_16x16x32_bf16 v[32:35], a[208:211], v[140:143], v[32:35]\n\t"
    "buffer_load_dwordx4 a[80:83], v196, s[4:7], s73 offen offset:2048 sc1\n\t"
    "v_mfma_f32_16x16x32_bf16 v[36:39], a[196:199], v[136:139], v[36:39]\n\t"
    "ds_read2st64_b64 v[132:135], v3 offset0:30 offset1:31\n\t"
    "v_mfma_f32_16x16x32_bf16 v[36:39], a[212:215], v[140:143], v[36:39]\n\t"
    "v_cvt_pk_bf16_f32 v122, v68, v69\n\t"
    "v_mfma_f32_16x16x32_bf16 v[40:43], a[200:203], v[136:139], v[40:43]\n\t"
    "v_fma_f32 v64, -v58, v80, v90\n\t"
    "v_mfma_f32_16x16x32_bf16 v[40:43], a[216:219], v[140:143], v[40:43]\n\t"
    "buffer_load_dwordx4 a[88:91], v196, s[4:7], s73 offen offset:3072 sc1\n\t"
    "v_mfma_f32_16x16x32_bf16 v[44:47], a[204:207], v[136:139], v[44:47]\n\t"
    "v_fma_f32 v65, -v58, v101, v112\n\t"
    "v_mfma_f32_16x16x32_bf16 v[44:47], a[220:223], v[140:143], v[44:47]\n\t"
    "s_waitcnt lgkmcnt(0)\n\t"
    "v_fma_f32 v64, v56, v22, v64 clamp\n\t"
    "v_mfma_f32_16x16x32_bf16 v[32:35], a[224:227], v[128:131], v[32:35]\n\t"
    "ds_read2st64_b64 v[136:139], v3 offset0:32 offset1:33\n\t"
    "s_add_u32 s26, s26, 18432\n\t"
    "v_mfma_f32_16x16x32_bf16 v[32:35], a[240:243], v[132:135], v[32:35]\n\t"
    "s_cmp_ge_u32 s26, 163840\n\t"
    "s_mov_b32 s73, 12288\n\t"
    "buffer_load_dwordx4 a[96:99], v196, s[4:7], s73 offen offset:0 sc1\n\t"
    "v_mfma_f32_16x16x32_bf16 v[36:39], a[228:231], v[128:131], v[36:39]\n\t"
    "ds_read2st64_b64 v[140:143], v3 offset0:34 offset1:35\n\t"
    "s_cselect_b32 s26, 71680, s26\n\t"
    "v_mfma_f32_16x16x32_bf16 v[36:39], a[244:247], v[132:135], v[36:39]\n\t"
    "v_fmac_f32 v65, v57, v30\n\t"
    "v_mfma_f32_16x16x32_bf16 v[40:43], a[232:235], v[128:131], v[40:43]\n\t"
    "v_mul_f32 v68, v64, v65\n\t"
    "v_mfma_f32_16x16x32_bf16 v[40:43], a[248:251], v[132:135], v[40:43]\n\t"
    "v_add_u32 v3, s26, v2\n\t"
    "v_mfma_f32_16x16x32_bf16 v[44:47], a[236:239], v[128:131], v[44:47]\n\t"
    "buffer_load_dwordx4 a[104:107], v196, s[4:7], s73 offen offset:1024 sc1\n\t"
    "v_mfma_f32_16x16x32_bf16 v[44:47], a[252:255], v[132:135], v[44:47]\n\t"
    "s_waitcnt lgkmcnt(0)\n\t"
    "s_waitcnt vmcnt(27)\n\t"
    "s_barrier\n\t"
    "v_fma_f32 v66, -v58, v81, v92\n\t"
    "v_mfma_f32_16x16x32_bf16 v[32:35], v[224:227], v[136:139], v[32:35]\n\t"
    "v_fma_f32 v67, -v58, v102, v113\n\t"
    "v_mfma_f32_16x16x32_bf16 v[32:35], v[240:243], v[140:143], v[32:35]\n\t"
    "v_cmp_ne_u32 vcc, s66, v49\n\t"
    "v_cmp_ne_u32 s[30:31], s67, v51\n\t"
    "v_mfma_f32_16x16x32_bf16 v[36:39], v[228:231], v[136:139], v[36:39]\n\t"
    "v_fma_f32 v66, v56, v23, v66 clamp\n\t"
    "v_mfma_f32_16x16x32_bf16 v[36:39], v[244:247], v[140:143], v[36:39]\n\t"
    "s_or_b64 vcc, vcc, s[30:31]\n\t"
    "s_cbranch_vccnz L_ss118_%=\n\t"
    "L_sr118_%=:\n\t"
    "v_mov_b32 v62, v48\n\t"
    "v_fmac_f32 v67, v57, v31\n\t"
    "v_mfma_f32_16x16x32_bf16 v[40:43], v[232:235], v[136:139], v[40:43]\n\t"
    "v_mul_f32 v60, s78, v50\n\t"
    "v_mul_f32 v69, v66, v67\n\t"
    "v_mfma_f32_16x16x32_bf16 v[40:43], v[248:251], v[140:143], v[40:43]\n\t"
    "v_mul_f32 v61, s79, v50\n\t"
    "v_cvt_pk_bf16_f32 v123, v68, v69\n\t"
    "v_mfma_f32_16x16x32_bf16 v[44:47], v[236:239], v[136:139], v[44:47]\n\t"
    "buffer_store_dwordx4 v[120:123], v15, s[16:19], s27 offen sc1\n\t"
    "v_mfma_f32_16x16x32_bf16 v[44:47], v[252:255], v[140:143], v[44:47]\n\t"
    "s_mov_b32 s27, s38\n\t"
    "s_add_u32 s38, s38, 1032192\n\t"
    "s_waitcnt vmcnt(15)\n\t"
    "s_barrier\n\t"
    "ds_read_b128 v[144:147], v204 offset:57344\n\t"
    "ds_read_b128 v[148:151], v204 offset:62464\n\t"
    "ds_read_b128 v[152:155], v204 offset:58368\n\t"
    "ds_read_b128 v[156:159], v204 offset:63488\n\t"
    "ds_read_b128 v[160:163], v204 offset:59392\n\t"
    "ds_read_b128 v[164:167], v204 offset:64512\n\t"
    "ds_read_b128 v[168:171], v204 offset:60416\n\t"
    "ds_read_b128 v[172:175], v205 offset:0\n\t"
    "s_nop 1\n\t"
    "v_fma_f32 v64, -v62, v72, v82\n\t"
    "v_fma_f32 v65, -v62, v93, v104\n\t"
    "v_fma_f32 v64, v60, v32, v64 clamp\n\t"
    "v_fmac_f32 v65, v61, v40\n\t"
    "v_mul_f32 v68, v64, v65\n\t"
    "v_fma_f32 v66, -v62, v73, v84\n\t"
    "v_fma_f32 v67, -v62, v94, v105\n\t"
    "v_fma_f32 v66, v60, v33, v66 clamp\n\t"
    "v_fmac_f32 v67, v61, v41\n\t"
    "v_mul_f32 v69, v66, v67\n\t"
    "v_cvt_pk_bf16_f32 v120, v68, v69\n\t"
    "v_fma_f32 v64, -v62, v74, v85\n\t"
    "v_fma_f32 v65, -v62, v96, v106\n\t"
    "v_fma_f32 v64, v60, v34, v64 clamp\n\t"
    "v_fmac_f32 v65, v61, v42\n\t"
    "v_mul_f32 v68, v64, v65\n\t"
    "v_fma_f32 v66, -v62, v76, v86\n\t"
    "v_fma_f32 v67, -v62, v97, v108\n\t"
    "v_fma_f32 v66, v60, v35, v66 clamp\n\t"
    "v_fmac_f32 v67, v61, v43\n\t"
    "v_mul_f32 v69, v66, v67\n\t"
    "v_cvt_pk_bf16_f32 v121, v68, v69\n\t"
    "v_fma_f32 v64, -v62, v77, v88\n\t"
    "v_fma_f32 v65, -v62, v98, v109\n\t"
    "v_fma_f32 v64, v60, v36, v64 clamp\n\t"
    "v_fmac_f32 v65, v61, v44\n\t"
    "v_mul_f32 v68, v64, v65\n\t"
    "v_fma_f32 v66, -v62, v78, v89\n\t"
    "v_fma_f32 v67, -v62, v100, v110\n\t"
    "v_fma_f32 v66, v60, v37, v66 clamp\n\t"
    "v_fmac_f32 v67, v61, v45\n\t"
    "v_mul_f32 v69, v66, v67\n\t"
    "v_cvt_pk_bf16_f32 v122, v68, v69\n\t"
    "v_fma_f32 v64, -v62, v80, v90\n\t"
    "v_fma_f32 v65, -v62, v101, v112\n\t"
    "v_fma_f32 v64, v60, v38, v64 clamp\n\t"
    "v_fmac_f32 v65, v61, v46\n\t"
    "v_mul_f32 v68, v64, v65\n\t"
    "v_fma_f32 v66, -v62, v81, v92\n\t"
    "v_fma_f32 v67, -v62, v102, v113\n\t"
    "v_fma_f32 v66, v60, v39, v66 clamp\n\t"
    "v_fmac_f32 v67, v61, v47\n\t"
    "v_mul_f32 v69, v66, v67\n\t"
    "v_cvt_pk_bf16_f32 v123, v68, v69\n\t"
    "buffer_store_dwordx4 v[120:123], v15, s[16:19], s27 offen sc1\n\t"
    "L_P_%=:\n\t"
    "ds_read_b128 v[88:91], v204 offset:61440\n\t"
    "ds_read_b128 v[92:95], v205 offset:1024\n\t"
    "ds_read_b128 v[96:99], v205 offset:2048\n\t"
    "ds_read_b128 v[100:103], v205 offset:7168\n\t"
    "s_waitcnt lgkmcnt(10) vmcnt(14)\n\t"
    "v_mfma_f32_16x16x32_bf16 v[32:35], v[144:147], a[0:3], 0\n\t"
    "ds_read_b128 v[104:107], v205 offset:3072\n\t"
    "s_mov_b32 s73, 16384\n\t"
    "buffer_load_dwordx4 a[144:147], v196, s[4:7], s73 offen offset:2048 sc1\n\t"
    "v_mfma_f32_16x16x32_bf16 v[32:35], v[148:151], a[8:11], v[32:35]\n\t"
    "ds_read_b128 v[108:111], v205 offset:8192\n\t"
    "s_waitcnt lgkmcnt(10)\n\t"
    "v_mfma_f32_16x16x32_bf16 v[40:43], v[152:155], a[0:3], 0\n\t"
    "ds_read_b128 v[112:115], v205 offset:4096\n\t"
    "buffer_load_dwordx4 a[152:155], v196, s[4:7], s73 offen offset:3072 sc1\n\t"
    "v_mfma_f32_16x16x32_bf16 v[40:43], v[156:159], a[8:11], v[40:43]\n\t"
    "ds_read_b128 v[116:119], v205 offset:9216\n\t"
    "s_waitcnt lgkmcnt(10)\n\t"
    "v_mfma_f32_16x16x32_bf16 v[48:51], v[160:163], a[0:3], 0\n\t"
    "ds_read_b128 v[144:147], v205 offset:5120\n\t"
    "s_mov_b32 s73, 20480\n\t"
    "buffer_load_dwordx4 a[160:163], v196, s[4:7], s73 offen offset:0 sc1\n\t"
    "v_mfma_f32_16x16x32_bf16 v[48:51], v[164:167], a[8:11], v[48:51]\n\t"
    "ds_read_b128 v[148:151], v205 offset:10240\n\t"
    "s_waitcnt lgkmcnt(10)\n\t"
    "v_mfma_f32_16x16x32_bf16 v[56:59], v[168:171], a[0:3], 0\n\t"
    "ds_read_b128 v[152:155], v205 offset:6144\n\t"
    "buffer_load_dwordx4 a[168:171], v196, s[4:7], s73 offen offset:1024 sc1\n\t"
    "v_mfma_f32_16x16x32_bf16 v[56:59], v[172:175], a[8:11], v[56:59]\n\t"
    "ds_read_b128 v[156:159], v205 offset:11264\n\t"
    "s_waitcnt lgkmcnt(10)\n\t"
    "v_mfma_f32_16x16x32_bf16 v[80:83], v[88:91], a[0:3], 0\n\t"
    "ds_read_b128 v[160:163], v15 offset:0\n\t"
    "buffer_load_dwordx4 a[176:179], v196, s[4:7], s73 offen offset:2048 sc1\n\t"
    "v_mfma_f32_16x16x32_bf16 v[80:83], v[92:95], a[8:11], v[80:83]\n\t"
    "ds_read_b128 v[164:167], v15 offset:18432\n\t"
    "s_waitcnt lgkmcnt(10) vmcnt(17)\n\t"
    "v_mfma_f32_16x16x32_bf16 v[32:35], v[96:99], a[16:19], v[32:35]\n\t"
    "ds_read_b128 v[168:171], v15 offset:1024\n\t"
    "buffer_load_dwordx4 a[184:187], v196, s[4:7], s73 offen offset:3072 sc1\n\t"
    "v_mfma_f32_16x16x32_bf16 v[32:35], v[100:103], a[24:27], v[32:35]\n\t"
    "ds_read_b128 v[172:175], v15 offset:19456\n\t"
    "s_waitcnt lgkmcnt(10)\n\t"
    "v_mfma_f32_16x16x32_bf16 v[40:43], v[104:107], a[16:19], v[40:43]\n\t"
    "s_waitcnt vmcnt(6)\n\t"
    "s_barrier\n\t"
    "s_lshl_b32 s28, s2, 7\n\t"
    "s_add_u32 s28, s28, 41947136\n\t"
    "s_cmp_lg_u32 s3, 0\n\t"
    "s_cbranch_scc1 L_pfg_%=\n\t"
    "v_mov_b32 v2, s66\n\t"
    "v_mov_b32 v3, s67\n\t"
    "v_mov_b32 v4, s28\n\t"
    "s_mov_b64 exec, 1\n\t"
    "global_store_dwordx2 v4, v[2:3], s[36:37] sc1\n\t"
    "s_mov_b64 exec, -1\n\t"
    "L_pfg_%=:\n\t"
    "ds_read_b128 v[88:91], v15 offset:36864\n\t"
    "s_mov_b32 s73, 24576\n\t"
    "buffer_load_dwordx4 a[192:195], v196, s[4:7], s73 offen offset:0 sc1\n\t"
    "v_mfma_f32_16x16x32_bf16 v[40:43], v[108:111], a[24:27], v[40:43]\n\t"
    "ds_read_b128 v[92:95], v15 offset:55296\n\t"
    "s_waitcnt lgkmcnt(10)\n\t"
    "v_mfma_f32_16x16x32_bf16 v[48:51], v[112:115], a[16:19], v[48:51]\n\t"
    "ds_read_b128 v[96:99], v15 offset:37888\n\t"
    "buffer_load_dwordx4 a[200:203], v196, s[4:7], s73 offen offset:1024 sc1\n\t"
    "v_accvgpr_write_b32 a112, v176\n\t"
    "v_mfma_f32_16x16x32_bf16 v[48:51], v[116:119], a[24:27], v[48:51]\n\t"
    "ds_read_b128 v[100:103], v15 offset:56320\n\t"
    "v_accvgpr_write_b32 a113, v177\n\t"
    "s_waitcnt lgkmcnt(10)\n\t"
    "v_mfma_f32_16x16x32_bf16 v[56:59], v[144:147], a[16:19], v[56:59]\n\t"
    "ds_read_b128 v[104:107], v15 offset:2048\n\t"
    "buffer_load_dwordx4 a[208:211], v196, s[4:7], s73 offen offset:2048 sc1\n\t"
    "v_accvgpr_write_b32 a114, v178\n\t"
    "v_mfma_f32_16x16x32_bf16 v[56:59], v[148:151], a[24:27], v[56:59]\n\t"
    "ds_read_b128 v[108:111], v15 offset:20480\n\t"
    "v_accvgpr_write_b32 a115, v179\n\t"
    "s_waitcnt lgkmcnt(10)\n\t"
    "v_mfma_f32_16x16x32_bf16 v[80:83], v[152:155], a[16:19], v[80:83]\n\t"
    "ds_read_b128 v[112:115], v15 offset:3072\n\t"
    "buffer_load_dwordx4 a[216:219], v196, s[4:7], s73 offen offset:3072 sc1\n\t"
    "v_accvgpr_write_b32 a120, v180\n\t"
    "v_mfma_f32_16x16x32_bf16 v[80:83], v[156:159], a[24:27], v[80:83]\n\t"
    "ds_read_b128 v[116:119], v15 offset:21504\n\t"
    "v_accvgpr_write_b32 a121, v181\n\t"
    "s_waitcnt lgkmcnt(10) vmcnt(24)\n\t"
    "v_mfma_f32_16x16x32_bf16 v[64:67], v[160:163], a[0:3], 0\n\t"
    "ds_read_b128 v[144:147], v15 offset:38912\n\t"
    "s_mov_b32 s73, 28672\n\t"
    "buffer_load_dwordx4 a[224:227], v196, s[4:7], s73 offen offset:0 sc1\n\t"
    "v_accvgpr_write_b32 a122, v182\n\t"
    "v_mfma_f32_16x16x32_bf16 v[64:67], v[164:167], a[8:11], v[64:67]\n\t"
    "ds_read_b128 v[148:151], v15 offset:57344\n\t"
    "v_accvgpr_write_b32 a123, v183\n\t"
    "s_waitcnt lgkmcnt(10)\n\t"
    "v_mfma_f32_16x16x32_bf16 v[72:75], v[168:171], a[0:3], 0\n\t"
    "ds_read_b128 v[152:155], v15 offset:39936\n\t"
    "buffer_load_dwordx4 a[232:235], v196, s[4:7], s73 offen offset:1024 sc1\n\t"
    "v_accvgpr_write_b32 a128, v184\n\t"
    "v_mfma_f32_16x16x32_bf16 v[72:75], v[172:175], a[8:11], v[72:75]\n\t"
    "ds_read_b128 v[156:159], v15 offset:58368\n\t"
    "v_accvgpr_write_b32 a129, v185\n\t"
    "s_waitcnt lgkmcnt(10) vmcnt(24)\n\t"
    "v_mfma_f32_16x16x32_bf16 v[64:67], v[88:91], a[16:19], v[64:67]\n\t"
    "ds_read_b128 v[160:163], v15 offset:4096\n\t"
    "buffer_load_dwordx4 a[240:243], v196, s[4:7], s73 offen offset:2048 sc1\n\t"
    "v_accvgpr_write_b32 a130, v186\n\t"
    "v_mfma_f32_16x16x32_bf16 v[64:67], v[92:95], a[24:27], v[64:67]\n\t"
    "ds_read_b128 v[164:167], v15 offset:22528\n\t"
    "v_accvgpr_write_b32 a131, v187\n\t"
    "s_waitcnt lgkmcnt(10)\n\t"
    "v_mfma_f32_16x16x32_bf16 v[72:75], v[96:99], a[16:19], v[72:75]\n\t"
    "ds_read_b128 v[168:171], v15 offset:5120\n\t"
    "buffer_load_dwordx4 a[248:251], v196, s[4:7], s73 offen offset:3072 sc1\n\t"
    "v_accvgpr_write_b32 a136, v188\n\t"
    "v_mfma_f32_16x16x32_bf16 v[72:75], v[100:103], a[24:27], v[72:75]\n\t"
    "ds_read_b128 v[172:175], v15 offset:23552\n\t"
    "v_accvgpr_write_b32 a137, v189\n\t"
    "s_waitcnt lgkmcnt(10) vmcnt(24)\n\t"
    "v_mfma_f32_16x16x32_bf16 v[64:67], v[104:107], a[32:35], v[64:67]\n\t"
    "ds_read_b128 v[88:91], v15 offset:40960\n\t"
    "s_mov_b32 s73, 32768\n\t"
    "buffer_load_dwordx4 v[224:227], v196, s[4:7], s73 offen offset:0 sc1\n\t"
    "v_accvgpr_write_b32 a138, v190\n\t"
    "v_mfma_f32_16x16x32_bf16 v[64:67], v[108:111], a[40:43], v[64:67]\n\t"
    "ds_read_b128 v[92:95], v15 offset:59392\n\t"
    "v_accvgpr_write_b32 a139, v191\n\t"
    "s_waitcnt lgkmcnt(10)\n\t"
    "v_mfma_f32_16x16x32_bf16 v[72:75], v[112:115], a[32:35], v[72:75]\n\t"
    "ds_read_b128 v[96:99], v15 offset:41984\n\t"
    "buffer_load_dwordx4 v[232:235], v196, s[4:7], s73 offen offset:1024 sc1\n\t"
    "v_mfma_f32_16x16x32_bf16 v[72:75], v[116:119], a[40:43], v[72:75]\n\t"
    "ds_read_b128 v[100:103], v15 offset:60416\n\t"
    "s_waitcnt lgkmcnt(10) vmcnt(24)\n\t"
    "v_mfma_f32_16x16x32_bf16 v[64:67], v[144:147], a[48:51], v[64:67]\n\t"
    "ds_read_b128 v[104:107], v15 offset:6144\n\t"
    "buffer_load_dwordx4 v[240:243], v196, s[4:7], s73 offen offset:2048 sc1\n\t"
    "v_mfma_f32_16x16x32_bf16 v[64:67], v[148:151], a[56:59], v[64:67]\n\t"
    "ds_read_b128 v[108:111], v15 offset:24576\n\t"
    "s_waitcnt lgkmcnt(10)\n\t"
    "v_mfma_f32_16x16x32_bf16 v[72:75], v[152:155], a[48:51], v[72:75]\n\t"
    "ds_read_b128 v[112:115], v15 offset:7168\n\t"
    "buffer_load_dwordx4 v[248:251], v196, s[4:7], s73 offen offset:3072 sc1\n\t"
    "v_mfma_f32_16x16x32_bf16 v[72:75], v[156:159], a[56:59], v[72:75]\n\t"
    "ds_read_b128 v[116:119], v15 offset:25600\n\t"
    "s_waitcnt lgkmcnt(10) vmcnt(24)\n\t"
    "v_mfma_f32_16x16x32_bf16 v[64:67], v[160:163], a[64:67], v[64:67]\n\t"
    "ds_read_b128 v[144:147], v15 offset:43008\n\t"
    "v_mfma_f32_16x16x32_bf16 v[64:67], v[164:167], a[72:75], v[64:67]\n\t"
    "ds_read_b128 v[148:151], v15 offset:61440\n\t"
    "s_waitcnt lgkmcnt(10)\n\t"
    "v_mfma_f32_16x16x32_bf16 v[72:75], v[168:171], a[64:67], v[72:75]\n\t"
    "ds_read_b128 v[152:155], v15 offset:44032\n\t"
    "v_mfma_f32_16x16x32_bf16 v[72:75], v[172:175], a[72:75], v[72:75]\n\t"
    "ds_read_b128 v[156:159], v15 offset:62464\n\t"
    "s_waitcnt lgkmcnt(10) vmcnt(22)\n\t"
    "v_mfma_f32_16x16x32_bf16 v[64:67], v[88:91], a[80:83], v[64:67]\n\t"
    "ds_read_b128 v[160:163], v15 offset:8192\n\t"
    "v_mfma_f32_16x16x32_bf16 v[64:67], v[92:95], a[88:91], v[64:67]\n\t"
    "ds_read_b128 v[164:167], v15 offset:26624\n\t"
    "s_waitcnt lgkmcnt(10)\n\t"
    "v_mfma_f32_16x16x32_bf16 v[72:75], v[96:99], a[80:83], v[72:75]\n\t"
    "ds_read_b128 v[168:171], v15 offset:9216\n\t"
    "v_mfma_f32_16x16x32_bf16 v[72:75], v[100:103], a[88:91], v[72:75]\n\t"
    "ds_read_b128 v[172:175], v15 offset:27648\n\t"
    "s_waitcnt lgkmcnt(10) vmcnt(20)\n\t"
    "v_mfma_f32_16x16x32_bf16 v[64:67], v[104:107], a[96:99], v[64:67]\n\t"
    "ds_read_b128 v[88:91], v15 offset:45056\n\t"
    "v_mfma_f32_16x16x32_bf16 v[64:67], v[108:111], a[104:107], v[64:67]\n\t"
    "ds_read_b128 v[92:95], v15 offset:63488\n\t"
    "s_waitcnt lgkmcnt(10)\n\t"
    "v_mfma_f32_16x16x32_bf16 v[72:75], v[112:115], a[96:99], v[72:75]\n\t"
    "ds_read_b128 v[96:99], v15 offset:46080\n\t"
    "v_mfma_f32_16x16x32_bf16 v[72:75], v[116:119], a[104:107], v[72:75]\n\t"
    "ds_read_b128 v[100:103], v15 offset:64512\n\t"
    "s_waitcnt lgkmcnt(10)\n\t"
    "v_mfma_f32_16x16x32_bf16 v[64:67], v[144:147], a[112:115], v[64:67]\n\t"
    "ds_read_b128 v[104:107], v15 offset:10240\n\t"
    "v_mfma_f32_16x16x32_bf16 v[64:67], v[148:151], a[120:123], v[64:67]\n\t"
    "ds_read_b128 v[108:111], v15 offset:28672\n\t"
    "s_waitcnt lgkmcnt(10)\n\t"
    "v_mfma_f32_16x16x32_bf16 v[72:75], v[152:155], a[112:115], v[72:75]\n\t"
    "ds_read_b128 v[112:115], v15 offset:11264\n\t"
    "v_mfma_f32_16x16x32_bf16 v[72:75], v[156:159], a[120:123], v[72:75]\n\t"
    "ds_read_b128 v[116:119], v15 offset:29696\n\t"
    "s_waitcnt lgkmcnt(10)\n\t"
    "v_mfma_f32_16x16x32_bf16 v[64:67], v[160:163], a[128:131], v[64:67]\n\t"
    "ds_read_b128 v[144:147], v15 offset:47104\n\t"
    "v_mfma_f32_16x16x32_bf16 v[64:67], v[164:167], a[136:139], v[64:67]\n\t"
    "ds_read_b128 v[148:151], v204 offset:0\n\t"
    "s_waitcnt lgkmcnt(10)\n\t"
    "v_mfma_f32_16x16x32_bf16 v[72:75], v[168:171], a[128:131], v[72:75]\n\t"
    "ds_read_b128 v[152:155], v15 offset:48128\n\t"
    "v_mfma_f32_16x16x32_bf16 v[72:75], v[172:175], a[136:139], v[72:75]\n\t"
    "ds_read_b128 v[156:159], v204 offset:1024\n\t"
    "s_waitcnt lgkmcnt(10) vmcnt(16)\n\t"
    "v_mfma_f32_16x16x32_bf16 v[64:67], v[88:91], a[144:147], v[64:67]\n\t"
    "ds_read_b128 v[160:163], v15 offset:12288\n\t"
    "v_mfma_f32_16x16x32_bf16 v[64:67], v[92:95], a[152:155], v[64:67]\n\t"
    "ds_read_b128 v[164:167], v15 offset:30720\n\t"
    "s_waitcnt lgkmcnt(10)\n\t"
    "v_mfma_f32_16x16x32_bf16 v[72:75], v[96:99], a[144:147], v[72:75]\n\t"
    "ds_read_b128 v[168:171], v15 offset:13312\n\t"
    "v_mfma_f32_16x16x32_bf16 v[72:75], v[100:103], a[152:155], v[72:75]\n\t"
    "ds_read_b128 v[172:175], v15 offset:31744\n\t"
    "s_waitcnt lgkmcnt(10) vmcnt(14)\n\t"
    "v_mfma_f32_16x16x32_bf16 v[64:67], v[104:107], a[160:163], v[64:67]\n\t"
    "ds_read_b128 v[88:91], v15 offset:49152\n\t"
    "v_mfma_f32_16x16x32_bf16 v[64:67], v[108:111], a[168:171], v[64:67]\n\t"
    "ds_read_b128 v[92:95], v204 offset:2048\n\t"
    "s_waitcnt lgkmcnt(10)\n\t"
    "v_mfma_f32_16x16x32_bf16 v[72:75], v[112:115], a[160:163], v[72:75]\n\t"
    "ds_read_b128 v[96:99], v15 offset:50176\n\t"
    "v_mfma_f32_16x16x32_bf16 v[72:75], v[116:119], a[168:171], v[72:75]\n\t"
    "ds_read_b128 v[100:103], v204 offset:3072\n\t"
    "s_waitcnt lgkmcnt(10) vmcnt(12)\n\t"
    "v_mfma_f32_16x16x32_bf16 v[64:67], v[144:147], a[176:179], v[64:67]\n\t"
    "ds_read_b128 v[104:107], v15 offset:14336\n\t"
    "v_mfma_f32_16x16x32_bf16 v[64:67], v[148:151], a[184:187], v[64:67]\n\t"
    "ds_read_b128 v[108:111], v15 offset:32768\n\t"
    "s_add_u32 m0, s75, 81920\n\t"
    "s_add_u32 s72, s71, 73728\n\t"
    "s_waitcnt lgkmcnt(10)\n\t"
    "v_mfma_f32_16x16x32_bf16 v[72:75], v[152:155], a[176:179], v[72:75]\n\t"
    "ds_read_b128 v[112:115], v15 offset:15360\n\t"
    "buffer_load_dwordx4 v199, s[60:63], s72 offen offset:0 sc1 lds\n\t"
    "v_mfma_f32_16x16x32_bf16 v[72:75], v[156:159], a[184:187], v[72:75]\n\t"
    "ds_read_b128 v[116:119], v15 offset:33792\n\t"
    "s_waitcnt lgkmcnt(10) vmcnt(11)\n\t"
    "v_mfma_f32_16x16x32_bf16 v[64:67], v[160:163], a[192:195], v[64:67]\n\t"
    "ds_read_b128 v[144:147], v15 offset:51200\n\t"
    "buffer_load_dwordx4 v199, s[60:63], s72 offen offset:1024 sc1 lds\n\t"
    "v_mfma_f32_16x16x32_bf16 v[64:67], v[164:167], a[200:203], v[64:67]\n\t"
    "ds_read_b128 v[148:151], v204 offset:4096\n\t"
    "s_add_u32 m0, s75, 83968\n\t"
    "s_waitcnt lgkmcnt(10)\n\t"
    "v_mfma_f32_16x16x32_bf16 v[72:75], v[168:171], a[192:195], v[72:75]\n\t"
    "ds_read_b128 v[152:155], v15 offset:52224\n\t"
    "buffer_load_dwordx4 v200, s[60:63], s72 offen offset:0 sc1 lds\n\t"
    "v_mfma_f32_16x16x32_bf16 v[72:75], v[172:175], a[200:203], v[72:75]\n\t"
    "ds_read_b128 v[156:159], v204 offset:5120\n\t"
    "s_waitcnt lgkmcnt(10) vmcnt(11)\n\t"
    "v_mfma_f32_16x16x32_bf16 v[64:67], v[88:91], a[208:211], v[64:67]\n\t"
    "ds_read_b128 v[160:163], v15 offset:16384\n\t"
    "buffer_load_dwordx4 v200, s[60:63], s72 offen offset:1024 sc1 lds\n\t"
    "v_mfma_f32_16x16x32_bf16 v[64:67], v[92:95], a[216:219], v[64:67]\n\t"
    "ds_read_b128 v[164:167], v15 offset:34816\n\t"
    "s_add_u32 m0, s75, 86016\n\t"
    "s_waitcnt lgkmcnt(10)\n\t"
    "v_mfma_f32_16x16x32_bf16 v[72:75], v[96:99], a[208:211], v[72:75]\n\t"
    "ds_read_b128 v[168:171], v15 offset:17408\n\t"
    "buffer_load_dwordx4 v202, s[60:63], s72 offen offset:0 sc1 lds\n\t"
    "v_mfma_f32_16x16x32_bf16 v[72:75], v[100:103], a[216:219], v[72:75]\n\t"
    "ds_read_b128 v[172:175], v15 offset:35840\n\t"
    "s_waitcnt lgkmcnt(10) vmcnt(11)\n\t"
    "v_mfma_f32_16x16x32_bf16 v[64:67], v[104:107], a[224:227], v[64:67]\n\t"
    "ds_read_b128 v[88:91], v15 offset:53248\n\t"
    "v_mfma_f32_16x16x32_bf16 v[64:67], v[108:111], a[232:235], v[64:67]\n\t"
    "ds_read_b128 v[92:95], v204 offset:6144\n\t"
    "s_waitcnt lgkmcnt(10)\n\t"
    "v_mfma_f32_16x16x32_bf16 v[72:75], v[112:115], a[224:227], v[72:75]\n\t"
    "ds_read_b128 v[96:99], v15 offset:54272\n\t"
    "global_load_dwordx2 v[220:221], v213, s[36:37] sc1\n\t"
    "v_mfma_f32_16x16x32_bf16 v[72:75], v[116:119], a[232:235], v[72:75]\n\t"
    "ds_read_b128 v[100:103], v204 offset:7168\n\t"
    "s_waitcnt lgkmcnt(10) vmcnt(10)\n\t"
    "v_mfma_f32_16x16x32_bf16 v[64:67], v[144:147], a[240:243], v[64:67]\n\t"
    "ds_read_b128 v[104:107], v205 offset:12288\n\t"
    "v_mfma_f32_16x16x32_bf16 v[64:67], v[148:151], a[248:251], v[64:67]\n\t"
    "ds_read_b128 v[108:111], v205 offset:17408\n\t"
    "s_waitcnt lgkmcnt(10)\n\t"
    "v_mfma_f32_16x16x32_bf16 v[72:75], v[152:155], a[240:243], v[72:75]\n\t"
    "ds_read_b128 v[112:115], v205 offset:13312\n\t"
    "v_mfma_f32_16x16x32_bf16 v[72:75], v[156:159], a[248:251], v[72:75]\n\t"
    "ds_read_b128 v[116:119], v205 offset:18432\n\t"
    "s_waitcnt lgkmcnt(10) vmcnt(8)\n\t"
    "v_mfma_f32_16x16x32_bf16 v[64:67], v[160:163], v[224:227], v[64:67]\n\t"
    "ds_read_b128 v[144:147], v205 offset:14336\n\t"
    "v_mfma_f32_16x16x32_bf16 v[64:67], v[164:167], v[232:235], v[64:67]\n\t"
    "ds_read_b128 v[148:151], v205 offset:19456\n\t"
    "s_waitcnt lgkmcnt(10)\n\t"
    "v_mfma_f32_16x16x32_bf16 v[72:75], v[168:171], v[224:227], v[72:75]\n\t"
    "ds_read_b128 v[152:155], v205 offset:15360\n\t"
    "v_mfma_f32_16x16x32_bf16 v[72:75], v[172:175], v[232:235], v[72:75]\n\t"
    "ds_read_b128 v[156:159], v205 offset:20480\n\t"
    "s_waitcnt lgkmcnt(10) vmcnt(6)\n\t"
    "v_mfma_f32_16x16x32_bf16 v[64:67], v[88:91], v[240:243], v[64:67]\n\t"
    "ds_read_b128 v[160:163], v205 offset:16384\n\t"
    "v_mfma_f32_16x16x32_bf16 v[64:67], v[92:95], v[248:251], v[64:67]\n\t"
    "ds_read_b128 v[164:167], v205 offset:21504\n\t"
    "s_waitcnt lgkmcnt(10)\n\t"
    "v_mfma_f32_16x16x32_bf16 v[72:75], v[96:99], v[240:243], v[72:75]\n\t"
    "ds_read_b128 v[168:171], v205 offset:22528\n\t"
    "v_mfma_f32_16x16x32_bf16 v[72:75], v[100:103], v[248:251], v[72:75]\n\t"
    "ds_read_b128 v[172:175], v205 offset:27648\n\t"
    "s_waitcnt vmcnt(0)\n\t"
    "v_cmp_ne_u64 vcc, s[66:67], v[220:221]\n\t"
    "s_cbranch_vccnz L_psg_%=\n\t"
    "L_pbg_%=:\n\t"
    "s_mov_b32 s73, 0\n\t"
    "buffer_load_dwordx4 a[4:7], v197, s[4:7], s73 offen offset:0 sc1\n\t"
    "buffer_load_dwordx4 a[12:15], v197, s[4:7], s73 offen offset:1024 sc1\n\t"
    "buffer_load_dwordx4 a[20:23], v197, s[4:7], s73 offen offset:2048 sc1\n\t"
    "buffer_load_dwordx4 a[28:31], v197, s[4:7], s73 offen offset:3072 sc1\n\t"
    "s_mov_b32 s73, 4096\n\t"
    "buffer_load_dwordx4 a[36:39], v197, s[4:7], s73 offen offset:0 sc1\n\t"
    "buffer_load_dwordx4 a[44:47], v197, s[4:7], s73 offen offset:1024 sc1\n\t"
    "s_waitcnt lgkmcnt(10)\n\t"
    "v_mfma_f32_16x16x32_bf16 v[32:35], v[104:107], a[32:35], v[32:35]\n\t"
    "ds_read_b128 v[88:91], v205 offset:23552\n\t"
    "v_mfma_f32_16x16x32_bf16 v[32:35], v[108:111], a[40:43], v[32:35]\n\t"
    "ds_read_b128 v[92:95], v205 offset:28672\n\t"
    "s_waitcnt lgkmcnt(10)\n\t"
    "v_mfma_f32_16x16x32_bf16 v[40:43], v[112:115], a[32:35], v[40:43]\n\t"
    "ds_read_b128 v[96:99], v205 offset:24576\n\t"
    "buffer_load_dwordx4 a[52:55], v197, s[4:7], s73 offen offset:2048 sc1\n\t"
    "v_mfma_f32_16x16x32_bf16 v[40:43], v[116:119], a[40:43], v[40:43]\n\t"
    "ds_read_b128 v[100:103], v205 offset:29696\n\t"
    "s_waitcnt lgkmcnt(10)\n\t"
    "v_mfma_f32_16x16x32_bf16 v[48:51], v[144:147], a[32:35], v[48:51]\n\t"
    "ds_read_b128 v[104:107], v205 offset:25600\n\t"
    "v_mfma_f32_16x16x32_bf16 v[48:51], v[148:151], a[40:43], v[48:51]\n\t"
    "ds_read_b128 v[108:111], v205 offset:30720\n\t"
    "s_waitcnt lgkmcnt(10)\n\t"
    "v_mfma_f32_16x16x32_bf16 v[56:59], v[152:155], a[32:35], v[56:59]\n\t"
    "ds_read_b128 v[112:115], v205 offset:26624\n\t"
    "buffer_load_dwordx4 a[60:63], v197, s[4:7], s73 offen offset:3072 sc1\n\t"
    "v_mfma_f32_16x16x32_bf16 v[56:59], v[156:159], a[40:43], v[56:59]\n\t"
    "ds_read_b128 v[116:119], v205 offset:31744\n\t"
    "s_waitcnt lgkmcnt(10)\n\t"
    "v_mfma_f32_16x16x32_bf16 v[80:83], v[160:163], a[32:35], v[80:83]\n\t"
    "ds_read_b128 v[144:147], v204 offset:57344\n\t"
    "v_mfma_f32_16x16x32_bf16 v[80:83], v[164:167], a[40:43], v[80:83]\n\t"
    "ds_read_b128 v[148:151], v204 offset:62464\n\t"
    "s_waitcnt lgkmcnt(10)\n\t"
    "v_mfma_f32_16x16x32_bf16 v[32:35], v[168:171], a[48:51], v[32:35]\n\t"
    "ds_read_b128 v[152:155], v204 offset:58368\n\t"
    "s_mov_b32 s73, 8192\n\t"
    "buffer_load_dwordx4 a[68:71], v197, s[4:7], s73 offen offset:0 sc1\n\t"
    "v_mfma_f32_16x16x32_bf16 v[32:35], v[172:175], a[56:59], v[32:35]\n\t"
    "ds_read_b128 v[156:159], v204 offset:63488\n\t"
    "s_waitcnt lgkmcnt(10)\n\t"
    "v_mfma_f32_16x16x32_bf16 v[40:43], v[88:91], a[48:51], v[40:43]\n\t"
    "ds_read_b128 v[160:163], v204 offset:59392\n\t"
    "v_mfma_f32_16x16x32_bf16 v[40:43], v[92:95], a[56:59], v[40:43]\n\t"
    "ds_read_b128 v[164:167], v204 offset:64512\n\t"
    "s_waitcnt lgkmcnt(10)\n\t"
    "v_mfma_f32_16x16x32_bf16 v[48:51], v[96:99], a[48:51], v[48:51]\n\t"
    "ds_read_b128 v[168:171], v204 offset:60416\n\t"
    "buffer_load_dwordx4 a[76:79], v197, s[4:7], s73 offen offset:1024 sc1\n\t"
    "v_mfma_f32_16x16x32_bf16 v[48:51], v[100:103], a[56:59], v[48:51]\n\t"
    "ds_read_b128 v[172:175], v205 offset:0\n\t"
    "s_waitcnt lgkmcnt(10)\n\t"
    "v_mfma_f32_16x16x32_bf16 v[56:59], v[104:107], a[48:51], v[56:59]\n\t"
    "ds_read_b128 v[88:91], v204 offset:61440\n\t"
    "v_mfma_f32_16x16x32_bf16 v[56:59], v[108:111], a[56:59], v[56:59]\n\t"
    "ds_read_b128 v[92:95], v205 offset:1024\n\t"
    "s_waitcnt lgkmcnt(10)\n\t"
    "v_mfma_f32_16x16x32_bf16 v[80:83], v[112:115], a[48:51], v[80:83]\n\t"
    "ds_read_b128 v[96:99], v205 offset:2048\n\t"
    "buffer_load_dwordx4 a[84:87], v197, s[4:7], s73 offen offset:2048 sc1\n\t"
    "v_mfma_f32_16x16x32_bf16 v[80:83], v[116:119], a[56:59], v[80:83]\n\t"
    "ds_read_b128 v[100:103], v205 offset:7168\n\t"
    "s_waitcnt lgkmcnt(10) vmcnt(9)\n\t"
    "v_mfma_f32_16x16x32_bf16 v[36:39], v[144:147], a[4:7], 0\n\t"
    "ds_read_b128 v[104:107], v205 offset:3072\n\t"
    "s_add_u32 m0, s75, 102400\n\t"
    "s_add_u32 s72, s71, 110592\n\t"
    "v_mfma_f32_16x16x32_bf16 v[36:39], v[148:151], a[12:15], v[36:39]\n\t"
    "ds_read_b128 v[108:111], v205 offset:8192\n\t"
    "buffer_load_dwordx4 v199, s[60:63], s72 offen offset:0 sc1 lds\n\t"
    "s_waitcnt lgkmcnt(10)\n\t"
    "v_mfma_f32_16x16x32_bf16 v[44:47], v[152:155], a[4:7], 0\n\t"
    "ds_read_b128 v[112:115], v205 offset:4096\n\t"
    "buffer_load_dwordx4 a[92:95], v197, s[4:7], s73 offen offset:3072 sc1\n\t"
    "v_mfma_f32_16x16x32_bf16 v[44:47], v[156:159], a[12:15], v[44:47]\n\t"
    "ds_read_b128 v[116:119], v205 offset:9216\n\t"
    "s_waitcnt lgkmcnt(10)\n\t"
    "v_mfma_f32_16x16x32_bf16 v[52:55], v[160:163], a[4:7], 0\n\t"
    "ds_read_b128 v[144:147], v205 offset:5120\n\t"
    "buffer_load_dwordx4 v199, s[60:63], s72 offen offset:1024 sc1 lds\n\t"
    "v_mfma_f32_16x16x32_bf16 v[52:55], v[164:167], a[12:15], v[52:55]\n\t"
    "ds_read_b128 v[148:151], v205 offset:10240\n\t"
    "s_waitcnt lgkmcnt(10)\n\t"
    "v_mfma_f32_16x16x32_bf16 v[60:63], v[168:171], a[4:7], 0\n\t"
    "ds_read_b128 v[152:155], v205 offset:6144\n\t"
    "s_mov_b32 s73, 12288\n\t"
    "buffer_load_dwordx4 a[100:103], v197, s[4:7], s73 offen offset:0 sc1\n\t"
    "s_add_u32 m0, s75, 104448\n\t"
    "v_mfma_f32_16x16x32_bf16 v[60:63], v[172:175], a[12:15], v[60:63]\n\t"
    "ds_read_b128 v[156:159], v205 offset:11264\n\t"
    "buffer_load_dwordx4 v200, s[60:63], s72 offen offset:0 sc1 lds\n\t"
    "s_waitcnt lgkmcnt(10)\n\t"
    "v_mfma_f32_16x16x32_bf16 v[84:87], v[88:91], a[4:7], 0\n\t"
    "ds_read_b128 v[160:163], v205 offset:12288\n\t"
    "v_mfma_f32_16x16x32_bf16 v[84:87], v[92:95], a[12:15], v[84:87]\n\t"
    "ds_read_b128 v[164:167], v205 offset:17408\n\t"
    "s_waitcnt lgkmcnt(10) vmcnt(12)\n\t"
    "v_mfma_f32_16x16x32_bf16 v[36:39], v[96:99], a[20:23], v[36:39]\n\t"
    "ds_read_b128 v[168:171], v205 offset:13312\n\t"
    "buffer_load_dwordx4 v200, s[60:63], s72 offen offset:1024 sc1 lds\n\t"
    "v_mfma_f32_16x16x32_bf16 v[36:39], v[100:103], a[28:31], v[36:39]\n\t"
    "ds_read_b128 v[172:175], v205 offset:18432\n\t"
    "buffer_load_dwordx4 a[108:111], v197, s[4:7], s73 offen offset:1024 sc1\n\t"
    "s_waitcnt lgkmcnt(10)\n\t"
    "v_mfma_f32_16x16x32_bf16 v[44:47], v[104:107], a[20:23], v[44:47]\n\t"
    "ds_read_b128 v[88:91], v205 offset:14336\n\t"
    "s_add_u32 m0, s75, 106496\n\t"
    "v_mfma_f32_16x16x32_bf16 v[44:47], v[108:111], a[28:31], v[44:47]\n\t"
    "ds_read_b128 v[92:95], v205 offset:19456\n\t"
    "buffer_load_dwordx4 v202, s[60:63], s72 offen offset:0 sc1 lds\n\t"
    "s_waitcnt lgkmcnt(10)\n\t"
    "v_mfma_f32_16x16x32_bf16 v[52:55], v[112:115], a[20:23], v[52:55]\n\t"
    "ds_read_b128 v[96:99], v205 offset:15360\n\t"
    "v_mfma_f32_16x16x32_bf16 v[52:55], v[116:119], a[28:31], v[52:55]\n\t"
    "ds_read_b128 v[100:103], v205 offset:20480\n\t"
    "buffer_load_dwordx4 a[116:119], v197, s[4:7], s73 offen offset:2048 sc1\n\t"
    "s_waitcnt lgkmcnt(10)\n\t"
    "v_mfma_f32_16x16x32_bf16 v[60:63], v[144:147], a[20:23], v[60:63]\n\t"
    "ds_read_b128 v[104:107], v205 offset:16384\n\t"
    "v_mfma_f32_16x16x32_bf16 v[60:63], v[148:151], a[28:31], v[60:63]\n\t"
    "ds_read_b128 v[108:111], v205 offset:21504\n\t"
    "s_waitcnt lgkmcnt(10)\n\t"
    "v_mfma_f32_16x16x32_bf16 v[84:87], v[152:155], a[20:23], v[84:87]\n\t"
    "ds_read_b128 v[112:115], v205 offset:22528\n\t"
    "v_mfma_f32_16x16x32_bf16 v[84:87], v[156:159], a[28:31], v[84:87]\n\t"
    "ds_read_b128 v[116:119], v205 offset:27648\n\t"
    "buffer_load_dwordx4 a[124:127], v197, s[4:7], s73 offen offset:3072 sc1\n\t"
    "s_waitcnt lgkmcnt(10) vmcnt(15)\n\t"
    "v_mfma_f32_16x16x32_bf16 v[36:39], v[160:163], a[36:39], v[36:39]\n\t"
    "ds_read_b128 v[144:147], v205 offset:23552\n\t"
    "v_mfma_f32_16x16x32_bf16 v[36:39], v[164:167], a[44:47], v[36:39]\n\t"
    "ds_read_b128 v[148:151], v205 offset:28672\n\t"
    "s_waitcnt lgkmcnt(10)\n\t"
    "v_mfma_f32_16x16x32_bf16 v[44:47], v[168:171], a[36:39], v[44:47]\n\t"
    "ds_read_b128 v[152:155], v205 offset:24576\n\t"
    "v_mfma_f32_16x16x32_bf16 v[44:47], v[172:175], a[44:47], v[44:47]\n\t"
    "ds_read_b128 v[156:159], v205 offset:29696\n\t"
    "s_mov_b32 s73, 16384\n\t"
    "buffer_load_dwordx4 a[132:135], v197, s[4:7], s73 offen offset:0 sc1\n\t"
    "s_waitcnt lgkmcnt(10)\n\t"
    "v_mfma_f32_16x16x32_bf16 v[52:55], v[88:91], a[36:39], v[52:55]\n\t"
    "ds_read_b128 v[160:163], v205 offset:25600\n\t"
    "v_mfma_f32_16x16x32_bf16 v[52:55], v[92:95], a[44:47], v[52:55]\n\t"
    "ds_read_b128 v[164:167], v205 offset:30720\n\t"
    "s_waitcnt lgkmcnt(10)\n\t"
    "v_mfma_f32_16x16x32_bf16 v[60:63], v[96:99], a[36:39], v[60:63]\n\t"
    "ds_read_b128 v[168:171], v205 offset:26624\n\t"
    "v_mfma_f32_16x16x32_bf16 v[60:63], v[100:103], a[44:47], v[60:63]\n\t"
    "ds_read_b128 v[172:175], v205 offset:31744\n\t"
    "s_waitcnt vmcnt(23)\n\t"
    "s_barrier\n\t"
    "s_waitcnt lgkmcnt(10)\n\t"
    "v_mfma_f32_16x16x32_bf16 v[84:87], v[104:107], a[36:39], v[84:87]\n\t"
    "ds_read_b128 v[88:91], v204 offset:16384\n\t"
    "v_mfma_f32_16x16x32_bf16 v[84:87], v[108:111], a[44:47], v[84:87]\n\t"
    "s_waitcnt lgkmcnt(9) vmcnt(14)\n\t"
    "v_mfma_f32_16x16x32_bf16 v[36:39], v[112:115], a[52:55], v[36:39]\n\t"
    "ds_read_b128 v[92:95], v204 offset:21504\n\t"
    "v_mfma_f32_16x16x32_bf16 v[36:39], v[116:119], a[60:63], v[36:39]\n\t"
    "buffer_load_dwordx4 a[140:143], v197, s[4:7], s73 offen offset:1024 sc1\n\t"
    "s_waitcnt lgkmcnt(8)\n\t"
    "v_mfma_f32_16x16x32_bf16 v[44:47], v[144:147], a[52:55], v[44:47]\n\t"
    "ds_read_b128 v[96:99], v204 offset:17408\n\t"
    "v_mfma_f32_16x16x32_bf16 v[44:47], v[148:151], a[60:63], v[44:47]\n\t"
    "s_waitcnt lgkmcnt(7)\n\t"
    "v_mfma_f32_16x16x32_bf16 v[52:55], v[152:155], a[52:55], v[52:55]\n\t"
    "ds_read_b128 v[100:103], v204 offset:22528\n\t"
    "v_mfma_f32_16x16x32_bf16 v[52:55], v[156:159], a[60:63], v[52:55]\n\t"
    "s_waitcnt lgkmcnt(6)\n\t"
    "v_mfma_f32_16x16x32_bf16 v[60:63], v[160:163], a[52:55], v[60:63]\n\t"
    "ds_read_b128 v[104:107], v204 offset:18432\n\t"
    "v_mfma_f32_16x16x32_bf16 v[60:63], v[164:167], a[60:63], v[60:63]\n\t"
    "s_waitcnt lgkmcnt(5)\n\t"
    "v_mfma_f32_16x16x32_bf16 v[84:87], v[168:171], a[52:55], v[84:87]\n\t"
    "ds_read_b128 v[108:111], v204 offset:23552\n\t"
    "v_mfma_f32_16x16x32_bf16 v[84:87], v[172:175], a[60:63], v[84:87]\n\t"
    "s_waitcnt lgkmcnt(4)\n\t"
    "v_mfma_f32_16x16x32_bf16 v[32:35], v[88:91], a[64:67], v[32:35]\n\t"
    "ds_read_b128 v[112:115], v204 offset:19456\n\t"
    "s_add_u32 m0, s75, 122880\n\t"
    "s_add_u32 s72, s71, 147456\n\t"
    "v_mfma_f32_16x16x32_bf16 v[32:35], v[92:95], a[72:75], v[32:35]\n\t"
    "buffer_load_dwordx4 v199, s[60:63], s72 offen offset:0 sc1 lds\n\t"
    "s_waitcnt vmcnt(14)\n\t"
    "v_mfma_f32_16x16x32_bf16 v[36:39], v[88:91], a[68:71], v[36:39]\n\t"
    "ds_read_b128 v[116:119], v204 offset:24576\n\t"
    "v_mfma_f32_16x16x32_bf16 v[36:39], v[92:95], a[76:79], v[36:39]\n\t"
    "buffer_load_dwordx4 a[148:151], v197, s[4:7], s73 offen offset:2048 sc1\n\t"
    "s_waitcnt lgkmcnt(4)\n\t"
    "v_mfma_f32_16x16x32_bf16 v[40:43], v[96:99], a[64:67], v[40:43]\n\t"
    "ds_read_b128 v[144:147], v204 offset:20480\n\t"
    "v_mfma_f32_16x16x32_bf16 v[40:43], v[100:103], a[72:75], v[40:43]\n\t"
    "buffer_load_dwordx4 v199, s[60:63], s72 offen offset:1024 sc1 lds\n\t"
    "v_mfma_f32_16x16x32_bf16 v[44:47], v[96:99], a[68:71], v[44:47]\n\t"
    "ds_read_b128 v[148:151], v204 offset:25600\n\t"
    "v_mfma_f32_16x16x32_bf16 v[44:47], v[100:103], a[76:79], v[44:47]\n\t"
    "s_waitcnt lgkmcnt(4)\n\t"
    "v_mfma_f32_16x16x32_bf16 v[48:51], v[104:107], a[64:67], v[48:51]\n\t"
    "ds_read_b128 v[152:155], v204 offset:26624\n\t"
    "s_add_u32 m0, s75, 124928\n\t"
    "v_mfma_f32_16x16x32_bf16 v[48:51], v[108:111], a[72:75], v[48:51]\n\t"
    "buffer_load_dwordx4 v200, s[60:63], s72 offen offset:0 sc1 lds\n\t"
    "v_mfma_f32_16x16x32_bf16 v[52:55], v[104:107], a[68:71], v[52:55]\n\t"
    "ds_read_b128 v[156:159], v204 offset:31744\n\t"
    "v_mfma_f32_16x16x32_bf16 v[52:55], v[108:111], a[76:79], v[52:55]\n\t"
    "buffer_load_dwordx4 a[156:159], v197, s[4:7], s73 offen offset:3072 sc1\n\t"
    "s_waitcnt lgkmcnt(4)\n\t"
    "v_mfma_f32_16x16x32_bf16 v[56:59], v[112:115], a[64:67], v[56:59]\n\t"
    "ds_read_b128 v[160:163], v204 offset:27648\n\t"
    "v_mfma_f32_16x16x32_bf16 v[56:59], v[116:119], a[72:75], v[56:59]\n\t"
    "buffer_load_dwordx4 v200, s[60:63], s72 offen offset:1024 sc1 lds\n\t"
    "v_mfma_f32_16x16x32_bf16 v[60:63], v[112:115], a[68:71], v[60:63]\n\t"
    "ds_read_b128 v[164:167], v204 offset:32768\n\t"
    "v_mfma_f32_16x16x32_bf16 v[60:63], v[116:119], a[76:79], v[60:63]\n\t"
    "s_waitcnt lgkmcnt(4)\n\t"
    "v_mfma_f32_16x16x32_bf16 v[80:83], v[144:147], a[64:67], v[80:83]\n\t"
    "ds_read_b128 v[168:171], v204 offset:28672\n\t"
    "s_add_u32 m0, s75, 126976\n\t"
    "v_mfma_f32_16x16x32_bf16 v[80:83], v[148:151], a[72:75], v[80:83]\n\t"
    "buffer_load_dwordx4 v202, s[60:63], s72 offen offset:0 sc1 lds\n\t"
    "v_mfma_f32_16x16x32_bf16 v[84:87], v[144:147], a[68:71], v[84:87]\n\t"
    "ds_read_b128 v[172:175], v204 offset:33792\n\t"
    "v_mfma_f32_16x16x32_bf16 v[84:87], v[148:151], a[76:79], v[84:87]\n\t"
    "s_waitcnt lgkmcnt(4)\n\t"
    "v_mfma_f32_16x16x32_bf16 v[32:35], v[152:155], a[80:83], v[32:35]\n\t"
    "ds_read_b128 v[88:91], v204 offset:29696\n\t"
    "v_mfma_f32_16x16x32_bf16 v[32:35], v[156:159], a[88:91], v[32:35]\n\t"
    "s_mov_b32 s73, 20480\n\t"
    "buffer_load_dwordx4 a[164:167], v197, s[4:7], s73 offen offset:0 sc1\n\t"
    "s_waitcnt vmcnt(18)\n\t"
    "v_mfma_f32_16x16x32_bf16 v[36:39], v[152:155], a[84:87], v[36:39]\n\t"
    "ds_read_b128 v[92:95], v204 offset:34816\n\t"
    "v_mfma_f32_16x16x32_bf16 v[36:39], v[156:159], a[92:95], v[36:39]\n\t"
    "s_waitcnt lgkmcnt(4)\n\t"
    "v_mfma_f32_16x16x32_bf16 v[40:43], v[160:163], a[80:83], v[40:43]\n\t"
    "ds_read_b128 v[96:99], v204 offset:30720\n\t"
    "v_mfma_f32_16x16x32_bf16 v[40:43], v[164:167], a[88:91], v[40:43]\n\t"
    "v_mfma_f32_16x16x32_bf16 v[44:47], v[160:163], a[84:87], v[44:47]\n\t"
    "ds_read_b128 v[100:103], v204 offset:35840\n\t"
    "v_mfma_f32_16x16x32_bf16 v[44:47], v[164:167], a[92:95], v[44:47]\n\t"
    "s_waitcnt vmcnt(12)\n\t"
    "s_barrier\n\t"
    "s_waitcnt lgkmcnt(4)\n\t"
    "v_mfma_f32_16x16x32_bf16 v[48:51], v[168:171], a[80:83], v[48:51]\n\t"
    "ds_read_b128 v[104:107], v204 offset:36864\n\t"
    "v_mfma_f32_16x16x32_bf16 v[48:51], v[172:175], a[88:91], v[48:51]\n\t"
    "v_mfma_f32_16x16x32_bf16 v[52:55], v[168:171], a[84:87], v[52:55]\n\t"
    "ds_read_b128 v[108:111], v204 offset:41984\n\t"
    "v_mfma_f32_16x16x32_bf16 v[52:55], v[172:175], a[92:95], v[52:55]\n\t"
    "buffer_load_dwordx4 a[172:175], v197, s[4:7], s73 offen offset:1024 sc1\n\t"
    "s_waitcnt lgkmcnt(4)\n\t"
    "v_mfma_f32_16x16x32_bf16 v[56:59], v[88:91], a[80:83], v[56:59]\n\t"
    "ds_read_b128 v[112:115], v204 offset:37888\n\t"
    "v_mfma_f32_16x16x32_bf16 v[56:59], v[92:95], a[88:91], v[56:59]\n\t"
    "v_mfma_f32_16x16x32_bf16 v[60:63], v[88:91], a[84:87], v[60:63]\n\t"
    "ds_read_b128 v[116:119], v204 offset:43008\n\t"
    "v_mfma_f32_16x16x32_bf16 v[60:63], v[92:95], a[92:95], v[60:63]\n\t"
    "s_waitcnt lgkmcnt(4)\n\t"
    "v_mfma_f32_16x16x32_bf16 v[80:83], v[96:99], a[80:83], v[80:83]\n\t"
    "ds_read_b128 v[144:147], v204 offset:38912\n\t"
    "v_mfma_f32_16x16x32_bf16 v[80:83], v[100:103], a[88:91], v[80:83]\n\t"
    "v_mfma_f32_16x16x32_bf16 v[84:87], v[96:99], a[84:87], v[84:87]\n\t"
    "ds_read_b128 v[148:151], v204 offset:44032\n\t"
    "v_mfma_f32_16x16x32_bf16 v[84:87], v[100:103], a[92:95], v[84:87]\n\t"
    "s_waitcnt lgkmcnt(4)\n\t"
    "v_mfma_f32_16x16x32_bf16 v[32:35], v[104:107], a[96:99], v[32:35]\n\t"
    "ds_read_b128 v[152:155], v204 offset:39936\n\t"
    "s_add_u32 m0, s75, 143360\n\t"
    "s_add_u32 s72, s71, 184320\n\t"
    "v_mfma_f32_16x16x32_bf16 v[32:35], v[108:111], a[104:107], v[32:35]\n\t"
    "buffer_load_dwordx4 v199, s[60:63], s72 offen offset:0 sc1 lds\n\t"
    "s_waitcnt vmcnt(15)\n\t"
    "v_mfma_f32_16x16x32_bf16 v[36:39], v[104:107], a[100:103], v[36:39]\n\t"
    "ds_read_b128 v[156:159], v204 offset:45056\n\t"
    "v_mfma_f32_16x16x32_bf16 v[36:39], v[108:111], a[108:111], v[36:39]\n\t"
    "buffer_load_dwordx4 a[180:183], v197, s[4:7], s73 offen offset:2048 sc1\n\t"
    "s_waitcnt lgkmcnt(4)\n\t"
    "v_mfma_f32_16x16x32_bf16 v[40:43], v[112:115], a[96:99], v[40:43]\n\t"
    "ds_read_b128 v[160:163], v204 offset:40960\n\t"
    "v_mfma_f32_16x16x32_bf16 v[40:43], v[116:119], a[104:107], v[40:43]\n\t"
    "buffer_load_dwordx4 v199, s[60:63], s72 offen offset:1024 sc1 lds\n\t"
    "v_mfma_f32_16x16x32_bf16 v[44:47], v[112:115], a[100:103], v[44:47]\n\t"
    "ds_read_b128 v[164:167], v204 offset:46080\n\t"
    "v_mfma_f32_16x16x32_bf16 v[44:47], v[116:119], a[108:111], v[44:47]\n\t"
    "s_waitcnt lgkmcnt(4)\n\t"
    "v_mfma_f32_16x16x32_bf16 v[48:51], v[144:147], a[96:99], v[48:51]\n\t"
    "ds_read_b128 v[168:171], v204 offset:47104\n\t"
    "s_add_u32 m0, s75, 145408\n\t"
    "v_mfma_f32_16x16x32_bf16 v[48:51], v[148:151], a[104:107], v[48:51]\n\t"
    "buffer_load_dwordx4 v200, s[60:63], s72 offen offset:0 sc1 lds\n\t"
    "v_mfma_f32_16x16x32_bf16 v[52:55], v[144:147], a[100:103], v[52:55]\n\t"
    "ds_read_b128 v[172:175], v204 offset:52224\n\t"
    "v_mfma_f32_16x16x32_bf16 v[52:55], v[148:151], a[108:111], v[52:55]\n\t"
    "buffer_load_dwordx4 a[188:191], v197, s[4:7], s73 offen offset:3072 sc1\n\t"
    "s_waitcnt lgkmcnt(4)\n\t"
    "v_mfma_f32_16x16x32_bf16 v[56:59], v[152:155], a[96:99], v[56:59]\n\t"
    "ds_read_b128 v[88:91], v204 offset:48128\n\t"
    "v_mfma_f32_16x16x32_bf16 v[56:59], v[156:159], a[104:107], v[56:59]\n\t"
    "buffer_load_dwordx4 v200, s[60:63], s72 offen offset:1024 sc1 lds\n\t"
    "v_mfma_f32_16x16x32_bf16 v[60:63], v[152:155], a[100:103], v[60:63]\n\t"
    "ds_read_b128 v[92:95], v204 offset:53248\n\t"
    "v_mfma_f32_16x16x32_bf16 v[60:63], v[156:159], a[108:111], v[60:63]\n\t"
    "s_waitcnt lgkmcnt(4)\n\t"
    "v_mfma_f32_16x16x32_bf16 v[80:83], v[160:163], a[96:99], v[80:83]\n\t"
    "ds_read_b128 v[96:99], v204 offset:49152\n\t"
    "s_add_u32 m0, s75, 147456\n\t"
    "v_mfma_f32_16x16x32_bf16 v[80:83], v[164:167], a[104:107], v[80:83]\n\t"
    "buffer_load_dwordx4 v202, s[60:63], s72 offen offset:0 sc1 lds\n\t"
    "v_mfma_f32_16x16x32_bf16 v[84:87], v[160:163], a[100:103], v[84:87]\n\t"
    "ds_read_b128 v[100:103], v204 offset:54272\n\t"
    "v_mfma_f32_16x16x32_bf16 v[84:87], v[164:167], a[108:111], v[84:87]\n\t"
    "s_waitcnt lgkmcnt(4)\n\t"
    "v_mfma_f32_16x16x32_bf16 v[32:35], v[168:171], a[112:115], v[32:35]\n\t"
    "ds_read_b128 v[104:107], v204 offset:50176\n\t"
    "v_mfma_f32_16x16x32_bf16 v[32:35], v[172:175], a[120:123], v[32:35]\n\t"
    "s_mov_b32 s73, 24576\n\t"
    "buffer_load_dwordx4 a[196:199], v197, s[4:7], s73 offen offset:0 sc1\n\t"
    "s_waitcnt vmcnt(19)\n\t"
    "v_mfma_f32_16x16x32_bf16 v[36:39], v[168:171], a[116:119], v[36:39]\n\t"
    "ds_read_b128 v[108:111], v204 offset:55296\n\t"
    "v_mfma_f32_16x16x32_bf16 v[36:39], v[172:175], a[124:127], v[36:39]\n\t"
    "s_waitcnt lgkmcnt(4)\n\t"
    "v_mfma_f32_16x16x32_bf16 v[40:43], v[88:91], a[112:115], v[40:43]\n\t"
    "ds_read_b128 v[112:115], v204 offset:51200\n\t"
    "v_mfma_f32_16x16x32_bf16 v[40:43], v[92:95], a[120:123], v[40:43]\n\t"
    "v_mfma_f32_16x16x32_bf16 v[44:47], v[88:91], a[116:119], v[44:47]\n\t"
    "ds_read_b128 v[116:119], v204 offset:56320\n\t"
    "v_mfma_f32_16x16x32_bf16 v[44:47], v[92:95], a[124:127], v[44:47]\n\t"
    "s_waitcnt vmcnt(10)\n\t"
    "s_barrier\n\t"
    "s_waitcnt lgkmcnt(4)\n\t"
    "v_mfma_f32_16x16x32_bf16 v[48:51], v[96:99], a[112:115], v[48:51]\n\t"
    "ds_read_b128 v[144:147], v204 offset:57344\n\t"
    "v_mfma_f32_16x16x32_bf16 v[48:51], v[100:103], a[120:123], v[48:51]\n\t"
    "v_mfma_f32_16x16x32_bf16 v[52:55], v[96:99], a[116:119], v[52:55]\n\t"
    "ds_read_b128 v[148:151], v204 offset:62464\n\t"
    "v_mfma_f32_16x16x32_bf16 v[52:55], v[100:103], a[124:127], v[52:55]\n\t"
    "buffer_load_dwordx4 a[204:207], v197, s[4:7], s73 offen offset:1024 sc1\n\t"
    "s_waitcnt lgkmcnt(4)\n\t"
    "v_mfma_f32_16x16x32_bf16 v[56:59], v[104:107], a[112:115], v[56:59]\n\t"
    "ds_read_b128 v[152:155], v204 offset:58368\n\t"
    "v_mfma_f32_16x16x32_bf16 v[56:59], v[108:111], a[120:123], v[56:59]\n\t"
    "v_mfma_f32_16x16x32_bf16 v[60:63], v[104:107], a[116:119], v[60:63]\n\t"
    "ds_read_b128 v[156:159], v204 offset:63488\n\t"
    "v_mfma_f32_16x16x32_bf16 v[60:63], v[108:111], a[124:127], v[60:63]\n\t"
    "s_waitcnt lgkmcnt(4)\n\t"
    "v_mfma_f32_16x16x32_bf16 v[80:83], v[112:115], a[112:115], v[80:83]\n\t"
    "ds_read_b128 v[160:163], v204 offset:59392\n\t"
    "v_mfma_f32_16x16x32_bf16 v[80:83], v[116:119], a[120:123], v[80:83]\n\t"
    "v_mfma_f32_16x16x32_bf16 v[84:87], v[112:115], a[116:119], v[84:87]\n\t"
    "ds_read_b128 v[164:167], v204 offset:64512\n\t"
    "v_mfma_f32_16x16x32_bf16 v[84:87], v[116:119], a[124:127], v[84:87]\n\t"
    "s_waitcnt lgkmcnt(4)\n\t"
    "v_mfma_f32_16x16x32_bf16 v[32:35], v[144:147], a[128:131], v[32:35]\n\t"
    "ds_read_b128 v[168:171], v204 offset:60416\n\t"
    "s_add_u32 m0, s75, 81920\n\t"
    "s_add_u32 s72, s71, 221184\n\t"
    "v_mfma_f32_16x16x32_bf16 v[32:35], v[148:151], a[136:139], v[32:35]\n\t"
    "buffer_load_dwordx4 v199, s[60:63], s72 offen offset:0 sc1 lds\n\t"
    "s_waitcnt vmcnt(19)\n\t"
    "v_mfma_f32_16x16x32_bf16 v[36:39], v[144:147], a[132:135], v[36:39]\n\t"
    "ds_read_b128 v[172:175], v205 offset:0\n\t"
    "v_mfma_f32_16x16x32_bf16 v[36:39], v[148:151], a[140:143], v[36:39]\n\t"
    "buffer_load_dwordx4 a[212:215], v197, s[4:7], s73 offen offset:2048 sc1\n\t"
    "s_waitcnt lgkmcnt(4)\n\t"
    "v_mfma_f32_16x16x32_bf16 v[40:43], v[152:155], a[128:131], v[40:43]\n\t"
    "ds_read_b128 v[88:91], v204 offset:61440\n\t"
    "v_mfma_f32_16x16x32_bf16 v[40:43], v[156:159], a[136:139], v[40:43]\n\t"
    "buffer_load_dwordx4 v199, s[60:63], s72 offen offset:1024 sc1 lds\n\t"
    "v_mfma_f32_16x16x32_bf16 v[44:47], v[152:155], a[132:135], v[44:47]\n\t"
    "ds_read_b128 v[92:95], v205 offset:1024\n\t"
    "v_mfma_f32_16x16x32_bf16 v[44:47], v[156:159], a[140:143], v[44:47]\n\t"
    "s_waitcnt lgkmcnt(4)\n\t"
    "v_mfma_f32_16x16x32_bf16 v[48:51], v[160:163], a[128:131], v[48:51]\n\t"
    "ds_read_b128 v[96:99], v205 offset:2048\n\t"
    "s_add_u32 m0, s75, 83968\n\t"
    "v_mfma_f32_16x16x32_bf16 v[48:51], v[164:167], a[136:139], v[48:51]\n\t"
    "buffer_load_dwordx4 v200, s[60:63], s72 offen offset:0 sc1 lds\n\t"
    "v_mfma_f32_16x16x32_bf16 v[52:55], v[160:163], a[132:135], v[52:55]\n\t"
    "ds_read_b128 v[100:103], v205 offset:7168\n\t"
    "v_mfma_f32_16x16x32_bf16 v[52:55], v[164:167], a[140:143], v[52:55]\n\t"
    "buffer_load_dwordx4 a[220:223], v197, s[4:7], s73 offen offset:3072 sc1\n\t"
    "s_waitcnt lgkmcnt(4)\n\t"
    "v_mfma_f32_16x16x32_bf16 v[56:59], v[168:171], a[128:131], v[56:59]\n\t"
    "ds_read_b128 v[104:107], v205 offset:3072\n\t"
    "v_mfma_f32_16x16x32_bf16 v[56:59], v[172:175], a[136:139], v[56:59]\n\t"
    "buffer_load_dwordx4 v200, s[60:63], s72 offen offset:1024 sc1 lds\n\t"
    "v_mfma_f32_16x16x32_bf16 v[60:63], v[168:171], a[132:135], v[60:63]\n\t"
    "ds_read_b128 v[108:111], v205 offset:8192\n\t"
    "v_mfma_f32_16x16x32_bf16 v[60:63], v[172:175], a[140:143], v[60:63]\n\t"
    "s_waitcnt lgkmcnt(4)\n\t"
    "v_mfma_f32_16x16x32_bf16 v[80:83], v[88:91], a[128:131], v[80:83]\n\t"
    "ds_read_b128 v[112:115], v205 offset:4096\n\t"
    "s_add_u32 m0, s75, 86016\n\t"
    "v_mfma_f32_16x16x32_bf16 v[80:83], v[92:95], a[136:139], v[80:83]\n\t"
    "buffer_load_dwordx4 v202, s[60:63], s72 offen offset:0 sc1 lds\n\t"
    "v_mfma_f32_16x16x32_bf16 v[84:87], v[88:91], a[132:135], v[84:87]\n\t"
    "ds_read_b128 v[116:119], v205 offset:9216\n\t"
    "v_mfma_f32_16x16x32_bf16 v[84:87], v[92:95], a[140:143], v[84:87]\n\t"
    "s_waitcnt lgkmcnt(4)\n\t"
    "v_mfma_f32_16x16x32_bf16 v[32:35], v[96:99], a[144:147], v[32:35]\n\t"
    "ds_read_b128 v[144:147], v205 offset:5120\n\t"
    "v_mfma_f32_16x16x32_bf16 v[32:35], v[100:103], a[152:155], v[32:35]\n\t"
    "s_mov_b32 s73, 28672\n\t"
    "buffer_load_dwordx4 a[228:231], v197, s[4:7], s73 offen offset:0 sc1\n\t"
    "s_waitcnt vmcnt(21)\n\t"
    "v_mfma_f32_16x16x32_bf16 v[36:39], v[96:99], a[148:151], v[36:39]\n\t"
    "ds_read_b128 v[148:151], v205 offset:10240\n\t"
    "v_mfma_f32_16x16x32_bf16 v[36:39], v[100:103], a[156:159], v[36:39]\n\t"
    "s_waitcnt lgkmcnt(4)\n\t"
    "v_mfma_f32_16x16x32_bf16 v[40:43], v[104:107], a[144:147], v[40:43]\n\t"
    "ds_read_b128 v[152:155], v205 offset:6144\n\t"
    "v_mfma_f32_16x16x32_bf16 v[40:43], v[108:111], a[152:155], v[40:43]\n\t"
    "v_mfma_f32_16x16x32_bf16 v[44:47], v[104:107], a[148:151], v[44:47]\n\t"
    "ds_read_b128 v[156:159], v205 offset:11264\n\t"
    "v_mfma_f32_16x16x32_bf16 v[44:47], v[108:111], a[156:159], v[44:47]\n\t"
    "s_waitcnt vmcnt(10)\n\t"
    "s_barrier\n\t"
    "s_waitcnt lgkmcnt(4)\n\t"
    "v_mfma_f32_16x16x32_bf16 v[48:51], v[112:115], a[144:147], v[48:51]\n\t"
    "ds_read_b128 v[160:163], v205 offset:12288\n\t"
    "v_mfma_f32_16x16x32_bf16 v[48:51], v[116:119], a[152:155], v[48:51]\n\t"
    "v_mfma_f32_16x16x32_bf16 v[52:55], v[112:115], a[148:151], v[52:55]\n\t"
    "ds_read_b128 v[164:167], v205 offset:17408\n\t"
    "v_mfma_f32_16x16x32_bf16 v[52:55], v[116:119], a[156:159], v[52:55]\n\t"
    "buffer_load_dwordx4 a[236:239], v197, s[4:7], s73 offen offset:1024 sc1\n\t"
    "s_waitcnt lgkmcnt(4)\n\t"
    "v_mfma_f32_16x16x32_bf16 v[56:59], v[144:147], a[144:147], v[56:59]\n\t"
    "ds_read_b128 v[168:171], v205 offset:13312\n\t"
    "v_mfma_f32_16x16x32_bf16 v[56:59], v[148:151], a[152:155], v[56:59]\n\t"
    "v_mfma_f32_16x16x32_bf16 v[60:63], v[144:147], a[148:151], v[60:63]\n\t"
    "ds_read_b128 v[172:175], v205 offset:18432\n\t"
    "v_mfma_f32_16x16x32_bf16 v[60:63], v[148:151], a[156:159], v[60:63]\n\t"
    "s_waitcnt lgkmcnt(4)\n\t"
    "v_mfma_f32_16x16x32_bf16 v[80:83], v[152:155], a[144:147], v[80:83]\n\t"
    "ds_read_b128 v[88:91], v205 offset:14336\n\t"
    "v_mfma_f32_16x16x32_bf16 v[80:83], v[156:159], a[152:155], v[80:83]\n\t"
    "v_mfma_f32_16x16x32_bf16 v[84:87], v[152:155], a[148:151], v[84:87]\n\t"
    "ds_read_b128 v[92:95], v205 offset:19456\n\t"
    "v_mfma_f32_16x16x32_bf16 v[84:87], v[156:159], a[156:159], v[84:87]\n\t"
    "s_waitcnt lgkmcnt(4)\n\t"
    "v_mfma_f32_16x16x32_bf16 v[32:35], v[160:163], a[160:163], v[32:35]\n\t"
    "ds_read_b128 v[96:99], v205 offset:15360\n\t"
    "s_add_u32 m0, s75, 102400\n\t"
    "s_add_u32 s72, s71, 258048\n\t"
    "v_mfma_f32_16x16x32_bf16 v[32:35], v[164:167], a[168:171], v[32:35]\n\t"
    "buffer_load_dwordx4 v199, s[60:63], s72 offen offset:0 sc1 lds\n\t"
    "s_waitcnt vmcnt(19)\n\t"
    "v_mfma_f32_16x16x32_bf16 v[36:39], v[160:163], a[164:167], v[36:39]\n\t"
    "ds_read_b128 v[100:103], v205 offset:20480\n\t"
    "v_mfma_f32_16x16x32_bf16 v[36:39], v[164:167], a[172:175], v[36:39]\n\t"
    "buffer_load_dwordx4 a[244:247], v197, s[4:7], s73 offen offset:2048 sc1\n\t"
    "s_waitcnt lgkmcnt(4)\n\t"
    "v_mfma_f32_16x16x32_bf16 v[40:43], v[168:171], a[160:163], v[40:43]\n\t"
    "ds_read_b128 v[104:107], v205 offset:16384\n\t"
    "v_mfma_f32_16x16x32_bf16 v[40:43], v[172:175], a[168:171], v[40:43]\n\t"
    "buffer_load_dwordx4 v199, s[60:63], s72 offen offset:1024 sc1 lds\n\t"
    "v_mfma_f32_16x16x32_bf16 v[44:47], v[168:171], a[164:167], v[44:47]\n\t"
    "ds_read_b128 v[108:111], v205 offset:21504\n\t"
    "v_mfma_f32_16x16x32_bf16 v[44:47], v[172:175], a[172:175], v[44:47]\n\t"
    "s_waitcnt lgkmcnt(4)\n\t"
    "v_mfma_f32_16x16x32_bf16 v[48:51], v[88:91], a[160:163], v[48:51]\n\t"
    "ds_read_b128 v[112:115], v205 offset:22528\n\t"
    "s_add_u32 m0, s75, 104448\n\t"
    "v_mfma_f32_16x16x32_bf16 v[48:51], v[92:95], a[168:171], v[48:51]\n\t"
    "buffer_load_dwordx4 v200, s[60:63], s72 offen offset:0 sc1 lds\n\t"
    "v_mfma_f32_16x16x32_bf16 v[52:55], v[88:91], a[164:167], v[52:55]\n\t"
    "ds_read_b128 v[116:119], v205 offset:27648\n\t"
    "v_mfma_f32_16x16x32_bf16 v[52:55], v[92:95], a[172:175], v[52:55]\n\t"
    "buffer_load_dwordx4 a[252:255], v197, s[4:7], s73 offen offset:3072 sc1\n\t"
    "s_waitcnt lgkmcnt(4)\n\t"
    "v_mfma_f32_16x16x32_bf16 v[56:59], v[96:99], a[160:163], v[56:59]\n\t"
    "ds_read_b128 v[144:147], v205 offset:23552\n\t"
    "v_mfma_f32_16x16x32_bf16 v[56:59], v[100:103], a[168:171], v[56:59]\n\t"
    "buffer_load_dwordx4 v200, s[60:63], s72 offen offset:1024 sc1 lds\n\t"
    "v_mfma_f32_16x16x32_bf16 v[60:63], v[96:99], a[164:167], v[60:63]\n\t"
    "ds_read_b128 v[148:151], v205 offset:28672\n\t"
    "v_mfma_f32_16x16x32_bf16 v[60:63], v[100:103], a[172:175], v[60:63]\n\t"
    "s_waitcnt lgkmcnt(4)\n\t"
    "v_mfma_f32_16x16x32_bf16 v[80:83], v[104:107], a[160:163], v[80:83]\n\t"
    "ds_read_b128 v[152:155], v205 offset:24576\n\t"
    "s_add_u32 m0, s75, 106496\n\t"
    "v_mfma_f32_16x16x32_bf16 v[80:83], v[108:111], a[168:171], v[80:83]\n\t"
    "buffer_load_dwordx4 v202, s[60:63], s72 offen offset:0 sc1 lds\n\t"
    "v_mfma_f32_16x16x32_bf16 v[84:87], v[104:107], a[164:167], v[84:87]\n\t"
    "ds_read_b128 v[156:159], v205 offset:29696\n\t"
    "v_mfma_f32_16x16x32_bf16 v[84:87], v[108:111], a[172:175], v[84:87]\n\t"
    "s_waitcnt lgkmcnt(4)\n\t"
    "v_mfma_f32_16x16x32_bf16 v[32:35], v[112:115], a[176:179], v[32:35]\n\t"
    "ds_read_b128 v[160:163], v205 offset:25600\n\t"
    "v_mfma_f32_16x16x32_bf16 v[32:35], v[116:119], a[184:187], v[32:35]\n\t"
    "s_mov_b32 s73, 32768\n\t"
    "buffer_load_dwordx4 v[228:231], v197, s[4:7], s73 offen offset:0 sc1\n\t"
    "s_waitcnt vmcnt(21)\n\t"
    "v_mfma_f32_16x16x32_bf16 v[36:39], v[112:115], a[180:183], v[36:39]\n\t"
    "ds_read_b128 v[164:167], v205 offset:30720\n\t"
    "v_mfma_f32_16x16x32_bf16 v[36:39], v[116:119], a[188:191], v[36:39]\n\t"
    "s_waitcnt lgkmcnt(4)\n\t"
    "v_mfma_f32_16x16x32_bf16 v[40:43], v[144:147], a[176:179], v[40:43]\n\t"
    "ds_read_b128 v[168:171], v205 offset:26624\n\t"
    "v_mfma_f32_16x16x32_bf16 v[40:43], v[148:151], a[184:187], v[40:43]\n\t"
    "v_mfma_f32_16x16x32_bf16 v[44:47], v[144:147], a[180:183], v[44:47]\n\t"
    "ds_read_b128 v[172:175], v205 offset:31744\n\t"
    "v_mfma_f32_16x16x32_bf16 v[44:47], v[148:151], a[188:191], v[44:47]\n\t"
    "s_waitcnt vmcnt(10)\n\t"
    "s_barrier\n\t"
    "s_waitcnt lgkmcnt(4)\n\t"
    "v_mfma_f32_16x16x32_bf16 v[48:51], v[152:155], a[176:179], v[48:51]\n\t"
    "ds_read_b128 v[88:91], v204 offset:16384\n\t"
    "v_mfma_f32_16x16x32_bf16 v[48:51], v[156:159], a[184:187], v[48:51]\n\t"
    "v_mfma_f32_16x16x32_bf16 v[52:55], v[152:155], a[180:183], v[52:55]\n\t"
    "ds_read_b128 v[92:95], v204 offset:21504\n\t"
    "v_mfma_f32_16x16x32_bf16 v[52:55], v[156:159], a[188:191], v[52:55]\n\t"
    "buffer_load_dwordx4 v[236:239], v197, s[4:7], s73 offen offset:1024 sc1\n\t"
    "s_waitcnt lgkmcnt(4)\n\t"
    "v_mfma_f32_16x16x32_bf16 v[56:59], v[160:163], a[176:179], v[56:59]\n\t"
    "ds_read_b128 v[96:99], v204 offset:17408\n\t"
    "v_mfma_f32_16x16x32_bf16 v[56:59], v[164:167], a[184:187], v[56:59]\n\t"
    "v_mfma_f32_16x16x32_bf16 v[60:63], v[160:163], a[180:183], v[60:63]\n\t"
    "ds_read_b128 v[100:103], v204 offset:22528\n\t"
    "v_mfma_f32_16x16x32_bf16 v[60:63], v[164:167], a[188:191], v[60:63]\n\t"
    "s_waitcnt lgkmcnt(4)\n\t"
    "v_mfma_f32_16x16x32_bf16 v[80:83], v[168:171], a[176:179], v[80:83]\n\t"
    "ds_read_b128 v[104:107], v204 offset:18432\n\t"
    "v_mfma_f32_16x16x32_bf16 v[80:83], v[172:175], a[184:187], v[80:83]\n\t"
    "v_mfma_f32_16x16x32_bf16 v[84:87], v[168:171], a[180:183], v[84:87]\n\t"
    "ds_read_b128 v[108:111], v204 offset:23552\n\t"
    "v_mfma_f32_16x16x32_bf16 v[84:87], v[172:175], a[188:191], v[84:87]\n\t"
    "s_waitcnt lgkmcnt(4)\n\t"
    "v_mfma_f32_16x16x32_bf16 v[32:35], v[88:91], a[192:195], v[32:35]\n\t"
    "ds_read_b128 v[112:115], v204 offset:19456\n\t"
    "s_add_u32 m0, s75, 122880\n\t"
    "s_add_u32 s72, s71, 294912\n\t"
    "v_mfma_f32_16x16x32_bf16 v[32:35], v[92:95], a[200:203], v[32:35]\n\t"
    "buffer_load_dwordx4 v199, s[60:63], s72 offen offset:0 sc1 lds\n\t"
    "s_waitcnt vmcnt(19)\n\t"
    "v_mfma_f32_16x16x32_bf16 v[36:39], v[88:91], a[196:199], v[36:39]\n\t"
    "ds_read_b128 v[116:119], v204 offset:24576\n\t"
    "v_mfma_f32_16x16x32_bf16 v[36:39], v[92:95], a[204:207], v[36:39]\n\t"
    "buffer_load_dwordx4 v[244:247], v197, s[4:7], s73 offen offset:2048 sc1\n\t"
    "s_waitcnt lgkmcnt(4)\n\t"
    "v_mfma_f32_16x16x32_bf16 v[40:43], v[96:99], a[192:195], v[40:43]\n\t"
    "ds_read_b128 v[144:147], v204 offset:20480\n\t"
    "v_mfma_f32_16x16x32_bf16 v[40:43], v[100:103], a[200:203], v[40:43]\n\t"
    "buffer_load_dwordx4 v199, s[60:63], s72 offen offset:1024 sc1 lds\n\t"
    "v_mfma_f32_16x16x32_bf16 v[44:47], v[96:99], a[196:199], v[44:47]\n\t"
    "ds_read_b128 v[148:151], v204 offset:25600\n\t"
    "v_mfma_f32_16x16x32_bf16 v[44:47], v[100:103], a[204:207], v[44:47]\n\t"
    "s_xor_b32 s28, s68, 3\n\t"
    "s_mul_i32 s28, s28, 40960\n\t"
    "s_add_u32 s28, s28, 4096\n\t"
    "v_cvt_pk_bf16_f32 v2, v64, v65\n\t"
    "s_waitcnt lgkmcnt(4)\n\t"
    "v_mfma_f32_16x16x32_bf16 v[48:51], v[104:107], a[192:195], v[48:51]\n\t"
    "ds_read_b128 v[152:155], v204 offset:26624\n\t"
    "s_add_u32 m0, s75, 124928\n\t"
    "v_mfma_f32_16x16x32_bf16 v[48:51], v[108:111], a[200:203], v[48:51]\n\t"
    "buffer_load_dwordx4 v200, s[60:63], s72 offen offset:0 sc1 lds\n\t"
    "v_mfma_f32_16x16x32_bf16 v[52:55], v[104:107], a[196:199], v[52:55]\n\t"
    "ds_read_b128 v[156:159], v204 offset:31744\n\t"
    "v_mfma_f32_16x16x32_bf16 v[52:55], v[108:111], a[204:207], v[52:55]\n\t"
    "buffer_load_dwordx4 v[252:255], v197, s[4:7], s73 offen offset:3072 sc1\n\t"
    "s_waitcnt lgkmcnt(4)\n\t"
    "v_mfma_f32_16x16x32_bf16 v[56:59], v[112:115], a[192:195], v[56:59]\n\t"
    "ds_read_b128 v[160:163], v204 offset:27648\n\t"
    "v_mfma_f32_16x16x32_bf16 v[56:59], v[116:119], a[200:203], v[56:59]\n\t"
    "buffer_load_dwordx4 v200, s[60:63], s72 offen offset:1024 sc1 lds\n\t"
    "v_mfma_f32_16x16x32_bf16 v[60:63], v[112:115], a[196:199], v[60:63]\n\t"
    "ds_read_b128 v[164:167], v204 offset:32768\n\t"
    "v_mfma_f32_16x16x32_bf16 v[60:63], v[116:119], a[204:207], v[60:63]\n\t"
    "v_cvt_pk_bf16_f32 v3, v66, v67\n\t"
    "s_waitcnt lgkmcnt(4)\n\t"
    "v_mfma_f32_16x16x32_bf16 v[80:83], v[144:147], a[192:195], v[80:83]\n\t"
    "ds_read_b128 v[168:171], v204 offset:28672\n\t"
    "s_add_u32 m0, s75, 126976\n\t"
    "v_mfma_f32_16x16x32_bf16 v[80:83], v[148:151], a[200:203], v[80:83]\n\t"
    "buffer_load_dwordx4 v202, s[60:63], s72 offen offset:0 sc1 lds\n\t"
    "v_mfma_f32_16x16x32_bf16 v[84:87], v[144:147], a[196:199], v[84:87]\n\t"
    "ds_read_b128 v[172:175], v204 offset:33792\n\t"
    "v_mfma_f32_16x16x32_bf16 v[84:87], v[148:151], a[204:207], v[84:87]\n\t"
    "v_cvt_pk_bf16_f32 v4, v72, v73\n\t"
    "s_waitcnt lgkmcnt(4)\n\t"
    "v_mfma_f32_16x16x32_bf16 v[32:35], v[152:155], a[208:211], v[32:35]\n\t"
    "ds_read_b128 v[88:91], v204 offset:29696\n\t"
    "v_mfma_f32_16x16x32_bf16 v[32:35], v[156:159], a[216:219], v[32:35]\n\t"
    "v_cvt_pk_bf16_f32 v5, v74, v75\n\t"
    "s_waitcnt vmcnt(20)\n\t"
    "v_mfma_f32_16x16x32_bf16 v[36:39], v[152:155], a[212:215], v[36:39]\n\t"
    "ds_read_b128 v[92:95], v204 offset:34816\n\t"
    "v_mfma_f32_16x16x32_bf16 v[36:39], v[156:159], a[220:223], v[36:39]\n\t"
    "buffer_store_dwordx4 v[2:5], v15, s[56:59], s28 offen offset:0 sc1\n\t"
    "s_waitcnt lgkmcnt(4)\n\t"
    "v_mfma_f32_16x16x32_bf16 v[40:43], v[160:163], a[208:211], v[40:43]\n\t"
    "ds_read_b128 v[96:99], v204 offset:30720\n\t"
    "v_mfma_f32_16x16x32_bf16 v[40:43], v[164:167], a[216:219], v[40:43]\n\t"
    "v_mfma_f32_16x16x32_bf16 v[44:47], v[160:163], a[212:215], v[44:47]\n\t"
    "ds_read_b128 v[100:103], v204 offset:35840\n\t"
    "v_mfma_f32_16x16x32_bf16 v[44:47], v[164:167], a[220:223], v[44:47]\n\t"
    "s_waitcnt vmcnt(10)\n\t"
    "s_barrier\n\t"
    "s_waitcnt lgkmcnt(4)\n\t"
    "v_mfma_f32_16x16x32_bf16 v[48:51], v[168:171], a[208:211], v[48:51]\n\t"
    "ds_read_b128 v[104:107], v204 offset:36864\n\t"
    "v_mfma_f32_16x16x32_bf16 v[48:51], v[172:175], a[216:219], v[48:51]\n\t"
    "v_mfma_f32_16x16x32_bf16 v[52:55], v[168:171], a[212:215], v[52:55]\n\t"
    "ds_read_b128 v[108:111], v204 offset:41984\n\t"
    "v_mfma_f32_16x16x32_bf16 v[52:55], v[172:175], a[220:223], v[52:55]\n\t"
    "s_waitcnt lgkmcnt(4)\n\t"
    "v_mfma_f32_16x16x32_bf16 v[56:59], v[88:91], a[208:211], v[56:59]\n\t"
    "ds_read_b128 v[112:115], v204 offset:37888\n\t"
    "v_mfma_f32_16x16x32_bf16 v[56:59], v[92:95], a[216:219], v[56:59]\n\t"
    "v_mfma_f32_16x16x32_bf16 v[60:63], v[88:91], a[212:215], v[60:63]\n\t"
    "ds_read_b128 v[116:119], v204 offset:43008\n\t"
    "v_mfma_f32_16x16x32_bf16 v[60:63], v[92:95], a[220:223], v[60:63]\n\t"
    "s_waitcnt lgkmcnt(4)\n\t"
    "v_mfma_f32_16x16x32_bf16 v[80:83], v[96:99], a[208:211], v[80:83]\n\t"
    "ds_read_b128 v[144:147], v204 offset:38912\n\t"
    "v_mfma_f32_16x16x32_bf16 v[80:83], v[100:103], a[216:219], v[80:83]\n\t"
    "v_mfma_f32_16x16x32_bf16 v[84:87], v[96:99], a[212:215], v[84:87]\n\t"
    "ds_read_b128 v[148:151], v204 offset:44032\n\t"
    "v_mfma_f32_16x16x32_bf16 v[84:87], v[100:103], a[220:223], v[84:87]\n\t"
    "s_waitcnt lgkmcnt(4)\n\t"
    "v_mfma_f32_16x16x32_bf16 v[32:35], v[104:107], a[224:227], v[32:35]\n\t"
    "ds_read_b128 v[152:155], v204 offset:39936\n\t"
    "s_add_u32 m0, s74, 143360\n\t"
    "s_add_u32 s72, s71, 0\n\t"
    "v_mfma_f32_16x16x32_bf16 v[32:35], v[108:111], a[232:235], v[32:35]\n\t"
    "buffer_load_dwordx4 v198, s[60:63], s72 offen offset:0 sc1 lds\n\t"
    "s_waitcnt vmcnt(18)\n\t"
    "v_mfma_f32_16x16x32_bf16 v[36:39], v[104:107], a[228:231], v[36:39]\n\t"
    "ds_read_b128 v[156:159], v204 offset:45056\n\t"
    "v_mfma_f32_16x16x32_bf16 v[36:39], v[108:111], a[236:239], v[36:39]\n\t"
    "s_waitcnt lgkmcnt(4)\n\t"
    "v_mfma_f32_16x16x32_bf16 v[40:43], v[112:115], a[224:227], v[40:43]\n\t"
    "ds_read_b128 v[160:163], v204 offset:40960\n\t"
    "v_mfma_f32_16x16x32_bf16 v[40:43], v[116:119], a[232:235], v[40:43]\n\t"
    "buffer_load_dwordx4 v198, s[60:63], s72 offen offset:1024 sc1 lds\n\t"
    "v_mfma_f32_16x16x32_bf16 v[44:47], v[112:115], a[228:231], v[44:47]\n\t"
    "ds_read_b128 v[164:167], v204 offset:46080\n\t"
    "v_mfma_f32_16x16x32_bf16 v[44:47], v[116:119], a[236:239], v[44:47]\n\t"
    "s_waitcnt lgkmcnt(4)\n\t"
    "v_mfma_f32_16x16x32_bf16 v[48:51], v[144:147], a[224:227], v[48:51]\n\t"
    "ds_read_b128 v[168:171], v204 offset:47104\n\t"
    "s_add_u32 m0, s74, 145408\n\t"
    "s_add_u32 s72, s71, 36864\n\t"
    "v_mfma_f32_16x16x32_bf16 v[48:51], v[148:151], a[232:235], v[48:51]\n\t"
    "buffer_load_dwordx4 v198, s[60:63], s72 offen offset:0 sc1 lds\n\t"
    "v_mfma_f32_16x16x32_bf16 v[52:55], v[144:147], a[228:231], v[52:55]\n\t"
    "ds_read_b128 v[172:175], v204 offset:52224\n\t"
    "v_mfma_f32_16x16x32_bf16 v[52:55], v[148:151], a[236:239], v[52:55]\n\t"
    "s_waitcnt lgkmcnt(4)\n\t"
    "v_mfma_f32_16x16x32_bf16 v[56:59], v[152:155], a[224:227], v[56:59]\n\t"
    "ds_read_b128 v[88:91], v204 offset:48128\n\t"
    "v_mfma_f32_16x16x32_bf16 v[56:59], v[156:159], a[232:235], v[56:59]\n\t"
    "buffer_load_dwordx4 v198, s[60:63], s72 offen offset:1024 sc1 lds\n\t"
    "v_mfma_f32_16x16x32_bf16 v[60:63], v[152:155], a[228:231], v[60:63]\n\t"
    "ds_read_b128 v[92:95], v204 offset:53248\n\t"
    "v_mfma_f32_16x16x32_bf16 v[60:63], v[156:159], a[236:239], v[60:63]\n\t"
    "s_waitcnt lgkmcnt(4)\n\t"
    "v_mfma_f32_16x16x32_bf16 v[80:83], v[160:163], a[224:227], v[80:83]\n\t"
    "ds_read_b128 v[96:99], v204 offset:49152\n\t"
    "v_mfma_f32_16x16x32_bf16 v[80:83], v[164:167], a[232:235], v[80:83]\n\t"
    "v_mfma_f32_16x16x32_bf16 v[84:87], v[160:163], a[228:231], v[84:87]\n\t"
    "ds_read_b128 v[100:103], v204 offset:54272\n\t"
    "v_mfma_f32_16x16x32_bf16 v[84:87], v[164:167], a[236:239], v[84:87]\n\t"
    "s_waitcnt lgkmcnt(4)\n\t"
    "v_mfma_f32_16x16x32_bf16 v[32:35], v[168:171], a[240:243], v[32:35]\n\t"
    "ds_read_b128 v[104:107], v204 offset:50176\n\t"
    "v_mfma_f32_16x16x32_bf16 v[32:35], v[172:175], a[248:251], v[32:35]\n\t"
    "global_load_dwordx2 v[120:121], v207, s[36:37] sc1\n\t"
    "s_waitcnt vmcnt(17)\n\t"
    "v_mfma_f32_16x16x32_bf16 v[36:39], v[168:171], a[244:247], v[36:39]\n\t"
    "ds_read_b128 v[108:111], v204 offset:55296\n\t"
    "v_mfma_f32_16x16x32_bf16 v[36:39], v[172:175], a[252:255], v[36:39]\n\t"
    "s_waitcnt lgkmcnt(4)\n\t"
    "v_mfma_f32_16x16x32_bf16 v[40:43], v[88:91], a[240:243], v[40:43]\n\t"
    "ds_read_b128 v[112:115], v204 offset:51200\n\t"
    "v_mfma_f32_16x16x32_bf16 v[40:43], v[92:95], a[248:251], v[40:43]\n\t"
    "global_load_dwordx2 v[122:123], v207, s[36:37] offset:128 sc1\n\t"
    "v_mfma_f32_16x16x32_bf16 v[44:47], v[88:91], a[244:247], v[44:47]\n\t"
    "ds_read_b128 v[116:119], v204 offset:56320\n\t"
    "v_mfma_f32_16x16x32_bf16 v[44:47], v[92:95], a[252:255], v[44:47]\n\t"
    "s_waitcnt vmcnt(7)\n\t"
    "s_barrier\n\t"
    "s_waitcnt lgkmcnt(4)\n\t"
    "v_mfma_f32_16x16x32_bf16 v[48:51], v[96:99], a[240:243], v[48:51]\n\t"
    "ds_read_b128 v[144:147], v204 offset:57344\n\t"
    "v_mfma_f32_16x16x32_bf16 v[48:51], v[100:103], a[248:251], v[48:51]\n\t"
    "v_add_u32 v207, 1179648, v207\n\t"
    "v_mfma_f32_16x16x32_bf16 v[52:55], v[96:99], a[244:247], v[52:55]\n\t"
    "ds_read_b128 v[148:151], v204 offset:62464\n\t"
    "v_mfma_f32_16x16x32_bf16 v[52:55], v[100:103], a[252:255], v[52:55]\n\t"
    "s_waitcnt lgkmcnt(4)\n\t"
    "v_mfma_f32_16x16x32_bf16 v[56:59], v[104:107], a[240:243], v[56:59]\n\t"
    "ds_read_b128 v[152:155], v204 offset:58368\n\t"
    "v_mfma_f32_16x16x32_bf16 v[56:59], v[108:111], a[248:251], v[56:59]\n\t"
    "global_load_dwordx2 v[124:125], v207, s[36:37] sc1\n\t"
    "v_mfma_f32_16x16x32_bf16 v[60:63], v[104:107], a[244:247], v[60:63]\n\t"
    "ds_read_b128 v[156:159], v204 offset:63488\n\t"
    "v_mfma_f32_16x16x32_bf16 v[60:63], v[108:111], a[252:255], v[60:63]\n\t"
    "s_waitcnt lgkmcnt(4)\n\t"
    "v_mfma_f32_16x16x32_bf16 v[80:83], v[112:115], a[240:243], v[80:83]\n\t"
    "ds_read_b128 v[160:163], v204 offset:59392\n\t"
    "v_mfma_f32_16x16x32_bf16 v[80:83], v[116:119], a[248:251], v[80:83]\n\t"
    "global_load_dwordx2 v[126:127], v207, s[36:37] offset:128 sc1\n\t"
    "v_mfma_f32_16x16x32_bf16 v[84:87], v[112:115], a[244:247], v[84:87]\n\t"
    "ds_read_b128 v[164:167], v204 offset:64512\n\t"
    "v_mfma_f32_16x16x32_bf16 v[84:87], v[116:119], a[252:255], v[84:87]\n\t"
    "s_waitcnt lgkmcnt(4)\n\t"
    "v_mfma_f32_16x16x32_bf16 v[32:35], v[144:147], v[224:227], v[32:35]\n\t"
    "ds_read_b128 v[168:171], v204 offset:60416\n\t"
    "s_add_u32 m0, s74, 81920\n\t"
    "s_add_u32 s72, s71, 73728\n\t"
    "v_mfma_f32_16x16x32_bf16 v[32:35], v[148:151], v[232:235], v[32:35]\n\t"
    "buffer_load_dwordx4 v198, s[60:63], s72 offen offset:0 sc1 lds\n\t"
    "s_waitcnt vmcnt(17)\n\t"
    "v_mfma_f32_16x16x32_bf16 v[36:39], v[144:147], v[228:231], v[36:39]\n\t"
    "ds_read_b128 v[172:175], v205 offset:0\n\t"
    "v_mfma_f32_16x16x32_bf16 v[36:39], v[148:151], v[236:239], v[36:39]\n\t"
    "global_load_dwordx2 v[128:129], v210, s[36:37] sc1\n\t"
    "s_waitcnt lgkmcnt(4)\n\t"
    "v_mfma_f32_16x16x32_bf16 v[40:43], v[152:155], v[224:227], v[40:43]\n\t"
    "ds_read_b128 v[88:91], v204 offset:61440\n\t"
    "v_mfma_f32_16x16x32_bf16 v[40:43], v[156:159], v[232:235], v[40:43]\n\t"
    "buffer_load_dwordx4 v198, s[60:63], s72 offen offset:1024 sc1 lds\n\t"
    "v_mfma_f32_16x16x32_bf16 v[44:47], v[152:155], v[228:231], v[44:47]\n\t"
    "ds_read_b128 v[92:95], v205 offset:1024\n\t"
    "v_mfma_f32_16x16x32_bf16 v[44:47], v[156:159], v[236:239], v[44:47]\n\t"
    "v_add_u32 v210, 1179648, v210\n\t"
    "s_waitcnt lgkmcnt(4)\n\t"
    "v_mfma_f32_16x16x32_bf16 v[48:51], v[160:163], v[224:227], v[48:51]\n\t"
    "ds_read_b128 v[96:99], v205 offset:2048\n\t"
    "s_add_u32 m0, s74, 83968\n\t"
    "s_add_u32 s72, s71, 110592\n\t"
    "v_mfma_f32_16x16x32_bf16 v[48:51], v[164:167], v[232:235], v[48:51]\n\t"
    "buffer_load_dwordx4 v198, s[60:63], s72 offen offset:0 sc1 lds\n\t"
    "v_mfma_f32_16x16x32_bf16 v[52:55], v[160:163], v[228:231], v[52:55]\n\t"
    "ds_read_b128 v[100:103], v205 offset:7168\n\t"
    "v_mfma_f32_16x16x32_bf16 v[52:55], v[164:167], v[236:239], v[52:55]\n\t"
    "global_load_dwordx2 v[130:131], v210, s[36:37] sc1\n\t"
    "s_waitcnt lgkmcnt(4)\n\t"
    "v_mfma_f32_16x16x32_bf16 v[56:59], v[168:171], v[224:227], v[56:59]\n\t"
    "ds_read_b128 v[104:107], v205 offset:3072\n\t"
    "v_mfma_f32_16x16x32_bf16 v[56:59], v[172:175], v[232:235], v[56:59]\n\t"
    "buffer_load_dwordx4 v198, s[60:63], s72 offen offset:1024 sc1 lds\n\t"
    "v_mfma_f32_16x16x32_bf16 v[60:63], v[168:171], v[228:231], v[60:63]\n\t"
    "ds_read_b128 v[108:111], v205 offset:8192\n\t"
    "v_mfma_f32_16x16x32_bf16 v[60:63], v[172:175], v[236:239], v[60:63]\n\t"
    "global_load_dwordx4 v[132:135], v209, s[36:37] sc1\n\t"
    "s_waitcnt lgkmcnt(4)\n\t"
    "v_mfma_f32_16x16x32_bf16 v[80:83], v[88:91], v[224:227], v[80:83]\n\t"
    "ds_read_b128 v[112:115], v205 offset:4096\n\t"
    "v_mfma_f32_16x16x32_bf16 v[80:83], v[92:95], v[232:235], v[80:83]\n\t"
    "v_mfma_f32_16x16x32_bf16 v[84:87], v[88:91], v[228:231], v[84:87]\n\t"
    "ds_read_b128 v[116:119], v205 offset:9216\n\t"
    "v_mfma_f32_16x16x32_bf16 v[84:87], v[92:95], v[236:239], v[84:87]\n\t"
    "global_load_dwordx4 v[136:139], v209, s[36:37] offset:16 sc1\n\t"
    "s_waitcnt lgkmcnt(4)\n\t"
    "v_mfma_f32_16x16x32_bf16 v[32:35], v[96:99], v[240:243], v[32:35]\n\t"
    "ds_read_b128 v[144:147], v205 offset:5120\n\t"
    "v_mfma_f32_16x16x32_bf16 v[32:35], v[100:103], v[248:251], v[32:35]\n\t"
    "s_waitcnt vmcnt(19)\n\t"
    "v_mfma_f32_16x16x32_bf16 v[36:39], v[96:99], v[244:247], v[36:39]\n\t"
    "ds_read_b128 v[148:151], v205 offset:10240\n\t"
    "v_mfma_f32_16x16x32_bf16 v[36:39], v[100:103], v[252:255], v[36:39]\n\t"
    "global_load_dwordx4 v[140:143], v212, s[36:37] sc1\n\t"
    "s_waitcnt lgkmcnt(4)\n\t"
    "v_mfma_f32_16x16x32_bf16 v[40:43], v[104:107], v[240:243], v[40:43]\n\t"
    "ds_read_b128 v[152:155], v205 offset:6144\n\t"
    "v_mfma_f32_16x16x32_bf16 v[40:43], v[108:111], v[248:251], v[40:43]\n\t"
    "v_mfma_f32_16x16x32_bf16 v[44:47], v[104:107], v[244:247], v[44:47]\n\t"
    "ds_read_b128 v[156:159], v205 offset:11264\n\t"
    "v_mfma_f32_16x16x32_bf16 v[44:47], v[108:111], v[252:255], v[44:47]\n\t"
    "s_waitcnt lgkmcnt(4)\n\t"
    "v_mfma_f32_16x16x32_bf16 v[48:51], v[112:115], v[240:243], v[48:51]\n\t"
    "ds_read_b128 v[160:163], v15 offset:0\n\t"
    "v_mfma_f32_16x16x32_bf16 v[48:51], v[116:119], v[248:251], v[48:51]\n\t"
    "ds_read_b128 v[164:167], v15 offset:18432\n\t"
    "v_mfma_f32_16x16x32_bf16 v[52:55], v[112:115], v[244:247], v[52:55]\n\t"
    "ds_read_b128 v[168:171], v15 offset:1024\n\t"
    "v_mfma_f32_16x16x32_bf16 v[52:55], v[116:119], v[252:255], v[52:55]\n\t"
    "ds_read_b128 v[172:175], v15 offset:19456\n\t"
    "s_waitcnt lgkmcnt(6)\n\t"
    "v_mfma_f32_16x16x32_bf16 v[56:59], v[144:147], v[240:243], v[56:59]\n\t"
    "ds_read_b128 v[88:91], v15 offset:36864\n\t"
    "v_mfma_f32_16x16x32_bf16 v[56:59], v[148:151], v[248:251], v[56:59]\n\t"
    "ds_read_b128 v[92:95], v15 offset:55296\n\t"
    "v_mfma_f32_16x16x32_bf16 v[60:63], v[144:147], v[244:247], v[60:63]\n\t"
    "ds_read_b128 v[96:99], v15 offset:37888\n\t"
    "v_mfma_f32_16x16x32_bf16 v[60:63], v[148:151], v[252:255], v[60:63]\n\t"
    "ds_read_b128 v[100:103], v15 offset:56320\n\t"
    "s_waitcnt lgkmcnt(8)\n\t"
    "v_mfma_f32_16x16x32_bf16 v[80:83], v[152:155], v[240:243], v[80:83]\n\t"
    "ds_read_b128 v[104:107], v15 offset:2048\n\t"
    "v_mfma_f32_16x16x32_bf16 v[80:83], v[156:159], v[248:251], v[80:83]\n\t"
    "ds_read_b128 v[108:111], v15 offset:20480\n\t"
    "v_mfma_f32_16x16x32_bf16 v[84:87], v[152:155], v[244:247], v[84:87]\n\t"
    "ds_read_b128 v[112:115], v15 offset:3072\n\t"
    "v_mfma_f32_16x16x32_bf16 v[84:87], v[156:159], v[252:255], v[84:87]\n\t"
    "ds_read_b128 v[116:119], v15 offset:21504\n\t"
    "s_waitcnt lgkmcnt(10)\n\t"
    "v_mfma_f32_16x16x32_bf16 v[68:71], v[160:163], a[4:7], 0\n\t"
    "ds_read_b128 v[144:147], v15 offset:38912\n\t"
    "s_barrier\n\t"
    "s_add_u32 m0, s74, 102400\n\t"
    "s_add_u32 s72, s71, 147456\n\t"
    "v_mfma_f32_16x16x32_bf16 v[68:71], v[164:167], a[12:15], v[68:71]\n\t"
    "ds_read_b128 v[148:151], v15 offset:57344\n\t"
    "buffer_load_dwordx4 v198, s[60:63], s72 offen offset:0 sc1 lds\n\t"
    "s_waitcnt lgkmcnt(10)\n\t"
    "v_mfma_f32_16x16x32_bf16 v[76:79], v[168:171], a[4:7], 0\n\t"
    "ds_read_b128 v[152:155], v15 offset:39936\n\t"
    "v_mfma_f32_16x16x32_bf16 v[76:79], v[172:175], a[12:15], v[76:79]\n\t"
    "ds_read_b128 v[156:159], v15 offset:58368\n\t"
    "s_xor_b32 s28, s68, 1\n\t"
    "s_mul_i32 s28, s28, 40960\n\t"
    "s_add_u32 s28, s28, 0\n\t"
    "v_cvt_pk_bf16_f32 v2, v32, v33\n\t"
    "v_cvt_pk_bf16_f32 v3, v34, v35\n\t"
    "s_waitcnt lgkmcnt(10)\n\t"
    "v_mfma_f32_16x16x32_bf16 v[68:71], v[88:91], a[20:23], v[68:71]\n\t"
    "ds_read_b128 v[160:163], v15 offset:4096\n\t"
    "buffer_load_dwordx4 v198, s[60:63], s72 offen offset:1024 sc1 lds\n\t"
    "v_cvt_pk_bf16_f32 v4, v40, v41\n\t"
    "v_mfma_f32_16x16x32_bf16 v[68:71], v[92:95], a[28:31], v[68:71]\n\t"
    "ds_read_b128 v[164:167], v15 offset:22528\n\t"
    "v_cvt_pk_bf16_f32 v5, v42, v43\n\t"
    "buffer_store_dwordx4 v[2:5], v15, s[56:59], s28 offen offset:0 sc1\n\t"
    "s_waitcnt lgkmcnt(10)\n\t"
    "v_mfma_f32_16x16x32_bf16 v[76:79], v[96:99], a[20:23], v[76:79]\n\t"
    "ds_read_b128 v[168:171], v15 offset:5120\n\t"
    "v_cvt_pk_bf16_f32 v6, v36, v37\n\t"
    "v_cvt_pk_bf16_f32 v7, v38, v39\n\t"
    "s_add_u32 m0, s74, 104448\n\t"
    "s_add_u32 s72, s71, 184320\n\t"
    "v_mfma_f32_16x16x32_bf16 v[76:79], v[100:103], a[28:31], v[76:79]\n\t"
    "ds_read_b128 v[172:175], v15 offset:23552\n\t"
    "buffer_load_dwordx4 v198, s[60:63], s72 offen offset:0 sc1 lds\n\t"
    "v_cvt_pk_bf16_f32 v8, v44, v45\n\t"
    "s_waitcnt lgkmcnt(10)\n\t"
    "v_mfma_f32_16x16x32_bf16 v[68:71], v[104:107], a[36:39], v[68:71]\n\t"
    "ds_read_b128 v[88:91], v15 offset:40960\n\t"
    "v_cvt_pk_bf16_f32 v9, v46, v47\n\t"
    "buffer_store_dwordx4 v[6:9], v15, s[56:59], s28 offen offset:1024 sc1\n\t"
    "v_mfma_f32_16x16x32_bf16 v[68:71], v[108:111], a[44:47], v[68:71]\n\t"
    "ds_read_b128 v[92:95], v15 offset:59392\n\t"
    "s_xor_b32 s28, s68, 2\n\t"
    "s_mul_i32 s28, s28, 40960\n\t"
    "s_add_u32 s28, s28, 2048\n\t"
    "v_cvt_pk_bf16_f32 v2, v48, v49\n\t"
    "v_cvt_pk_bf16_f32 v3, v50, v51\n\t"
    "s_waitcnt lgkmcnt(10)\n\t"
    "v_mfma_f32_16x16x32_bf16 v[76:79], v[112:115], a[36:39], v[76:79]\n\t"
    "ds_read_b128 v[96:99], v15 offset:41984\n\t"
    "buffer_load_dwordx4 v198, s[60:63], s72 offen offset:1024 sc1 lds\n\t"
    "v_cvt_pk_bf16_f32 v4, v56, v57\n\t"
    "v_mfma_f32_16x16x32_bf16 v[76:79], v[116:119], a[44:47], v[76:79]\n\t"
    "ds_read_b128 v[100:103], v15 offset:60416\n\t"
    "v_cvt_pk_bf16_f32 v5, v58, v59\n\t"
    "buffer_store_dwordx4 v[2:5], v15, s[56:59], s28 offen offset:0 sc1\n\t"
    "s_waitcnt lgkmcnt(10)\n\t"
    "v_mfma_f32_16x16x32_bf16 v[68:71], v[144:147], a[52:55], v[68:71]\n\t"
    "ds_read_b128 v[104:107], v15 offset:6144\n\t"
    "v_cvt_pk_bf16_f32 v6, v52, v53\n\t"
    "v_cvt_pk_bf16_f32 v7, v54, v55\n\t"
    "v_mfma_f32_16x16x32_bf16 v[68:71], v[148:151], a[60:63], v[68:71]\n\t"
    "ds_read_b128 v[108:111], v15 offset:24576\n\t"
    "v_cvt_pk_bf16_f32 v8, v60, v61\n\t"
    "v_cvt_pk_bf16_f32 v9, v62, v63\n\t"
    "s_add_u32 m0, s74, 122880\n\t"
    "s_add_u32 s72, s71, 221184\n\t"
    "s_waitcnt lgkmcnt(10)\n\t"
    "v_mfma_f32_16x16x32_bf16 v[76:79], v[152:155], a[52:55], v[76:79]\n\t"
    "ds_read_b128 v[112:115], v15 offset:7168\n\t"
    "buffer_load_dwordx4 v198, s[60:63], s72 offen offset:0 sc1 lds\n\t"
    "buffer_store_dwordx4 v[6:9], v15, s[56:59], s28 offen offset:1024 sc1\n\t"
    "v_mfma_f32_16x16x32_bf16 v[76:79], v[156:159], a[60:63], v[76:79]\n\t"
    "ds_read_b128 v[116:119], v15 offset:25600\n\t"
    "s_xor_b32 s28, s68, s3\n\t"
    "s_cselect_b32 s28, s28, 4\n\t"
    "s_add_u32 s28, s28, 5\n\t"
    "s_lshl_b32 s28, s28, 10\n\t"
    "s_mul_i32 s29, s3, 40960\n\t"
    "s_add_u32 s28, s28, s29\n\t"
    "v_cvt_pk_bf16_f32 v2, v80, v81\n\t"
    "v_cvt_pk_bf16_f32 v3, v82, v83\n\t"
    "s_waitcnt lgkmcnt(10)\n\t"
    "v_mfma_f32_16x16x32_bf16 v[68:71], v[160:163], a[68:71], v[68:71]\n\t"
    "ds_read_b128 v[144:147], v15 offset:43008\n\t"
    "v_cvt_pk_bf16_f32 v4, v84, v85\n\t"
    "v_cvt_pk_bf16_f32 v5, v86, v87\n\t"
    "v_mfma_f32_16x16x32_bf16 v[68:71], v[164:167], a[76:79], v[68:71]\n\t"
    "ds_read_b128 v[148:151], v15 offset:61440\n\t"
    "buffer_load_dwordx4 v198, s[60:63], s72 offen offset:1024 sc1 lds\n\t"
    "buffer_store_dwordx4 v[2:5], v15, s[56:59], s28 offen sc1\n\t"
    "s_waitcnt lgkmcnt(10)\n\t"
    "v_mfma_f32_16x16x32_bf16 v[76:79], v[168:171], a[68:71], v[76:79]\n\t"
    "ds_read_b128 v[152:155], v15 offset:44032\n\t"
    "v_mfma_f32_16x16x32_bf16 v[76:79], v[172:175], a[76:79], v[76:79]\n\t"
    "ds_read_b128 v[156:159], v15 offset:62464\n\t"
    "s_add_u32 m0, s74, 124928\n\t"
    "s_add_u32 s72, s71, 258048\n\t"
    "s_waitcnt lgkmcnt(10)\n\t"
    "v_mfma_f32_16x16x32_bf16 v[68:71], v[88:91], a[84:87], v[68:71]\n\t"
    "ds_read_b128 v[160:163], v15 offset:8192\n\t"
    "buffer_load_dwordx4 v198, s[60:63], s72 offen offset:0 sc1 lds\n\t"
    "v_mfma_f32_16x16x32_bf16 v[68:71], v[92:95], a[92:95], v[68:71]\n\t"
    "ds_read_b128 v[164:167], v15 offset:26624\n\t"
    "s_waitcnt lgkmcnt(10)\n\t"
    "v_mfma_f32_16x16x32_bf16 v[76:79], v[96:99], a[84:87], v[76:79]\n\t"
    "ds_read_b128 v[168:171], v15 offset:9216\n\t"
    "v_mfma_f32_16x16x32_bf16 v[76:79], v[100:103], a[92:95], v[76:79]\n\t"
    "ds_read_b128 v[172:175], v15 offset:27648\n\t"
    "buffer_load_dwordx4 v198, s[60:63], s72 offen offset:1024 sc1 lds\n\t"
    "s_waitcnt lgkmcnt(10)\n\t"
    "v_mfma_f32_16x16x32_bf16 v[68:71], v[104:107], a[100:103], v[68:71]\n\t"
    "ds_read_b128 v[88:91], v15 offset:45056\n\t"
    "v_mfma_f32_16x16x32_bf16 v[68:71], v[108:111], a[108:111], v[68:71]\n\t"
    "ds_read_b128 v[92:95], v15 offset:63488\n\t"
    "s_waitcnt lgkmcnt(10)\n\t"
    "v_mfma_f32_16x16x32_bf16 v[76:79], v[112:115], a[100:103], v[76:79]\n\t"
    "ds_read_b128 v[96:99], v15 offset:46080\n\t"
    "v_mfma_f32_16x16x32_bf16 v[76:79], v[116:119], a[108:111], v[76:79]\n\t"
    "ds_read_b128 v[100:103], v15 offset:64512\n\t"
    "s_waitcnt lgkmcnt(10)\n\t"
    "v_mfma_f32_16x16x32_bf16 v[68:71], v[144:147], a[116:119], v[68:71]\n\t"
    "ds_read_b128 v[104:107], v15 offset:10240\n\t"
    "v_mfma_f32_16x16x32_bf16 v[68:71], v[148:151], a[124:127], v[68:71]\n\t"
    "ds_read_b128 v[108:111], v15 offset:28672\n\t"
    "s_waitcnt lgkmcnt(10)\n\t"
    "v_mfma_f32_16x16x32_bf16 v[76:79], v[152:155], a[116:119], v[76:79]\n\t"
    "ds_read_b128 v[112:115], v15 offset:11264\n\t"
    "v_mfma_f32_16x16x32_bf16 v[76:79], v[156:159], a[124:127], v[76:79]\n\t"
    "ds_read_b128 v[116:119], v15 offset:29696\n\t"
    "s_waitcnt lgkmcnt(10)\n\t"
    "v_mfma_f32_16x16x32_bf16 v[68:71], v[160:163], a[132:135], v[68:71]\n\t"
    "ds_read_b128 v[144:147], v15 offset:47104\n\t"
    "v_mfma_f32_16x16x32_bf16 v[68:71], v[164:167], a[140:143], v[68:71]\n\t"
    "ds_read_b128 v[148:151], v204 offset:0\n\t"
    "s_waitcnt lgkmcnt(10)\n\t"
    "v_mfma_f32_16x16x32_bf16 v[76:79], v[168:171], a[132:135], v[76:79]\n\t"
    "ds_read_b128 v[152:155], v15 offset:48128\n\t"
    "v_mfma_f32_16x16x32_bf16 v[76:79], v[172:175], a[140:143], v[76:79]\n\t"
    "ds_read_b128 v[156:159], v204 offset:1024\n\t"
    "s_waitcnt lgkmcnt(10)\n\t"
    "v_mfma_f32_16x16x32_bf16 v[68:71], v[88:91], a[148:151], v[68:71]\n\t"
    "ds_read_b128 v[160:163], v15 offset:12288\n\t"
    "v_mfma_f32_16x16x32_bf16 v[68:71], v[92:95], a[156:159], v[68:71]\n\t"
    "ds_read_b128 v[164:167], v15 offset:30720\n\t"
    "s_waitcnt lgkmcnt(10)\n\t"
    "v_mfma_f32_16x16x32_bf16 v[76:79], v[96:99], a[148:151], v[76:79]\n\t"
    "ds_read_b128 v[168:171], v15 offset:13312\n\t"
    "v_mfma_f32_16x16x32_bf16 v[76:79], v[100:103], a[156:159], v[76:79]\n\t"
    "ds_read_b128 v[172:175], v15 offset:31744\n\t"
    "s_waitcnt lgkmcnt(10)\n\t"
    "v_mfma_f32_16x16x32_bf16 v[68:71], v[104:107], a[164:167], v[68:71]\n\t"
    "ds_read_b128 v[88:91], v15 offset:49152\n\t"
    "v_mfma_f32_16x16x32_bf16 v[68:71], v[108:111], a[172:175], v[68:71]\n\t"
    "ds_read_b128 v[92:95], v204 offset:2048\n\t"
    "s_waitcnt lgkmcnt(10)\n\t"
    "v_mfma_f32_16x16x32_bf16 v[76:79], v[112:115], a[164:167], v[76:79]\n\t"
    "ds_read_b128 v[96:99], v15 offset:50176\n\t"
    "v_mfma_f32_16x16x32_bf16 v[76:79], v[116:119], a[172:175], v[76:79]\n\t"
    "ds_read_b128 v[100:103], v204 offset:3072\n\t"
    "s_waitcnt lgkmcnt(10)\n\t"
    "v_mfma_f32_16x16x32_bf16 v[68:71], v[144:147], a[180:183], v[68:71]\n\t"
    "ds_read_b128 v[104:107], v15 offset:14336\n\t"
    "v_mfma_f32_16x16x32_bf16 v[68:71], v[148:151], a[188:191], v[68:71]\n\t"
    "ds_read_b128 v[108:111], v15 offset:32768\n\t"
    "s_waitcnt lgkmcnt(10)\n\t"
    "v_mfma_f32_16x16x32_bf16 v[76:79], v[152:155], a[180:183], v[76:79]\n\t"
    "ds_read_b128 v[112:115], v15 offset:15360\n\t"
    "v_mfma_f32_16x16x32_bf16 v[76:79], v[156:159], a[188:191], v[76:79]\n\t"
    "ds_read_b128 v[116:119], v15 offset:33792\n\t"
    "s_waitcnt lgkmcnt(10)\n\t"
    "v_mfma_f32_16x16x32_bf16 v[68:71], v[160:163], a[196:199], v[68:71]\n\t"
    "ds_read_b128 v[144:147], v15 offset:51200\n\t"
    "v_mfma_f32_16x16x32_bf16 v[68:71], v[164:167], a[204:207], v[68:71]\n\t"
    "ds_read_b128 v[148:151], v204 offset:4096\n\t"
    "s_waitcnt lgkmcnt(10)\n\t"
    "v_mfma_f32_16x16x32_bf16 v[76:79], v[168:171], a[196:199], v[76:79]\n\t"
    "ds_read_b128 v[152:155], v15 offset:52224\n\t"
    "v_mfma_f32_16x16x32_bf16 v[76:79], v[172:175], a[204:207], v[76:79]\n\t"
    "ds_read_b128 v[156:159], v204 offset:5120\n\t"
    "s_waitcnt lgkmcnt(10)\n\t"
    "v_mfma_f32_16x16x32_bf16 v[68:71], v[88:91], a[212:215], v[68:71]\n\t"
    "ds_read_b128 v[160:163], v15 offset:16384\n\t"
    "v_mfma_f32_16x16x32_bf16 v[68:71], v[92:95], a[220:223], v[68:71]\n\t"
    "ds_read_b128 v[164:167], v15 offset:34816\n\t"
    "s_waitcnt lgkmcnt(10)\n\t"
    "v_mfma_f32_16x16x32_bf16 v[76:79], v[96:99], a[212:215], v[76:79]\n\t"
    "ds_read_b128 v[168:171], v15 offset:17408\n\t"
    "v_mfma_f32_16x16x32_bf16 v[76:79], v[100:103], a[220:223], v[76:79]\n\t"
    "ds_read_b128 v[172:175], v15 offset:35840\n\t"
    "s_waitcnt lgkmcnt(10)\n\t"
    "v_mfma_f32_16x16x32_bf16 v[68:71], v[104:107], a[228:231], v[68:71]\n\t"
    "ds_read_b128 v[88:91], v15 offset:53248\n\t"
    "v_mfma_f32_16x16x32_bf16 v[68:71], v[108:111], a[236:239], v[68:71]\n\t"
    "ds_read_b128 v[92:95], v204 offset:6144\n\t"
    "s_waitcnt lgkmcnt(10)\n\t"
    "v_mfma_f32_16x16x32_bf16 v[76:79], v[112:115], a[228:231], v[76:79]\n\t"
    "ds_read_b128 v[96:99], v15 offset:54272\n\t"
    "v_mfma_f32_16x16x32_bf16 v[76:79], v[116:119], a[236:239], v[76:79]\n\t"
    "ds_read_b128 v[100:103], v204 offset:7168\n\t"
    "s_waitcnt vmcnt(26)\n\t"
    "s_barrier\n\t"
    "s_waitcnt lgkmcnt(10)\n\t"
    "v_mfma_f32_16x16x32_bf16 v[68:71], v[144:147], a[244:247], v[68:71]\n\t"
    "ds_read_b128 v[104:107], v205 offset:12288\n\t"
    "v_mfma_f32_16x16x32_bf16 v[68:71], v[148:151], a[252:255], v[68:71]\n\t"
    "s_waitcnt lgkmcnt(9)\n\t"
    "v_mfma_f32_16x16x32_bf16 v[76:79], v[152:155], a[244:247], v[76:79]\n\t"
    "ds_read_b128 v[108:111], v205 offset:16384\n\t"
    "v_mfma_f32_16x16x32_bf16 v[76:79], v[156:159], a[252:255], v[76:79]\n\t"
    "s_waitcnt lgkmcnt(8)\n\t"
    "v_mfma_f32_16x16x32_bf16 v[68:71], v[160:163], v[228:231], v[68:71]\n\t"
    "ds_read_b128 v[112:115], v205 offset:13312\n\t"
    "v_mfma_f32_16x16x32_bf16 v[68:71], v[164:167], v[236:239], v[68:71]\n\t"
    "s_waitcnt lgkmcnt(7)\n\t"
    "v_mfma_f32_16x16x32_bf16 v[76:79], v[168:171], v[228:231], v[76:79]\n\t"
    "ds_read_b128 v[116:119], v205 offset:17408\n\t"
    "v_mfma_f32_16x16x32_bf16 v[76:79], v[172:175], v[236:239], v[76:79]\n\t"
    "s_waitcnt lgkmcnt(6)\n\t"
    "v_mfma_f32_16x16x32_bf16 v[68:71], v[88:91], v[244:247], v[68:71]\n\t"
    "v_mfma_f32_16x16x32_bf16 v[68:71], v[92:95], v[252:255], v[68:71]\n\t"
    "s_waitcnt lgkmcnt(4)\n\t"
    "v_mfma_f32_16x16x32_bf16 v[76:79], v[96:99], v[244:247], v[76:79]\n\t"
    "ds_read_b128 v[88:91], v205 offset:20480\n\t"
    "v_mfma_f32_16x16x32_bf16 v[76:79], v[100:103], v[252:255], v[76:79]\n\t"
    "s_waitcnt lgkmcnt(3)\n\t"
    "v_mfma_f32_16x16x32_bf16 v[16:19], v[104:107], a[0:3], 0\n\t"
    "ds_read_b128 v[92:95], v205 offset:24576\n\t"
    "v_mfma_f32_16x16x32_bf16 v[16:19], v[108:111], a[8:11], v[16:19]\n\t"
    "ds_read_b128 v[96:99], v205 offset:21504\n\t"
    "v_mfma_f32_16x16x32_bf16 v[20:23], v[104:107], a[4:7], 0\n\t"
    "v_mfma_f32_16x16x32_bf16 v[20:23], v[108:111], a[12:15], v[20:23]\n\t"
    "ds_read_b128 v[100:103], v205 offset:25600\n\t"
    "v_cvt_pk_bf16_f32 v6, v68, v69\n\t"
    "s_waitcnt lgkmcnt(4)\n\t"
    "v_mfma_f32_16x16x32_bf16 v[24:27], v[112:115], a[0:3], 0\n\t"
    "ds_read_b128 v[104:107], v205 offset:14336\n\t"
    "v_cvt_pk_bf16_f32 v7, v70, v71\n\t"
    "v_mfma_f32_16x16x32_bf16 v[24:27], v[116:119], a[8:11], v[24:27]\n\t"
    "v_cvt_pk_bf16_f32 v8, v76, v77\n\t"
    "v_cvt_pk_bf16_f32 v9, v78, v79\n\t"
    "v_mfma_f32_16x16x32_bf16 v[28:31], v[112:115], a[4:7], 0\n\t"
    "ds_read_b128 v[108:111], v205 offset:18432\n\t"
    "v_mfma_f32_16x16x32_bf16 v[28:31], v[116:119], a[12:15], v[28:31]\n\t"
    "s_waitcnt lgkmcnt(4)\n\t"
    "v_mfma_f32_16x16x32_bf16 v[16:19], v[88:91], a[16:19], v[16:19]\n\t"
    "ds_read_b128 v[112:115], v205 offset:15360\n\t"
    "v_mfma_f32_16x16x32_bf16 v[16:19], v[92:95], a[24:27], v[16:19]\n\t"
    "s_mul_i32 s29, s68, 40960\n\t"
    "s_add_u32 s29, s29, 0\n\t"
    "buffer_load_dwordx4 v[144:147], v15, s[56:59], s29 offen offset:0 sc1\n\t"
    "v_mfma_f32_16x16x32_bf16 v[20:23], v[88:91], a[20:23], v[20:23]\n\t"
    "ds_read_b128 v[116:119], v205 offset:19456\n\t"
    "v_mfma_f32_16x16x32_bf16 v[20:23], v[92:95], a[28:31], v[20:23]\n\t"
    "buffer_load_dwordx4 v[148:151], v15, s[56:59], s29 offen offset:1024 sc1\n\t"
    "s_waitcnt lgkmcnt(4)\n\t"
    "v_mfma_f32_16x16x32_bf16 v[24:27], v[96:99], a[16:19], v[24:27]\n\t"
    "ds_read_b128 v[88:91], v205 offset:22528\n\t"
    "v_mfma_f32_16x16x32_bf16 v[24:27], v[100:103], a[24:27], v[24:27]\n\t"
    "v_mfma_f32_16x16x32_bf16 v[28:31], v[96:99], a[20:23], v[28:31]\n\t"
    "ds_read_b128 v[92:95], v205 offset:26624\n\t"
    "v_mfma_f32_16x16x32_bf16 v[28:31], v[100:103], a[28:31], v[28:31]\n\t"
    "buffer_load_dwordx4 v[152:155], v15, s[56:59], s29 offen offset:2048 sc1\n\t"
    "s_waitcnt lgkmcnt(4)\n\t"
    "v_mfma_f32_16x16x32_bf16 v[16:19], v[104:107], a[32:35], v[16:19]\n\t"
    "ds_read_b128 v[96:99], v205 offset:23552\n\t"
    "v_mfma_f32_16x16x32_bf16 v[16:19], v[108:111], a[40:43], v[16:19]\n\t"
    "buffer_load_dwordx4 v[156:159], v15, s[56:59], s29 offen offset:3072 sc1\n\t"
    "v_mfma_f32_16x16x32_bf16 v[20:23], v[104:107], a[36:39], v[20:23]\n\t"
    "ds_read_b128 v[100:103], v205 offset:27648\n\t"
    "v_mfma_f32_16x16x32_bf16 v[20:23], v[108:111], a[44:47], v[20:23]\n\t"
    "s_waitcnt vmcnt(20)\n\t"
    "s_barrier\n\t"
    "s_waitcnt lgkmcnt(4)\n\t"
    "v_mfma_f32_16x16x32_bf16 v[24:27], v[112:115], a[32:35], v[24:27]\n\t"
    "ds_read_b128 v[104:107], v204 offset:16384\n\t"
    "v_mfma_f32_16x16x32_bf16 v[24:27], v[116:119], a[40:43], v[24:27]\n\t"
    "v_mfma_f32_16x16x32_bf16 v[28:31], v[112:115], a[36:39], v[28:31]\n\t"
    "ds_read_b128 v[108:111], v204 offset:20480\n\t"
    "v_mfma_f32_16x16x32_bf16 v[28:31], v[116:119], a[44:47], v[28:31]\n\t"
    "s_waitcnt lgkmcnt(4)\n\t"
    "v_mfma_f32_16x16x32_bf16 v[16:19], v[88:91], a[48:51], v[16:19]\n\t"
    "ds_read_b128 v[112:115], v204 offset:17408\n\t"
    "v_mfma_f32_16x16x32_bf16 v[16:19], v[92:95], a[56:59], v[16:19]\n\t"
    "s_mul_i32 s29, s68, 40960\n\t"
    "s_add_u32 s29, s29, 4096\n\t"
    "buffer_load_dwordx4 v[168:171], v15, s[56:59], s29 offen offset:2048 sc1\n\t"
    "v_mfma_f32_16x16x32_bf16 v[20:23], v[88:91], a[52:55], v[20:23]\n\t"
    "ds_read_b128 v[116:119], v204 offset:21504\n\t"
    "v_mfma_f32_16x16x32_bf16 v[20:23], v[92:95], a[60:63], v[20:23]\n\t"
    "buffer_load_dwordx4 v[172:175], v15, s[56:59], s29 offen offset:3072 sc1\n\t"
    "s_waitcnt lgkmcnt(4)\n\t"
    "v_mfma_f32_16x16x32_bf16 v[24:27], v[96:99], a[48:51], v[24:27]\n\t"
    "ds_read_b128 v[88:91], v204 offset:24576\n\t"
    "v_mfma_f32_16x16x32_bf16 v[24:27], v[100:103], a[56:59], v[24:27]\n\t"
    "s_mul_i32 s29, s68, 40960\n\t"
    "s_add_u32 s29, s29, 8192\n\t"
    "buffer_load_dwordx4 v[176:179], v15, s[56:59], s29 offen offset:0 sc1\n\t"
    "v_mfma_f32_16x16x32_bf16 v[28:31], v[96:99], a[52:55], v[28:31]\n\t"
    "ds_read_b128 v[92:95], v204 offset:28672\n\t"
    "v_mfma_f32_16x16x32_bf16 v[28:31], v[100:103], a[60:63], v[28:31]\n\t"
    "s_xor_b32 s28, s68, 3\n\t"
    "s_mul_i32 s28, s28, 40960\n\t"
    "s_add_u32 s28, s28, 4096\n\t"
    "buffer_store_dwordx4 v[6:9], v15, s[56:59], s28 offen offset:1024 sc1\n\t"
    "s_waitcnt lgkmcnt(4)\n\t"
    "v_mfma_f32_16x16x32_bf16 v[16:19], v[104:107], a[64:67], v[16:19]\n\t"
    "ds_read_b128 v[96:99], v204 offset:25600\n\t"
    "v_mfma_f32_16x16x32_bf16 v[16:19], v[108:111], a[72:75], v[16:19]\n\t"
    "v_mfma_f32_16x16x32_bf16 v[20:23], v[104:107], a[68:71], v[20:23]\n\t"
    "ds_read_b128 v[100:103], v204 offset:29696\n\t"
    "v_mfma_f32_16x16x32_bf16 v[20:23], v[108:111], a[76:79], v[20:23]\n\t"
    "s_waitcnt lgkmcnt(4)\n\t"
    "v_mfma_f32_16x16x32_bf16 v[24:27], v[112:115], a[64:67], v[24:27]\n\t"
    "ds_read_b128 v[104:107], v204 offset:18432\n\t"
    "v_mfma_f32_16x16x32_bf16 v[24:27], v[116:119], a[72:75], v[24:27]\n\t"
    "v_mfma_f32_16x16x32_bf16 v[28:31], v[112:115], a[68:71], v[28:31]\n\t"
    "ds_read_b128 v[108:111], v204 offset:22528\n\t"
    "v_mfma_f32_16x16x32_bf16 v[28:31], v[116:119], a[76:79], v[28:31]\n\t"
    "s_waitcnt vmcnt(21)\n\t"
    "s_waitcnt lgkmcnt(4)\n\t"
    "v_mfma_f32_16x16x32_bf16 v[16:19], v[88:91], a[80:83], v[16:19]\n\t"
    "ds_read_b128 v[112:115], v204 offset:19456\n\t"
    "v_lshlrev_b32 v180, 16, v120\n\t"
    "v_mfma_f32_16x16x32_bf16 v[16:19], v[92:95], a[88:91], v[16:19]\n\t"
    "v_and_b32 v181, 0xffff0000, v120\n\t"
    "v_lshlrev_b32 v182, 16, v121\n\t"
    "v_mfma_f32_16x16x32_bf16 v[20:23], v[88:91], a[84:87], v[20:23]\n\t"
    "ds_read_b128 v[116:119], v204 offset:23552\n\t"
    "v_and_b32 v183, 0xffff0000, v121\n\t"
    "v_mfma_f32_16x16x32_bf16 v[20:23], v[92:95], a[92:95], v[20:23]\n\t"
    "v_lshlrev_b32 v184, 16, v122\n\t"
    "v_and_b32 v185, 0xffff0000, v122\n\t"
    "s_waitcnt lgkmcnt(4)\n\t"
    "v_mfma_f32_16x16x32_bf16 v[24:27], v[96:99], a[80:83], v[24:27]\n\t"
    "ds_read_b128 v[88:91], v204 offset:26624\n\t"
    "v_lshlrev_b32 v186, 16, v123\n\t"
    "v_mfma_f32_16x16x32_bf16 v[24:27], v[100:103], a[88:91], v[24:27]\n\t"
    "v_and_b32 v187, 0xffff0000, v123\n\t"
    "v_lshlrev_b32 v188, 16, v124\n\t"
    "v_mfma_f32_16x16x32_bf16 v[28:31], v[96:99], a[84:87], v[28:31]\n\t"
    "ds_read_b128 v[92:95], v204 offset:30720\n\t"
    "v_and_b32 v189, 0xffff0000, v124\n\t"
    "v_mfma_f32_16x16x32_bf16 v[28:31], v[100:103], a[92:95], v[28:31]\n\t"
    "v_lshlrev_b32 v190, 16, v125\n\t"
    "v_and_b32 v191, 0xffff0000, v125\n\t"
    "s_waitcnt lgkmcnt(4)\n\t"
    "v_mfma_f32_16x16x32_bf16 v[16:19], v[104:107], a[96:99], v[16:19]\n\t"
    "ds_read_b128 v[96:99], v204 offset:27648\n\t"
    "v_lshlrev_b32 v192, 16, v126\n\t"
    "v_mfma_f32_16x16x32_bf16 v[16:19], v[108:111], a[104:107], v[16:19]\n\t"
    "v_and_b32 v193, 0xffff0000, v126\n\t"
    "v_lshlrev_b32 v194, 16, v127\n\t"
    "v_mfma_f32_16x16x32_bf16 v[20:23], v[104:107], a[100:103], v[20:23]\n\t"
    "ds_read_b128 v[100:103], v204 offset:31744\n\t"
    "v_and_b32 v195, 0xffff0000, v127\n\t"
    "v_mfma_f32_16x16x32_bf16 v[20:23], v[108:111], a[108:111], v[20:23]\n\t"
    "s_waitcnt vmcnt(15)\n\t"
    "s_barrier\n\t"
    "s_waitcnt lgkmcnt(4)\n\t"
    "v_mfma_f32_16x16x32_bf16 v[24:27], v[112:115], a[96:99], v[24:27]\n\t"
    "ds_read_b128 v[104:107], v204 offset:36864\n\t"
    "v_mfma_f32_16x16x32_bf16 v[24:27], v[116:119], a[104:107], v[24:27]\n\t"
    "v_mfma_f32_16x16x32_bf16 v[28:31], v[112:115], a[100:103], v[28:31]\n\t"
    "ds_read_b128 v[108:111], v204 offset:40960\n\t"
    "v_mfma_f32_16x16x32_bf16 v[28:31], v[116:119], a[108:111], v[28:31]\n\t"
    "s_waitcnt lgkmcnt(4)\n\t"
    "v_mfma_f32_16x16x32_bf16 v[16:19], v[88:91], a[112:115], v[16:19]\n\t"
    "ds_read_b128 v[112:115], v204 offset:37888\n\t"
    "v_mfma_f32_16x16x32_bf16 v[16:19], v[92:95], a[120:123], v[16:19]\n\t"
    "v_mfma_f32_16x16x32_bf16 v[20:23], v[88:91], a[116:119], v[20:23]\n\t"
    "ds_read_b128 v[116:119], v204 offset:41984\n\t"
    "v_mfma_f32_16x16x32_bf16 v[20:23], v[92:95], a[124:127], v[20:23]\n\t"
    "s_waitcnt lgkmcnt(4)\n\t"
    "v_mfma_f32_16x16x32_bf16 v[24:27], v[96:99], a[112:115], v[24:27]\n\t"
    "ds_read_b128 v[88:91], v204 offset:45056\n\t"
    "v_mfma_f32_16x16x32_bf16 v[24:27], v[100:103], a[120:123], v[24:27]\n\t"
    "v_mfma_f32_16x16x32_bf16 v[28:31], v[96:99], a[116:119], v[28:31]\n\t"
    "ds_read_b128 v[92:95], v204 offset:49152\n\t"
    "v_mfma_f32_16x16x32_bf16 v[28:31], v[100:103], a[124:127], v[28:31]\n\t"
    "s_waitcnt lgkmcnt(4)\n\t"
    "v_mfma_f32_16x16x32_bf16 v[16:19], v[104:107], a[128:131], v[16:19]\n\t"
    "ds_read_b128 v[96:99], v204 offset:46080\n\t"
    "s_add_u32 m0, s74, 143360\n\t"
    "s_add_u32 s72, s71, 294912\n\t"
    "v_mfma_f32_16x16x32_bf16 v[16:19], v[108:111], a[136:139], v[16:19]\n\t"
    "buffer_load_dwordx4 v198, s[60:63], s72 offen offset:0 sc1 lds\n\t"
    "v_mfma_f32_16x16x32_bf16 v[20:23], v[104:107], a[132:135], v[20:23]\n\t"
    "ds_read_b128 v[100:103], v204 offset:50176\n\t"
    "v_mfma_f32_16x16x32_bf16 v[20:23], v[108:111], a[140:143], v[20:23]\n\t"
    "s_waitcnt lgkmcnt(4)\n\t"
    "v_mfma_f32_16x16x32_bf16 v[24:27], v[112:115], a[128:131], v[24:27]\n\t"
    "ds_read_b128 v[104:107], v204 offset:38912\n\t"
    "v_mfma_f32_16x16x32_bf16 v[24:27], v[116:119], a[136:139], v[24:27]\n\t"
    "buffer_load_dwordx4 v198, s[60:63], s72 offen offset:1024 sc1 lds\n\t"
    "v_mfma_f32_16x16x32_bf16 v[28:31], v[112:115], a[132:135], v[28:31]\n\t"
    "ds_read_b128 v[108:111], v204 offset:43008\n\t"
    "v_mfma_f32_16x16x32_bf16 v[28:31], v[116:119], a[140:143], v[28:31]\n\t"
    "s_waitcnt lgkmcnt(4)\n\t"
    "v_mfma_f32_16x16x32_bf16 v[16:19], v[88:91], a[144:147], v[16:19]\n\t"
    "ds_read_b128 v[112:115], v204 offset:39936\n\t"
    "v_mfma_f32_16x16x32_bf16 v[16:19], v[92:95], a[152:155], v[16:19]\n\t"
    "v_mfma_f32_16x16x32_bf16 v[20:23], v[88:91], a[148:151], v[20:23]\n\t"
    "ds_read_b128 v[116:119], v204 offset:44032\n\t"
    "v_mfma_f32_16x16x32_bf16 v[20:23], v[92:95], a[156:159], v[20:23]\n\t"
    "s_waitcnt lgkmcnt(4)\n\t"
    "v_mfma_f32_16x16x32_bf16 v[24:27], v[96:99], a[144:147], v[24:27]\n\t"
    "ds_read_b128 v[88:91], v204 offset:47104\n\t"
    "v_mfma_f32_16x16x32_bf16 v[24:27], v[100:103], a[152:155], v[24:27]\n\t"
    "v_mfma_f32_16x16x32_bf16 v[28:31], v[96:99], a[148:151], v[28:31]\n\t"
    "ds_read_b128 v[92:95], v204 offset:51200\n\t"
    "v_mfma_f32_16x16x32_bf16 v[28:31], v[100:103], a[156:159], v[28:31]\n\t"
    "s_waitcnt lgkmcnt(4)\n\t"
    "v_mfma_f32_16x16x32_bf16 v[16:19], v[104:107], a[160:163], v[16:19]\n\t"
    "ds_read_b128 v[96:99], v204 offset:48128\n\t"
    "v_mfma_f32_16x16x32_bf16 v[16:19], v[108:111], a[168:171], v[16:19]\n\t"
    "v_mfma_f32_16x16x32_bf16 v[20:23], v[104:107], a[164:167], v[20:23]\n\t"
    "ds_read_b128 v[100:103], v204 offset:52224\n\t"
    "v_mfma_f32_16x16x32_bf16 v[20:23], v[108:111], a[172:175], v[20:23]\n\t"
    "s_waitcnt vmcnt(10)\n\t"
    "s_barrier\n\t"
    "s_waitcnt lgkmcnt(4)\n\t"
    "v_mfma_f32_16x16x32_bf16 v[24:27], v[112:115], a[160:163], v[24:27]\n\t"
    "ds_read_b128 v[104:107], v204 offset:57344\n\t"
    "v_mfma_f32_16x16x32_bf16 v[24:27], v[116:119], a[168:171], v[24:27]\n\t"
    "v_mfma_f32_16x16x32_bf16 v[28:31], v[112:115], a[164:167], v[28:31]\n\t"
    "ds_read_b128 v[108:111], v204 offset:61440\n\t"
    "s_waitcnt vmcnt(3)\n\t"
    "v_mfma_f32_16x16x32_bf16 v[28:31], v[116:119], a[172:175], v[28:31]\n\t"
    "v_max_u32 v220, v168, v170\n\t"
    "v_max3_u32 v220, v172, v174, v220\n\t"
    "s_waitcnt lgkmcnt(4)\n\t"
    "v_mfma_f32_16x16x32_bf16 v[16:19], v[88:91], a[176:179], v[16:19]\n\t"
    "ds_read_b128 v[112:115], v204 offset:58368\n\t"
    "v_max3_u32 v220, v176, v178, v220\n\t"
    "v_mfma_f32_16x16x32_bf16 v[16:19], v[92:95], a[184:187], v[16:19]\n\t"
    "v_and_b32 v220, s77, v220\n\t"
    "v_max3_u32 v220, v144, v146, v220\n\t"
    "v_mfma_f32_16x16x32_bf16 v[20:23], v[88:91], a[180:183], v[20:23]\n\t"
    "ds_read_b128 v[116:119], v204 offset:62464\n\t"
    "v_max3_u32 v220, v148, v150, v220\n\t"
    "v_mfma_f32_16x16x32_bf16 v[20:23], v[92:95], a[188:191], v[20:23]\n\t"
    "v_max3_u32 v220, v152, v154, v220\n\t"
    "v_max3_u32 v220, v156, v158, v220\n\t"
    "s_waitcnt lgkmcnt(4)\n\t"
    "v_mfma_f32_16x16x32_bf16 v[24:27], v[96:99], a[176:179], v[24:27]\n\t"
    "ds_read_b128 v[88:91], v205 offset:0\n\t"
    "v_cmp_eq_u32 vcc, -1, v220\n\t"
    "s_cbranch_vccnz L_slA_%=\n\t"
    "L_bkA_%=:\n\t"
    "v_mfma_f32_16x16x32_bf16 v[24:27], v[100:103], a[184:187], v[24:27]\n\t"
    "v_lshlrev_b32 v10, 16, v144\n\t"
    "v_and_b32 v11, 0xffff0000, v144\n\t"
    "v_mfma_f32_16x16x32_bf16 v[28:31], v[96:99], a[180:183], v[28:31]\n\t"
    "ds_read_b128 v[92:95], v205 offset:4096\n\t"
    "v_fmac_f32 v180, v132, v10\n\t"
    "v_mfma_f32_16x16x32_bf16 v[28:31], v[100:103], a[188:191], v[28:31]\n\t"
    "v_fmac_f32 v181, v133, v11\n\t"
    "v_lshlrev_b32 v10, 16, v145\n\t"
    "s_waitcnt lgkmcnt(4)\n\t"
    "v_mfma_f32_16x16x32_bf16 v[16:19], v[104:107], a[192:195], v[16:19]\n\t"
    "ds_read_b128 v[96:99], v205 offset:1024\n\t"
    "v_and_b32 v11, 0xffff0000, v145\n\t"
    "v_mfma_f32_16x16x32_bf16 v[16:19], v[108:111], a[200:203], v[16:19]\n\t"
    "s_mul_i32 s29, s68, 40960\n\t"
    "s_add_u32 s29, s29, 4096\n\t"
    "buffer_load_dwordx4 v[160:163], v15, s[56:59], s29 offen offset:0 sc1\n\t"
    "v_fmac_f32 v182, v134, v10\n\t"
    "v_mfma_f32_16x16x32_bf16 v[20:23], v[104:107], a[196:199], v[20:23]\n\t"
    "ds_read_b128 v[100:103], v205 offset:5120\n\t"
    "v_fmac_f32 v183, v135, v11\n\t"
    "v_mfma_f32_16x16x32_bf16 v[20:23], v[108:111], a[204:207], v[20:23]\n\t"
    "v_lshlrev_b32 v10, 16, v146\n\t"
    "v_and_b32 v11, 0xffff0000, v146\n\t"
    "s_waitcnt lgkmcnt(4)\n\t"
    "v_mfma_f32_16x16x32_bf16 v[24:27], v[112:115], a[192:195], v[24:27]\n\t"
    "ds_read_b128 v[104:107], v204 offset:59392\n\t"
    "v_fmac_f32 v184, v136, v10\n\t"
    "v_mfma_f32_16x16x32_bf16 v[24:27], v[116:119], a[200:203], v[24:27]\n\t"
    "v_fmac_f32 v185, v137, v11\n\t"
    "v_lshlrev_b32 v10, 16, v147\n\t"
    "v_mfma_f32_16x16x32_bf16 v[28:31], v[112:115], a[196:199], v[28:31]\n\t"
    "ds_read_b128 v[108:111], v204 offset:63488\n\t"
    "v_and_b32 v11, 0xffff0000, v147\n\t"
    "v_mfma_f32_16x16x32_bf16 v[28:31], v[116:119], a[204:207], v[28:31]\n\t"
    "v_fmac_f32 v186, v138, v10\n\t"
    "v_fmac_f32 v187, v139, v11\n\t"
    "s_waitcnt lgkmcnt(4)\n\t"
    "v_mfma_f32_16x16x32_bf16 v[16:19], v[88:91], a[208:211], v[16:19]\n\t"
    "ds_read_b128 v[112:115], v204 offset:60416\n\t"
    "v_lshlrev_b32 v10, 16, v148\n\t"
    "v_mfma_f32_16x16x32_bf16 v[16:19], v[92:95], a[216:219], v[16:19]\n\t"
    "v_and_b32 v11, 0xffff0000, v148\n\t"
    "v_fmac_f32 v188, v132, v10\n\t"
    "v_mfma_f32_16x16x32_bf16 v[20:23], v[88:91], a[212:215], v[20:23]\n\t"
    "ds_read_b128 v[116:119], v204 offset:64512\n\t"
    "v_fmac_f32 v189, v133, v11\n\t"
    "v_mfma_f32_16x16x32_bf16 v[20:23], v[92:95], a[220:223], v[20:23]\n\t"
    "v_lshlrev_b32 v10, 16, v149\n\t"
    "v_and_b32 v11, 0xffff0000, v149\n\t"
    "s_waitcnt lgkmcnt(4)\n\t"
    "v_mfma_f32_16x16x32_bf16 v[24:27], v[96:99], a[208:211], v[24:27]\n\t"
    "ds_read_b128 v[88:91], v205 offset:2048\n\t"
    "v_fmac_f32 v190, v134, v10\n\t"
    "v_mfma_f32_16x16x32_bf16 v[24:27], v[100:103], a[216:219], v[24:27]\n\t"
    "v_fmac_f32 v191, v135, v11\n\t"
    "v_lshlrev_b32 v10, 16, v150\n\t"
    "v_mfma_f32_16x16x32_bf16 v[28:31], v[96:99], a[212:215], v[28:31]\n\t"
    "ds_read_b128 v[92:95], v205 offset:6144\n\t"
    "v_and_b32 v11, 0xffff0000, v150\n\t"
    "v_mfma_f32_16x16x32_bf16 v[28:31], v[100:103], a[220:223], v[28:31]\n\t"
    "v_fmac_f32 v192, v136, v10\n\t"
    "v_fmac_f32 v193, v137, v11\n\t"
    "s_waitcnt lgkmcnt(4)\n\t"
    "v_mfma_f32_16x16x32_bf16 v[16:19], v[104:107], a[224:227], v[16:19]\n\t"
    "ds_read_b128 v[96:99], v205 offset:3072\n\t"
    "v_lshlrev_b32 v10, 16, v151\n\t"
    "v_mfma_f32_16x16x32_bf16 v[16:19], v[108:111], a[232:235], v[16:19]\n\t"
    "v_and_b32 v11, 0xffff0000, v151\n\t"
    "v_fmac_f32 v194, v138, v10\n\t"
    "v_mfma_f32_16x16x32_bf16 v[20:23], v[104:107], a[228:231], v[20:23]\n\t"
    "ds_read_b128 v[100:103], v205 offset:7168\n\t"
    "v_fmac_f32 v195, v139, v11\n\t"
    "v_mfma_f32_16x16x32_bf16 v[20:23], v[108:111], a[236:239], v[20:23]\n\t"
    "s_waitcnt vmcnt(1)\n\t"
    "s_barrier\n\t"
    "s_mul_i32 s29, s68, 40960\n\t"
    "s_add_u32 s29, s29, 4096\n\t"
    "buffer_load_dwordx4 v[164:167], v15, s[56:59], s29 offen offset:1024 sc1\n\t"
    "v_lshlrev_b32 v10, 16, v152\n\t"
    "s_waitcnt lgkmcnt(4)\n\t"
    "v_mfma_f32_16x16x32_bf16 v[24:27], v[112:115], a[224:227], v[24:27]\n\t"
    "ds_read_b128 v[104:107], v205 offset:12288\n\t"
    "v_and_b32 v11, 0xffff0000, v152\n\t"
    "v_mfma_f32_16x16x32_bf16 v[24:27], v[116:119], a[232:235], v[24:27]\n\t"
    "v_fmac_f32 v180, v132, v10\n\t"
    "v_fmac_f32 v181, v133, v11\n\t"
    "v_mfma_f32_16x16x32_bf16 v[28:31], v[112:115], a[228:231], v[28:31]\n\t"
    "ds_read_b128 v[108:111], v205 offset:16384\n\t"
    "v_lshlrev_b32 v10, 16, v153\n\t"
    "v_mfma_f32_16x16x32_bf16 v[28:31], v[116:119], a[236:239], v[28:31]\n\t"
    "v_and_b32 v11, 0xffff0000, v153\n\t"
    "v_fmac_f32 v182, v134, v10\n\t"
    "s_waitcnt lgkmcnt(4)\n\t"
    "v_mfma_f32_16x16x32_bf16 v[16:19], v[88:91], a[240:243], v[16:19]\n\t"
    "ds_read_b128 v[112:115], v205 offset:13312\n\t"
    "v_fmac_f32 v183, v135, v11\n\t"
    "v_mfma_f32_16x16x32_bf16 v[16:19], v[92:95], a[248:251], v[16:19]\n\t"
    "v_lshlrev_b32 v10, 16, v154\n\t"
    "v_and_b32 v11, 0xffff0000, v154\n\t"
    "v_mfma_f32_16x16x32_bf16 v[20:23], v[88:91], a[244:247], v[20:23]\n\t"
    "ds_read_b128 v[116:119], v205 offset:17408\n\t"
    "v_fmac_f32 v184, v136, v10\n\t"
    "v_mfma_f32_16x16x32_bf16 v[20:23], v[92:95], a[252:255], v[20:23]\n\t"
    "v_fmac_f32 v185, v137, v11\n\t"
    "v_lshlrev_b32 v10, 16, v155\n\t"
    "s_waitcnt lgkmcnt(4)\n\t"
    "v_mfma_f32_16x16x32_bf16 v[24:27], v[96:99], a[240:243], v[24:27]\n\t"
    "ds_read_b128 v[88:91], v205 offset:20480\n\t"
    "v_and_b32 v11, 0xffff0000, v155\n\t"
    "v_mfma_f32_16x16x32_bf16 v[24:27], v[100:103], a[248:251], v[24:27]\n\t"
    "v_fmac_f32 v186, v138, v10\n\t"
    "v_fmac_f32 v187, v139, v11\n\t"
    "v_mfma_f32_16x16x32_bf16 v[28:31], v[96:99], a[244:247], v[28:31]\n\t"
    "ds_read_b128 v[92:95], v205 offset:24576\n\t"
    "v_lshlrev_b32 v10, 16, v156\n\t"
    "v_mfma_f32_16x16x32_bf16 v[28:31], v[100:103], a[252:255], v[28:31]\n\t"
    "v_and_b32 v11, 0xffff0000, v156\n\t"
    "v_fmac_f32 v188, v132, v10\n\t"
    "s_waitcnt lgkmcnt(4)\n\t"
    "v_mfma_f32_16x16x32_bf16 v[16:19], v[104:107], v[224:227], v[16:19]\n\t"
    "ds_read_b128 v[96:99], v205 offset:21504\n\t"
    "v_fmac_f32 v189, v133, v11\n\t"
    "v_mfma_f32_16x16x32_bf16 v[16:19], v[108:111], v[232:235], v[16:19]\n\t"
    "v_lshlrev_b32 v10, 16, v157\n\t"
    "v_and_b32 v11, 0xffff0000, v157\n\t"
    "v_mfma_f32_16x16x32_bf16 v[20:23], v[104:107], v[228:231], v[20:23]\n\t"
    "ds_read_b128 v[100:103], v205 offset:25600\n\t"
    "v_fmac_f32 v190, v134, v10\n\t"
    "v_mfma_f32_16x16x32_bf16 v[20:23], v[108:111], v[236:239], v[20:23]\n\t"
    "v_fmac_f32 v191, v135, v11\n\t"
    "v_lshlrev_b32 v10, 16, v158\n\t"
    "s_waitcnt lgkmcnt(4)\n\t"
    "v_mfma_f32_16x16x32_bf16 v[24:27], v[112:115], v[224:227], v[24:27]\n\t"
    "v_and_b32 v11, 0xffff0000, v158\n\t"
    "v_fmac_f32 v192, v136, v10\n\t"
    "v_mfma_f32_16x16x32_bf16 v[24:27], v[116:119], v[232:235], v[24:27]\n\t"
    "v_fmac_f32 v193, v137, v11\n\t"
    "v_lshlrev_b32 v10, 16, v159\n\t"
    "v_mfma_f32_16x16x32_bf16 v[28:31], v[112:115], v[228:231], v[28:31]\n\t"
    "v_and_b32 v11, 0xffff0000, v159\n\t"
    "v_fmac_f32 v194, v138, v10\n\t"
    "v_mfma_f32_16x16x32_bf16 v[28:31], v[116:119], v[236:239], v[28:31]\n\t"
    "v_fmac_f32 v195, v139, v11\n\t"
    "v_lshlrev_b32 v10, 16, v168\n\t"
    "s_waitcnt lgkmcnt(2)\n\t"
    "v_mfma_f32_16x16x32_bf16 v[16:19], v[88:91], v[240:243], v[16:19]\n\t"
    "v_and_b32 v11, 0xffff0000, v168\n\t"
    "v_add_f32 v80, v10, v80\n\t"
    "v_mfma_f32_16x16x32_bf16 v[16:19], v[92:95], v[248:251], v[16:19]\n\t"
    "v_add_f32 v81, v11, v81\n\t"
    "v_lshlrev_b32 v10, 16, v169\n\t"
    "v_mfma_f32_16x16x32_bf16 v[20:23], v[88:91], v[244:247], v[20:23]\n\t"
    "v_and_b32 v11, 0xffff0000, v169\n\t"
    "v_add_f32 v82, v10, v82\n\t"
    "v_mfma_f32_16x16x32_bf16 v[20:23], v[92:95], v[252:255], v[20:23]\n\t"
    "v_add_f32 v83, v11, v83\n\t"
    "v_lshlrev_b32 v10, 16, v170\n\t"
    "s_waitcnt lgkmcnt(0)\n\t"
    "v_mfma_f32_16x16x32_bf16 v[24:27], v[96:99], v[240:243], v[24:27]\n\t"
    "v_and_b32 v11, 0xffff0000, v170\n\t"
    "v_add_f32 v84, v10, v84\n\t"
    "v_mfma_f32_16x16x32_bf16 v[24:27], v[100:103], v[248:251], v[24:27]\n\t"
    "v_add_f32 v85, v11, v85\n\t"
    "v_lshlrev_b32 v10, 16, v171\n\t"
    "v_mfma_f32_16x16x32_bf16 v[28:31], v[96:99], v[244:247], v[28:31]\n\t"
    "v_and_b32 v11, 0xffff0000, v171\n\t"
    "v_add_f32 v86, v10, v86\n\t"
    "v_mfma_f32_16x16x32_bf16 v[28:31], v[100:103], v[252:255], v[28:31]\n\t"
    "v_add_f32 v87, v11, v87\n\t"
    "v_lshlrev_b32 v10, 16, v172\n\t"
    "v_and_b32 v11, 0xffff0000, v172\n\t"
    "v_add_f32 v80, v10, v80\n\t"
    "v_add_f32 v81, v11, v81\n\t"
    "v_lshlrev_b32 v10, 16, v173\n\t"
    "v_and_b32 v11, 0xffff0000, v173\n\t"
    "v_add_f32 v82, v10, v82\n\t"
    "v_add_f32 v83, v11, v83\n\t"
    "v_lshlrev_b32 v10, 16, v174\n\t"
    "v_and_b32 v11, 0xffff0000, v174\n\t"
    "v_add_f32 v84, v10, v84\n\t"
    "v_add_f32 v85, v11, v85\n\t"
    "v_lshlrev_b32 v10, 16, v175\n\t"
    "v_and_b32 v11, 0xffff0000, v175\n\t"
    "v_add_f32 v86, v10, v86\n\t"
    "v_add_f32 v87, v11, v87\n\t"
    "v_lshlrev_b32 v10, 16, v176\n\t"
    "v_and_b32 v11, 0xffff0000, v176\n\t"
    "v_add_f32 v80, v10, v80\n\t"
    "v_add_f32 v81, v11, v81\n\t"
    "v_lshlrev_b32 v10, 16, v177\n\t"
    "v_and_b32 v11, 0xffff0000, v177\n\t"
    "v_add_f32 v82, v10, v82\n\t"
    "v_add_f32 v83, v11, v83\n\t"
    "v_lshlrev_b32 v10, 16, v178\n\t"
    "v_and_b32 v11, 0xffff0000, v178\n\t"
    "v_add_f32 v84, v10, v84\n\t"
    "v_add_f32 v85, v11, v85\n\t"
    "v_lshlrev_b32 v10, 16, v179\n\t"
    "v_and_b32 v11, 0xffff0000, v179\n\t"
    "v_add_f32 v86, v10, v86\n\t"
    "v_add_f32 v87, v11, v87\n\t"
    "v_lshlrev_b32 v10, 16, v128\n\t"
    "v_and_b32 v11, 0xffff0000, v128\n\t"
    "v_fmac_f32 v10, v140, v80\n\t"
    "v_fmac_f32 v11, v141, v81\n\t"
    "v_cvt_pk_bf16_f32 v2, v10, v11\n\t"
    "v_lshlrev_b32 v10, 16, v129\n\t"
    "v_and_b32 v11, 0xffff0000, v129\n\t"
    "v_fmac_f32 v10, v142, v82\n\t"
    "v_fmac_f32 v11, v143, v83\n\t"
    "v_cvt_pk_bf16_f32 v3, v10, v11\n\t"
    "v_lshlrev_b32 v10, 16, v130\n\t"
    "v_and_b32 v11, 0xffff0000, v130\n\t"
    "v_fmac_f32 v10, v140, v84\n\t"
    "v_fmac_f32 v11, v141, v85\n\t"
    "v_cvt_pk_bf16_f32 v4, v10, v11\n\t"
    "v_lshlrev_b32 v10, 16, v131\n\t"
    "v_and_b32 v11, 0xffff0000, v131\n\t"
    "v_fmac_f32 v10, v142, v86\n\t"
    "v_fmac_f32 v11, v143, v87\n\t"
    "v_cvt_pk_bf16_f32 v5, v10, v11\n\t"
    "s_nop 3\n\t"
    "v_fmac_f32 v180, v132, v16\n\t"
    "v_fmac_f32 v181, v133, v17\n\t"
    "v_fmac_f32 v182, v134, v18\n\t"
    "v_fmac_f32 v183, v135, v19\n\t"
    "v_fmac_f32 v184, v136, v24\n\t"
    "v_fmac_f32 v185, v137, v25\n\t"
    "v_fmac_f32 v186, v138, v26\n\t"
    "v_fmac_f32 v187, v139, v27\n\t"
    "v_fmac_f32 v188, v132, v20\n\t"
    "v_fmac_f32 v189, v133, v21\n\t"
    "v_fmac_f32 v190, v134, v22\n\t"
    "v_fmac_f32 v191, v135, v23\n\t"
    "v_fmac_f32 v192, v136, v28\n\t"
    "v_fmac_f32 v193, v137, v29\n\t"
    "v_fmac_f32 v194, v138, v30\n\t"
    "v_fmac_f32 v195, v139, v31\n\t"
    "s_waitcnt vmcnt(0)\n\t"
    "v_max_u32 v220, v160, v162\n\t"
    "v_max3_u32 v220, v164, v166, v220\n\t"
    "v_cmp_eq_u32 vcc, -1, v220\n\t"
    "s_cbranch_vccnz L_slB_%=\n\t"
    "L_bkB_%=:\n\t"
    "s_cmp_lg_u32 s3, s68\n\t"
    "s_cbranch_scc1 L_n8_%=\n\t"
    "global_store_dwordx2 v211, v[2:3], s[64:65] sc1\n\t"
    "v_add_u32 v211, 1179648, v211\n\t"
    "global_store_dwordx2 v211, v[4:5], s[64:65] sc1\n\t"
    "L_n8_%=:\n\t"
    "v_lshlrev_b32 v10, 16, v160\n\t"
    "v_and_b32 v11, 0xffff0000, v160\n\t"
    "v_fmac_f32 v180, v132, v10\n\t"
    "v_fmac_f32 v181, v133, v11\n\t"
    "v_lshlrev_b32 v10, 16, v161\n\t"
    "v_and_b32 v11, 0xffff0000, v161\n\t"
    "v_fmac_f32 v182, v134, v10\n\t"
    "v_fmac_f32 v183, v135, v11\n\t"
    "v_lshlrev_b32 v10, 16, v162\n\t"
    "v_and_b32 v11, 0xffff0000, v162\n\t"
    "v_fmac_f32 v184, v136, v10\n\t"
    "v_fmac_f32 v185, v137, v11\n\t"
    "v_lshlrev_b32 v10, 16, v163\n\t"
    "v_and_b32 v11, 0xffff0000, v163\n\t"
    "v_fmac_f32 v186, v138, v10\n\t"
    "v_fmac_f32 v187, v139, v11\n\t"
    "v_lshlrev_b32 v10, 16, v164\n\t"
    "v_and_b32 v11, 0xffff0000, v164\n\t"
    "v_fmac_f32 v188, v132, v10\n\t"
    "v_fmac_f32 v189, v133, v11\n\t"
    "v_lshlrev_b32 v10, 16, v165\n\t"
    "v_and_b32 v11, 0xffff0000, v165\n\t"
    "v_fmac_f32 v190, v134, v10\n\t"
    "v_fmac_f32 v191, v135, v11\n\t"
    "v_lshlrev_b32 v10, 16, v166\n\t"
    "v_and_b32 v11, 0xffff0000, v166\n\t"
    "v_fmac_f32 v192, v136, v10\n\t"
    "v_fmac_f32 v193, v137, v11\n\t"
    "v_lshlrev_b32 v10, 16, v167\n\t"
    "v_and_b32 v11, 0xffff0000, v167\n\t"
    "v_fmac_f32 v194, v138, v10\n\t"
    "v_fmac_f32 v195, v139, v11\n\t"
    "v_cvt_pk_bf16_f32 v2, v180, v181\n\t"
    "v_cvt_pk_bf16_f32 v3, v182, v183\n\t"
    "v_cvt_pk_bf16_f32 v4, v184, v185\n\t"
    "v_cvt_pk_bf16_f32 v5, v186, v187\n\t"
    "global_store_dwordx4 v208, v[2:5], s[64:65] sc1\n\t"
    "v_add_u32 v208, 1179648, v208\n\t"
    "v_cvt_pk_bf16_f32 v6, v188, v189\n\t"
    "v_cvt_pk_bf16_f32 v7, v190, v191\n\t"
    "v_cvt_pk_bf16_f32 v8, v192, v193\n\t"
    "v_cvt_pk_bf16_f32 v9, v194, v195\n\t"
    "global_store_dwordx4 v208, v[6:9], s[64:65] sc1\n\t"
    "s_cmp_lg_u32 s2, 0\n\t"
    "s_cbranch_scc1 L_nb_%=\n\t"
    "s_mov_b32 s28, 41943040\n\t"
    "s_cmp_lg_u32 s3, 0\n\t"
    "s_cbranch_scc1 L_pfz_%=\n\t"
    "v_mov_b32 v2, s66\n\t"
    "v_mov_b32 v3, s67\n\t"
    "v_mov_b32 v4, s28\n\t"
    "s_mov_b64 exec, 1\n\t"
    "global_store_dwordx2 v4, v[2:3], s[36:37] sc1\n\t"
    "s_mov_b64 exec, -1\n\t"
    "L_pfz_%=:\n\t"
    "L_nb_%=:\n\t"
    "s_branch L_end_%=\n\t"
    "L_gss_%=:\n\t"
    "s_mov_b32 s35, 0\n\t"
    "L_gqs_%=:\n\t"
    "s_sleep 4\n\t"
    "global_load_dwordx4 v[40:43], v91, s[36:37] offset:-4096 sc1\n\t"
    "global_load_dwordx4 v[44:47], v91, s[36:37] offset:0 sc1\n\t"
    "global_load_dwordx4 v[48:51], v92, s[36:37] offset:-4096 sc1\n\t"
    "global_load_dwordx4 v[52:55], v92, s[36:37] offset:0 sc1\n\t"
    "s_add_u32 s35, s35, 1\n\t"
    "s_waitcnt vmcnt(0)\n\t"
    "s_cmp_gt_u32 s35, 0x8000\n\t"
    "s_cbranch_scc1 L_gbs_%=\n\t"
    "v_cmpx_eq_u32 vcc, s66, v43\n\t"
    "v_cmpx_eq_u32 vcc, s66, v47\n\t"
    "v_cmpx_eq_u32 vcc, s66, v51\n\t"
    "v_cmpx_eq_u32 vcc, s66, v55\n\t"
    "s_cmp_eq_u64 exec, -1\n\t"
    "s_mov_b64 exec, -1\n\t"
    "s_cbranch_scc0 L_gqs_%=\n\t"
    "s_branch L_gbs_%=\n\t"
    "L_gsm_%=:\n\t"
    "s_mov_b32 s35, 0\n\t"
    "L_gqm_%=:\n\t"
    "s_sleep 4\n\t"
    "global_load_dwordx2 v[72:73], v78, s[36:37] sc1\n\t"
    "global_load_dwordx2 v[74:75], v79, s[36:37] sc1\n\t"
    "global_load_dwordx2 v[76:77], v80, s[36:37] sc1\n\t"
    "s_add_u32 s35, s35, 1\n\t"
    "s_waitcnt vmcnt(0)\n\t"
    "s_cmp_gt_u32 s35, 0x8000\n\t"
    "s_cbranch_scc1 L_gbm_%=\n\t"
    "v_cmpx_eq_u32 vcc, v81, v73\n\t"
    "v_cmpx_eq_u32 vcc, v81, v75\n\t"
    "v_cmpx_eq_u32 vcc, v81, v77\n\t"
    "s_cmp_eq_u64 exec, -1\n\t"
    "s_mov_b64 exec, -1\n\t"
    "s_cbranch_scc0 L_gqm_%=\n\t"
    "s_branch L_gbm_%=\n\t"
    "L_gsc_%=:\n\t"
    "s_mov_b32 s35, 0\n\t"
    "L_gqc_%=:\n\t"
    "s_sleep 4\n\t"
    "global_load_dwordx2 v[94:95], v14, s[36:37] sc1\n\t"
    "s_add_u32 s35, s35, 1\n\t"
    "s_waitcnt vmcnt(0)\n\t"
    "s_cmp_gt_u32 s35, 0x8000\n\t"
    "s_cbranch_scc1 L_gbc_%=\n\t"
    "v_cmp_ne_u32 vcc, s72, v95\n\t"
    "s_cbranch_vccnz L_gqc_%=\n\t"
    "s_branch L_gbc_%=\n\t"
    "L_gsf_%=:\n\t"
    "s_mov_b32 s35, 0\n\t"
    "L_gqf_%=:\n\t"
    "s_sleep 4\n\t"
    "global_load_dwordx4 v[16:19], v80, s[36:37] sc1\n\t"
    "global_load_dwordx4 v[20:23], v81, s[36:37] sc1\n\t"
    "global_load_dwordx4 v[24:27], v82, s[36:37] sc1\n\t"
    "global_load_dwordx4 v[28:31], v83, s[36:37] sc1\n\t"
    "global_load_dwordx4 v[32:35], v84, s[36:37] sc1\n\t"
    "global_load_dwordx4 v[36:39], v85, s[36:37] sc1\n\t"
    "global_load_dwordx4 v[40:43], v86, s[36:37] sc1\n\t"
    "global_load_dwordx4 v[44:47], v87, s[36:37] sc1\n\t"
    "global_load_dwordx4 v[48:51], v88, s[36:37] sc1\n\t"
    "global_load_dwordx4 v[52:55], v89, s[36:37] sc1\n\t"
    "global_load_dwordx4 v[56:59], v90, s[36:37] sc1\n\t"
    "global_load_dwordx4 v[60:63], v91, s[36:37] sc1\n\t"
    "s_add_u32 s35, s35, 1\n\t"
    "s_waitcnt vmcnt(0)\n\t"
    "s_cmp_gt_u32 s35, 0x8000\n\t"
    "s_cbranch_scc1 L_gbf_%=\n\t"
    "v_cmpx_eq_u32 vcc, s66, v17\n\t"
    "v_cmpx_eq_u32 vcc, s67, v19\n\t"
    "v_cmpx_eq_u32 vcc, s66, v21\n\t"
    "v_cmpx_eq_u32 vcc, s67, v23\n\t"
    "v_cmpx_eq_u32 vcc, s66, v25\n\t"
    "v_cmpx_eq_u32 vcc, s67, v27\n\t"
    "v_cmpx_eq_u32 vcc, s66, v29\n\t"
    "v_cmpx_eq_u32 vcc, s67, v31\n\t"
    "v_cmpx_eq_u32 vcc, s66, v33\n\t"
    "v_cmpx_eq_u32 vcc, s67, v35\n\t"
    "v_cmpx_eq_u32 vcc, s66, v37\n\t"
    "v_cmpx_eq_u32 vcc, s67, v39\n\t"
    "v_cmpx_eq_u32 vcc, s66, v41\n\t"
    "v_cmpx_eq_u32 vcc, s67, v43\n\t"
    "v_cmpx_eq_u32 vcc, s66, v45\n\t"
    "v_cmpx_eq_u32 vcc, s67, v47\n\t"
    "v_cmpx_eq_u32 vcc, s66, v49\n\t"
    "v_cmpx_eq_u32 vcc, s67, v51\n\t"
    "v_cmpx_eq_u32 vcc, s66, v53\n\t"
    "v_cmpx_eq_u32 vcc, s67, v55\n\t"
    "v_cmpx_eq_u32 vcc, s66, v57\n\t"
    "v_cmpx_eq_u32 vcc, s67, v59\n\t"
    "v_cmpx_eq_u32 vcc, s66, v61\n\t"
    "v_cmpx_eq_u32 vcc, s67, v63\n\t"
    "s_cmp_eq_u64 exec, -1\n\t"
    "s_mov_b64 exec, -1\n\t"
    "s_cbranch_scc0 L_gqf_%=\n\t"
    "s_branch L_gbf_%=\n\t"
    "L_is0_%=:\n\t"
    "s_mov_b32 s35, 0\n\t"
    "L_iq0_%=:\n\t"
    "s_mul_i32 s28, s33, 18432\n\t"
    "buffer_load_dwordx4 v[16:19], v15, s[12:15], s28 offen offset:0 sc1\n\t"
    "buffer_load_dwordx4 v[20:23], v15, s[12:15], s28 offen offset:1024 sc1\n\t"
    "buffer_load_dwordx4 v[24:27], v15, s[12:15], s28 offen offset:2048 sc1\n\t"
    "buffer_load_dwordx4 v[28:31], v15, s[12:15], s28 offen offset:3072 sc1\n\t"
    "s_add_u32 s28, s28, 4096\n\t"
    "buffer_load_dwordx4 v[32:35], v15, s[12:15], s28 offen offset:0 sc1\n\t"
    "buffer_load_dwordx4 v[36:39], v15, s[12:15], s28 offen offset:1024 sc1\n\t"
    "buffer_load_dwordx4 v[40:43], v15, s[12:15], s28 offen offset:2048 sc1\n\t"
    "buffer_load_dwordx4 v[44:47], v15, s[12:15], s28 offen offset:3072 sc1\n\t"
    "s_add_u32 s28, s28, 4096\n\t"
    "buffer_load_dwordx4 v[48:51], v15, s[12:15], s28 offen offset:0 sc1\n\t"
    "buffer_load_dwordx4 v[52:55], v15, s[12:15], s28 offen offset:1024 sc1\n\t"
    "buffer_load_dwordx4 v[56:59], v15, s[12:15], s28 offen offset:2048 sc1\n\t"
    "buffer_load_dwordx4 v[60:63], v15, s[12:15], s28 offen offset:3072 sc1\n\t"
    "s_add_u32 s28, s28, 4096\n\t"
    "buffer_load_dwordx4 v[64:67], v15, s[12:15], s28 offen offset:0 sc1\n\t"
    "buffer_load_dwordx4 v[68:71], v15, s[12:15], s28 offen offset:1024 sc1\n\t"
    "buffer_load_dwordx4 v[72:75], v15, s[12:15], s28 offen offset:2048 sc1\n\t"
    "buffer_load_dwordx4 v[76:79], v15, s[12:15], s28 offen offset:3072 sc1\n\t"
    "s_add_u32 s28, s28, 4096\n\t"
    "buffer_load_dwordx4 v[80:83], v15, s[12:15], s28 offen offset:0 sc1\n\t"
    "buffer_load_dwordx4 v[84:87], v15, s[12:15], s28 offen offset:1024 sc1\n\t"
    "s_add_u32 s35, s35, 1\n\t"
    "s_waitcnt vmcnt(0)\n\t"
    "v_max_u32 v8, v16, v20\n\t"
    "v_max3_u32 v8, v24, v28, v8\n\t"
    "v_max3_u32 v8, v32, v36, v8\n\t"
    "v_max3_u32 v8, v40, v44, v8\n\t"
    "v_max3_u32 v8, v48, v52, v8\n\t"
    "v_max3_u32 v8, v56, v60, v8\n\t"
    "v_max3_u32 v8, v64, v68, v8\n\t"
    "v_max3_u32 v8, v72, v76, v8\n\t"
    "v_max3_u32 v8, v80, v84, v8\n\t"
    "v_cmp_eq_u32 vcc, -1, v8\n\t"
    "s_cbranch_vccz L_ib0_%=\n\t"
    "s_cmp_lt_u32 s35, 0x8000\n\t"
    "s_cbranch_scc1 L_iq0_%=\n\t"
    "s_branch L_ib0_%=\n\t"
    "L_is1_%=:\n\t"
    "s_mov_b32 s35, 0\n\t"
    "L_iq1_%=:\n\t"
    "s_mul_i32 s28, s33, 18432\n\t"
    "buffer_load_dwordx4 v[88:91], v15, s[12:15], s28 offen offset:0 sc1\n\t"
    "buffer_load_dwordx4 v[92:95], v15, s[12:15], s28 offen offset:1024 sc1\n\t"
    "buffer_load_dwordx4 v[96:99], v15, s[12:15], s28 offen offset:2048 sc1\n\t"
    "buffer_load_dwordx4 v[100:103], v15, s[12:15], s28 offen offset:3072 sc1\n\t"
    "s_add_u32 s28, s28, 4096\n\t"
    "buffer_load_dwordx4 v[104:107], v15, s[12:15], s28 offen offset:0 sc1\n\t"
    "buffer_load_dwordx4 v[108:111], v15, s[12:15], s28 offen offset:1024 sc1\n\t"
    "buffer_load_dwordx4 v[112:115], v15, s[12:15], s28 offen offset:2048 sc1\n\t"
    "buffer_load_dwordx4 v[116:119], v15, s[12:15], s28 offen offset:3072 sc1\n\t"
    "s_add_u32 s28, s28, 4096\n\t"
    "buffer_load_dwordx4 v[120:123], v15, s[12:15], s28 offen offset:0 sc1\n\t"
    "buffer_load_dwordx4 v[124:127], v15, s[12:15], s28 offen offset:1024 sc1\n\t"
    "buffer_load_dwordx4 v[128:131], v15, s[12:15], s28 offen offset:2048 sc1\n\t"
    "buffer_load_dwordx4 v[132:135], v15, s[12:15], s28 offen offset:3072 sc1\n\t"
    "s_add_u32 s28, s28, 4096\n\t"
    "buffer_load_dwordx4 v[136:139], v15, s[12:15], s28 offen offset:0 sc1\n\t"
    "buffer_load_dwordx4 v[140:143], v15, s[12:15], s28 offen offset:1024 sc1\n\t"
    "buffer_load_dwordx4 v[144:147], v15, s[12:15], s28 offen offset:2048 sc1\n\t"
    "buffer_load_dwordx4 v[148:151], v15, s[12:15], s28 offen offset:3072 sc1\n\t"
    "s_add_u32 s28, s28, 4096\n\t"
    "buffer_load_dwordx4 v[152:155], v15, s[12:15], s28 offen offset:0 sc1\n\t"
    "buffer_load_dwordx4 v[156:159], v15, s[12:15], s28 offen offset:1024 sc1\n\t"
    "s_add_u32 s35, s35, 1\n\t"
    "s_waitcnt vmcnt(0)\n\t"
    "v_max_u32 v8, v88, v92\n\t"
    "v_max3_u32 v8, v96, v100, v8\n\t"
    "v_max3_u32 v8, v104, v108, v8\n\t"
    "v_max3_u32 v8, v112, v116, v8\n\t"
    "v_max3_u32 v8, v120, v124, v8\n\t"
    "v_max3_u32 v8, v128, v132, v8\n\t"
    "v_max3_u32 v8, v136, v140, v8\n\t"
    "v_max3_u32 v8, v144, v148, v8\n\t"
    "v_max3_u32 v8, v152, v156, v8\n\t"
    "v_cmp_eq_u32 vcc, -1, v8\n\t"
    "s_cbranch_vccz L_ib1_%=\n\t"
    "s_cmp_lt_u32 s35, 0x8000\n\t"
    "s_cbranch_scc1 L_iq1_%=\n\t"
    "s_branch L_ib1_%=\n\t"
    "L_gsv_%=:\n\t"
    "s_mov_b32 s35, 0\n\t"
    "L_gqv_%=:\n\t"
    "s_sleep 4\n\t"
    "global_load_dwordx4 v[72:75], v9, s[36:37] offset:0 sc1\n\t"
    "global_load_dwordx4 v[76:79], v9, s[36:37] offset:16 sc1\n\t"
    "global_load_dwordx4 v[80:83], v9, s[36:37] offset:32 sc1\n\t"
    "global_load_dwordx4 v[84:87], v9, s[36:37] offset:48 sc1\n\t"
    "global_load_dwordx4 v[88:91], v9, s[36:37] offset:64 sc1\n\t"
    "global_load_dwordx4 v[92:95], v9, s[36:37] offset:80 sc1\n\t"
    "global_load_dwordx4 v[96:99], v9, s[36:37] offset:96 sc1\n\t"
    "global_load_dwordx4 v[100:103], v9, s[36:37] offset:112 sc1\n\t"
    "global_load_dwordx4 v[104:107], v9, s[36:37] offset:128 sc1\n\t"
    "global_load_dwordx4 v[108:111], v9, s[36:37] offset:144 sc1\n\t"
    "global_load_dwordx4 v[112:115], v9, s[36:37] offset:160 sc1\n\t"
    "s_add_u32 s35, s35, 1\n\t"
    "s_waitcnt vmcnt(0)\n\t"
    "s_cmp_gt_u32 s35, 0x8000\n\t"
    "s_cbranch_scc1 L_gbv_%=\n\t"
    "v_cmpx_eq_u32 vcc, s66, v75\n\t"
    "v_cmpx_eq_u32 vcc, s66, v79\n\t"
    "v_cmpx_eq_u32 vcc, s66, v83\n\t"
    "v_cmpx_eq_u32 vcc, s66, v87\n\t"
    "v_cmpx_eq_u32 vcc, s66, v91\n\t"
    "v_cmpx_eq_u32 vcc, s66, v95\n\t"
    "v_cmpx_eq_u32 vcc, s66, v99\n\t"
    "v_cmpx_eq_u32 vcc, s66, v103\n\t"
    "v_cmpx_eq_u32 vcc, s66, v107\n\t"
    "v_cmpx_eq_u32 vcc, s66, v111\n\t"
    "v_cmpx_eq_u32 vcc, s66, v115\n\t"
    "s_cmp_eq_u64 exec, -1\n\t"
    "s_mov_b64 exec, -1\n\t"
    "s_cbranch_scc0 L_gqv_%=\n\t"
    "s_branch L_gbv_%=\n\t"
    "L_pse_%=:\n\t"
    "s_mov_b32 s28, 0\n\t"
    "L_pqe_%=:\n\t"
    "s_sleep 6\n\t"
    "global_load_dwordx2 v[130:131], v128, s[36:37] offset:0 sc1\n\t"
    "s_add_u32 s28, s28, 1\n\t"
    "s_waitcnt vmcnt(0)\n\t"
    "s_cmp_gt_u32 s28, 0x10000\n\t"
    "s_cbranch_scc1 L_pbe_%=\n\t"
    "v_cmp_ne_u64 vcc, s[66:67], v[130:131]\n\t"
    "s_cbranch_vccnz L_pqe_%=\n\t"
    "s_branch L_pbe_%=\n\t"
    "L_psm_%=:\n\t"
    "s_mov_b32 s28, 0\n\t"
    "L_pqm_%=:\n\t"
    "s_sleep 6\n\t"
    "global_load_dwordx2 v[124:125], v126, s[36:37] offset:0 sc1\n\t"
    "s_add_u32 s28, s28, 1\n\t"
    "s_waitcnt vmcnt(0)\n\t"
    "s_cmp_gt_u32 s28, 0x10000\n\t"
    "s_cbranch_scc1 L_pbm_%=\n\t"
    "v_cmp_ne_u64 vcc, s[66:67], v[124:125]\n\t"
    "s_cbranch_vccnz L_pqm_%=\n\t"
    "s_branch L_pbm_%=\n\t"
    "L_psf_%=:\n\t"
    "s_mov_b32 s28, 0\n\t"
    "L_pqf_%=:\n\t"
    "s_sleep 6\n\t"
    "global_load_dwordx2 v[124:125], v126, s[36:37] offset:0 sc1\n\t"
    "s_add_u32 s28, s28, 1\n\t"
    "s_waitcnt vmcnt(0)\n\t"
    "s_cmp_gt_u32 s28, 0x10000\n\t"
    "s_cbranch_scc1 L_pbf_%=\n\t"
    "v_cmp_ne_u64 vcc, s[66:67], v[124:125]\n\t"
    "s_cbranch_vccnz L_pqf_%=\n\t"
    "s_branch L_pbf_%=\n\t"
    "L_ss1a_%=:\n\t"
    "s_mov_b32 s28, 0\n\t"
    "L_sq1a_%=:\n\t"
    "global_load_dwordx4 v[48:51], v5, s[36:37] offset:-3584 sc1\n\t"
    "s_add_u32 s28, s28, 1\n\t"
    "s_waitcnt vmcnt(0)\n\t"
    "s_cmp_gt_u32 s28, 0x10000\n\t"
    "s_cbranch_scc1 L_sr1a_%=\n\t"
    "v_cmp_ne_u32 vcc, s66, v49\n\t"
    "v_cmp_ne_u32 s[30:31], s67, v51\n\t"
    "s_or_b64 vcc, vcc, s[30:31]\n\t"
    "s_cbranch_vccnz L_sq1a_%=\n\t"
    "s_branch L_sr1a_%=\n\t"
    "L_ss01_%=:\n\t"
    "s_mov_b32 s28, 0\n\t"
    "L_sq01_%=:\n\t"
    "global_load_dwordx4 v[52:55], v5, s[36:37] offset:-3584 sc1\n\t"
    "s_add_u32 s28, s28, 1\n\t"
    "s_waitcnt vmcnt(0)\n\t"
    "s_cmp_gt_u32 s28, 0x10000\n\t"
    "s_cbranch_scc1 L_sr01_%=\n\t"
    "v_cmp_ne_u32 vcc, s66, v53\n\t"
    "v_cmp_ne_u32 s[30:31], s67, v55\n\t"
    "s_or_b64 vcc, vcc, s[30:31]\n\t"
    "s_cbranch_vccnz L_sq01_%=\n\t"
    "s_branch L_sr01_%=\n\t"
    "L_ss12_%=:\n\t"
    "s_mov_b32 s28, 0\n\t"
    "L_sq12_%=:\n\t"
    "global_load_dwordx4 v[48:51], v5, s[36:37] offset:-3584 sc1\n\t"
    "s_add_u32 s28, s28, 1\n\t"
    "s_waitcnt vmcnt(0)\n\t"
    "s_cmp_gt_u32 s28, 0x10000\n\t"
    "s_cbranch_scc1 L_sr12_%=\n\t"
    "v_cmp_ne_u32 vcc, s66, v49\n\t"
    "v_cmp_ne_u32 s[30:31], s67, v51\n\t"
    "s_or_b64 vcc, vcc, s[30:31]\n\t"
    "s_cbranch_vccnz L_sq12_%=\n\t"
    "s_branch L_sr12_%=\n\t"
    "L_ss03_%=:\n\t"
    "s_mov_b32 s28, 0\n\t"
    "L_sq03_%=:\n\t"
    "global_load_dwordx4 v[52:55], v5, s[36:37] offset:-3584 sc1\n\t"
    "s_add_u32 s28, s28, 1\n\t"
    "s_waitcnt vmcnt(0)\n\t"
    "s_cmp_gt_u32 s28, 0x10000\n\t"
    "s_cbranch_scc1 L_sr03_%=\n\t"
    "v_cmp_ne_u32 vcc, s66, v53\n\t"
    "v_cmp_ne_u32 s[30:31], s67, v55\n\t"
    "s_or_b64 vcc, vcc, s[30:31]\n\t"
    "s_cbranch_vccnz L_sq03_%=\n\t"
    "s_branch L_sr03_%=\n\t"
    "L_ss14_%=:\n\t"
    "s_mov_b32 s28, 0\n\t"
    "L_sq14_%=:\n\t"
    "global_load_dwordx4 v[48:51], v5, s[36:37] offset:-1792 sc1\n\t"
    "s_add_u32 s28, s28, 1\n\t"
    "s_waitcnt vmcnt(0)\n\t"
    "s_cmp_gt_u32 s28, 0x10000\n\t"
    "s_cbranch_scc1 L_sr14_%=\n\t"
    "v_cmp_ne_u32 vcc, s66, v49\n\t"
    "v_cmp_ne_u32 s[30:31], s67, v51\n\t"
    "s_or_b64 vcc, vcc, s[30:31]\n\t"
    "s_cbranch_vccnz L_sq14_%=\n\t"
    "s_branch L_sr14_%=\n\t"
    "L_ss09_%=:\n\t"
    "s_mov_b32 s28, 0\n\t"
    "L_sq09_%=:\n\t"
    "global_load_dwordx4 v[52:55], v5, s[36:37] offset:-3584 sc1\n\t"
    "s_add_u32 s28, s28, 1\n\t"
    "s_waitcnt vmcnt(0)\n\t"
    "s_cmp_gt_u32 s28, 0x10000\n\t"
    "s_cbranch_scc1 L_sr09_%=\n\t"
    "v_cmp_ne_u32 vcc, s66, v53\n\t"
    "v_cmp_ne_u32 s[30:31], s67, v55\n\t"
    "s_or_b64 vcc, vcc, s[30:31]\n\t"
    "s_cbranch_vccnz L_sq09_%=\n\t"
    "s_branch L_sr09_%=\n\t"
    "L_ss110_%=:\n\t"
    "s_mov_b32 s28, 0\n\t"
    "L_sq110_%=:\n\t"
    "global_load_dwordx4 v[48:51], v5, s[36:37] offset:-3584 sc1\n\t"
    "s_add_u32 s28, s28, 1\n\t"
    "s_waitcnt vmcnt(0)\n\t"
    "s_cmp_gt_u32 s28, 0x10000\n\t"
    "s_cbranch_scc1 L_sr110_%=\n\t"
    "v_cmp_ne_u32 vcc, s66, v49\n\t"
    "v_cmp_ne_u32 s[30:31], s67, v51\n\t"
    "s_or_b64 vcc, vcc, s[30:31]\n\t"
    "s_cbranch_vccnz L_sq110_%=\n\t"
    "s_branch L_sr110_%=\n\t"
    "L_ss05_%=:\n\t"
    "s_mov_b32 s28, 0\n\t"
    "L_sq05_%=:\n\t"
    "global_load_dwordx4 v[52:55], v5, s[36:37] offset:-3584 sc1\n\t"
    "s_add_u32 s28, s28, 1\n\t"
    "s_waitcnt vmcnt(0)\n\t"
    "s_cmp_gt_u32 s28, 0x10000\n\t"
    "s_cbranch_scc1 L_sr05_%=\n\t"
    "v_cmp_ne_u32 vcc, s66, v53\n\t"
    "v_cmp_ne_u32 s[30:31], s67, v55\n\t"
    "s_or_b64 vcc, vcc, s[30:31]\n\t"
    "s_cbranch_vccnz L_sq05_%=\n\t"
    "s_branch L_sr05_%=\n\t"
    "L_ss16_%=:\n\t"
    "s_mov_b32 s28, 0\n\t"
    "L_sq16_%=:\n\t"
    "global_load_dwordx4 v[48:51], v5, s[36:37] offset:-3584 sc1\n\t"
    "s_add_u32 s28, s28, 1\n\t"
    "s_waitcnt vmcnt(0)\n\t"
    "s_cmp_gt_u32 s28, 0x10000\n\t"
    "s_cbranch_scc1 L_sr16_%=\n\t"
    "v_cmp_ne_u32 vcc, s66, v49\n\t"
    "v_cmp_ne_u32 s[30:31], s67, v51\n\t"
    "s_or_b64 vcc, vcc, s[30:31]\n\t"
    "s_cbranch_vccnz L_sq16_%=\n\t"
    "s_branch L_sr16_%=\n\t"
    "L_ss07_%=:\n\t"
    "s_mov_b32 s28, 0\n\t"
    "L_sq07_%=:\n\t"
    "global_load_dwordx4 v[52:55], v5, s[36:37] offset:-3584 sc1\n\t"
    "s_add_u32 s28, s28, 1\n\t"
    "s_waitcnt vmcnt(0)\n\t"
    "s_cmp_gt_u32 s28, 0x10000\n\t"
    "s_cbranch_scc1 L_sr07_%=\n\t"
    "v_cmp_ne_u32 vcc, s66, v53\n\t"
    "v_cmp_ne_u32 s[30:31], s67, v55\n\t"
    "s_or_b64 vcc, vcc, s[30:31]\n\t"
    "s_cbranch_vccnz L_sq07_%=\n\t"
    "s_branch L_sr07_%=\n\t"
    "L_ss18_%=:\n\t"
    "s_mov_b32 s28, 0\n\t"
    "L_sq18_%=:\n\t"
    "global_load_dwordx4 v[48:51], v5, s[36:37] offset:-3584 sc1\n\t"
    "s_add_u32 s28, s28, 1\n\t"
    "s_waitcnt vmcnt(0)\n\t"
    "s_cmp_gt_u32 s28, 0x10000\n\t"
    "s_cbranch_scc1 L_sr18_%=\n\t"
    "v_cmp_ne_u32 vcc, s66, v49\n\t"
    "v_cmp_ne_u32 s[30:31], s67, v51\n\t"
    "s_or_b64 vcc, vcc, s[30:31]\n\t"
    "s_cbranch_vccnz L_sq18_%=\n\t"
    "s_branch L_sr18_%=\n\t"
    "L_ss0_%=:\n\t"
    "s_mov_b32 s28, 0\n\t"
    "L_sq0_%=:\n\t"
    "global_load_dwordx4 v[52:55], v5, s[36:37] offset:-3584 sc1\n\t"
    "s_add_u32 s28, s28, 1\n\t"
    "s_waitcnt vmcnt(0)\n\t"
    "s_cmp_gt_u32 s28, 0x10000\n\t"
    "s_cbranch_scc1 L_sr0_%=\n\t"
    "v_cmp_ne_u32 vcc, s66, v53\n\t"
    "v_cmp_ne_u32 s[30:31], s67, v55\n\t"
    "s_or_b64 vcc, vcc, s[30:31]\n\t"
    "s_cbranch_vccnz L_sq0_%=\n\t"
    "s_branch L_sr0_%=\n\t"
    "L_ss1_%=:\n\t"
    "s_mov_b32 s28, 0\n\t"
    "L_sq1_%=:\n\t"
    "global_load_dwordx4 v[48:51], v5, s[36:37] offset:-3584 sc1\n\t"
    "s_add_u32 s28, s28, 1\n\t"
    "s_waitcnt vmcnt(0)\n\t"
    "s_cmp_gt_u32 s28, 0x10000\n\t"
    "s_cbranch_scc1 L_sr1_%=\n\t"
    "v_cmp_ne_u32 vcc, s66, v49\n\t"
    "v_cmp_ne_u32 s[30:31], s67, v51\n\t"
    "s_or_b64 vcc, vcc, s[30:31]\n\t"
    "s_cbranch_vccnz L_sq1_%=\n\t"
    "s_branch L_sr1_%=\n\t"
    "L_slA_%=:\n\t"
    "s_mov_b32 s28, 0\n\t"
    "L_spA_%=:\n\t"
    "s_mul_i32 s29, s68, 40960\n\t"
    "s_add_u32 s29, s29, 0\n\t"
    "buffer_load_dwordx4 v[144:147], v15, s[56:59], s29 offen offset:0 sc1\n\t"
    "buffer_load_dwordx4 v[148:151], v15, s[56:59], s29 offen offset:1024 sc1\n\t"
    "buffer_load_dwordx4 v[152:155], v15, s[56:59], s29 offen offset:2048 sc1\n\t"
    "buffer_load_dwordx4 v[156:159], v15, s[56:59], s29 offen offset:3072 sc1\n\t"
    "s_mul_i32 s29, s68, 40960\n\t"
    "s_add_u32 s29, s29, 4096\n\t"
    "buffer_load_dwordx4 v[168:171], v15, s[56:59], s29 offen offset:2048 sc1\n\t"
    "buffer_load_dwordx4 v[172:175], v15, s[56:59], s29 offen offset:3072 sc1\n\t"
    "s_mul_i32 s29, s68, 40960\n\t"
    "s_add_u32 s29, s29, 8192\n\t"
    "buffer_load_dwordx4 v[176:179], v15, s[56:59], s29 offen offset:0 sc1\n\t"
    "s_add_u32 s28, s28, 1\n\t"
    "s_waitcnt vmcnt(0)\n\t"
    "s_cmp_gt_u32 s28, 0x8000\n\t"
    "s_cbranch_scc1 L_bkA_%=\n\t"
    "v_max_u32 v220, v168, v170\n\t"
    "v_max3_u32 v220, v172, v174, v220\n\t"
    "v_max3_u32 v220, v176, v178, v220\n\t"
    "v_and_b32 v220, s77, v220\n\t"
    "v_max3_u32 v220, v144, v146, v220\n\t"
    "v_max3_u32 v220, v148, v150, v220\n\t"
    "v_max3_u32 v220, v152, v154, v220\n\t"
    "v_max3_u32 v220, v156, v158, v220\n\t"
    "v_cmp_eq_u32 vcc, -1, v220\n\t"
    "s_cbranch_vccnz L_spA_%=\n\t"
    "s_branch L_bkA_%=\n\t"
    "L_slB_%=:\n\t"
    "s_mov_b32 s28, 0\n\t"
    "L_spB_%=:\n\t"
    "s_mul_i32 s29, s68, 40960\n\t"
    "s_add_u32 s29, s29, 4096\n\t"
    "buffer_load_dwordx4 v[160:163], v15, s[56:59], s29 offen offset:0 sc1\n\t"
    "buffer_load_dwordx4 v[164:167], v15, s[56:59], s29 offen offset:1024 sc1\n\t"
    "s_add_u32 s28, s28, 1\n\t"
    "s_waitcnt vmcnt(0)\n\t"
    "s_cmp_gt_u32 s28, 0x8000\n\t"
    "s_cbranch_scc1 L_bkB_%=\n\t"
    "v_max_u32 v220, v160, v162\n\t"
    "v_max3_u32 v220, v164, v166, v220\n\t"
    "v_cmp_eq_u32 vcc, -1, v220\n\t"
    "s_cbranch_vccnz L_spB_%=\n\t"
    "s_branch L_bkB_%=\n\t"
    "L_psg_%=:\n\t"
    "s_mov_b32 s28, 0\n\t"
    "L_pqg_%=:\n\t"
    "s_sleep 6\n\t"
    "global_load_dwordx2 v[220:221], v213, s[36:37] offset:0 sc1\n\t"
    "s_add_u32 s28, s28, 1\n\t"
    "s_waitcnt vmcnt(0)\n\t"
    "s_cmp_gt_u32 s28, 0x10000\n\t"
    "s_cbranch_scc1 L_pbg_%=\n\t"
    "v_cmp_ne_u64 vcc, s[66:67], v[220:221]\n\t"
    "s_cbranch_vccnz L_pqg_%=\n\t"
    "s_branch L_pbg_%=\n\t"
    "L_ss017_%=:\n\t"
    "s_mov_b32 s28, 0\n\t"
    "L_sq017_%=:\n\t"
    "global_load_dwordx4 v[52:55], v5, s[36:37] offset:-3584 sc1\n\t"
    "s_add_u32 s28, s28, 1\n\t"
    "s_waitcnt vmcnt(0)\n\t"
    "s_cmp_gt_u32 s28, 0x10000\n\t"
    "s_cbranch_scc1 L_sr017_%=\n\t"
    "v_cmp_ne_u32 vcc, s66, v53\n\t"
    "v_cmp_ne_u32 s[30:31], s67, v55\n\t"
    "s_or_b64 vcc, vcc, s[30:31]\n\t"
    "s_cbranch_vccnz L_sq017_%=\n\t"
    "s_branch L_sr017_%=\n\t"
    "L_ss118_%=:\n\t"
    "s_mov_b32 s28, 0\n\t"
    "L_sq118_%=:\n\t"
    "global_load_dwordx4 v[48:51], v5, s[36:37] offset:-1792 sc1\n\t"
    "s_add_u32 s28, s28, 1\n\t"
    "s_waitcnt vmcnt(0)\n\t"
    "s_cmp_gt_u32 s28, 0x10000\n\t"
    "s_cbranch_scc1 L_sr118_%=\n\t"
    "v_cmp_ne_u32 vcc, s66, v49\n\t"
    "v_cmp_ne_u32 s[30:31], s67, v51\n\t"
    "s_or_b64 vcc, vcc, s[30:31]\n\t"
    "s_cbranch_vccnz L_sq118_%=\n\t"
    "s_branch L_sr118_%=\n\t"
    "L_psh_%=:\n\t"
    "s_mov_b32 s28, 0\n\t"
    "L_pqh_%=:\n\t"
    "s_sleep 6\n\t"
    "global_load_dwordx2 v[220:221], v216, s[36:37] offset:0 sc1\n\t"
    "s_add_u32 s28, s28, 1\n\t"
    "s_waitcnt vmcnt(0)\n\t"
    "s_cmp_gt_u32 s28, 0x10000\n\t"
    "s_cbranch_scc1 L_pbh_%=\n\t"
    "v_cmp_ne_u64 vcc, s[66:67], v[220:221]\n\t"
    "s_cbranch_vccnz L_pqh_%=\n\t"
    "s_branch L_pbh_%=\n\t"
    "L_psj_%=:\n\t"
    "s_mov_b32 s28, 0\n\t"
    "L_pqj_%=:\n\t"
    "s_sleep 6\n\t"
    "global_load_dwordx2 v[220:221], v216, s[36:37] offset:0 sc1\n\t"
    "s_add_u32 s28, s28, 1\n\t"
    "s_waitcnt vmcnt(0)\n\t"
    "s_cmp_gt_u32 s28, 0x10000\n\t"
    "s_cbranch_scc1 L_pbj_%=\n\t"
    "v_cmp_ne_u64 vcc, s[66:67], v[220:221]\n\t"
    "s_cbranch_vccnz L_pqj_%=\n\t"
    "s_branch L_pbj_%=\n\t"
    "L_ss015_%=:\n\t"
    "s_mov_b32 s28, 0\n\t"
    "L_sq015_%=:\n\t"
    "global_load_dwordx4 v[52:55], v5, s[36:37] offset:-3584 sc1\n\t"
    "s_add_u32 s28, s28, 1\n\t"
    "s_waitcnt vmcnt(0)\n\t"
    "s_cmp_gt_u32 s28, 0x10000\n\t"
    "s_cbranch_scc1 L_sr015_%=\n\t"
    "v_cmp_ne_u32 vcc, s66, v53\n\t"
    "v_cmp_ne_u32 s[30:31], s67, v55\n\t"
    "s_or_b64 vcc, vcc, s[30:31]\n\t"
    "s_cbranch_vccnz L_sq015_%=\n\t"
    "s_branch L_sr015_%=\n\t"
    "L_ss116_%=:\n\t"
    "s_mov_b32 s28, 0\n\t"
    "L_sq116_%=:\n\t"
    "global_load_dwordx4 v[48:51], v5, s[36:37] offset:-3584 sc1\n\t"
    "s_add_u32 s28, s28, 1\n\t"
    "s_waitcnt vmcnt(0)\n\t"
    "s_cmp_gt_u32 s28, 0x10000\n\t"
    "s_cbranch_scc1 L_sr116_%=\n\t"
    "v_cmp_ne_u32 vcc, s66, v49\n\t"
    "v_cmp_ne_u32 s[30:31], s67, v51\n\t"
    "s_or_b64 vcc, vcc, s[30:31]\n\t"
    "s_cbranch_vccnz L_sq116_%=\n\t"
    "s_branch L_sr116_%=\n\t"
    "L_psw_%=:\n\t"
    "s_mov_b32 s28, 0\n\t"
    "L_pqw_%=:\n\t"
    "s_sleep 6\n\t"
    "global_load_dwordx2 v[220:221], v218, s[36:37] offset:0 sc1\n\t"
    "s_add_u32 s28, s28, 1\n\t"
    "s_waitcnt vmcnt(0)\n\t"
    "s_cmp_gt_u32 s28, 0x10000\n\t"
    "s_cbranch_scc1 L_pbw_%=\n\t"
    "v_cmp_ne_u64 vcc, s[66:67], v[220:221]\n\t"
    "s_cbranch_vccnz L_pqw_%=\n\t"
    "s_branch L_pbw_%=\n\t"
    "L_psk_%=:\n\t"
    "s_mov_b32 s28, 0\n\t"
    "L_pqk_%=:\n\t"
    "s_sleep 6\n\t"
    "global_load_dwordx2 v[220:221], v218, s[36:37] offset:0 sc1\n\t"
    "s_add_u32 s28, s28, 1\n\t"
    "s_waitcnt vmcnt(0)\n\t"
    "s_cmp_gt_u32 s28, 0x10000\n\t"
    "s_cbranch_scc1 L_pbk_%=\n\t"
    "v_cmp_ne_u64 vcc, s[66:67], v[220:221]\n\t"
    "s_cbranch_vccnz L_pqk_%=\n\t"
    "s_branch L_pbk_%=\n\t"
    "L_end_%=:\n\t"
      : "+{v0}"(tid), "+{s[0:1]}"(ka), "+{s2}"(bid)
      : "s"((unsigned)(size_t)(__attribute__((address_space(3))) char*)lds)
      : "v1", "v2", "v3", "v4", "v5", "v6", "v7", "v8", "v9", "v10", "v11", "v12", "v13", "v14", "v15", "v16", "v17", "v18", "v19", "v20", "v21", "v22", "v23", "v24",
      "v25", "v26", "v27", "v28", "v29", "v30", "v31", "v32", "v33", "v34", "v35", "v36", "v37", "v38", "v39", "v40", "v41", "v42", "v43", "v44", "v45", "v46", "v47", "v48",
      "v49", "v50", "v51", "v52", "v53", "v54", "v55", "v56", "v57", "v58", "v59", "v60", "v61", "v62", "v63", "v64", "v65", "v66", "v67", "v68", "v69", "v70", "v71", "v72",
      "v73", "v74", "v75", "v76", "v77", "v78", "v79", "v80", "v81", "v82", "v83", "v84", "v85", "v86", "v87", "v88", "v89", "v90", "v91", "v92", "v93", "v94", "v95", "v96",
      "v97", "v98", "v99", "v100", "v101", "v102", "v103", "v104", "v105", "v106", "v107", "v108", "v109", "v110", "v111", "v112", "v113", "v114", "v115", "v116", "v117", "v118", "v119", "v120",
      "v121", "v122", "v123", "v124", "v125", "v126", "v127", "v128", "v129", "v130", "v131", "v132", "v133", "v134", "v135", "v136", "v137", "v138", "v139", "v140", "v141", "v142", "v143", "v144",
      "v145", "v146", "v147", "v148", "v149", "v150", "v151", "v152", "v153", "v154", "v155", "v156", "v157", "v158", "v159", "v160", "v161", "v162", "v163", "v164", "v165", "v166", "v167", "v168",
      "v169", "v170", "v171", "v172", "v173", "v174", "v175", "v176", "v177", "v178", "v179", "v180", "v181", "v182", "v183", "v184", "v185", "v186", "v187", "v188", "v189", "v190", "v191", "v192",
      "v193", "v194", "v195", "v196", "v197", "v198", "v199", "v200", "v201", "v202", "v203", "v204", "v205", "v206", "v207", "v208", "v209", "v210", "v211", "v212", "v213", "v214", "v215", "v216",
      "v217", "v218", "v219", "v220", "v221", "v222", "v223", "v224", "v225", "v226", "v227", "v228", "v229", "v230", "v231", "v232", "v233", "v234", "v235", "v236", "v237", "v238", "v239", "v240",
      "v241", "v242", "v243", "v244", "v245", "v246", "v247", "v248", "v249", "v250", "v251", "v252", "v253", "v254", "v255", "a0", "a1", "a2", "a3", "a4", "a5", "a6", "a7", "a8",
      "a9", "a10", "a11", "a12", "a13", "a14", "a15", "a16", "a17", "a18", "a19", "a20", "a21", "a22", "a23", "a24", "a25", "a26", "a27", "a28", "a29", "a30", "a31", "a32",
      "a33", "a34", "a35", "a36", "a37", "a38", "a39", "a40", "a41", "a42", "a43", "a44", "a45", "a46", "a47", "a48", "a49", "a50", "a51", "a52", "a53", "a54", "a55", "a56",
      "a57", "a58", "a59", "a60", "a61", "a62", "a63", "a64", "a65", "a66", "a67", "a68", "a69", "a70", "a71", "a72", "a73", "a74", "a75", "a76", "a77", "a78", "a79", "a80",
      "a81", "a82", "a83", "a84", "a85", "a86", "a87", "a88", "a89", "a90", "a91", "a92", "a93", "a94", "a95", "a96", "a97", "a98", "a99", "a100", "a101", "a102", "a103", "a104",
      "a105", "a106", "a107", "a108", "a109", "a110", "a111", "a112", "a113", "a114", "a115", "a116", "a117", "a118", "a119", "a120", "a121", "a122", "a123", "a124", "a125", "a126", "a127", "a128",
      "a129", "a130", "a131", "a132", "a133", "a134", "a135", "a136", "a137", "a138", "a139", "a140", "a141", "a142", "a143", "a144", "a145", "a146", "a147", "a148", "a149", "a150", "a151", "a152",
      "a153", "a154", "a155", "a156", "a157", "a158", "a159", "a160", "a161", "a162", "a163", "a164", "a165", "a166", "a167", "a168", "a169", "a170", "a171", "a172", "a173", "a174", "a175", "a176",
      "a177", "a178", "a179", "a180", "a181", "a182", "a183", "a184", "a185", "a186", "a187", "a188", "a189", "a190", "a191", "a192", "a193", "a194", "a195", "a196", "a197", "a198", "a199", "a200",
      "a201", "a202", "a203", "a204", "a205", "a206", "a207", "a208", "a209", "a210", "a211", "a212", "a213", "a214", "a215", "a216", "a217", "a218", "a219", "a220", "a221", "a222", "a223", "a224",
      "a225", "a226", "a227", "a228", "a229", "a230", "a231", "a232", "a233", "a234", "a235", "a236", "a237", "a238", "a239", "a240", "a241", "a242", "a243", "a244", "a245", "a246", "a247", "a248",
      "a249", "a250", "a251", "a252", "a253", "a254", "a255", "s3", "s4", "s5", "s6", "s7", "s8", "s9", "s10", "s11", "s12", "s13", "s14", "s15", "s16", "s17", "s18", "s19",
      "s20", "s21", "s22", "s23", "s24", "s25", "s26", "s27", "s28", "s29", "s30", "s31", "s33", "s34", "s35", "s36", "s37", "s38", "s39", "s40", "s41", "s42", "s43", "s44",
      "s45", "s46", "s47", "s48", "s49", "s50", "s51", "s52", "s53", "s54", "s55", "s56", "s57", "s58", "s59", "s60", "s61", "s62", "s63", "s64", "s65", "s66", "s67", "s68",
      "s69", "s70", "s71", "s72", "s73", "s74", "s75", "s76", "s77", "s78", "s79", "s80", "s81", "s82", "s83", "s84", "s85", "s86", "s87", "s88", "s89", "s90", "s91", "s92",
      "s93", "s94", "vcc", "scc", "memory");
#endif
}

extern "C" void kernel_launch(void* const* d_in, const int* in_sizes, int n_in, void* d_out, int out_size,
                              void* d_ws, size_t ws_size, hipStream_t stream) {
  k_mega<<<256, 256, 0, stream>>>(d_in[0], d_in[1], d_in[2], d_in[3], d_in[4], d_in[5], d_in[6], d_in[7], d_in[8], d_in[9], d_in[10], d_in[11], d_in[12], d_in[13], d_in[14], d_out, d_ws);
}
```
